```python
import torch

def _softmax(x, axis=-1):
    return torch.softmax(x, dim=axis)

def _gelu(x):
    return torch.nn.functional.gelu(x, approximate='tanh')

def _silu(x):
    return torch.nn.functional.silu(x)

def _sigmoid(x):
    return torch.sigmoid(x)

def _layernorm(x, axis=-1, eps=1e-05):
    m = x.mean(dim=axis, keepdim=True)
    v = x.var(dim=axis, keepdim=True, unbiased=False)
    return (x - m) / torch.sqrt(v + eps)

def _scatter_add(tgt, idx, src, axis=0):
    return tgt.clone().index_add_(axis, idx, src)

def _segment_sum(src, seg, n):
    out = torch.zeros((n,) + src.shape[1:], dtype=src.dtype)
    return out.index_add_(0, seg, src)

def _pad(x, pw):
    flat = tuple((v for pair in reversed(pw) for v in pair))
    return torch.nn.functional.pad(x, flat)
kernel_name = 'adaLN_cond__grouped_kv_mqa__swiglu_ffn__prompt'

def setup_inputs():

    _w = lambda a: (lambda t: t.to(torch.bfloat16) if t.is_floating_point() else t)(torch.as_tensor(a))

    def _A_setup():
        B, S, D, H = (1, 2048, 576, 32)
        Dh = D // H
        return {'x': _w((lambda *s: torch.randn(*s, dtype=torch.bfloat16) * 0.05)(B, S, D)), 'wq': _w((lambda *s: torch.randn(*s, dtype=torch.bfloat16) * 0.05)(D, H * Dh)), 'wk': _w((lambda *s: torch.randn(*s, dtype=torch.bfloat16) * 0.05)(D, Dh)), 'wv': _w((lambda *s: torch.randn(*s, dtype=torch.bfloat16) * 0.05)(D, Dh)), 'wo': _w((lambda *s: torch.randn(*s, dtype=torch.bfloat16) * 0.05)(H * Dh, D))}
    _A_in = _A_setup()

    def _B_setup():
        B, S, D, F = (1, 2048, 576, 4608)
        return {'x': _w((lambda *s: torch.randn(*s, dtype=torch.bfloat16) * 0.05)(B, S, D)), 'w_gate': _w((lambda *s: torch.randn(*s, dtype=torch.bfloat16) * 0.05)(D, F)), 'w_up': _w((lambda *s: torch.randn(*s, dtype=torch.bfloat16) * 0.05)(D, F)), 'w_down': _w((lambda *s: torch.randn(*s, dtype=torch.bfloat16) * 0.05)(F, D))}
    _B_in = _B_setup()
    return {'A_x': _A_in['x'], 'A_wq': _A_in['wq'], 'A_wk': _A_in['wk'], 'A_wv': _A_in['wv'], 'A_wo': _A_in['wo'], 'B_x': _B_in['x'], 'B_w_gate': _B_in['w_gate'], 'B_w_up': _B_in['w_up'], 'B_w_down': _B_in['w_down'], 'c': _w((lambda *s: torch.randn(*s, dtype=torch.bfloat16) * 0.1)(1, 576) * 8.0), 'w_mod': _w((lambda *s: torch.randn(*s, dtype=torch.bfloat16) * 0.1)(576, 6 * 576) * 576 ** (-0.5) * 8.0)}

def reference(A_x, A_wq, A_wk, A_wv, A_wo, B_x, B_w_gate, B_w_up, B_w_down, c, w_mod):

    def _A_ref(x, wq, wk, wv, wo):
        B, S, D = x.shape
        H = 32
        Dh = D // H
        q = (lambda x, *d: x.permute(*d))((x @ wq).reshape(B, S, H, Dh), 0, 2, 1, 3)
        k = x @ wk
        v = x @ wv
        scores = torch.einsum('bhqd,bkd->bhqk', q, k) * Dh ** (-0.5)
        p = _softmax(scores, axis=-1)
        o = torch.einsum('bhqk,bkd->bhqd', p, v)
        return (lambda x, *d: x.permute(*d))(o, 0, 2, 1, 3).reshape(B, S, H * Dh) @ wo

    def _B_ref(x, w_gate, w_up, w_down):
        g = _silu(x @ w_gate)
        u = x @ w_up
        return g * u @ w_down
    x = A_x
    mod = (_silu(c) @ w_mod).reshape(c.shape[0], 6, 1, -1)
    sa, ca, ga, sb, cb, gb = (mod[:, 0].clone(), mod[:, 1].clone(), mod[:, 2].clone(), mod[:, 3].clone(), mod[:, 4].clone(), mod[:, 5].clone())
    h = _layernorm(x, axis=-1) * (1 + ca) + sa
    x = x + ga * _A_ref(h, A_wq, A_wk, A_wv, A_wo)
    h = _layernorm(x, axis=-1) * (1 + cb) + sb
    x = x + gb * _B_ref(h, B_w_gate, B_w_up, B_w_down)
    return x
```

```cpp
#include <hip/hip_runtime.h>
#include <hip/hip_bf16.h>
typedef unsigned short u16;
__device__ __forceinline__ float bf2f(u16 v){ return __uint_as_float(((unsigned)v)<<16); }
__device__ __forceinline__ u16 f2bf(float f){ unsigned u=__float_as_uint(f); u += 0x7fffu + ((u>>16)&1u); return (u16)(u>>16); }
constexpr int S=2048, D=576, H=32, DH=18, F=4608;

__global__ void k_mod(const u16* c, const u16* wmod, float* mod){
  int n = blockIdx.x*blockDim.x+threadIdx.x; if(n>=6*D) return;
  float acc=0; for(int k=0;k<D;++k){ float cv=bf2f(c[k]); float s=cv/(1.f+__expf(-cv)); acc+=s*bf2f(wmod[k*6*D+n]); }
  mod[n]=acc;
}
__global__ void k_ln(const float* x, const float* shift, const float* scale, float* h){
  int r=blockIdx.x; __shared__ float red[2][64];
  float s=0; for(int i=threadIdx.x;i<D;i+=64) s+=x[r*D+i];
  for(int o=32;o>0;o>>=1) s+=__shfl_xor(s,o);
  float m=s/D; float v=0; for(int i=threadIdx.x;i<D;i+=64){ float d=x[r*D+i]-m; v+=d*d; }
  for(int o=32;o>0;o>>=1) v+=__shfl_xor(v,o);
  float rs=rsqrtf(v/D+1e-5f);
  for(int i=threadIdx.x;i<D;i+=64) h[r*D+i]=(x[r*D+i]-m)*rs*(1.f+scale[i])+shift[i];
}
__global__ void k_cvt(const u16* x, float* y, int n){ int i=blockIdx.x*blockDim.x+threadIdx.x; if(i<n) y[i]=bf2f(x[i]); }
__global__ void k_gemm(const float* A, const u16* W, float* C, int M, int N, int K){
  __shared__ float As[16][17]; __shared__ float Ws[16][17];
  int tx=threadIdx.x&15, ty=threadIdx.x>>4; int row=blockIdx.y*16+ty, col=blockIdx.x*16+tx; float acc=0;
  for(int k0=0;k0<K;k0+=16){
    As[ty][tx]=A[row*K+k0+tx];
    Ws[ty][tx]= col<N ? bf2f(W[(k0+ty)*N+col]) : 0.f;
    __syncthreads();
    for(int k=0;k<16;++k) acc+=As[ty][k]*Ws[k][tx];
    __syncthreads();
  }
  if(col<N) C[row*N+col]=acc;
}
__global__ void k_attn(const float* q, const float* k, const float* v, float* o){
  int idx=blockIdx.x*blockDim.x+threadIdx.x; int t=idx/H, h=idx%H;
  float qq[DH]; for(int d=0;d<DH;++d) qq[d]=q[t*D+h*DH+d]*0.23570226039551584f;
  float m=-1e30f,l=0; float acc[DH]; for(int d=0;d<DH;++d) acc[d]=0;
  for(int j=0;j<S;++j){ float s=0; for(int d=0;d<DH;++d) s+=qq[d]*k[j*DH+d];
    float mn=fmaxf(m,s); float a=__expf(m-mn), p=__expf(s-mn); l=l*a+p; for(int d=0;d<DH;++d) acc[d]=acc[d]*a+p*v[j*DH+d]; m=mn; }
  for(int d=0;d<DH;++d) o[t*D+h*DH+d]=acc[d]/l;
}
__global__ void k_res(const float* x, const float* g, const float* y, float* out){ int i=blockIdx.x*blockDim.x+threadIdx.x; if(i<S*D) out[i]=x[i]+g[i%D]*y[i]; }
__global__ void k_swiglu(const float* g, const float* u, float* a){ int i=blockIdx.x*blockDim.x+threadIdx.x; if(i<S*F){ float gv=g[i]; a[i]=gv/(1.f+__expf(-gv))*u[i]; } }
__global__ void k_out(const float* x, const float* g, const float* y, u16* out){ int i=blockIdx.x*blockDim.x+threadIdx.x; if(i<S*D) out[i]=f2bf(x[i]+g[i%D]*y[i]); }

extern "C" void kernel_launch(void* const* d_in, const int* in_sizes, int n_in, void* d_out, int out_size, void* d_ws, size_t ws_size, hipStream_t stream){
  const u16* x=(const u16*)d_in[0]; const u16* wq=(const u16*)d_in[1]; const u16* wk=(const u16*)d_in[2]; const u16* wv=(const u16*)d_in[3]; const u16* wo=(const u16*)d_in[4];
  const u16* wg=(const u16*)d_in[6]; const u16* wu=(const u16*)d_in[7]; const u16* wd=(const u16*)d_in[8]; const u16* c=(const u16*)d_in[9]; const u16* wmod=(const u16*)d_in[10];
  float* ws=(float*)d_ws; size_t o=0; auto alloc=[&](size_t n){ float* p=ws+o; o+=(n+63)/64*64; return p; };
  float* mod=alloc(6*D); float* xf=alloc(S*D); float* h=alloc(S*D); float* q=alloc(S*D); float* k=alloc(S*DH); float* v=alloc(S*DH); float* oo=alloc(S*D); float* ao=alloc(S*D); float* x1=alloc(S*D);
  float* g=alloc((size_t)S*F); float* u=alloc((size_t)S*F); float* a=alloc((size_t)S*F); float* y=alloc(S*D);
  k_mod<<<(6*D+63)/64,64,0,stream>>>(c,wmod,mod);
  k_cvt<<<S*D/256,256,0,stream>>>(x,xf,S*D);
  k_ln<<<S,64,0,stream>>>(xf,mod+0*D,mod+1*D,h);
  k_gemm<<<dim3(D/16,S/16),256,0,stream>>>(h,wq,q,S,D,D);
  k_gemm<<<dim3(2,S/16),256,0,stream>>>(h,wk,k,S,DH,D);
  k_gemm<<<dim3(2,S/16),256,0,stream>>>(h,wv,v,S,DH,D);
  k_attn<<<S*H/64,64,0,stream>>>(q,k,v,oo);
  k_gemm<<<dim3(D/16,S/16),256,0,stream>>>(oo,wo,ao,S,D,D);
  k_res<<<S*D/256,256,0,stream>>>(xf,mod+2*D,ao,x1);
  k_ln<<<S,64,0,stream>>>(x1,mod+3*D,mod+4*D,h);
  k_gemm<<<dim3(F/16,S/16),256,0,stream>>>(h,wg,g,S,F,D);
  k_gemm<<<dim3(F/16,S/16),256,0,stream>>>(h,wu,u,S,F,D);
  k_swiglu<<<S*F/256,256,0,stream>>>(g,u,a);
  k_gemm<<<dim3(D/16,S/16),256,0,stream>>>(a,wd,y,S,D,F);
  k_out<<<S*D/256,256,0,stream>>>(x1,mod+5*D,y,(u16*)d_out);
}
```

```cpp
#include <hip/hip_runtime.h>
#include <hip/hip_bf16.h>

typedef unsigned short u16;
using bf16x8 = __attribute__((ext_vector_type(8))) __bf16;
using bf16x2 = __attribute__((ext_vector_type(2))) __bf16;
using s16x4  = __attribute__((ext_vector_type(4))) short;
using f32x2  = __attribute__((ext_vector_type(2))) float;
using f32x4  = __attribute__((ext_vector_type(4))) float;
using f32x16 = __attribute__((ext_vector_type(16))) float;
using u32x2  = __attribute__((ext_vector_type(2))) unsigned;
using u32x4  = __attribute__((ext_vector_type(4))) unsigned;
#define DI __device__ __forceinline__
#define LDSP __attribute__((address_space(3)))
#define SBAR() __builtin_amdgcn_sched_barrier(0)
#define PIN(x) asm volatile("" : "+v"(x))
#define BC8(x) __builtin_bit_cast(bf16x8, (x))
#define MFMA16(a, b, c) __builtin_amdgcn_mfma_f32_16x16x32_bf16(BC8(a), BC8(b), (c), 0, 0, 0)
#define MFMA32(a, b, c) __builtin_amdgcn_mfma_f32_32x32x16_bf16(BC8(a), BC8(b), (c), 0, 0, 0)
#define MFMA32K8(a, b, c) __builtin_amdgcn_mfma_f32_32x32x8bf16_1k(__builtin_bit_cast(s16x4, (a)), __builtin_bit_cast(s16x4, (b)), (c), 0, 0, 0)
#define WAITVM(n) asm volatile("s_waitcnt vmcnt(" #n ")" ::: "memory")
#define BARRIER() asm volatile("s_waitcnt lgkmcnt(0)\n\ts_barrier" ::: "memory")

constexpr int S = 2048, D = 576, F = 4608;
constexpr size_t OFF_MOD  = 0;
constexpr size_t OFF_KMAX = 16384;
constexpr size_t OFF_WQKV = 32768;
constexpr size_t OFF_WO   = OFF_WQKV + 40 * 18 * 1024;
constexpr size_t OFF_Q    = OFF_WO + 18 * 36 * 1024;
constexpr size_t OFF_K    = OFF_Q + (size_t)S * D * 2;
constexpr size_t OFF_K1   = OFF_K + 65536;
constexpr size_t OFF_V    = OFF_K1 + 8192;
constexpr size_t OFF_X1   = OFF_V + 64 * 4 * 304;
constexpr size_t OFF_H2   = OFF_X1 + (size_t)S * D * 2;
constexpr size_t OFF_WGU  = OFF_H2 + (size_t)S * D * 2;
constexpr size_t OFF_WD   = OFF_WGU + (size_t)288 * 36864;
constexpr size_t OFF_ACT  = OFF_WD + (size_t)4 * 144 * 9216;
constexpr size_t OFF_PART = OFF_ACT + (size_t)128 * 144 * 1024;
constexpr size_t OFF_DBG  = OFF_PART + (size_t)4 * 128 * 36 * 1024;
constexpr size_t WS_END   = OFF_DBG + 8 * 1024 * 256;
#define STAMP(kid, i) do {} while (0)

DI float bf2f(u16 v) { return __uint_as_float(((unsigned)v) << 16); }
DI float bflo(unsigned v) { return __uint_as_float(v << 16); }
DI float bfhi(unsigned v) { return __uint_as_float(v & 0xffff0000u); }
DI unsigned pk(float a, float b) { f32x2 v = {a, b}; return __builtin_bit_cast(unsigned, __builtin_convertvector(v, bf16x2)); }
DI float rbf(float a) { return bflo(pk(a, a)); }
DI int rfl(int x) { return __builtin_amdgcn_readfirstlane(x); }
template <int CTRL> DI float dpp(float x) { return __int_as_float(__builtin_amdgcn_update_dpp(0, __float_as_int(x), CTRL, 0xf, 0xf, false)); }
DI float row16_sum(float x) { x += dpp<0x128>(x); x += dpp<0x124>(x); x += dpp<0x122>(x); x += dpp<0x121>(x); return x; }
DI float row16_max(float x) { x = fmaxf(x, dpp<0x128>(x)); x = fmaxf(x, dpp<0x124>(x)); x = fmaxf(x, dpp<0x122>(x)); x = fmaxf(x, dpp<0x121>(x)); return x; }
DI float xor16(float x) { auto r = __builtin_amdgcn_permlane16_swap(__float_as_uint(x), __float_as_uint(x), false, false); return __uint_as_float(r[0]) + __uint_as_float(r[1]); }
DI float xor32(float x) { auto r = __builtin_amdgcn_permlane32_swap(__float_as_uint(x), __float_as_uint(x), false, false); return __uint_as_float(r[0]) + __uint_as_float(r[1]); }
DI float max16(float x) { auto r = __builtin_amdgcn_permlane16_swap(__float_as_uint(x), __float_as_uint(x), false, false); return fmaxf(__uint_as_float(r[0]), __uint_as_float(r[1])); }
DI float max32(float x) { auto r = __builtin_amdgcn_permlane32_swap(__float_as_uint(x), __float_as_uint(x), false, false); return fmaxf(__uint_as_float(r[0]), __uint_as_float(r[1])); }

template <int N> DI void glds(const void* base, unsigned voff, unsigned lds) {
  unsigned sv;
  if constexpr (N == 1)
    asm volatile("s_mov_b32 %0, m0\n\ts_mov_b32 m0, %3\n\ts_nop 2\n\tglobal_load_lds_dwordx4 %1, %2\n\ts_mov_b32 m0, %0" : "=&s"(sv) : "v"(voff), "s"(base), "s"(lds) : "memory");
  else if constexpr (N == 4)
    asm volatile("s_mov_b32 %0, m0\n\ts_mov_b32 m0, %3\n\ts_nop 2\n\tglobal_load_lds_dwordx4 %1, %2\n\tglobal_load_lds_dwordx4 %1, %2 offset:1024\n\t"
                 "global_load_lds_dwordx4 %1, %2 offset:2048\n\tglobal_load_lds_dwordx4 %1, %2 offset:3072\n\ts_mov_b32 m0, %0" : "=&s"(sv) : "v"(voff), "s"(base), "s"(lds) : "memory");
}
template <bool Z> DI void mf16(f32x4& c, const u32x4& a, const u32x4& b) {
  if constexpr (Z) asm volatile("v_mfma_f32_16x16x32_bf16 %0, %1, %2, 0" : "=v"(c) : "v"(a), "v"(b));
  else asm volatile("v_mfma_f32_16x16x32_bf16 %0, %1, %2, %0" : "+v"(c) : "v"(a), "v"(b));
}
#define MF16(Z, c, a, b) do { if (Z) mf16<true>(c, a, b); else mf16<false>(c, a, b); } while (0)
#define MFMA_DRAIN() asm volatile("s_nop 7\n\ts_nop 3" ::: "memory")
DI u32x4 ldsr128(unsigned a) { return *(const LDSP u32x4*)(a); }
DI u32x2 ldsr64(unsigned a) { return *(const LDSP u32x2*)(a); }
DI void ldsw128(unsigned a, u32x4 v) { *(LDSP u32x4*)(a) = v; }
DI void ldsw64(unsigned a, u32x2 v) { *(LDSP u32x2*)(a) = v; }
DI void ldsw32(unsigned a, unsigned v) { *(LDSP unsigned*)(a) = v; }
DI float ldsrf(unsigned a) { return *(const LDSP float*)(a); }
DI u32x2 ldstr(unsigned a) { return __builtin_bit_cast(u32x2, __builtin_amdgcn_ds_read_tr16_b64_v4i16((LDSP s16x4*)(a))); }

DI int wo_step_ks(int n) { return n < 8 ? n : (n == 8 ? 16 : (n < 17 ? n - 1 : 17)); }
DI int wo_row(int ks, int q, int j) {
  if (ks < 16) return 18 * (2 * ks + (j >> 2)) + 4 * q + (j & 3);
  return 18 * (16 * (ks - 16) + 4 * q + (j >> 1)) + 16 + (j & 1);
}
__global__ __launch_bounds__(384) void k1_mod_pack(const u16* __restrict__ c, const u16* __restrict__ wmod, const u16* __restrict__ wq, const u16* __restrict__ wk,
                                                   const u16* __restrict__ wv, const u16* __restrict__ wo, char* __restrict__ ws) {
  __shared__ float red[6][16];
  const int b = blockIdx.x, tid = threadIdx.x, lane = tid & 63, wave = rfl(tid >> 6);
  if (b < 216) {
    const int cg = (b & 7) * 27 + (b >> 3);
    const int half = tid & 1, r0 = tid >> 1;
    u32x4 w[3]; u16 cv[3];
#pragma unroll
    for (int i = 0; i < 3; ++i) { w[i] = *(const u32x4*)(wmod + (size_t)(r0 + 192 * i) * 3456 + cg * 16 + half * 8); cv[i] = c[r0 + 192 * i]; }
    float a[8] = {0, 0, 0, 0, 0, 0, 0, 0};
#pragma unroll
    for (int i = 0; i < 3; ++i) {
      const float x = bf2f(cv[i]);
      const float s = rbf(x / (1.f + __expf(-x)));
#pragma unroll
      for (int e = 0; e < 4; ++e) { a[2 * e] += s * bflo(w[i][e]); a[2 * e + 1] += s * bfhi(w[i][e]); }
    }
#pragma unroll
    for (int e = 0; e < 8; ++e) { float v = a[e]; v += dpp<0x128>(v); v += dpp<0x124>(v); v += dpp<0x122>(v); v = xor16(v); v = xor32(v); a[e] = v; }
    if (lane < 2) {
#pragma unroll
      for (int e = 0; e < 8; ++e) red[wave][lane * 8 + e] = a[e];
    }
    __syncthreads();
    if (tid < 16) {
      float v = 0;
#pragma unroll
      for (int i = 0; i < 6; ++i) v += red[i][tid];
      v = rbf(v);
      const int n = cg * 16 + tid, sec = n / D;
      if (sec == 1 || sec == 4) v = rbf(1.f + v);
      ((float*)(ws + OFF_MOD))[n] = v;
    }
    return;
  }
  const int r = lane & 15, q = lane >> 4;
#pragma unroll
  for (int it = 0; it < 2; ++it) {
    const int f = ((b - 216) * 6 + wave) * 2 + it;
    u16 v[8];
    if (f < 720) {
      const int T = f / 18, ks = f % 18, g = T / 10, i = T % 10;
      const u16* src; int ld, col; bool ok = true;
      if (g < 2 && i >= 8) { src = g == 0 ? wk : wv; ld = 18; col = (i - 8) * 16 + r; ok = col < 18; }
      else { src = wq; ld = D; col = (g < 2 ? g * 8 + i : 16 + (g - 2) * 10 + i) * 16 + r; }
      if (!ok) col = 0;
#pragma unroll
      for (int j = 0; j < 8; ++j) { const u16 t = src[(32 * ks + 8 * q + j) * ld + col]; v[j] = ok ? t : (u16)0; }
      u32x4 o = {v[0] | (unsigned)v[1] << 16, v[2] | (unsigned)v[3] << 16, v[4] | (unsigned)v[5] << 16, v[6] | (unsigned)v[7] << 16};
      *(u32x4*)(ws + OFF_WQKV + (size_t)f * 1024 + lane * 16) = o;
    } else {
      const int ff = f - 720, n = ff / 36, t = ff % 36, ks = wo_step_ks(n);
#pragma unroll
      for (int j = 0; j < 8; ++j) v[j] = wo[wo_row(ks, q, j) * D + t * 16 + r];
      u32x4 o = {v[0] | (unsigned)v[1] << 16, v[2] | (unsigned)v[3] << 16, v[4] | (unsigned)v[5] << 16, v[6] | (unsigned)v[7] << 16};
      *(u32x4*)(ws + OFF_WO + (size_t)ff * 1024 + lane * 16) = o;
    }
  }
}

constexpr float QSCALE = 0.23570226039551584f * 1.4426950408889634f;
__global__ __launch_bounds__(640) void k2_qkv(const u16* __restrict__ x, char* __restrict__ ws) {
  __shared__ __attribute__((aligned(16))) char hs[2 * 18 * 1024];
  const int b = blockIdx.x, tid = threadIdx.x, lane = tid & 63, wave = rfl(tid >> 6);
  const int g = (b >> 3) & 3, tt = (b & 7) + 8 * (b >> 5);
  const unsigned hb = (unsigned)(size_t)hs;
  u32x4 wf[18];
  const int l16 = lane & 15;
  const int row = wave * 4 + (lane >> 4);
  u32x4 xv[5];
  if (wave < 8) {
    const u16* xr = x + (size_t)(tt * 32 + row) * D;
#pragma unroll
    for (int i = 0; i < 4; ++i) xv[i] = *(const u32x4*)(xr + (i * 16 + l16) * 8);
    xv[4] = *(const u32x4*)(xr + (64 + (l16 & 7)) * 8);
  }
  {
    const char* wp = ws + OFF_WQKV + (size_t)(g * 10 + wave) * 18 * 1024 + lane * 16;
#pragma unroll
    for (int ks = 0; ks < 18; ++ks) wf[ks] = *(const u32x4*)(wp + ks * 1024);
  }
  if (wave < 8) {
    const float* mod = (const float*)(ws + OFF_MOD);
    const float m4 = l16 < 8 ? 1.f : 0.f;
    float s = 0;
#pragma unroll
    for (int i = 0; i < 5; ++i) { float t = 0;
#pragma unroll
      for (int e = 0; e < 4; ++e) t += bflo(xv[i][e]) + bfhi(xv[i][e]);
      s += i < 4 ? t : t * m4; }
    const float mean = row16_sum(s) * (1.f / D);
    float v = 0;
#pragma unroll
    for (int i = 0; i < 5; ++i) { float t = 0;
#pragma unroll
      for (int e = 0; e < 4; ++e) { const float a = bflo(xv[i][e]) - mean, bb = bfhi(xv[i][e]) - mean; t += a * a + bb * bb; }
      v += i < 4 ? t : t * m4; }
    const float rs = rsqrtf(row16_sum(v) * (1.f / D) + 1e-5f);
#pragma unroll
    for (int i = 0; i < 5; ++i) {
      const int c = i < 4 ? i * 16 + l16 : 64 + (l16 & 7);
      const f32x4 sh0 = *(const f32x4*)(mod + c * 8), sh1 = *(const f32x4*)(mod + c * 8 + 4);
      const f32x4 sc0 = *(const f32x4*)(mod + D + c * 8), sc1 = *(const f32x4*)(mod + D + c * 8 + 4);
      u32x4 o;
      o[0] = pk((bflo(xv[i][0]) - mean) * rs * sc0[0] + sh0[0], (bfhi(xv[i][0]) - mean) * rs * sc0[1] + sh0[1]);
      o[1] = pk((bflo(xv[i][1]) - mean) * rs * sc0[2] + sh0[2], (bfhi(xv[i][1]) - mean) * rs * sc0[3] + sh0[3]);
      o[2] = pk((bflo(xv[i][2]) - mean) * rs * sc1[0] + sh1[0], (bfhi(xv[i][2]) - mean) * rs * sc1[1] + sh1[1]);
      o[3] = pk((bflo(xv[i][3]) - mean) * rs * sc1[2] + sh1[2], (bfhi(xv[i][3]) - mean) * rs * sc1[3] + sh1[3]);
      const int ks = c >> 2, q = c & 3, gsw = q | ((ks & 1) ? 12 : 0);
      if (i < 4 || l16 < 8) ldsw128(hb + (row >> 4) * 18432 + ks * 1024 + (q * 16 + ((row & 15) ^ gsw)) * 16, o);
    }
  }
  __syncthreads();
  const int q = lane >> 4;
  const unsigned ra0 = hb + (q * 16 + (l16 ^ q)) * 16, ra1 = hb + (q * 16 + (l16 ^ (q | 12))) * 16;
  f32x4 acc0 = {0, 0, 0, 0}, acc1 = {0, 0, 0, 0};
  const bool isv = (g == 1 && wave >= 8);
  if (!isv) {
#pragma unroll
    for (int ks = 0; ks < 18; ++ks) {
      const unsigned ra = ((ks & 1) ? ra1 : ra0) + ks * 1024;
      acc0 = MFMA16(wf[ks], ldsr128(ra), acc0);
      acc1 = MFMA16(wf[ks], ldsr128(ra + 18432), acc1);
    }
  } else {
#pragma unroll
    for (int ks = 0; ks < 18; ++ks) {
      const unsigned ra = ((ks & 1) ? ra1 : ra0) + ks * 1024;
      acc0 = MFMA16(ldsr128(ra), wf[ks], acc0);
      acc1 = MFMA16(ldsr128(ra + 18432), wf[ks], acc1);
    }
  }
  if (g >= 2 || wave < 8) {
    const int n0 = (g < 2 ? g * 8 + wave : 16 + (g - 2) * 10 + wave) * 16 + 4 * q;
    u16* qp = (u16*)(ws + OFF_Q) + (size_t)(tt * 32 + l16) * D + n0;
    *(u32x2*)qp = u32x2{pk(acc0[0] * QSCALE, acc0[1] * QSCALE), pk(acc0[2] * QSCALE, acc0[3] * QSCALE)};
    *(u32x2*)(qp + 16 * D) = u32x2{pk(acc1[0] * QSCALE, acc1[1] * QSCALE), pk(acc1[2] * QSCALE, acc1[3] * QSCALE)};
  } else if (g == 0) {
    u32x2 k0 = {pk(acc0[0], acc0[1]), pk(acc0[2], acc0[3])}, k1 = {pk(acc1[0], acc1[1]), pk(acc1[2], acc1[3])};
    float n0 = bflo(k0[0]) * bflo(k0[0]) + bfhi(k0[0]) * bfhi(k0[0]) + bflo(k0[1]) * bflo(k0[1]) + bfhi(k0[1]) * bfhi(k0[1]);
    float n1 = bflo(k1[0]) * bflo(k1[0]) + bfhi(k1[0]) * bfhi(k1[0]) + bflo(k1[1]) * bflo(k1[1]) + bfhi(k1[1]) * bfhi(k1[1]);
    n0 = xor32(xor16(n0)); n1 = xor32(xor16(n1));
    const float nm = row16_max(fmaxf(n0, n1));
    if (lane == 0) ((float*)(ws + OFF_KMAX))[(wave - 8) * 64 + tt] = nm;
    if (wave == 8) {
      char* kp = ws + OFF_K + (size_t)tt * 1024 + (q >> 1) * 512 + l16 * 16 + (q & 1) * 8;
      *(u32x2*)kp = k0; *(u32x2*)(kp + 256) = k1;
    } else if (q == 0) {
      char* kp = ws + OFF_K1 + (size_t)(tt >> 2) * 512 + l16 * 16 + (tt & 3) * 4;
      *(unsigned*)kp = k0[0]; *(unsigned*)(kp + 256) = k1[0];
    }
  } else {
    const int dim = (wave - 8) * 16 + l16;
    if (dim < 19) {
      char* vp = ws + OFF_V + (size_t)tt * 1216 + (q & 1) * 304 + dim * 16 + (q >> 1) * 8;
      const u32x2 one = {0x3f803f80u, 0x3f803f80u};
      *(u32x2*)vp = dim < 18 ? u32x2{pk(acc0[0], acc0[1]), pk(acc0[2], acc0[3])} : one;
      *(u32x2*)(vp + 608) = dim < 18 ? u32x2{pk(acc1[0], acc1[1]), pk(acc1[2], acc1[3])} : one;
    }
  }
}

constexpr int L3_QF0 = 0, L3_QF1 = 8192, L3_PART = 12288, L3_OB = L3_PART + 2 * 20480, L3_RING = L3_OB + 18 * 512, L3_XM = L3_RING + 8 * 2 * 5120,
              L3_MISC = L3_XM + 16384, L3_SIZE = L3_MISC + 1024;
constexpr float SHIFT_SAFE = 48.f;

DI void split3(float m, unsigned& hi, unsigned& mid, unsigned& lo) {
  const unsigned h = __float_as_uint(m) & 0xffff0000u; const float r1 = m - __uint_as_float(h);
  const unsigned md = __float_as_uint(r1) & 0xffff0000u; const float r2 = r1 - __uint_as_float(md);
  hi = h >> 16; mid = md >> 16; lo = __float_as_uint(r2) >> 16;
}
#define EX2(x) __builtin_amdgcn_exp2f(x)

__global__ __launch_bounds__(512, 2) void k3_attn(const u16* __restrict__ x, char* __restrict__ ws) {
  extern __shared__ __attribute__((aligned(16))) char smem[];
  const unsigned lb = (unsigned)(size_t)smem;
  const int b = blockIdx.x, tid = threadIdx.x, lane = tid & 63, wave = rfl(tid >> 6);
  const int r32 = lane & 31, hh = lane >> 5, l16 = lane & 15, q4 = lane >> 4;
  const int tok0 = b * 8;
  STAMP(3, 0);
  u32x4 q0; unsigned q1; float kmA, kmB;
  {
    const u16* qp = (const u16*)(ws + OFF_Q) + (size_t)(tok0 + (r32 & 7)) * D + (4 * wave + (r32 >> 3)) * 18;
    const unsigned* qd = (const unsigned*)(qp + 8 * hh);
    q0 = u32x4{qd[0], qd[1], qd[2], qd[3]};
    q1 = *(const unsigned*)(qp + 16);
    kmA = ((const float*)(ws + OFF_KMAX))[lane]; kmB = ((const float*)(ws + OFF_KMAX))[64 + lane];
  }
  SBAR();
  u32x4 K0[8], V0[8], V1[8], K1[2];
  const char* kp = ws + OFF_K + (size_t)wave * 8192 + lane * 16;
  const char* vp = ws + OFF_V + (size_t)wave * 8 * 1216 + hh * 304 + (r32 < 18 ? r32 : 18) * 16;
#define LDK(t) K0[t] = *(const u32x4*)(kp + (t) * 1024)
#define LDV(t) do { V0[t] = *(const u32x4*)(vp + (t) * 1216); V1[t] = *(const u32x4*)(vp + (t) * 1216 + 608); } while (0)
  {
    const char* k1p = hh == 0 ? ws + OFF_K1 + (size_t)wave * 1024 + r32 * 16 : ws + OFF_V + 288;
    LDK(0); K1[0] = *(const u32x4*)k1p; LDK(1); LDV(0); K1[1] = *(const u32x4*)(k1p + (hh == 0 ? 512 : 0)); LDK(2); LDV(1);
  }
  SBAR();
  {
    float n = 0;
#pragma unroll
    for (int e = 0; e < 4; ++e) n += bflo(q0[e]) * bflo(q0[e]) + bfhi(q0[e]) * bfhi(q0[e]);
    n = xor32(n) + bflo(q1) * bflo(q1) + bfhi(q1) * bfhi(q1);
    kmA = max32(max16(row16_max(kmA))); kmB = max32(max16(row16_max(kmB)));
    const float mh = __builtin_amdgcn_sqrtf(n * (kmA + kmB));
    const bool bad = !(mh <= SHIFT_SAFE);
    const unsigned nm = pk(-mh, 0.f);
    ldsw128(lb + L3_QF0 + wave * 1024 + lane * 16, q0);
    ldsw64(lb + L3_QF1 + wave * 512 + lane * 8, hh == 0 ? u32x2{q1, 0u} : u32x2{nm, 0u});
    const unsigned anybad = __builtin_amdgcn_ballot_w64(bad) != 0;
    if (lane == 0) ldsw32(lb + L3_MISC + wave * 4, anybad);
  }
  BARRIER();
  const f32x16 zero16 = {0, 0, 0, 0, 0, 0, 0, 0, 0, 0, 0, 0, 0, 0, 0, 0};
#define K1P(t) (u32x2{K1[(t) >> 2][(t) & 2], K1[(t) >> 2][((t) & 2) + 1]})
  {
    const u32x4 fl0 = ldsr128(lb + L3_MISC), fl1 = ldsr128(lb + L3_MISC + 16);
    if (rfl(fl0[0] | fl0[1] | fl0[2] | fl0[3] | fl1[0] | fl1[1] | fl1[2] | fl1[3])) {
#pragma unroll 1
      for (int g = 0; g < 8; ++g) {
        const u32x4 Q0 = ldsr128(lb + L3_QF0 + g * 1024 + lane * 16);
        const unsigned qq = hh == 0 ? ldsr64(lb + L3_QF1 + g * 512 + lane * 8)[0] : 0u;
        float m = -3.0e38f;
#pragma unroll 1
        for (int t = 0; t < 8; ++t) {
          const u32x4 k0 = *(const u32x4*)(kp + t * 1024);
          const unsigned k1 = *(const unsigned*)(ws + OFF_K1 + (size_t)wave * 1024 + (t >> 2) * 512 + r32 * 16 + (t & 3) * 4);
          f32x16 s = MFMA32(k0, Q0, zero16); s = MFMA32K8((u32x2{k1, 0u}), (u32x2{qq, 0u}), s);
#pragma unroll
          for (int i = 0; i < 16; ++i) m = fmaxf(m, s[i]);
        }
        m = max32(m);
        if (hh == 0) *(LDSP float*)(lb + L3_PART + (g * 8 + wave) * 128 + r32 * 4) = m;
      }
      BARRIER();
      float m = -3.0e38f;
#pragma unroll
      for (int w = 0; w < 8; ++w) m = fmaxf(m, ldsrf(lb + L3_PART + (wave * 8 + w) * 128 + r32 * 4));
      unsigned hi, mid, lo; split3(-m, hi, mid, lo);
      if (hh == 1) ldsw64(lb + L3_QF1 + wave * 512 + lane * 8, u32x2{hi | mid << 16, lo});
      BARRIER();
    }
  }
  STAMP(3, 1);
  const int nt0 = wave < 4 ? wave * 5 : 20 + (wave - 4) * 4;
  const char* wob = ws + OFF_WO + (size_t)nt0 * 1024;
  const unsigned ring = lb + L3_RING + wave * 10240;
  f32x4 pa[5];
#pragma unroll
  for (int i = 0; i < 5; ++i) pa[i] = f32x4{0, 0, 0, 0};
  auto wo_issue = [&](int n) {
    const char* src = wob + (size_t)n * 36864; const unsigned dst = ring + (n & 1) * 5120;
    glds<4>(src, lane * 16, dst);
    glds<1>(src + (wave < 4 ? 4096 : 0), lane * 16, dst + 4096);
  };
  unsigned rw_addr, rw_step; int bp_idx;
  {
    const int c4 = lane / 10, u = lane - 10 * c4, hd = wave >> 1, tk = (wave & 1) * 4 + c4;
    bp_idx = (c4 * 10 + 9) * 4;
    if (lane < 40 && u < 8) { rw_addr = lb + L3_OB + (hd >> 1) * 512 + (u >> 1) * 128 + tk * 16 + (hd & 1) * 8 + (u & 1) * 4; rw_step = 1024; }
    else if (lane < 40 && u == 8) { rw_addr = lb + L3_OB + 8192 + tk * 16 + hd * 4; rw_step = 128; }
    else { rw_addr = lb + L3_MISC + 768 + (lane & 31) * 4; rw_step = 0; }
  }
  const unsigned rd_addr = lb + L3_PART + wave * 320 + lane * 8;
  const unsigned pw_addr = lb + L3_PART + wave * 2560 + r32 * 80 + hh * 16;
  const unsigned ob_addr = lb + L3_OB + q4 * 128 + (lane & 7) * 16;
  f32x2 rp[8]; float rl; u32x4 ob, wa[5];
  u32x4 wt[3][5];

  u32x4 Q0 = ldsr128(lb + L3_QF0 + lane * 16), Q0n;
  u32x2 Qa = ldsr64(lb + L3_QF1 + lane * 8), Qb, Qan;
  Qb = hh == 0 ? u32x2{0u, Qa[0]} : Qa;
  f32x16 sA = MFMA32(K0[0], Q0, zero16), sB, O;
  sA = MFMA32K8(K1P(0), Qa, sA);
  f32x4 ep, ea, eb; u32x4 p0, p1;
  int n = 0;

#define TILE(PH, t, SC, SN, X0, X1, X2, X3) do { \
    if (!((PH) == 2 && (t) == 7)) { SN = MFMA32(K0[((t) + 1) & 7], (t) == 7 ? Q0n : Q0, zero16); PIN(SN); } \
    SBAR(); \
    if (!((PH) == 0 && (t) == 0)) { p1[2] = pk(ep[0], ep[1]); p1[3] = pk(ep[2], ep[3]); PIN(p1); SBAR(); } \
    ea = f32x4{EX2(SC[0]), EX2(SC[1]), EX2(SC[2]), EX2(SC[3])}; PIN(ea); SBAR(); X0; SBAR(); \
    if (!((PH) == 0 && (t) == 0)) { O = MFMA32(V1[((t) + 7) & 7], p1, O); PIN(O); } \
    SBAR(); \
    p0[0] = pk(ea[0], ea[1]); p0[1] = pk(ea[2], ea[3]); PIN(p0); SBAR(); \
    eb = f32x4{EX2(SC[4]), EX2(SC[5]), EX2(SC[6]), EX2(SC[7])}; PIN(eb); SBAR(); X1; SBAR(); \
    if ((t) == 7) { Q0 = Q0n; Qa = Qan; } \
    if (!((PH) == 2 && (t) == 7)) { SN = MFMA32K8(K1P(((t) + 1) & 7), (((t) + 1) & 1) ? Qb : Qa, SN); PIN(SN); } \
    SBAR(); \
    p0[2] = pk(eb[0], eb[1]); p0[3] = pk(eb[2], eb[3]); PIN(p0); SBAR(); \
    ea = f32x4{EX2(SC[8]), EX2(SC[9]), EX2(SC[10]), EX2(SC[11])}; PIN(ea); SBAR(); X2; SBAR(); \
    O = MFMA32(V0[t], p0, (t) == 0 ? zero16 : O); PIN(O); \
    SBAR(); \
    p1[0] = pk(ea[0], ea[1]); p1[1] = pk(ea[2], ea[3]); PIN(p1); SBAR(); \
    ep = f32x4{EX2(SC[12]), EX2(SC[13]), EX2(SC[14]), EX2(SC[15])}; PIN(ep); SBAR(); X3; SBAR(); \
  } while (0)
#define NOP_ do {} while (0)
#define WRITE_O(par) do { PIN(O); const unsigned pw_ = pw_addr + (par) * 20480; \
    ldsw128(pw_, __builtin_bit_cast(u32x4, f32x4{O[0], O[1], O[2], O[3]})); ldsw128(pw_ + 32, __builtin_bit_cast(u32x4, f32x4{O[4], O[5], O[6], O[7]})); \
    if (hh == 0) ldsw128(pw_ + 64, __builtin_bit_cast(u32x4, f32x4{O[8], O[9], O[10], O[11]})); } while (0)
#define RED_RD(par) do { _Pragma("unroll") for (int w_ = 0; w_ < 8; ++w_) rp[w_] = __builtin_bit_cast(f32x2, ldsr64(rd_addr + (par) * 20480 + w_ * 2560)); } while (0)
#define RED_ADD() do { rp[0] = ((rp[0] + rp[1]) + (rp[2] + rp[3])) + ((rp[4] + rp[5]) + (rp[6] + rp[7])); \
    rl = __int_as_float(__builtin_amdgcn_ds_bpermute(bp_idx, __float_as_int(rp[0][0]))); } while (0)
#define RED_FIN() do { const float inv_ = __builtin_amdgcn_rcpf(rl); ldsw32(rw_addr, pk(rp[0][0] * inv_, rp[0][1] * inv_)); rw_addr += rw_step; } while (0)
#define OP_RD() do { WAITVM(5); ob = ldsr128(ob_addr + wo_step_ks(n) * 512); const unsigned s_ = ring + (n & 1) * 5120 + lane * 16; \
    _Pragma("unroll") for (int i_ = 0; i_ < 5; ++i_) wa[i_] = ldsr128(s_ + i_ * 1024); } while (0)
#define OP_M(i) do { pa[i] = MFMA16(wa[i], ob, pa[i]); PIN(pa[i]); } while (0)
#define OP_END(ISS) do { if (ISS) wo_issue(n + 2); ++n; } while (0)
#define LBAR() BARRIER()
#define QNEXT(gn) do { Q0n = ldsr128(lb + L3_QF0 + (gn) * 1024 + lane * 16); Qan = ldsr64(lb + L3_QF1 + (gn) * 512 + lane * 8); } while (0)
#define QB() do { Qb = hh == 0 ? u32x2{0u, Qa[0]} : Qa; PIN(Qb); } while (0)

  TILE(0, 0, sA, sB, LDK(3), LDV(2), NOP_, NOP_);
  TILE(0, 1, sB, sA, LDK(4), LDV(3), NOP_, NOP_);
  TILE(0, 2, sA, sB, LDK(5), LDV(4), NOP_, NOP_);
  TILE(0, 3, sB, sA, LDK(6), LDV(5), NOP_, NOP_);
  TILE(0, 4, sA, sB, LDK(7), LDV(6), NOP_, NOP_);
  TILE(0, 5, sB, sA, NOP_, LDV(7), NOP_, NOP_);
  TILE(0, 6, sA, sB, QNEXT(1), NOP_, NOP_, NOP_);
  TILE(0, 7, sB, sA, NOP_, NOP_, NOP_, QB());
  PIN(V1[7]);
  wo_issue(0); wo_issue(1);
  {
    const char* s0 = (const char*)(x + (size_t)tok0 * D) + wave * 1024;
    const char* s1 = wave == 0 ? (const char*)(x + (size_t)tok0 * D) + 8192 : ws + OFF_MOD + 2 * D * 4 + (wave - 1) * 1024;
    glds<1>(s0, lane * 16, lb + L3_XM + wave * 1024);
    glds<1>(s1, lane * 16, lb + L3_XM + 8192 + wave * 1024);
  }
  TILE(1, 0, sA, sB, NOP_, NOP_, WRITE_O(0), LBAR());
  TILE(1, 1, sB, sA, RED_RD(0), NOP_, NOP_, RED_ADD());
  TILE(1, 2, sA, sB, NOP_, NOP_, RED_FIN(), NOP_);
  TILE(1, 3, sB, sA, NOP_, NOP_, NOP_, NOP_);
  TILE(1, 4, sA, sB, NOP_, NOP_, NOP_, NOP_);
  TILE(1, 5, sB, sA, NOP_, NOP_, NOP_, NOP_);
  TILE(1, 6, sA, sB, QNEXT(2), NOP_, NOP_, NOP_);
  TILE(1, 7, sB, sA, NOP_, NOP_, NOP_, QB());
  WAITVM(0);
#pragma unroll 1
  for (int g = 2; g < 7; ++g) {
    const int par = (g - 1) & 1;
    TILE(1, 0, sA, sB, NOP_, NOP_, WRITE_O(par), LBAR());
    TILE(1, 1, sB, sA, RED_RD(par), NOP_, NOP_, RED_ADD());
    TILE(1, 2, sA, sB, OP_RD(), NOP_, RED_FIN(), OP_M(0));
    TILE(1, 3, sB, sA, OP_M(1), OP_M(2), OP_M(3), { OP_M(4); OP_END(true); });
    TILE(1, 4, sA, sB, OP_RD(), NOP_, NOP_, OP_M(0));
    TILE(1, 5, sB, sA, OP_M(1), OP_M(2), OP_M(3), { OP_M(4); OP_END(true); });
    TILE(1, 6, sA, sB, QNEXT(g + 1), NOP_, NOP_, NOP_);
    TILE(1, 7, sB, sA, NOP_, NOP_, NOP_, QB());
    if (g == 5) { OP_RD(); OP_M(0); OP_M(1); OP_M(2); OP_M(3); OP_M(4); OP_END(true); SBAR(); }
  }
#define LDWT(i) do { _Pragma("unroll") for (int j_ = 0; j_ < 5; ++j_) wt[i][j_] = *(const u32x4*)(wob + (size_t)(15 + (i)) * 36864 + (j_ < 4 || wave < 4 ? j_ : 0) * 1024 + lane * 16); } while (0)
  TILE(2, 0, sA, sB, NOP_, NOP_, WRITE_O(0), LBAR());
  TILE(2, 1, sB, sA, RED_RD(0), NOP_, NOP_, RED_ADD());
  TILE(2, 2, sA, sB, OP_RD(), NOP_, RED_FIN(), OP_M(0));
  TILE(2, 3, sB, sA, OP_M(1), OP_M(2), OP_M(3), { OP_M(4); OP_END(true); });
  TILE(2, 4, sA, sB, OP_RD(), NOP_, NOP_, OP_M(0));
  TILE(2, 5, sB, sA, OP_M(1), OP_M(2), OP_M(3), { OP_M(4); OP_END(true); });
  TILE(2, 6, sA, sB, LDWT(0), NOP_, LDWT(1), NOP_);
  TILE(2, 7, sB, sA, LDWT(2), NOP_, NOP_, NOP_);
  p1[2] = pk(ep[0], ep[1]); p1[3] = pk(ep[2], ep[3]);
  O = MFMA32(V1[7], p1, O);
  WRITE_O(1);
  BARRIER();
  STAMP(3, 9);
  RED_RD(1);
  WAITVM(15);
#pragma unroll
  for (int i = 0; i < 2; ++i) {
    ob = ldsr128(ob_addr + wo_step_ks(13 + i) * 512);
#pragma unroll
    for (int j = 0; j < 5; ++j) pa[j] = MFMA16(ldsr128(ring + ((13 + i) & 1) * 5120 + lane * 16 + j * 1024), ob, pa[j]);
    if (i == 0) RED_ADD();
  }
  RED_FIN();
  const int tk = lane & 7;
  u32x2 xr[5]; f32x4 ga[5], sb[5], cb[5];
#pragma unroll
  for (int j = 0; j < 5; ++j) {
    const int nn = (nt0 + (j < 4 || wave < 4 ? j : 0)) * 16 + 4 * q4;
    xr[j] = ldsr64(lb + L3_XM + tk * 1152 + nn * 2);
    ga[j] = __builtin_bit_cast(f32x4, ldsr128(lb + L3_XM + 9216 + nn * 4));
    sb[j] = __builtin_bit_cast(f32x4, ldsr128(lb + L3_XM + 9216 + 2304 + nn * 4));
    cb[j] = __builtin_bit_cast(f32x4, ldsr128(lb + L3_XM + 9216 + 4608 + nn * 4));
  }
  BARRIER();
#pragma unroll
  for (int i = 0; i < 3; ++i) {
    ob = ldsr128(ob_addr + wo_step_ks(15 + i) * 512);
#pragma unroll
    for (int j = 0; j < 5; ++j) pa[j] = MFMA16(wt[i][j], ob, pa[j]);
  }
  float xv[5][4];
  const int nv = wave < 4 ? 5 : 4;
  float sum = 0;
#pragma unroll
  for (int j = 0; j < 5; ++j) {
    const unsigned a = pk(bflo(xr[j][0]) + rbf(ga[j][0] * rbf(pa[j][0])), bfhi(xr[j][0]) + rbf(ga[j][1] * rbf(pa[j][1])));
    const unsigned c = pk(bflo(xr[j][1]) + rbf(ga[j][2] * rbf(pa[j][2])), bfhi(xr[j][1]) + rbf(ga[j][3] * rbf(pa[j][3])));
    xv[j][0] = bflo(a); xv[j][1] = bfhi(a); xv[j][2] = bflo(c); xv[j][3] = bfhi(c);
    if (j < nv) {
      sum += (xv[j][0] + xv[j][1]) + (xv[j][2] + xv[j][3]);
      if (l16 < 8) *(u32x2*)((u16*)(ws + OFF_X1) + (size_t)(tok0 + tk) * D + (nt0 + j) * 16 + 4 * q4) = u32x2{a, c};
    }
  }
  sum = xor32(xor16(sum));
  const float mw = sum * (wave < 4 ? 1.f / 80.f : 1.f / 64.f);
  float m2 = 0;
#pragma unroll
  for (int j = 0; j < 5; ++j) if (j < nv) {
#pragma unroll
    for (int e = 0; e < 4; ++e) { const float d = xv[j][e] - mw; m2 += d * d; }
  }
  m2 = xor32(xor16(m2));
  if (lane < 8) ldsw64(lb + L3_MISC + 64 + tk * 64 + wave * 8, u32x2{__float_as_uint(mw), __float_as_uint(m2)});
  BARRIER();
  float mean, rs;
  {
    u32x4 st[4];
#pragma unroll
    for (int i = 0; i < 4; ++i) st[i] = ldsr128(lb + L3_MISC + 64 + tk * 64 + i * 16);
    float tot = 0;
#pragma unroll
    for (int w = 0; w < 8; ++w) tot += __uint_as_float(st[w >> 1][(w & 1) * 2]) * (w < 4 ? 80.f : 64.f);
    mean = tot * (1.f / D);
    float M2 = 0;
#pragma unroll
    for (int w = 0; w < 8; ++w) { const float d = __uint_as_float(st[w >> 1][(w & 1) * 2]) - mean; M2 += __uint_as_float(st[w >> 1][(w & 1) * 2 + 1]) + d * d * (w < 4 ? 80.f : 64.f); }
    rs = rsqrtf(M2 * (1.f / D) + 1e-5f);
  }
  char* h2 = ws + OFF_H2 + (size_t)(b >> 1) * 18432 + (q4 * 16 + (b & 1) * 8 + tk) * 16;
#pragma unroll
  for (int j = 0; j < 5; ++j) if (j < nv && l16 < 8) {
    const int nt = nt0 + j;
    const u32x2 o = {pk((xv[j][0] - mean) * rs * cb[j][0] + sb[j][0], (xv[j][1] - mean) * rs * cb[j][1] + sb[j][1]),
                     pk((xv[j][2] - mean) * rs * cb[j][2] + sb[j][2], (xv[j][3] - mean) * rs * cb[j][3] + sb[j][3])};
    *(u32x2*)(h2 + (nt >> 1) * 1024 + (nt & 1) * 8) = o;
  }
  STAMP(3, 10);
}

__global__ __launch_bounds__(256) void k4_pack(const u16* __restrict__ wg, const u16* __restrict__ wu, const u16* __restrict__ wd, char* __restrict__ ws) {
  __shared__ __attribute__((aligned(16))) char st[4 * 4096];
  const int lane = threadIdx.x & 63, wave = rfl(threadIdx.x >> 6);
  const int u = blockIdx.x * 4 + wave;
  const unsigned sb = (unsigned)(size_t)st + wave * 4096;
  const int mat = u / 1296, r = u % 1296;
  const char* src; int ld; char* dst; int t0, ks;
  if (mat < 2) { ks = r / 72; t0 = (r % 72) * 4; src = (const char*)(mat == 0 ? wg : wu); ld = F * 2; }
  else { ks = r / 9; t0 = (r % 9) * 4; src = (const char*)wd; ld = D * 2; }
  src += (size_t)ks * 32 * ld + t0 * 32;
#pragma unroll
  for (int i = 0; i < 4; ++i) glds<1>(src + (size_t)i * 8 * ld, (lane >> 3) * ld + (lane & 7) * 16, sb + i * 1024);
  WAITVM(0);
  const unsigned ta = sb + ((lane >> 4) * 4 + ((lane >> 2) & 3)) * 128 + (lane & 3) * 8;
#pragma unroll
  for (int t = 0; t < 4; ++t) {
    const u32x2 lo = ldstr(ta + t * 32), hi = ldstr(ta + t * 32 + 2048);
    const int tt = t0 + t;
    if (mat < 2) dst = ws + OFF_WGU + (size_t)tt * 36864 + ks * 2048 + mat * 1024;
    else dst = ws + OFF_WD + (size_t)(tt / 9) * (144 * 9216) + ks * 9216 + (tt % 9) * 1024;
    *(u32x4*)(dst + lane * 16) = u32x4{lo[0], lo[1], hi[0], hi[1]};
  }
}

DI void ring_issue(const char* src, unsigned lb, int slot, int par, int wave, int lane) {
  glds<4>(src + wave * 4096, lane * 16, lb + slot * 36864 + wave * 4096);
  if ((wave >> 2) == par) glds<1>(src + 32768 + (wave & 3) * 1024, lane * 16, lb + slot * 36864 + 32768 + (wave & 3) * 1024);
}

DI float silu_mul(float g, float u) { return g * u * __builtin_amdgcn_rcpf(1.f + __builtin_amdgcn_exp2f(g * -1.4426950408889634f)); }

#define RAWBAR() asm volatile("s_barrier" ::: "memory")
__global__ __launch_bounds__(512, 2) void k5_gateup(char* __restrict__ ws) {
  extern __shared__ __attribute__((aligned(16))) char smem[];
  const unsigned lb = (unsigned)(size_t)smem;
  const int b = blockIdx.x, lane = threadIdx.x & 63, wave = rfl(threadIdx.x >> 6);
  const int xcd = b & 7, i = b >> 3;
  const int rt = (xcd >> 2) * 4 + (i & 3), ft = (xcd & 3) * 8 + (i >> 2);
  const int odd = ft & 1;
  STAMP(5, 0);
  const char* wsrc = ws + OFF_WGU + (size_t)ft * 9 * 36864;
  auto csrc = [&](int c) { int cc = c + odd; if (cc == 9) cc = 0; return wsrc + (size_t)cc * 36864; };
  ring_issue(csrc(0), lb, 0, 0, wave, lane);
  u32x4 hb[2][18];
  const char* hp = ws + OFF_H2 + (size_t)(rt * 16 + 2 * wave) * 18432 + lane * 16;
#pragma unroll
  for (int ks = 0; ks < 18; ++ks) {
    hb[0][ks] = *(const u32x4*)(hp + ks * 1024); hb[1][ks] = *(const u32x4*)(hp + 18432 + ks * 1024);
    SBAR();
    if (ks == 8) { ring_issue(csrc(1), lb, 1, 1, wave, lane); SBAR(); }
  }
  char* ap = ws + OFF_ACT + (size_t)(rt * 16 + 2 * wave) * (144 * 1024) + (size_t)((ft * 9 + odd) >> 1) * 1024 + lane * 16;
  constexpr int PD = 3;
  u32x4 ag[PD], au[PD];
  f32x4 acc[2][4];
  unsigned ko[2][4];
  float et[2], gu[2];
  const unsigned sa = lb + lane * 16;
  WAITVM(40);
  RAWBAR();
  STAMP(5, 1);
#pragma unroll
  for (int j = 0; j < PD; ++j) { ag[j] = ldsr128(sa + j * 2048); au[j] = ldsr128(sa + j * 2048 + 1024); }
  SBAR();
#pragma unroll
  for (int c = 0; c <= 9; ++c) {
    const int P = c & 1, Q = P ^ 1;
#pragma unroll
    for (int ks = 0; ks < 18; ++ks) {
      if (c < 9) {
        if (ks == 9 && c < 8) {
          if (c == 0) WAITVM(18); else if (c < 7) WAITVM(4); else WAITVM(0);
          RAWBAR();
          if (c >= 1 && c + 3 <= 8) ring_issue(csrc(c + 3), lb, (c + 3) & 3, (c + 3) & 1, wave, lane);
          SBAR();
        }
        const int fi = c * 18 + ks, sl = fi % PD;
        MF16(ks == 0, acc[P][0], ag[sl], hb[0][ks]);
        MF16(ks == 0, acc[P][1], ag[sl], hb[1][ks]);
        SBAR();
        const int fn = fi + PD, cn = fn / 18, kn = fn % 18;
        if (fn < 162) { ag[sl] = ldsr128(sa + (cn & 3) * 36864 + kn * 2048); SBAR(); }
        MF16(ks == 0, acc[P][2], au[sl], hb[0][ks]);
        MF16(ks == 0, acc[P][3], au[sl], hb[1][ks]);
        SBAR();
        if (fn < 162) { au[sl] = ldsr128(sa + (cn & 3) * 36864 + kn * 2048 + 1024); SBAR(); }
        if (c == 0 && ks == 17) { ring_issue(csrc(2), lb, 2, 0, wave, lane); ring_issue(csrc(3), lb, 3, 1, wave, lane); SBAR(); }
      }
      if (c == 9 && ks == 0) { MFMA_DRAIN(); SBAR(); }
      if (c >= 1 && ks >= 1 && ks <= 16) {
        const int v = (ks - 1) >> 1, tt = v >> 2, e = v & 3, pc = c - 1;
        if ((ks - 1) & 1) {
          const float r = __builtin_amdgcn_rcpf(1.f + et[e & 1]);
          gu[e & 1] *= r; PIN(gu[e & 1]);
          if (e & 1) { ko[tt][(pc & 1) * 2 + (e >> 1)] = pk(gu[0], gu[1]); PIN(ko[tt][(pc & 1) * 2 + (e >> 1)]); }
        } else {
          const float g = acc[Q][tt][e];
          et[e & 1] = EX2(g * -1.4426950408889634f); PIN(et[e & 1]);
          gu[e & 1] = g * acc[Q][2 + tt][e]; PIN(gu[e & 1]);
        }
        if (c < 9) SBAR();
      }
      if (c >= 1 && ks == 17) {
        const int pc = c - 1;
        if (pc == 8) {
          char* p = odd ? ap - 1024 + 8 : ap + 4096;
          *(u32x2*)p = u32x2{ko[0][0], ko[0][1]}; *(u32x2*)(p + 144 * 1024) = u32x2{ko[1][0], ko[1][1]};
        } else if (pc & 1) {
          *(u32x4*)(ap + (pc >> 1) * 1024) = u32x4{ko[0][0], ko[0][1], ko[0][2], ko[0][3]};
          *(u32x4*)(ap + (pc >> 1) * 1024 + 144 * 1024) = u32x4{ko[1][0], ko[1][1], ko[1][2], ko[1][3]};
        }
        SBAR();
      }
    }
    STAMP(5, 2 + c);
  }
}

__global__ __launch_bounds__(512, 2) void k6_down(char* __restrict__ ws) {
  extern __shared__ __attribute__((aligned(16))) char smem[];
  const unsigned lb = (unsigned)(size_t)smem;
  const int b = blockIdx.x, lane = threadIdx.x & 63, wave = rfl(threadIdx.x >> 6);
  const int xcd = b & 7, i = b >> 3;
  const int kq = xcd & 3, rt = (xcd >> 2) * 8 + (i & 7), ng = i >> 3;
  const int tg = wave & 3, kp = wave >> 2;
  STAMP(6, 0);
  const char* wsrc = ws + OFF_WD + (size_t)ng * (144 * 9216) + (size_t)kq * 36 * 9216;
  const char* ap = ws + OFF_ACT + (size_t)(rt * 8 + 2 * tg) * (144 * 1024) + (size_t)(kq * 36 + kp) * 1024 + lane * 16;
  u32x4 ab[18][2];
#pragma unroll
  for (int s = 0; s < 9; ++s) {
    if (s < 4) { ring_issue(wsrc + s * 36864, lb, s, s & 1, wave, lane); SBAR(); }
#pragma unroll
    for (int h = 0; h < 2; ++h) { ab[2 * s + h][0] = *(const u32x4*)(ap + (4 * s + 2 * h) * 1024); ab[2 * s + h][1] = *(const u32x4*)(ap + (4 * s + 2 * h) * 1024 + 144 * 1024); SBAR(); }
  }
  constexpr int PD = 5;
  f32x4 acc[2][9];
  u32x4 af[PD];
  const unsigned sa = lb + kp * 9216 + lane * 16;
  WAITVM(49);
  RAWBAR();
  STAMP(6, 1);
#pragma unroll
  for (int j = 0; j < PD; ++j) af[j] = ldsr128(sa + j * 1024);
  SBAR();
#pragma unroll
  for (int s = 0; s < 9; ++s) {
#pragma unroll
    for (int f = 0; f < 18; ++f) {
      if (f == 9 && s < 8) {
        if (s == 0) WAITVM(41); else if (s == 1) WAITVM(32); else if (s == 2) WAITVM(28); else if (s < 7) WAITVM(4); else WAITVM(0);
        RAWBAR();
        if (s >= 1 && s + 3 <= 8) ring_issue(wsrc + (s + 3) * 36864, lb, (s + 3) & 3, (s + 3) & 1, wave, lane);
        SBAR();
      }
      const int h = f / 9, n = f % 9, fi = s * 18 + f, sl = fi % PD;
      MF16(fi < 9, acc[0][n], af[sl], ab[2 * s + h][0]);
      MF16(fi < 9, acc[1][n], af[sl], ab[2 * s + h][1]);
      SBAR();
      const int fn = fi + PD, sn = fn / 18, hn = (fn % 18) / 9, nn = fn % 9;
      if (fn < 162) { af[sl] = ldsr128(sa + (sn & 3) * 36864 + hn * 18432 + nn * 1024); SBAR(); }
    }
    STAMP(6, 2 + s);
  }
  MFMA_DRAIN(); SBAR();
  const unsigned xw = lb + 36864 + wave * 9216 + lane * 16, xr = lb + 36864 + (wave ^ 4) * 9216 + lane * 16;
#pragma unroll
  for (int n = 0; n < 9; ++n) ldsw128(xw + n * 1024, __builtin_bit_cast(u32x4, kp ? acc[0][n] : acc[1][n]));
  BARRIER();
  char* pp = ws + OFF_PART + ((size_t)(kq * 128 + rt * 8 + 2 * tg + kp) * 36 + ng * 9) * 1024 + lane * 16;
#pragma unroll
  for (int n = 0; n < 9; ++n) {
    const f32x4 o = __builtin_bit_cast(f32x4, ldsr128(xr + n * 1024)) + (kp ? acc[1][n] : acc[0][n]);
    *(f32x4*)(pp + n * 1024) = o;
  }
  STAMP(6, 11);
}

__global__ __launch_bounds__(256) void k7_out(const char* __restrict__ ws, u16* __restrict__ out) {
  const int lane = threadIdx.x & 63, gw = blockIdx.x * 4 + (threadIdx.x >> 6);
  const int tt = gw / 18, np = gw % 18;
  const int tok = tt * 16 + (lane & 15), q = lane >> 4;
  f32x4 p[2][4]; u32x2 xr[2]; f32x4 gb[2];
#pragma unroll
  for (int j = 0; j < 2; ++j) {
#pragma unroll
    for (int k = 0; k < 4; ++k) p[j][k] = *(const f32x4*)(ws + OFF_PART + ((size_t)(k * 128 + tt) * 36 + np * 2 + j) * 1024 + lane * 16);
    const int n = (np * 2 + j) * 16 + 4 * q;
    xr[j] = *(const u32x2*)(ws + OFF_X1 + ((size_t)tok * D + n) * 2);
    gb[j] = *(const f32x4*)(ws + OFF_MOD + (5 * D + n) * 4);
  }
#pragma unroll
  for (int j = 0; j < 2; ++j) {
    const f32x4 y = (p[j][0] + p[j][1]) + (p[j][2] + p[j][3]);
    const int n = (np * 2 + j) * 16 + 4 * q;
    const u32x2 o = {pk(bflo(xr[j][0]) + rbf(gb[j][0] * rbf(y[0])), bfhi(xr[j][0]) + rbf(gb[j][1] * rbf(y[1]))),
                     pk(bflo(xr[j][1]) + rbf(gb[j][2] * rbf(y[2])), bfhi(xr[j][1]) + rbf(gb[j][3] * rbf(y[3])))};
    *(u32x2*)(out + (size_t)tok * D + n) = o;
  }
}

extern "C" void kernel_launch(void* const* d_in, const int* in_sizes, int n_in, void* d_out, int out_size, void* d_ws, size_t ws_size, hipStream_t stream) {
  const u16* x = (const u16*)d_in[0]; const u16* wq = (const u16*)d_in[1]; const u16* wk = (const u16*)d_in[2]; const u16* wv = (const u16*)d_in[3]; const u16* wo = (const u16*)d_in[4];
  const u16* wg = (const u16*)d_in[6]; const u16* wu = (const u16*)d_in[7]; const u16* wd = (const u16*)d_in[8]; const u16* c = (const u16*)d_in[9]; const u16* wmod = (const u16*)d_in[10];
  char* ws = (char*)d_ws;
  (void)hipFuncSetAttribute((const void*)k3_attn, hipFuncAttributeMaxDynamicSharedMemorySize, L3_SIZE);
  (void)hipFuncSetAttribute((const void*)k5_gateup, hipFuncAttributeMaxDynamicSharedMemorySize, 4 * 36864);
  (void)hipFuncSetAttribute((const void*)k6_down, hipFuncAttributeMaxDynamicSharedMemorySize, 4 * 36864);
  k1_mod_pack<<<216 + 114, 384, 0, stream>>>(c, wmod, wq, wk, wv, wo, ws);
  k4_pack<<<972, 256, 0, stream>>>(wg, wu, wd, ws);
  k2_qkv<<<256, 640, 0, stream>>>(x, ws);
  k3_attn<<<256, 512, L3_SIZE, stream>>>(x, ws);
  k5_gateup<<<256, 512, 4 * 36864, stream>>>(ws);
  k6_down<<<256, 512, 4 * 36864, stream>>>(ws);
  k7_out<<<576, 256, 0, stream>>>(ws, (u16*)d_out);
}
```

```cpp
#include <hip/hip_runtime.h>
#include <hip/hip_bf16.h>

typedef unsigned short u16;
using bf16x8 = __attribute__((ext_vector_type(8))) __bf16;
using bf16x2 = __attribute__((ext_vector_type(2))) __bf16;
using s16x4  = __attribute__((ext_vector_type(4))) short;
using f32x2  = __attribute__((ext_vector_type(2))) float;
using f32x4  = __attribute__((ext_vector_type(4))) float;
using f32x16 = __attribute__((ext_vector_type(16))) float;
using u32x2  = __attribute__((ext_vector_type(2))) unsigned;
using u32x4  = __attribute__((ext_vector_type(4))) unsigned;
#define DI __device__ __forceinline__
#define LDSP __attribute__((address_space(3)))
#define SBAR() __builtin_amdgcn_sched_barrier(0)
#define PIN(x) asm volatile("" : "+v"(x))
#define BC8(x) __builtin_bit_cast(bf16x8, (x))
#define MFMA16(a, b, c) __builtin_amdgcn_mfma_f32_16x16x32_bf16(BC8(a), BC8(b), (c), 0, 0, 0)
#define MFMA32(a, b, c) __builtin_amdgcn_mfma_f32_32x32x16_bf16(BC8(a), BC8(b), (c), 0, 0, 0)
#define MFMA32K8(a, b, c) __builtin_amdgcn_mfma_f32_32x32x8bf16_1k(__builtin_bit_cast(s16x4, (a)), __builtin_bit_cast(s16x4, (b)), (c), 0, 0, 0)
#define WAITVM(n) asm volatile("s_waitcnt vmcnt(" #n ")" ::: "memory")
#define BARRIER() asm volatile("s_waitcnt lgkmcnt(0)\n\ts_barrier" ::: "memory")

constexpr int S = 2048, D = 576, F = 4608;
constexpr size_t OFF_MOD  = 0;
constexpr size_t OFF_KMAX = 16384;
constexpr size_t OFF_WQKV = 32768;
constexpr size_t OFF_WO   = OFF_WQKV + 40 * 18 * 1024;
constexpr size_t OFF_Q    = OFF_WO + 18 * 36 * 1024;
constexpr size_t OFF_K    = OFF_Q + (size_t)S * D * 2;
constexpr size_t OFF_K1   = OFF_K + 65536;
constexpr size_t OFF_V    = OFF_K1 + 8192;
constexpr size_t OFF_X1   = OFF_V + 64 * 4 * 304;
constexpr size_t OFF_H2   = OFF_X1 + (size_t)S * D * 2;
constexpr size_t OFF_WGU  = OFF_H2 + (size_t)S * D * 2;
constexpr size_t OFF_WD   = OFF_WGU + (size_t)288 * 36864;
constexpr size_t OFF_ACT  = OFF_WD + (size_t)4 * 144 * 9216;
constexpr size_t OFF_PART = OFF_ACT + (size_t)128 * 144 * 1024;
constexpr size_t OFF_DBG  = OFF_PART + (size_t)4 * 128 * 36 * 1024;
constexpr size_t WS_END   = OFF_DBG + 8 * 1024 * 256;
#define STAMP(kid, i) do {} while (0)

DI float bf2f(u16 v) { return __uint_as_float(((unsigned)v) << 16); }
DI float bflo(unsigned v) { return __uint_as_float(v << 16); }
DI float bfhi(unsigned v) { return __uint_as_float(v & 0xffff0000u); }
DI unsigned pk(float a, float b) { f32x2 v = {a, b}; return __builtin_bit_cast(unsigned, __builtin_convertvector(v, bf16x2)); }
DI float rbf(float a) { return bflo(pk(a, a)); }
DI int rfl(int x) { return __builtin_amdgcn_readfirstlane(x); }
template <int CTRL> DI float dpp(float x) { return __int_as_float(__builtin_amdgcn_update_dpp(0, __float_as_int(x), CTRL, 0xf, 0xf, false)); }
DI float row16_sum(float x) { x += dpp<0x128>(x); x += dpp<0x124>(x); x += dpp<0x122>(x); x += dpp<0x121>(x); return x; }
DI float row16_max(float x) { x = fmaxf(x, dpp<0x128>(x)); x = fmaxf(x, dpp<0x124>(x)); x = fmaxf(x, dpp<0x122>(x)); x = fmaxf(x, dpp<0x121>(x)); return x; }
DI float xor16(float x) { auto r = __builtin_amdgcn_permlane16_swap(__float_as_uint(x), __float_as_uint(x), false, false); return __uint_as_float(r[0]) + __uint_as_float(r[1]); }
DI float xor32(float x) { auto r = __builtin_amdgcn_permlane32_swap(__float_as_uint(x), __float_as_uint(x), false, false); return __uint_as_float(r[0]) + __uint_as_float(r[1]); }
DI float max16(float x) { auto r = __builtin_amdgcn_permlane16_swap(__float_as_uint(x), __float_as_uint(x), false, false); return fmaxf(__uint_as_float(r[0]), __uint_as_float(r[1])); }
DI float max32(float x) { auto r = __builtin_amdgcn_permlane32_swap(__float_as_uint(x), __float_as_uint(x), false, false); return fmaxf(__uint_as_float(r[0]), __uint_as_float(r[1])); }

template <int N> DI void glds(const void* base, unsigned voff, unsigned lds) {
  unsigned sv;
  if constexpr (N == 1)
    asm volatile("s_mov_b32 %0, m0\n\ts_mov_b32 m0, %3\n\ts_nop 2\n\tglobal_load_lds_dwordx4 %1, %2\n\ts_mov_b32 m0, %0" : "=&s"(sv) : "v"(voff), "s"(base), "s"(lds) : "memory");
  else if constexpr (N == 4)
    asm volatile("s_mov_b32 %0, m0\n\ts_mov_b32 m0, %3\n\ts_nop 2\n\tglobal_load_lds_dwordx4 %1, %2\n\tglobal_load_lds_dwordx4 %1, %2 offset:1024\n\t"
                 "global_load_lds_dwordx4 %1, %2 offset:2048\n\tglobal_load_lds_dwordx4 %1, %2 offset:3072\n\ts_mov_b32 m0, %0" : "=&s"(sv) : "v"(voff), "s"(base), "s"(lds) : "memory");
}
template <bool Z> DI void mf16(f32x4& c, const u32x4& a, const u32x4& b) {
  if constexpr (Z) asm volatile("v_mfma_f32_16x16x32_bf16 %0, %1, %2, 0" : "=v"(c) : "v"(a), "v"(b));
  else asm volatile("v_mfma_f32_16x16x32_bf16 %0, %1, %2, %0" : "+v"(c) : "v"(a), "v"(b));
}
#define MF16(Z, c, a, b) do { if (Z) mf16<true>(c, a, b); else mf16<false>(c, a, b); } while (0)
#define MFMA_DRAIN() asm volatile("s_nop 7\n\ts_nop 3" ::: "memory")
DI u32x4 ldsr128(unsigned a) { return *(const LDSP u32x4*)(a); }
DI u32x2 ldsr64(unsigned a) { return *(const LDSP u32x2*)(a); }
DI void ldsw128(unsigned a, u32x4 v) { *(LDSP u32x4*)(a) = v; }
DI void ldsw64(unsigned a, u32x2 v) { *(LDSP u32x2*)(a) = v; }
DI void ldsw32(unsigned a, unsigned v) { *(LDSP unsigned*)(a) = v; }
DI float ldsrf(unsigned a) { return *(const LDSP float*)(a); }
DI u32x2 ldstr(unsigned a) { return __builtin_bit_cast(u32x2, __builtin_amdgcn_ds_read_tr16_b64_v4i16((LDSP s16x4*)(a))); }

DI int wo_step_ks(int n) { return n < 8 ? n : (n == 8 ? 16 : (n < 17 ? n - 1 : 17)); }
DI int wo_row(int ks, int q, int j) {
  if (ks < 16) return 18 * (2 * ks + (j >> 2)) + 4 * q + (j & 3);
  return 18 * (16 * (ks - 16) + 4 * q + (j >> 1)) + 16 + (j & 1);
}
__global__ __launch_bounds__(384) void k1_mod_pack(const u16* __restrict__ c, const u16* __restrict__ wmod, const u16* __restrict__ wq, const u16* __restrict__ wk,
                                                   const u16* __restrict__ wv, const u16* __restrict__ wo, char* __restrict__ ws) {
  __shared__ float red[6][16];
  const int b = blockIdx.x, tid = threadIdx.x, lane = tid & 63, wave = rfl(tid >> 6);
  if (b >= 330) {
    unsigned long long pc; asm volatile("s_getpc_b64 %0" : "=s"(pc));
    const char* p = (const char*)(pc & ~255ull) + ((b - 330) >> 3) * 6144;
    u32x4 t; asm volatile("global_load_dwordx4 %0, %1, %2\n\ts_waitcnt vmcnt(0)" : "=v"(t) : "v"(tid * 16), "s"(p) : "memory");
    return;
  }
  if (b < 216) {
    const int cg = (b & 7) * 27 + (b >> 3);
    const int half = tid & 1, r0 = tid >> 1;
    u32x4 w[3]; u16 cv[3];
#pragma unroll
    for (int i = 0; i < 3; ++i) { w[i] = *(const u32x4*)(wmod + (size_t)(r0 + 192 * i) * 3456 + cg * 16 + half * 8); cv[i] = c[r0 + 192 * i]; }
    float a[8] = {0, 0, 0, 0, 0, 0, 0, 0};
#pragma unroll
    for (int i = 0; i < 3; ++i) {
      const float x = bf2f(cv[i]);
      const float s = rbf(x / (1.f + __expf(-x)));
#pragma unroll
      for (int e = 0; e < 4; ++e) { a[2 * e] += s * bflo(w[i][e]); a[2 * e + 1] += s * bfhi(w[i][e]); }
    }
#pragma unroll
    for (int e = 0; e < 8; ++e) { float v = a[e]; v += dpp<0x128>(v); v += dpp<0x124>(v); v += dpp<0x122>(v); v = xor16(v); v = xor32(v); a[e] = v; }
    if (lane < 2) {
#pragma unroll
      for (int e = 0; e < 8; ++e) red[wave][lane * 8 + e] = a[e];
    }
    __syncthreads();
    if (tid < 16) {
      float v = 0;
#pragma unroll
      for (int i = 0; i < 6; ++i) v += red[i][tid];
      v = rbf(v);
      const int n = cg * 16 + tid, sec = n / D;
      if (sec == 1 || sec == 4) v = rbf(1.f + v);
      ((float*)(ws + OFF_MOD))[n] = v;
    }
    return;
  }
  const int r = lane & 15, q = lane >> 4;
#pragma unroll
  for (int it = 0; it < 2; ++it) {
    const int f = ((b - 216) * 6 + wave) * 2 + it;
    u16 v[8];
    if (f < 720) {
      const int T = f / 18, ks = f % 18, g = T / 10, i = T % 10;
      const u16* src; int ld, col; bool ok = true;
      if (g < 2 && i >= 8) { src = g == 0 ? wk : wv; ld = 18; col = (i - 8) * 16 + r; ok = col < 18; }
      else { src = wq; ld = D; col = (g < 2 ? g * 8 + i : 16 + (g - 2) * 10 + i) * 16 + r; }
      if (!ok) col = 0;
#pragma unroll
      for (int j = 0; j < 8; ++j) { const u16 t = src[(32 * ks + 8 * q + j) * ld + col]; v[j] = ok ? t : (u16)0; }
      u32x4 o = {v[0] | (unsigned)v[1] << 16, v[2] | (unsigned)v[3] << 16, v[4] | (unsigned)v[5] << 16, v[6] | (unsigned)v[7] << 16};
      *(u32x4*)(ws + OFF_WQKV + (size_t)f * 1024 + lane * 16) = o;
    } else {
      const int ff = f - 720, n = ff / 36, t = ff % 36, ks = wo_step_ks(n);
#pragma unroll
      for (int j = 0; j < 8; ++j) v[j] = wo[wo_row(ks, q, j) * D + t * 16 + r];
      u32x4 o = {v[0] | (unsigned)v[1] << 16, v[2] | (unsigned)v[3] << 16, v[4] | (unsigned)v[5] << 16, v[6] | (unsigned)v[7] << 16};
      *(u32x4*)(ws + OFF_WO + (size_t)ff * 1024 + lane * 16) = o;
    }
  }
}

constexpr float QSCALE = 0.23570226039551584f * 1.4426950408889634f;
__global__ __launch_bounds__(640) void k2_qkv(const u16* __restrict__ x, char* __restrict__ ws) {
  __shared__ __attribute__((aligned(16))) char hs[2 * 18 * 1024];
  const int b = blockIdx.x, tid = threadIdx.x, lane = tid & 63, wave = rfl(tid >> 6);
  const int g = (b >> 3) & 3, tt = (b & 7) + 8 * (b >> 5);
  const unsigned hb = (unsigned)(size_t)hs;
  u32x4 wf[18];
  const int l16 = lane & 15;
  const int row = wave * 4 + (lane >> 4);
  u32x4 xv[5];
  if (wave < 8) {
    const u16* xr = x + (size_t)(tt * 32 + row) * D;
#pragma unroll
    for (int i = 0; i < 4; ++i) xv[i] = *(const u32x4*)(xr + (i * 16 + l16) * 8);
    xv[4] = *(const u32x4*)(xr + (64 + (l16 & 7)) * 8);
  }
  {
    const char* wp = ws + OFF_WQKV + (size_t)(g * 10 + wave) * 18 * 1024 + lane * 16;
#pragma unroll
    for (int ks = 0; ks < 18; ++ks) wf[ks] = *(const u32x4*)(wp + ks * 1024);
  }
  if (wave < 8) {
    const float* mod = (const float*)(ws + OFF_MOD);
    const float m4 = l16 < 8 ? 1.f : 0.f;
    float s = 0;
#pragma unroll
    for (int i = 0; i < 5; ++i) { float t = 0;
#pragma unroll
      for (int e = 0; e < 4; ++e) t += bflo(xv[i][e]) + bfhi(xv[i][e]);
      s += i < 4 ? t : t * m4; }
    const float mean = row16_sum(s) * (1.f / D);
    float v = 0;
#pragma unroll
    for (int i = 0; i < 5; ++i) { float t = 0;
#pragma unroll
      for (int e = 0; e < 4; ++e) { const float a = bflo(xv[i][e]) - mean, bb = bfhi(xv[i][e]) - mean; t += a * a + bb * bb; }
      v += i < 4 ? t : t * m4; }
    const float rs = rsqrtf(row16_sum(v) * (1.f / D) + 1e-5f);
#pragma unroll
    for (int i = 0; i < 5; ++i) {
      const int c = i < 4 ? i * 16 + l16 : 64 + (l16 & 7);
      const f32x4 sh0 = *(const f32x4*)(mod + c * 8), sh1 = *(const f32x4*)(mod + c * 8 + 4);
      const f32x4 sc0 = *(const f32x4*)(mod + D + c * 8), sc1 = *(const f32x4*)(mod + D + c * 8 + 4);
      u32x4 o;
      o[0] = pk((bflo(xv[i][0]) - mean) * rs * sc0[0] + sh0[0], (bfhi(xv[i][0]) - mean) * rs * sc0[1] + sh0[1]);
      o[1] = pk((bflo(xv[i][1]) - mean) * rs * sc0[2] + sh0[2], (bfhi(xv[i][1]) - mean) * rs * sc0[3] + sh0[3]);
      o[2] = pk((bflo(xv[i][2]) - mean) * rs * sc1[0] + sh1[0], (bfhi(xv[i][2]) - mean) * rs * sc1[1] + sh1[1]);
      o[3] = pk((bflo(xv[i][3]) - mean) * rs * sc1[2] + sh1[2], (bfhi(xv[i][3]) - mean) * rs * sc1[3] + sh1[3]);
      const int ks = c >> 2, q = c & 3, gsw = q | ((ks & 1) ? 12 : 0);
      if (i < 4 || l16 < 8) ldsw128(hb + (row >> 4) * 18432 + ks * 1024 + (q * 16 + ((row & 15) ^ gsw)) * 16, o);
    }
  }
  __syncthreads();
  const int q = lane >> 4;
  const unsigned ra0 = hb + (q * 16 + (l16 ^ q)) * 16, ra1 = hb + (q * 16 + (l16 ^ (q | 12))) * 16;
  f32x4 acc0 = {0, 0, 0, 0}, acc1 = {0, 0, 0, 0};
  const bool isv = (g == 1 && wave >= 8);
  if (!isv) {
#pragma unroll
    for (int ks = 0; ks < 18; ++ks) {
      const unsigned ra = ((ks & 1) ? ra1 : ra0) + ks * 1024;
      acc0 = MFMA16(wf[ks], ldsr128(ra), acc0);
      acc1 = MFMA16(wf[ks], ldsr128(ra + 18432), acc1);
    }
  } else {
#pragma unroll
    for (int ks = 0; ks < 18; ++ks) {
      const unsigned ra = ((ks & 1) ? ra1 : ra0) + ks * 1024;
      acc0 = MFMA16(ldsr128(ra), wf[ks], acc0);
      acc1 = MFMA16(ldsr128(ra + 18432), wf[ks], acc1);
    }
  }
  if (g >= 2 || wave < 8) {
    const int n0 = (g < 2 ? g * 8 + wave : 16 + (g - 2) * 10 + wave) * 16 + 4 * q;
    u16* qp = (u16*)(ws + OFF_Q) + (size_t)(tt * 32 + l16) * D + n0;
    *(u32x2*)qp = u32x2{pk(acc0[0] * QSCALE, acc0[1] * QSCALE), pk(acc0[2] * QSCALE, acc0[3] * QSCALE)};
    *(u32x2*)(qp + 16 * D) = u32x2{pk(acc1[0] * QSCALE, acc1[1] * QSCALE), pk(acc1[2] * QSCALE, acc1[3] * QSCALE)};
  } else if (g == 0) {
    u32x2 k0 = {pk(acc0[0], acc0[1]), pk(acc0[2], acc0[3])}, k1 = {pk(acc1[0], acc1[1]), pk(acc1[2], acc1[3])};
    float n0 = bflo(k0[0]) * bflo(k0[0]) + bfhi(k0[0]) * bfhi(k0[0]) + bflo(k0[1]) * bflo(k0[1]) + bfhi(k0[1]) * bfhi(k0[1]);
    float n1 = bflo(k1[0]) * bflo(k1[0]) + bfhi(k1[0]) * bfhi(k1[0]) + bflo(k1[1]) * bflo(k1[1]) + bfhi(k1[1]) * bfhi(k1[1]);
    n0 = xor32(xor16(n0)); n1 = xor32(xor16(n1));
    const float nm = row16_max(fmaxf(n0, n1));
    if (lane == 0) ((float*)(ws + OFF_KMAX))[(wave - 8) * 64 + tt] = nm;
    if (wave == 8) {
      char* kp = ws + OFF_K + (size_t)tt * 1024 + (q >> 1) * 512 + l16 * 16 + (q & 1) * 8;
      *(u32x2*)kp = k0; *(u32x2*)(kp + 256) = k1;
    } else if (q == 0) {
      char* kp = ws + OFF_K1 + (size_t)(tt >> 2) * 512 + l16 * 16 + (tt & 3) * 4;
      *(unsigned*)kp = k0[0]; *(unsigned*)(kp + 256) = k1[0];
    }
  } else {
    const int dim = (wave - 8) * 16 + l16;
    if (dim < 19) {
      char* vp = ws + OFF_V + (size_t)tt * 1216 + (q & 1) * 304 + dim * 16 + (q >> 1) * 8;
      const u32x2 one = {0x3f803f80u, 0x3f803f80u};
      *(u32x2*)vp = dim < 18 ? u32x2{pk(acc0[0], acc0[1]), pk(acc0[2], acc0[3])} : one;
      *(u32x2*)(vp + 608) = dim < 18 ? u32x2{pk(acc1[0], acc1[1]), pk(acc1[2], acc1[3])} : one;
    }
  }
}

constexpr int L3_QF0 = 0, L3_QF1 = 8192, L3_PART = 12288, L3_OB = L3_PART + 2 * 20480, L3_RING = L3_OB + 18 * 512, L3_XM = L3_RING + 8 * 2 * 5120,
              L3_MISC = L3_XM + 16384, L3_SIZE = L3_MISC + 1024;
constexpr float SHIFT_SAFE = 48.f;

DI void split3(float m, unsigned& hi, unsigned& mid, unsigned& lo) {
  const unsigned h = __float_as_uint(m) & 0xffff0000u; const float r1 = m - __uint_as_float(h);
  const unsigned md = __float_as_uint(r1) & 0xffff0000u; const float r2 = r1 - __uint_as_float(md);
  hi = h >> 16; mid = md >> 16; lo = __float_as_uint(r2) >> 16;
}
#define EX2(x) __builtin_amdgcn_exp2f(x)

__global__ __launch_bounds__(512, 2) void k3_attn(const u16* __restrict__ x, char* __restrict__ ws) {
  extern __shared__ __attribute__((aligned(16))) char smem[];
  const unsigned lb = (unsigned)(size_t)smem;
  const int b = blockIdx.x, tid = threadIdx.x, lane = tid & 63, wave = rfl(tid >> 6);
  const int r32 = lane & 31, hh = lane >> 5, l16 = lane & 15, q4 = lane >> 4;
  const int tok0 = b * 8;
  STAMP(3, 0);
  u32x4 q0; unsigned q1; float kmA, kmB;
  {
    const u16* qp = (const u16*)(ws + OFF_Q) + (size_t)(tok0 + (r32 & 7)) * D + (4 * wave + (r32 >> 3)) * 18;
    const unsigned* qd = (const unsigned*)(qp + 8 * hh);
    q0 = u32x4{qd[0], qd[1], qd[2], qd[3]};
    q1 = *(const unsigned*)(qp + 16);
    kmA = ((const float*)(ws + OFF_KMAX))[lane]; kmB = ((const float*)(ws + OFF_KMAX))[64 + lane];
  }
  SBAR();
  u32x4 K0[8], V0[8], V1[8], K1[2];
  const char* kp = ws + OFF_K + (size_t)wave * 8192 + lane * 16;
  const char* vp = ws + OFF_V + (size_t)wave * 8 * 1216 + hh * 304 + (r32 < 18 ? r32 : 18) * 16;
#define LDK(t) K0[t] = *(const u32x4*)(kp + (t) * 1024)
#define LDV(t) do { V0[t] = *(const u32x4*)(vp + (t) * 1216); V1[t] = *(const u32x4*)(vp + (t) * 1216 + 608); } while (0)
  {
    const char* k1p = hh == 0 ? ws + OFF_K1 + (size_t)wave * 1024 + r32 * 16 : ws + OFF_V + 288;
    LDK(0); K1[0] = *(const u32x4*)k1p; LDK(1); LDV(0); K1[1] = *(const u32x4*)(k1p + (hh == 0 ? 512 : 0)); LDK(2); LDV(1);
  }
  SBAR();
  {
    float n = 0;
#pragma unroll
    for (int e = 0; e < 4; ++e) n += bflo(q0[e]) * bflo(q0[e]) + bfhi(q0[e]) * bfhi(q0[e]);
    n = xor32(n) + bflo(q1) * bflo(q1) + bfhi(q1) * bfhi(q1);
    kmA = max32(max16(row16_max(kmA))); kmB = max32(max16(row16_max(kmB)));
    const float mh = __builtin_amdgcn_sqrtf(n * (kmA + kmB));
    const bool bad = !(mh <= SHIFT_SAFE);
    const unsigned nm = pk(-mh, 0.f);
    ldsw128(lb + L3_QF0 + wave * 1024 + lane * 16, q0);
    ldsw64(lb + L3_QF1 + wave * 512 + lane * 8, hh == 0 ? u32x2{q1, 0u} : u32x2{nm, 0u});
    const unsigned anybad = __builtin_amdgcn_ballot_w64(bad) != 0;
    if (lane == 0) ldsw32(lb + L3_MISC + wave * 4, anybad);
  }
  BARRIER();
  const f32x16 zero16 = {0, 0, 0, 0, 0, 0, 0, 0, 0, 0, 0, 0, 0, 0, 0, 0};
#define K1P(t) (u32x2{K1[(t) >> 2][(t) & 2], K1[(t) >> 2][((t) & 2) + 1]})
  {
    const u32x4 fl0 = ldsr128(lb + L3_MISC), fl1 = ldsr128(lb + L3_MISC + 16);
    if (rfl(fl0[0] | fl0[1] | fl0[2] | fl0[3] | fl1[0] | fl1[1] | fl1[2] | fl1[3])) {
#pragma unroll 1
      for (int g = 0; g < 8; ++g) {
        const u32x4 Q0 = ldsr128(lb + L3_QF0 + g * 1024 + lane * 16);
        const unsigned qq = hh == 0 ? ldsr64(lb + L3_QF1 + g * 512 + lane * 8)[0] : 0u;
        float m = -3.0e38f;
#pragma unroll 1
        for (int t = 0; t < 8; ++t) {
          const u32x4 k0 = *(const u32x4*)(kp + t * 1024);
          const unsigned k1 = *(const unsigned*)(ws + OFF_K1 + (size_t)wave * 1024 + (t >> 2) * 512 + r32 * 16 + (t & 3) * 4);
          f32x16 s = MFMA32(k0, Q0, zero16); s = MFMA32K8((u32x2{k1, 0u}), (u32x2{qq, 0u}), s);
#pragma unroll
          for (int i = 0; i < 16; ++i) m = fmaxf(m, s[i]);
        }
        m = max32(m);
        if (hh == 0) *(LDSP float*)(lb + L3_PART + (g * 8 + wave) * 128 + r32 * 4) = m;
      }
      BARRIER();
      float m = -3.0e38f;
#pragma unroll
      for (int w = 0; w < 8; ++w) m = fmaxf(m, ldsrf(lb + L3_PART + (wave * 8 + w) * 128 + r32 * 4));
      unsigned hi, mid, lo; split3(-m, hi, mid, lo);
      if (hh == 1) ldsw64(lb + L3_QF1 + wave * 512 + lane * 8, u32x2{hi | mid << 16, lo});
      BARRIER();
    }
  }
  STAMP(3, 1);
  const int nt0 = wave < 4 ? wave * 5 : 20 + (wave - 4) * 4;
  const char* wob = ws + OFF_WO + (size_t)nt0 * 1024;
  const unsigned ring = lb + L3_RING + wave * 10240;
  f32x4 pa[5];
#pragma unroll
  for (int i = 0; i < 5; ++i) pa[i] = f32x4{0, 0, 0, 0};
  auto wo_issue = [&](int n) {
    const char* src = wob + (size_t)n * 36864; const unsigned dst = ring + (n & 1) * 5120;
    glds<4>(src, lane * 16, dst);
    glds<1>(src + (wave < 4 ? 4096 : 0), lane * 16, dst + 4096);
  };
  unsigned rw_addr, rw_step; int bp_idx;
  {
    const int c4 = lane / 10, u = lane - 10 * c4, hd = wave >> 1, tk = (wave & 1) * 4 + c4;
    bp_idx = (c4 * 10 + 9) * 4;
    if (lane < 40 && u < 8) { rw_addr = lb + L3_OB + (hd >> 1) * 512 + (u >> 1) * 128 + tk * 16 + (hd & 1) * 8 + (u & 1) * 4; rw_step = 1024; }
    else if (lane < 40 && u == 8) { rw_addr = lb + L3_OB + 8192 + tk * 16 + hd * 4; rw_step = 128; }
    else { rw_addr = lb + L3_MISC + 768 + (lane & 31) * 4; rw_step = 0; }
  }
  const unsigned rd_addr = lb + L3_PART + wave * 320 + lane * 8;
  const unsigned pw_addr = lb + L3_PART + wave * 2560 + r32 * 80 + hh * 16;
  const unsigned ob_addr = lb + L3_OB + q4 * 128 + (lane & 7) * 16;
  f32x2 rp[8]; float rl; u32x4 ob, wa[5];
  u32x4 wt[3][5];

  u32x4 Q0 = ldsr128(lb + L3_QF0 + lane * 16), Q0n;
  u32x2 Qa = ldsr64(lb + L3_QF1 + lane * 8), Qb, Qan;
  Qb = hh == 0 ? u32x2{0u, Qa[0]} : Qa;
  f32x16 sA = MFMA32(K0[0], Q0, zero16), sB, O;
  sA = MFMA32K8(K1P(0), Qa, sA);
  f32x4 ep, ea, eb; u32x4 p0, p1;
  int n = 0;

#define TILE(PH, t, SC, SN, X0, X1, X2, X3) do { \
    if (!((PH) == 2 && (t) == 7)) { SN = MFMA32(K0[((t) + 1) & 7], (t) == 7 ? Q0n : Q0, zero16); PIN(SN); } \
    SBAR(); \
    if (!((PH) == 0 && (t) == 0)) { p1[2] = pk(ep[0], ep[1]); p1[3] = pk(ep[2], ep[3]); PIN(p1); SBAR(); } \
    ea = f32x4{EX2(SC[0]), EX2(SC[1]), EX2(SC[2]), EX2(SC[3])}; PIN(ea); SBAR(); X0; SBAR(); \
    if (!((PH) == 0 && (t) == 0)) { O = MFMA32(V1[((t) + 7) & 7], p1, O); PIN(O); } \
    SBAR(); \
    p0[0] = pk(ea[0], ea[1]); p0[1] = pk(ea[2], ea[3]); PIN(p0); SBAR(); \
    eb = f32x4{EX2(SC[4]), EX2(SC[5]), EX2(SC[6]), EX2(SC[7])}; PIN(eb); SBAR(); X1; SBAR(); \
    if ((t) == 7) { Q0 = Q0n; Qa = Qan; } \
    if (!((PH) == 2 && (t) == 7)) { SN = MFMA32K8(K1P(((t) + 1) & 7), (((t) + 1) & 1) ? Qb : Qa, SN); PIN(SN); } \
    SBAR(); \
    p0[2] = pk(eb[0], eb[1]); p0[3] = pk(eb[2], eb[3]); PIN(p0); SBAR(); \
    ea = f32x4{EX2(SC[8]), EX2(SC[9]), EX2(SC[10]), EX2(SC[11])}; PIN(ea); SBAR(); X2; SBAR(); \
    O = MFMA32(V0[t], p0, (t) == 0 ? zero16 : O); PIN(O); \
    SBAR(); \
    p1[0] = pk(ea[0], ea[1]); p1[1] = pk(ea[2], ea[3]); PIN(p1); SBAR(); \
    ep = f32x4{EX2(SC[12]), EX2(SC[13]), EX2(SC[14]), EX2(SC[15])}; PIN(ep); SBAR(); X3; SBAR(); \
  } while (0)
#define NOP_ do {} while (0)
#define WRITE_O(par) do { PIN(O); const unsigned pw_ = pw_addr + (par) * 20480; \
    ldsw128(pw_, __builtin_bit_cast(u32x4, f32x4{O[0], O[1], O[2], O[3]})); ldsw128(pw_ + 32, __builtin_bit_cast(u32x4, f32x4{O[4], O[5], O[6], O[7]})); \
    if (hh == 0) ldsw128(pw_ + 64, __builtin_bit_cast(u32x4, f32x4{O[8], O[9], O[10], O[11]})); } while (0)
#define RED_RD(par) do { _Pragma("unroll") for (int w_ = 0; w_ < 8; ++w_) rp[w_] = __builtin_bit_cast(f32x2, ldsr64(rd_addr + (par) * 20480 + w_ * 2560)); } while (0)
#define RED_ADD() do { rp[0] = ((rp[0] + rp[1]) + (rp[2] + rp[3])) + ((rp[4] + rp[5]) + (rp[6] + rp[7])); \
    rl = __int_as_float(__builtin_amdgcn_ds_bpermute(bp_idx, __float_as_int(rp[0][0]))); } while (0)
#define RED_FIN() do { const float inv_ = __builtin_amdgcn_rcpf(rl); ldsw32(rw_addr, pk(rp[0][0] * inv_, rp[0][1] * inv_)); rw_addr += rw_step; } while (0)
#define OP_RD() do { WAITVM(5); ob = ldsr128(ob_addr + wo_step_ks(n) * 512); const unsigned s_ = ring + (n & 1) * 5120 + lane * 16; \
    _Pragma("unroll") for (int i_ = 0; i_ < 5; ++i_) wa[i_] = ldsr128(s_ + i_ * 1024); } while (0)
#define OP_M(i) do { pa[i] = MFMA16(wa[i], ob, pa[i]); PIN(pa[i]); } while (0)
#define OP_END(ISS) do { if (ISS) wo_issue(n + 2); ++n; } while (0)
#define LBAR() BARRIER()
#define QNEXT(gn) do { Q0n = ldsr128(lb + L3_QF0 + (gn) * 1024 + lane * 16); Qan = ldsr64(lb + L3_QF1 + (gn) * 512 + lane * 8); } while (0)
#define QB() do { Qb = hh == 0 ? u32x2{0u, Qa[0]} : Qa; PIN(Qb); } while (0)

  TILE(0, 0, sA, sB, LDK(3), LDV(2), NOP_, NOP_);
  TILE(0, 1, sB, sA, LDK(4), LDV(3), NOP_, NOP_);
  TILE(0, 2, sA, sB, LDK(5), LDV(4), NOP_, NOP_);
  TILE(0, 3, sB, sA, LDK(6), LDV(5), NOP_, NOP_);
  TILE(0, 4, sA, sB, LDK(7), LDV(6), NOP_, NOP_);
  TILE(0, 5, sB, sA, NOP_, LDV(7), NOP_, NOP_);
  TILE(0, 6, sA, sB, QNEXT(1), NOP_, NOP_, NOP_);
  TILE(0, 7, sB, sA, NOP_, NOP_, NOP_, QB());
  PIN(V1[7]);
  wo_issue(0); wo_issue(1);
  {
    const char* s0 = (const char*)(x + (size_t)tok0 * D) + wave * 1024;
    const char* s1 = wave == 0 ? (const char*)(x + (size_t)tok0 * D) + 8192 : ws + OFF_MOD + 2 * D * 4 + (wave - 1) * 1024;
    glds<1>(s0, lane * 16, lb + L3_XM + wave * 1024);
    glds<1>(s1, lane * 16, lb + L3_XM + 8192 + wave * 1024);
  }
  TILE(1, 0, sA, sB, NOP_, NOP_, WRITE_O(0), LBAR());
  TILE(1, 1, sB, sA, RED_RD(0), NOP_, NOP_, RED_ADD());
  TILE(1, 2, sA, sB, NOP_, NOP_, RED_FIN(), NOP_);
  TILE(1, 3, sB, sA, NOP_, NOP_, NOP_, NOP_);
  TILE(1, 4, sA, sB, NOP_, NOP_, NOP_, NOP_);
  TILE(1, 5, sB, sA, NOP_, NOP_, NOP_, NOP_);
  TILE(1, 6, sA, sB, QNEXT(2), NOP_, NOP_, NOP_);
  TILE(1, 7, sB, sA, NOP_, NOP_, NOP_, QB());
  WAITVM(0);
#pragma unroll 1
  for (int g = 2; g < 7; ++g) {
    const int par = (g - 1) & 1;
    TILE(1, 0, sA, sB, NOP_, NOP_, WRITE_O(par), LBAR());
    TILE(1, 1, sB, sA, RED_RD(par), NOP_, NOP_, RED_ADD());
    TILE(1, 2, sA, sB, OP_RD(), NOP_, RED_FIN(), OP_M(0));
    TILE(1, 3, sB, sA, OP_M(1), OP_M(2), OP_M(3), { OP_M(4); OP_END(true); });
    TILE(1, 4, sA, sB, OP_RD(), NOP_, NOP_, OP_M(0));
    TILE(1, 5, sB, sA, OP_M(1), OP_M(2), OP_M(3), { OP_M(4); OP_END(true); });
    TILE(1, 6, sA, sB, QNEXT(g + 1), NOP_, NOP_, NOP_);
    TILE(1, 7, sB, sA, NOP_, NOP_, NOP_, QB());
    if (g == 5) { OP_RD(); OP_M(0); OP_M(1); OP_M(2); OP_M(3); OP_M(4); OP_END(true); SBAR(); }
  }
#define LDWT(i) do { _Pragma("unroll") for (int j_ = 0; j_ < 5; ++j_) wt[i][j_] = *(const u32x4*)(wob + (size_t)(15 + (i)) * 36864 + (j_ < 4 || wave < 4 ? j_ : 0) * 1024 + lane * 16); } while (0)
  TILE(2, 0, sA, sB, NOP_, NOP_, WRITE_O(0), LBAR());
  TILE(2, 1, sB, sA, RED_RD(0), NOP_, NOP_, RED_ADD());
  TILE(2, 2, sA, sB, OP_RD(), NOP_, RED_FIN(), OP_M(0));
  TILE(2, 3, sB, sA, OP_M(1), OP_M(2), OP_M(3), { OP_M(4); OP_END(true); });
  TILE(2, 4, sA, sB, OP_RD(), NOP_, NOP_, OP_M(0));
  TILE(2, 5, sB, sA, OP_M(1), OP_M(2), OP_M(3), { OP_M(4); OP_END(true); });
  TILE(2, 6, sA, sB, LDWT(0), NOP_, LDWT(1), NOP_);
  TILE(2, 7, sB, sA, LDWT(2), NOP_, NOP_, NOP_);
  p1[2] = pk(ep[0], ep[1]); p1[3] = pk(ep[2], ep[3]);
  O = MFMA32(V1[7], p1, O);
  WRITE_O(1);
  BARRIER();
  STAMP(3, 9);
  RED_RD(1);
  WAITVM(15);
#pragma unroll
  for (int i = 0; i < 2; ++i) {
    ob = ldsr128(ob_addr + wo_step_ks(13 + i) * 512);
#pragma unroll
    for (int j = 0; j < 5; ++j) pa[j] = MFMA16(ldsr128(ring + ((13 + i) & 1) * 5120 + lane * 16 + j * 1024), ob, pa[j]);
    if (i == 0) RED_ADD();
  }
  RED_FIN();
  const int tk = lane & 7;
  u32x2 xr[5]; f32x4 ga[5], sb[5], cb[5];
#pragma unroll
  for (int j = 0; j < 5; ++j) {
    const int nn = (nt0 + (j < 4 || wave < 4 ? j : 0)) * 16 + 4 * q4;
    xr[j] = ldsr64(lb + L3_XM + tk * 1152 + nn * 2);
    ga[j] = __builtin_bit_cast(f32x4, ldsr128(lb + L3_XM + 9216 + nn * 4));
    sb[j] = __builtin_bit_cast(f32x4, ldsr128(lb + L3_XM + 9216 + 2304 + nn * 4));
    cb[j] = __builtin_bit_cast(f32x4, ldsr128(lb + L3_XM + 9216 + 4608 + nn * 4));
  }
  BARRIER();
#pragma unroll
  for (int i = 0; i < 3; ++i) {
    ob = ldsr128(ob_addr + wo_step_ks(15 + i) * 512);
#pragma unroll
    for (int j = 0; j < 5; ++j) pa[j] = MFMA16(wt[i][j], ob, pa[j]);
  }
  float xv[5][4];
  const int nv = wave < 4 ? 5 : 4;
  float sum = 0;
#pragma unroll
  for (int j = 0; j < 5; ++j) {
    const unsigned a = pk(bflo(xr[j][0]) + rbf(ga[j][0] * rbf(pa[j][0])), bfhi(xr[j][0]) + rbf(ga[j][1] * rbf(pa[j][1])));
    const unsigned c = pk(bflo(xr[j][1]) + rbf(ga[j][2] * rbf(pa[j][2])), bfhi(xr[j][1]) + rbf(ga[j][3] * rbf(pa[j][3])));
    xv[j][0] = bflo(a); xv[j][1] = bfhi(a); xv[j][2] = bflo(c); xv[j][3] = bfhi(c);
    if (j < nv) {
      sum += (xv[j][0] + xv[j][1]) + (xv[j][2] + xv[j][3]);
      if (l16 < 8) *(u32x2*)((u16*)(ws + OFF_X1) + (size_t)(tok0 + tk) * D + (nt0 + j) * 16 + 4 * q4) = u32x2{a, c};
    }
  }
  sum = xor32(xor16(sum));
  const float mw = sum * (wave < 4 ? 1.f / 80.f : 1.f / 64.f);
  float m2 = 0;
#pragma unroll
  for (int j = 0; j < 5; ++j) if (j < nv) {
#pragma unroll
    for (int e = 0; e < 4; ++e) { const float d = xv[j][e] - mw; m2 += d * d; }
  }
  m2 = xor32(xor16(m2));
  if (lane < 8) ldsw64(lb + L3_MISC + 64 + tk * 64 + wave * 8, u32x2{__float_as_uint(mw), __float_as_uint(m2)});
  BARRIER();
  float mean, rs;
  {
    u32x4 st[4];
#pragma unroll
    for (int i = 0; i < 4; ++i) st[i] = ldsr128(lb + L3_MISC + 64 + tk * 64 + i * 16);
    float tot = 0;
#pragma unroll
    for (int w = 0; w < 8; ++w) tot += __uint_as_float(st[w >> 1][(w & 1) * 2]) * (w < 4 ? 80.f : 64.f);
    mean = tot * (1.f / D);
    float M2 = 0;
#pragma unroll
    for (int w = 0; w < 8; ++w) { const float d = __uint_as_float(st[w >> 1][(w & 1) * 2]) - mean; M2 += __uint_as_float(st[w >> 1][(w & 1) * 2 + 1]) + d * d * (w < 4 ? 80.f : 64.f); }
    rs = rsqrtf(M2 * (1.f / D) + 1e-5f);
  }
  char* h2 = ws + OFF_H2 + (size_t)(b >> 1) * 18432 + (q4 * 16 + (b & 1) * 8 + tk) * 16;
#pragma unroll
  for (int j = 0; j < 5; ++j) if (j < nv && l16 < 8) {
    const int nt = nt0 + j;
    const u32x2 o = {pk((xv[j][0] - mean) * rs * cb[j][0] + sb[j][0], (xv[j][1] - mean) * rs * cb[j][1] + sb[j][1]),
                     pk((xv[j][2] - mean) * rs * cb[j][2] + sb[j][2], (xv[j][3] - mean) * rs * cb[j][3] + sb[j][3])};
    *(u32x2*)(h2 + (nt >> 1) * 1024 + (nt & 1) * 8) = o;
  }
  STAMP(3, 10);
}

__global__ __launch_bounds__(256) void k4_pack(const u16* __restrict__ wg, const u16* __restrict__ wu, const u16* __restrict__ wd, char* __restrict__ ws) {
  __shared__ __attribute__((aligned(16))) char st[4 * 4096];
  const int lane = threadIdx.x & 63, wave = rfl(threadIdx.x >> 6);
  const int u = blockIdx.x * 4 + wave;
  const unsigned sb = (unsigned)(size_t)st + wave * 4096;
  const int mat = u / 1296, r = u % 1296;
  const char* src; int ld; char* dst; int t0, ks;
  if (mat < 2) { ks = r / 72; t0 = (r % 72) * 4; src = (const char*)(mat == 0 ? wg : wu); ld = F * 2; }
  else { ks = r / 9; t0 = (r % 9) * 4; src = (const char*)wd; ld = D * 2; }
  src += (size_t)ks * 32 * ld + t0 * 32;
#pragma unroll
  for (int i = 0; i < 4; ++i) glds<1>(src + (size_t)i * 8 * ld, (lane >> 3) * ld + (lane & 7) * 16, sb + i * 1024);
  WAITVM(0);
  const unsigned ta = sb + ((lane >> 4) * 4 + ((lane >> 2) & 3)) * 128 + (lane & 3) * 8;
#pragma unroll
  for (int t = 0; t < 4; ++t) {
    const u32x2 lo = ldstr(ta + t * 32), hi = ldstr(ta + t * 32 + 2048);
    const int tt = t0 + t;
    if (mat < 2) dst = ws + OFF_WGU + (size_t)tt * 36864 + ks * 2048 + mat * 1024;
    else dst = ws + OFF_WD + (size_t)(tt / 9) * (144 * 9216) + ks * 9216 + (tt % 9) * 1024;
    *(u32x4*)(dst + lane * 16) = u32x4{lo[0], lo[1], hi[0], hi[1]};
  }
}

DI void ring_issue(const char* src, unsigned lb, int slot, int par, int wave, int lane) {
  glds<4>(src + wave * 4096, lane * 16, lb + slot * 36864 + wave * 4096);
  if ((wave >> 2) == par) glds<1>(src + 32768 + (wave & 3) * 1024, lane * 16, lb + slot * 36864 + 32768 + (wave & 3) * 1024);
}

DI float silu_mul(float g, float u) { return g * u * __builtin_amdgcn_rcpf(1.f + __builtin_amdgcn_exp2f(g * -1.4426950408889634f)); }

#define RAWBAR() asm volatile("s_barrier" ::: "memory")
__global__ __launch_bounds__(512, 2) void k5_gateup(char* __restrict__ ws) {
  extern __shared__ __attribute__((aligned(16))) char smem[];
  const unsigned lb = (unsigned)(size_t)smem;
  const int b = blockIdx.x, lane = threadIdx.x & 63, wave = rfl(threadIdx.x >> 6);
  const int xcd = b & 7, i = b >> 3;
  const int rt = (xcd >> 2) * 4 + (i & 3), ft = (xcd & 3) * 8 + (i >> 2);
  const int odd = ft & 1;
  STAMP(5, 0);
  const char* wsrc = ws + OFF_WGU + (size_t)ft * 9 * 36864;
  auto csrc = [&](int c) { int cc = c + odd; if (cc == 9) cc = 0; return wsrc + (size_t)cc * 36864; };
  ring_issue(csrc(0), lb, 0, 0, wave, lane);
  u32x4 hb[2][18];
  const char* hp = ws + OFF_H2 + (size_t)(rt * 16 + 2 * wave) * 18432 + lane * 16;
#pragma unroll
  for (int ks = 0; ks < 18; ++ks) {
    hb[0][ks] = *(const u32x4*)(hp + ks * 1024); hb[1][ks] = *(const u32x4*)(hp + 18432 + ks * 1024);
    SBAR();
    if (ks == 8) { ring_issue(csrc(1), lb, 1, 1, wave, lane); SBAR(); }
  }
  char* ap = ws + OFF_ACT + (size_t)(rt * 16 + 2 * wave) * (144 * 1024) + (size_t)((ft * 9 + odd) >> 1) * 1024 + lane * 16;
  constexpr int PD = 3;
  u32x4 ag[PD], au[PD];
  f32x4 acc[2][4];
  unsigned ko[2][4];
  float et[2], gu[2];
  const unsigned sa = lb + lane * 16;
  WAITVM(40);
  RAWBAR();
  STAMP(5, 1);
#pragma unroll
  for (int j = 0; j < PD; ++j) { ag[j] = ldsr128(sa + j * 2048); au[j] = ldsr128(sa + j * 2048 + 1024); }
  SBAR();
#pragma unroll
  for (int c = 0; c <= 9; ++c) {
    const int P = c & 1, Q = P ^ 1;
#pragma unroll
    for (int ks = 0; ks < 18; ++ks) {
      if (c < 9) {
        if (ks == 9 && c < 8) {
          if (c == 0) WAITVM(18); else if (c < 7) WAITVM(4); else WAITVM(0);
          RAWBAR();
          if (c >= 1 && c + 3 <= 8) ring_issue(csrc(c + 3), lb, (c + 3) & 3, (c + 3) & 1, wave, lane);
          SBAR();
        }
        const int fi = c * 18 + ks, sl = fi % PD;
        MF16(ks == 0, acc[P][0], ag[sl], hb[0][ks]);
        MF16(ks == 0, acc[P][1], ag[sl], hb[1][ks]);
        SBAR();
        const int fn = fi + PD, cn = fn / 18, kn = fn % 18;
        if (fn < 162) { ag[sl] = ldsr128(sa + (cn & 3) * 36864 + kn * 2048); SBAR(); }
        MF16(ks == 0, acc[P][2], au[sl], hb[0][ks]);
        MF16(ks == 0, acc[P][3], au[sl], hb[1][ks]);
        SBAR();
        if (fn < 162) { au[sl] = ldsr128(sa + (cn & 3) * 36864 + kn * 2048 + 1024); SBAR(); }
        if (c == 0 && ks == 17) { ring_issue(csrc(2), lb, 2, 0, wave, lane); ring_issue(csrc(3), lb, 3, 1, wave, lane); SBAR(); }
      }
      if (c == 9 && ks == 0) { MFMA_DRAIN(); SBAR(); }
      if (c >= 1 && ks >= 1 && ks <= 16) {
        const int v = (ks - 1) >> 1, tt = v >> 2, e = v & 3, pc = c - 1;
        if ((ks - 1) & 1) {
          const float r = __builtin_amdgcn_rcpf(1.f + et[e & 1]);
          gu[e & 1] *= r; PIN(gu[e & 1]);
          if (e & 1) { ko[tt][(pc & 1) * 2 + (e >> 1)] = pk(gu[0], gu[1]); PIN(ko[tt][(pc & 1) * 2 + (e >> 1)]); }
        } else {
          const float g = acc[Q][tt][e];
          et[e & 1] = EX2(g * -1.4426950408889634f); PIN(et[e & 1]);
          gu[e & 1] = g * acc[Q][2 + tt][e]; PIN(gu[e & 1]);
        }
        if (c < 9) SBAR();
      }
      if (c >= 1 && ks == 17) {
        const int pc = c - 1;
        if (pc == 8) {
          char* p = odd ? ap - 1024 + 8 : ap + 4096;
          *(u32x2*)p = u32x2{ko[0][0], ko[0][1]}; *(u32x2*)(p + 144 * 1024) = u32x2{ko[1][0], ko[1][1]};
        } else if (pc & 1) {
          *(u32x4*)(ap + (pc >> 1) * 1024) = u32x4{ko[0][0], ko[0][1], ko[0][2], ko[0][3]};
          *(u32x4*)(ap + (pc >> 1) * 1024 + 144 * 1024) = u32x4{ko[1][0], ko[1][1], ko[1][2], ko[1][3]};
        }
        SBAR();
      }
    }
    STAMP(5, 2 + c);
  }
}

__global__ __launch_bounds__(512, 2) void k6_down(char* __restrict__ ws) {
  extern __shared__ __attribute__((aligned(16))) char smem[];
  const unsigned lb = (unsigned)(size_t)smem;
  const int b = blockIdx.x, lane = threadIdx.x & 63, wave = rfl(threadIdx.x >> 6);
  const int xcd = b & 7, i = b >> 3;
  const int kq = xcd & 3, rt = (xcd >> 2) * 8 + (i & 7), ng = i >> 3;
  const int tg = wave & 3, kp = wave >> 2;
  STAMP(6, 0);
  const char* wsrc = ws + OFF_WD + (size_t)ng * (144 * 9216) + (size_t)kq * 36 * 9216;
  const char* ap = ws + OFF_ACT + (size_t)(rt * 8 + 2 * tg) * (144 * 1024) + (size_t)(kq * 36 + kp) * 1024 + lane * 16;
  u32x4 ab[18][2];
#pragma unroll
  for (int s = 0; s < 9; ++s) {
    if (s < 4) { ring_issue(wsrc + s * 36864, lb, s, s & 1, wave, lane); SBAR(); }
#pragma unroll
    for (int h = 0; h < 2; ++h) { ab[2 * s + h][0] = *(const u32x4*)(ap + (4 * s + 2 * h) * 1024); ab[2 * s + h][1] = *(const u32x4*)(ap + (4 * s + 2 * h) * 1024 + 144 * 1024); SBAR(); }
  }
  constexpr int PD = 5;
  f32x4 acc[2][9];
  u32x4 af[PD];
  const unsigned sa = lb + kp * 9216 + lane * 16;
  WAITVM(49);
  RAWBAR();
  STAMP(6, 1);
#pragma unroll
  for (int j = 0; j < PD; ++j) af[j] = ldsr128(sa + j * 1024);
  SBAR();
#pragma unroll
  for (int s = 0; s < 9; ++s) {
#pragma unroll
    for (int f = 0; f < 18; ++f) {
      if (f == 9 && s < 8) {
        if (s == 0) WAITVM(41); else if (s == 1) WAITVM(32); else if (s == 2) WAITVM(28); else if (s < 7) WAITVM(4); else WAITVM(0);
        RAWBAR();
        if (s >= 1 && s + 3 <= 8) ring_issue(wsrc + (s + 3) * 36864, lb, (s + 3) & 3, (s + 3) & 1, wave, lane);
        SBAR();
      }
      const int h = f / 9, n = f % 9, fi = s * 18 + f, sl = fi % PD;
      MF16(fi < 9, acc[0][n], af[sl], ab[2 * s + h][0]);
      MF16(fi < 9, acc[1][n], af[sl], ab[2 * s + h][1]);
      SBAR();
      const int fn = fi + PD, sn = fn / 18, hn = (fn % 18) / 9, nn = fn % 9;
      if (fn < 162) { af[sl] = ldsr128(sa + (sn & 3) * 36864 + hn * 18432 + nn * 1024); SBAR(); }
    }
    STAMP(6, 2 + s);
  }
  MFMA_DRAIN(); SBAR();
  const unsigned xw = lb + 36864 + wave * 9216 + lane * 16, xr = lb + 36864 + (wave ^ 4) * 9216 + lane * 16;
#pragma unroll
  for (int n = 0; n < 9; ++n) ldsw128(xw + n * 1024, __builtin_bit_cast(u32x4, kp ? acc[0][n] : acc[1][n]));
  BARRIER();
  char* pp = ws + OFF_PART + ((size_t)(kq * 128 + rt * 8 + 2 * tg + kp) * 36 + ng * 9) * 1024 + lane * 16;
#pragma unroll
  for (int n = 0; n < 9; ++n) {
    const f32x4 o = __builtin_bit_cast(f32x4, ldsr128(xr + n * 1024)) + (kp ? acc[1][n] : acc[0][n]);
    *(f32x4*)(pp + n * 1024) = o;
  }
  STAMP(6, 11);
}

__global__ __launch_bounds__(256) void k7_out(const char* __restrict__ ws, u16* __restrict__ out) {
  const int lane = threadIdx.x & 63, gw = blockIdx.x * 4 + (threadIdx.x >> 6);
  const int tt = gw / 18, np = gw % 18;
  const int tok = tt * 16 + (lane & 15), q = lane >> 4;
  f32x4 p[2][4]; u32x2 xr[2]; f32x4 gb[2];
#pragma unroll
  for (int j = 0; j < 2; ++j) {
#pragma unroll
    for (int k = 0; k < 4; ++k) p[j][k] = *(const f32x4*)(ws + OFF_PART + ((size_t)(k * 128 + tt) * 36 + np * 2 + j) * 1024 + lane * 16);
    const int n = (np * 2 + j) * 16 + 4 * q;
    xr[j] = *(const u32x2*)(ws + OFF_X1 + ((size_t)tok * D + n) * 2);
    gb[j] = *(const f32x4*)(ws + OFF_MOD + (5 * D + n) * 4);
  }
#pragma unroll
  for (int j = 0; j < 2; ++j) {
    const f32x4 y = (p[j][0] + p[j][1]) + (p[j][2] + p[j][3]);
    const int n = (np * 2 + j) * 16 + 4 * q;
    const u32x2 o = {pk(bflo(xr[j][0]) + rbf(gb[j][0] * rbf(y[0])), bfhi(xr[j][0]) + rbf(gb[j][1] * rbf(y[1]))),
                     pk(bflo(xr[j][1]) + rbf(gb[j][2] * rbf(y[2])), bfhi(xr[j][1]) + rbf(gb[j][3] * rbf(y[3])))};
    *(u32x2*)(out + (size_t)tok * D + n) = o;
  }
}

extern "C" void kernel_launch(void* const* d_in, const int* in_sizes, int n_in, void* d_out, int out_size, void* d_ws, size_t ws_size, hipStream_t stream) {
  const u16* x = (const u16*)d_in[0]; const u16* wq = (const u16*)d_in[1]; const u16* wk = (const u16*)d_in[2]; const u16* wv = (const u16*)d_in[3]; const u16* wo = (const u16*)d_in[4];
  const u16* wg = (const u16*)d_in[6]; const u16* wu = (const u16*)d_in[7]; const u16* wd = (const u16*)d_in[8]; const u16* c = (const u16*)d_in[9]; const u16* wmod = (const u16*)d_in[10];
  char* ws = (char*)d_ws;
  (void)hipFuncSetAttribute((const void*)k3_attn, hipFuncAttributeMaxDynamicSharedMemorySize, L3_SIZE);
  (void)hipFuncSetAttribute((const void*)k5_gateup, hipFuncAttributeMaxDynamicSharedMemorySize, 4 * 36864);
  (void)hipFuncSetAttribute((const void*)k6_down, hipFuncAttributeMaxDynamicSharedMemorySize, 4 * 36864);
  k1_mod_pack<<<216 + 114 + 64, 384, 0, stream>>>(c, wmod, wq, wk, wv, wo, ws);
  k4_pack<<<972, 256, 0, stream>>>(wg, wu, wd, ws);
  k2_qkv<<<256, 640, 0, stream>>>(x, ws);
  k3_attn<<<256, 512, L3_SIZE, stream>>>(x, ws);
  k5_gateup<<<256, 512, 4 * 36864, stream>>>(ws);
  k6_down<<<256, 512, 4 * 36864, stream>>>(ws);
  k7_out<<<576, 256, 0, stream>>>(ws, (u16*)d_out);
}
```

```cpp
#include <hip/hip_runtime.h>
#include <hip/hip_bf16.h>

typedef unsigned short u16;
using bf16x8 = __attribute__((ext_vector_type(8))) __bf16;
using bf16x2 = __attribute__((ext_vector_type(2))) __bf16;
using s16x4  = __attribute__((ext_vector_type(4))) short;
using f32x2  = __attribute__((ext_vector_type(2))) float;
using f32x4  = __attribute__((ext_vector_type(4))) float;
using f32x16 = __attribute__((ext_vector_type(16))) float;
using u32x2  = __attribute__((ext_vector_type(2))) unsigned;
using u32x4  = __attribute__((ext_vector_type(4))) unsigned;
using u32x8  = __attribute__((ext_vector_type(8))) unsigned;
using u32x16 = __attribute__((ext_vector_type(16))) unsigned;
#define DI __device__ __forceinline__
#define LDSP __attribute__((address_space(3)))
#define SBAR() __builtin_amdgcn_sched_barrier(0)
#define PIN(x) asm volatile("" : "+v"(x))
#define BC8(x) __builtin_bit_cast(bf16x8, (x))
#define MFMA16(a, b, c) __builtin_amdgcn_mfma_f32_16x16x32_bf16(BC8(a), BC8(b), (c), 0, 0, 0)
#define MFMA32(a, b, c) __builtin_amdgcn_mfma_f32_32x32x16_bf16(BC8(a), BC8(b), (c), 0, 0, 0)
#define MFMA32K8(a, b, c) __builtin_amdgcn_mfma_f32_32x32x8bf16_1k(__builtin_bit_cast(s16x4, (a)), __builtin_bit_cast(s16x4, (b)), (c), 0, 0, 0)
#define WAITVM(n) asm volatile("s_waitcnt vmcnt(" #n ")" ::: "memory")
#define BARRIER() asm volatile("s_waitcnt lgkmcnt(0)\n\ts_barrier" ::: "memory")

constexpr int S = 2048, D = 576, F = 4608;
constexpr size_t OFF_MOD  = 0;
constexpr size_t OFF_CTR  = 16384;
constexpr size_t OFF_ARR  = OFF_CTR + 512;
constexpr size_t OFF_KMAX = OFF_CTR + 4608;
constexpr size_t OFF_TAB  = 32768;
constexpr size_t OFF_GR   = OFF_TAB + 16384;
constexpr size_t OFF_WO   = OFF_GR + 16 * 5120;
constexpr size_t OFF_Q    = OFF_WO + 18 * 36 * 1024;
constexpr size_t OFF_SH   = OFF_Q + (size_t)S * D * 2;
constexpr size_t OFF_K    = OFF_SH + (size_t)S * D * 2 + 1024;
constexpr size_t OFF_K1   = OFF_K + 65536;
constexpr size_t OFF_V    = OFF_K1 + 8192;
constexpr size_t OFF_X1   = OFF_V + 64 * 1536;
constexpr size_t OFF_H2   = OFF_X1 + (size_t)S * D * 2;
constexpr size_t OFF_PG   = OFF_H2 + (size_t)S * D * 2;
constexpr size_t OFF_PD   = OFF_PG + 32 * 36864;
constexpr size_t OFF_ACT  = OFF_PD + 16 * 36864;
constexpr size_t OFF_PART = (OFF_ACT + (size_t)S * F * 2 + 32767) / 32768 * 32768;
constexpr size_t OFF_FLAG = OFF_PART + (size_t)256 * 32768;
constexpr size_t OFF_DBG  = OFF_FLAG + 1024 * 256;
constexpr size_t OFF_DBG2 = OFF_DBG + 8 * 1024 * 256;
constexpr size_t WS_END   = OFF_DBG2 + 2 * 2048 * 256;
constexpr float LOG2E = 1.4426950408889634f;
#define STAMP(kid, i) do {} while (0)
#define STAMPW(kid, i, w) do {} while (0)

DI float bf2f(u16 v) { return __uint_as_float(((unsigned)v) << 16); }
DI float bflo(unsigned v) { return __uint_as_float(v << 16); }
DI float bfhi(unsigned v) { return __uint_as_float(v & 0xffff0000u); }
DI unsigned pk(float a, float b) { f32x2 v = {a, b}; return __builtin_bit_cast(unsigned, __builtin_convertvector(v, bf16x2)); }
DI float rbf(float a) { return bflo(pk(a, a)); }
DI int rfl(int x) { return __builtin_amdgcn_readfirstlane(x); }
template <int CTRL> DI float dpp(float x) { return __int_as_float(__builtin_amdgcn_update_dpp(0, __float_as_int(x), CTRL, 0xf, 0xf, false)); }
DI float row16_sum(float x) { x += dpp<0x128>(x); x += dpp<0x124>(x); x += dpp<0x122>(x); x += dpp<0x121>(x); return x; }
DI float row16_max(float x) { x = fmaxf(x, dpp<0x128>(x)); x = fmaxf(x, dpp<0x124>(x)); x = fmaxf(x, dpp<0x122>(x)); x = fmaxf(x, dpp<0x121>(x)); return x; }
DI float xor16(float x) { auto r = __builtin_amdgcn_permlane16_swap(__float_as_uint(x), __float_as_uint(x), false, false); return __uint_as_float(r[0]) + __uint_as_float(r[1]); }
DI float xor32(float x) { auto r = __builtin_amdgcn_permlane32_swap(__float_as_uint(x), __float_as_uint(x), false, false); return __uint_as_float(r[0]) + __uint_as_float(r[1]); }
DI float max16(float x) { auto r = __builtin_amdgcn_permlane16_swap(__float_as_uint(x), __float_as_uint(x), false, false); return fmaxf(__uint_as_float(r[0]), __uint_as_float(r[1])); }
DI float max32(float x) { auto r = __builtin_amdgcn_permlane32_swap(__float_as_uint(x), __float_as_uint(x), false, false); return fmaxf(__uint_as_float(r[0]), __uint_as_float(r[1])); }

DI unsigned umax16(unsigned x) {
#define UMX(C) do { const unsigned t_ = __builtin_amdgcn_update_dpp(0u, x, C, 0xf, 0xf, false); x = x > t_ ? x : t_; } while (0)
  UMX(0x128); UMX(0x124); UMX(0x122); UMX(0x121);
  auto r = __builtin_amdgcn_permlane16_swap(x, x, false, false); return r[0] > r[1] ? r[0] : r[1];
}
template <int N> DI void glds(const void* base, unsigned voff, unsigned lds) {
  unsigned sv;
  if constexpr (N == 1)
    asm volatile("s_mov_b32 %0, m0\n\ts_mov_b32 m0, %3\n\ts_nop 2\n\tglobal_load_lds_dwordx4 %1, %2\n\ts_mov_b32 m0, %0" : "=&s"(sv) : "v"(voff), "s"(base), "s"(lds) : "memory");
  else if constexpr (N == 4)
    asm volatile("s_mov_b32 %0, m0\n\ts_mov_b32 m0, %3\n\ts_nop 2\n\tglobal_load_lds_dwordx4 %1, %2\n\tglobal_load_lds_dwordx4 %1, %2 offset:1024\n\t"
                 "global_load_lds_dwordx4 %1, %2 offset:2048\n\tglobal_load_lds_dwordx4 %1, %2 offset:3072\n\ts_mov_b32 m0, %0" : "=&s"(sv) : "v"(voff), "s"(base), "s"(lds) : "memory");
}
template <bool Z> DI void mf16(f32x4& c, const u32x4& a, const u32x4& b) {
  if constexpr (Z) asm volatile("v_mfma_f32_16x16x32_bf16 %0, %1, %2, 0" : "=v"(c) : "v"(a), "v"(b));
  else asm volatile("v_mfma_f32_16x16x32_bf16 %0, %1, %2, %0" : "+v"(c) : "v"(a), "v"(b));
}
#define MF16(Z, c, a, b) do { if (Z) mf16<true>(c, a, b); else mf16<false>(c, a, b); } while (0)
#define MFMA_DRAIN() asm volatile("s_nop 7\n\ts_nop 3" ::: "memory")
DI u32x4 ldsr128(unsigned a) { return *(const LDSP u32x4*)(a); }
DI u32x2 ldsr64(unsigned a) { return *(const LDSP u32x2*)(a); }
DI void ldsw128(unsigned a, u32x4 v) { *(LDSP u32x4*)(a) = v; }
DI void ldsw64(unsigned a, u32x2 v) { *(LDSP u32x2*)(a) = v; }
DI void ldsw32(unsigned a, unsigned v) { *(LDSP unsigned*)(a) = v; }
DI float ldsrf(unsigned a) { return *(const LDSP float*)(a); }
DI u32x2 ldstr(unsigned a) { return __builtin_bit_cast(u32x2, __builtin_amdgcn_ds_read_tr16_b64_v4i16((LDSP s16x4*)(a))); }

DI u32x4 srd(const void* p, unsigned bytes) { return u32x4{(unsigned)(size_t)p, (unsigned)((size_t)p >> 32), bytes, 0x00020000u}; }

constexpr unsigned L1_SIZE = 163392, L3_SIZE = 46080 + 8192;
constexpr float QSCALE = 0.23570226039551584f * 1.4426950408889634f;
__global__ __launch_bounds__(512) __attribute__((aligned(4096))) void k12_qkv(const u16* __restrict__ c, const u16* __restrict__ wmod, const u16* __restrict__ wq, const u16* __restrict__ wk,
    const u16* __restrict__ wv, const u16* __restrict__ wo, const u16* __restrict__ x, const u16* __restrict__ wg, const u16* __restrict__ wu, const u16* __restrict__ wd, char* __restrict__ ws) {
  asm volatile(
".LK1_top:\n"
"s_load_dwordx16 s[8:23], s[0:1], 0x0\n"
"s_load_dwordx2 s[40:41], s[0:1], 0x50\n"
"s_load_dwordx4 s[24:27], s[0:1], 0x40\n"
"v_readfirstlane_b32 s30, v0\n"
"v_and_b32_e32 v1, 63, v0\n"
"s_lshr_b32 s37, s2, 3\n"
"s_and_b32 s36, s2, 7\n"
"s_lshr_b32 s30, s30, 6\n"
"s_mov_b32 s35, 0x20000\n"
"s_cmp_eq_u32 s30, 7\n"
"s_cbranch_scc1 .LK1_w7\n"
"s_sub_u32 s48, s37, 8\n"
"s_mul_i32 s38, s48, 43\n"
"s_lshr_b32 s38, s38, 7\n"
"s_mul_i32 s49, s38, 3\n"
"s_sub_u32 s31, s48, s49\n"
"s_lshl3_add_u32 s39, s38, s36\n"
"s_cmp_lt_u32 s2, 64\n"
"s_cmov_b32 s31, 3\n"
"s_cmov_b32 s39, s2\n"
"v_lshrrev_b32_e32 v2, 3, v1\n"
"v_lshlrev_b32_e32 v4, 4, v1\n"
"v_and_b32_e32 v3, 0x70, v4\n"
"s_cmp_lt_u32 s30, 4\n"
"s_cbranch_scc1 .LK1_ln\n"
"v_and_b32_e32 v5, 5, v2\n"
"v_and_b32_e32 v6, 2, v2\n"
"s_cmp_lt_u32 s31, 3\n"
"s_cbranch_scc0 .LK1_g3\n"
"v_lshl_add_u32 v5, v6, 2, v5\n"
"v_mul_u32_u24_e32 v8, 0x480, v5\n"
"v_add_u32_e32 v8, v8, v3\n"
"v_add_u32_e32 v9, 0x4c0, v8\n"
"v_add_u32_e32 v10, 0x3f80, v8\n"
"v_add_u32_e32 v11, 0x4440, v8\n"
"s_mul_i32 s48, s31, 3\n"
"s_add_u32 s48, s48, s30\n"
"s_lshl_b32 s48, s48, 7\n"
"s_sub_u32 s48, s48, 0x200\n"
"s_mov_b32 s49, s48\n"
"s_mul_i32 s50, s30, 0x8800\n"
"s_add_u32 s50, s50, 0xfffec640\n"
"s_mov_b32 m0, s50\n"
"s_mov_b32 s34, 0xa2000\n"
"s_add_u32 s51, s48, 0x48000\n"
"s_barrier\n"
"s_waitcnt lgkmcnt(0)\n"
"s_mov_b64 s[32:33], s[12:13]\n"
".LK1_ql:\n"
"buffer_load_dwordx4 v8, s[32:35], s49 offen lds\n"
"buffer_load_dwordx4 v9, s[32:35], s49 offen offset:1088 lds\n"
"buffer_load_dwordx4 v10, s[32:35], s49 offen offset:2176 lds\n"
"buffer_load_dwordx4 v11, s[32:35], s49 offen offset:3264 lds\n"
"s_add_u32 s49, s49, 0x9000\n"
"s_addk_i32 m0, 0x1100\n"
"s_cmp_lt_u32 s49, s51\n"
"s_cbranch_scc1 .LK1_ql\n"
"s_branch .LK1_q2\n"
".LK1_ln:\n"
"v_mul_u32_u24_e32 v5, 0x480, v2\n"
"v_add_u32_e32 v5, v5, v3\n"
"v_and_b32_e32 v6, 6, v2\n"
"v_mul_u32_u24_e32 v6, 0x480, v6\n"
"v_and_b32_e32 v7, 0xf0, v4\n"
"v_add_u32_e32 v6, v6, v7\n"
"s_lshl2_add_u32 s48, s39, s30\n"
"s_mul_i32 s48, s48, 0x2400\n"
"s_mov_b32 s34, 0x240000\n"
"s_cmp_lt_u32 s2, 54\n"
"s_cbranch_scc0 .LK1_lx\n"
"s_barrier\n"
"s_cmp_eq_u32 s2, s2\n"
".LK1_lx:\n"
"s_waitcnt lgkmcnt(0)\n"
"s_mov_b64 s[32:33], s[20:21]\n"
"buffer_load_dwordx4 v[28:31], v5, s[32:35], s48 offen\n"
"buffer_load_dwordx4 v[32:35], v6, s[32:35], s48 offen offset:128\n"
"buffer_load_dwordx4 v[36:39], v6, s[32:35], s48 offen offset:1280\n"
"buffer_load_dwordx4 v[40:43], v6, s[32:35], s48 offen offset:384\n"
"buffer_load_dwordx4 v[44:47], v6, s[32:35], s48 offen offset:1536\n"
"buffer_load_dwordx4 v[48:51], v6, s[32:35], s48 offen offset:640\n"
"buffer_load_dwordx4 v[52:55], v6, s[32:35], s48 offen offset:1792\n"
"buffer_load_dwordx4 v[56:59], v6, s[32:35], s48 offen offset:896\n"
"buffer_load_dwordx4 v[60:63], v6, s[32:35], s48 offen offset:2048\n"
"s_cbranch_scc1 .LK1_ln2\n"
"s_barrier\n"
"s_branch .LK1_ln2\n"
".LK1_w7:\n"
"s_getpc_b64 s[4:5]\n"
".LK1_pc:\n"
"s_and_b32 s31, s37, 3\n"
"v_lshl_add_u32 v2, v1, 2, s31\n"
"v_lshlrev_b32_e32 v2, 7, v2\n"
"v_min_u32_e32 v2, 0x3704, v2\n"
"global_load_dword v8, v2, s[4:5]\n"
"v_mov_b32_e32 v4, -1\n"
"v_lshlrev_b32_e32 v3, 2, v1\n"
"ds_write_b32 v3, v4 offset:39680\n"
"v_mov_b32_e32 v4, 0\n"
"v_mov_b32_e32 v5, 0\n"
"ds_write_b64 v4, v[4:5] offset:41984\n"
"v_mul_u32_u24_e32 v2, 57, v1\n"
"v_lshrrev_b32_e32 v2, 10, v2\n"
"v_mul_u32_u24_e32 v2, 0x1e200, v2\n"
"v_mul_u32_u24_e32 v3, 0x1b00, v1\n"
"v_sub_u32_e32 v2, v3, v2\n"
"s_mul_i32 s48, s37, 0x1e600\n"
"s_lshl_b32 s49, s36, 7\n"
"s_add_u32 s48, s48, s49\n"
"v_add_u32_e32 v2, s48, v2\n"
"s_waitcnt lgkmcnt(0)\n"
"s_load_dword s28, s[40:41], 0x4000\n"
"global_load_dword v9, v2, s[10:11]\n"
"s_barrier\n"
"s_branch .LK1_w72\n"
".LK1_g3:\n"
"s_mov_b32 s47, s35\n"
"s_cmp_eq_u32 s30, 6\n"
"s_cbranch_scc1 .LK1_kt\n"
"v_lshl_add_u32 v5, v6, 3, v5\n"
"v_mul_u32_u24_e32 v8, 0x1b00, v5\n"
"v_add_u32_e32 v8, v8, v3\n"
"v_lshlrev_b32_e32 v7, 2, v1\n"
"v_add_u32_e32 v9, 0x31e0, v8\n"
"v_add_u32_e32 v10, 0xcfc0, v8\n"
"v_add_u32_e32 v11, 0x101a0, v8\n"
"s_mul_i32 s50, s30, 0x9480\n"
"s_add_u32 s50, s50, 0xfffe5240\n"
"s_cmp_lt_u32 s2, 54\n"
"s_cbranch_scc0 .LK1_ib\n"
"s_setprio 3\n"
".LK1_u:\n"
"s_mov_b32 m0, s50\n"
"s_mov_b32 s34, 0x3cc000\n"
"s_movk_i32 s46, 0x480\n"
"s_and_b32 s48, s30, 1\n"
"s_mul_i32 s49, s48, 0x1e6000\n"
"s_mulk_i32 s48, 0x240\n"
"v_add_u32_e32 v15, s48, v7\n"
"s_lshl_b32 s51, s2, 7\n"
"s_add_u32 s49, s49, s51\n"
"s_add_u32 s51, s49, 0x1e6000\n"
"s_waitcnt lgkmcnt(0)\n"
"s_mov_b64 s[44:45], s[8:9]\n"
"s_mov_b64 s[32:33], s[10:11]\n"
"s_load_dword s28, s[40:41], 0x4000\n"
"buffer_load_dword v12, v15, s[44:47], 0 offen\n"
"buffer_load_dword v13, v15, s[44:47], 0 offen offset:256\n"
"buffer_load_dword v14, v15, s[44:47], 0 offen offset:512\n"
".LK1_ml:\n"
"buffer_load_dwordx4 v8, s[32:35], s49 offen lds\n"
"buffer_load_dwordx4 v9, s[32:35], s49 offen offset:1056 lds\n"
"buffer_load_dwordx4 v10, s[32:35], s49 offen offset:2112 lds\n"
"buffer_load_dwordx4 v11, s[32:35], s49 offen offset:3168 lds\n"
"s_add_u32 s49, s49, 0x36000\n"
"s_addk_i32 m0, 0x1080\n"
"s_cmp_lt_u32 s49, s51\n"
"s_cbranch_scc1 .LK1_ml\n"
".LK1_bU:\n"
"s_cmp_lt_u32 s2, 54\n"
"s_cbranch_scc0 .LK1_mb\n"
"s_barrier\n"
".LK1_mb:\n"
"s_mov_b32 s42, 0x28e0000\n"
"s_mov_b32 s43, s35\n"
"s_mul_i32 s48, s30, 0x500\n"
"s_add_u32 s48, s48, 0x1b940\n"
"v_add_u32_e32 v7, s48, v7\n"
"v_and_b32_e32 v6, 48, v1\n"
"v_add_u32_e32 v6, s48, v6\n"
"v_and_b32_e32 v15, 3, v2\n"
"v_mul_u32_u24_e32 v5, 0x420, v15\n"
"v_lshrrev_b32_e32 v15, 5, v1\n"
"v_lshl_add_u32 v5, v15, 8, v5\n"
"v_bfe_u32 v15, v1, 2, 1\n"
"v_lshl_add_u32 v5, v15, 7, v5\n"
"v_and_b32_e32 v15, 3, v1\n"
"v_lshlrev_b32_e32 v15, 3, v15\n"
"v_add3_u32 v5, v5, v15, s50\n"
"s_waitcnt vmcnt(38)\n"
"v_lshlrev_b32_e32 v92, 16, v12\n"
"v_and_b32_e32 v93, 0xffff0000, v12\n"
"v_mul_f32_e32 v94, 0xbfb8aa3b, v92\n"
"v_mul_f32_e32 v95, 0xbfb8aa3b, v93\n"
"v_exp_f32_e32 v94, v94\n"
"v_exp_f32_e32 v95, v95\n"
"s_waitcnt vmcnt(37)\n"
"v_lshlrev_b32_e32 v96, 16, v13\n"
"v_and_b32_e32 v97, 0xffff0000, v13\n"
"v_mul_f32_e32 v98, 0xbfb8aa3b, v96\n"
"v_mul_f32_e32 v99, 0xbfb8aa3b, v97\n"
"v_exp_f32_e32 v98, v98\n"
"v_exp_f32_e32 v99, v99\n"
"s_waitcnt vmcnt(36)\n"
"v_lshlrev_b32_e32 v100, 16, v14\n"
"v_and_b32_e32 v101, 0xffff0000, v14\n"
"v_mul_f32_e32 v102, 0xbfb8aa3b, v100\n"
"v_mul_f32_e32 v103, 0xbfb8aa3b, v101\n"
"v_exp_f32_e32 v102, v102\n"
"v_exp_f32_e32 v103, v103\n"
"v_add_f32_e32 v94, 1.0, v94\n"
"v_add_f32_e32 v95, 1.0, v95\n"
"v_rcp_f32_e32 v94, v94\n"
"v_rcp_f32_e32 v95, v95\n"
"v_add_f32_e32 v98, 1.0, v98\n"
"v_add_f32_e32 v99, 1.0, v99\n"
"v_rcp_f32_e32 v98, v98\n"
"v_rcp_f32_e32 v99, v99\n"
"v_add_f32_e32 v102, 1.0, v102\n"
"v_add_f32_e32 v103, 1.0, v103\n"
"v_rcp_f32_e32 v102, v102\n"
"v_rcp_f32_e32 v103, v103\n"
"v_mul_f32_e32 v92, v92, v94\n"
"v_mul_f32_e32 v93, v93, v95\n"
"v_cvt_pk_bf16_f32 v92, v92, v93\n"
"ds_write_b32 v7, v92 offset:0\n"
"v_mul_f32_e32 v96, v96, v98\n"
"v_mul_f32_e32 v97, v97, v99\n"
"v_cvt_pk_bf16_f32 v96, v96, v97\n"
"ds_write_b32 v7, v96 offset:256\n"
"v_mul_f32_e32 v100, v100, v102\n"
"v_mul_f32_e32 v101, v101, v103\n"
"v_cvt_pk_bf16_f32 v100, v100, v101\n"
"ds_write_b32 v7, v100 offset:512\n"
"ds_read_b128 v[140:143], v6 offset:0\n"
"s_waitcnt vmcnt(32)\n"
"ds_read_b64_tr_b16 v[92:93], v5 offset:0\n"
"ds_read_b64_tr_b16 v[94:95], v5 offset:512\n"
"ds_read_b64_tr_b16 v[96:97], v5 offset:32\n"
"ds_read_b64_tr_b16 v[98:99], v5 offset:544\n"
"ds_read_b64_tr_b16 v[100:101], v5 offset:64\n"
"ds_read_b64_tr_b16 v[102:103], v5 offset:576\n"
"ds_read_b64_tr_b16 v[104:105], v5 offset:96\n"
"ds_read_b64_tr_b16 v[106:107], v5 offset:608\n"
"s_waitcnt lgkmcnt(0)\n"
"s_mul_i32 s28, s28, 0x19660d\n"
"s_add_u32 s28, s28, 0x3c6ef35f\n"
"s_mul_i32 s48, s2, 57\n"
"s_lshr_b32 s48, s48, 9\n"
"s_mov_b32 s51, 0\n"
"s_bitcmp1_b32 18, s48\n"
"s_cmov_b32 s51, 1.0\n"
"s_lshl_b32 s49, s2, 8\n"
"s_add_u32 s52, s49, 0xc000\n"
"ds_read_b128 v[144:147], v6 offset:64\n"
"s_waitcnt vmcnt(28)\n"
"ds_read_b64_tr_b16 v[108:109], v5 offset:4224\n"
"ds_read_b64_tr_b16 v[110:111], v5 offset:4736\n"
"ds_read_b64_tr_b16 v[112:113], v5 offset:4256\n"
"ds_read_b64_tr_b16 v[114:115], v5 offset:4768\n"
"ds_read_b64_tr_b16 v[116:117], v5 offset:4288\n"
"ds_read_b64_tr_b16 v[118:119], v5 offset:4800\n"
"ds_read_b64_tr_b16 v[120:121], v5 offset:4320\n"
"ds_read_b64_tr_b16 v[122:123], v5 offset:4832\n"
"v_mfma_f32_16x16x32_bf16 v[124:127], v[92:95], v[140:143], 0\n"
"v_mfma_f32_16x16x32_bf16 v[128:131], v[96:99], v[140:143], 0\n"
"v_mfma_f32_16x16x32_bf16 v[132:135], v[100:103], v[140:143], 0\n"
"v_mfma_f32_16x16x32_bf16 v[136:139], v[104:107], v[140:143], 0\n"
"v_and_b32_e32 v17, 0xf0, v4\n"
"v_lshrrev_b32_e32 v16, 4, v1\n"
"v_mul_u32_u24_e32 v16, 0x1400, v16\n"
"v_add_u32_e32 v16, v16, v17\n"
"v_add_u32_e32 v17, 0x1cd40, v17\n"
"v_mov_b32_e32 v13, s28\n"
"v_mov_b32_e32 v15, s28\n"
"v_mov_b32_e32 v7, 0\n"
"s_add_u32 s53, s52, 0x5000\n"
"s_add_u32 s54, s53, 0x5000\n"
"s_add_u32 s55, s54, 0x5000\n"
"ds_read_b128 v[148:151], v6 offset:128\n"
"s_waitcnt vmcnt(24)\n"
"ds_read_b64_tr_b16 v[92:93], v5 offset:8448\n"
"ds_read_b64_tr_b16 v[94:95], v5 offset:8960\n"
"ds_read_b64_tr_b16 v[96:97], v5 offset:8480\n"
"ds_read_b64_tr_b16 v[98:99], v5 offset:8992\n"
"ds_read_b64_tr_b16 v[100:101], v5 offset:8512\n"
"ds_read_b64_tr_b16 v[102:103], v5 offset:9024\n"
"ds_read_b64_tr_b16 v[104:105], v5 offset:8544\n"
"ds_read_b64_tr_b16 v[106:107], v5 offset:9056\n"
"s_waitcnt lgkmcnt(9)\n"
"v_mfma_f32_16x16x32_bf16 v[124:127], v[108:111], v[144:147], v[124:127]\n"
"v_mfma_f32_16x16x32_bf16 v[128:131], v[112:115], v[144:147], v[128:131]\n"
"v_mfma_f32_16x16x32_bf16 v[132:135], v[116:119], v[144:147], v[132:135]\n"
"v_mfma_f32_16x16x32_bf16 v[136:139], v[120:123], v[144:147], v[136:139]\n"
"ds_read_b128 v[152:155], v6 offset:192\n"
"s_waitcnt vmcnt(20)\n"
"ds_read_b64_tr_b16 v[108:109], v5 offset:12672\n"
"ds_read_b64_tr_b16 v[110:111], v5 offset:13184\n"
"ds_read_b64_tr_b16 v[112:113], v5 offset:12704\n"
"ds_read_b64_tr_b16 v[114:115], v5 offset:13216\n"
"ds_read_b64_tr_b16 v[116:117], v5 offset:12736\n"
"ds_read_b64_tr_b16 v[118:119], v5 offset:13248\n"
"ds_read_b64_tr_b16 v[120:121], v5 offset:12768\n"
"ds_read_b64_tr_b16 v[122:123], v5 offset:13280\n"
"s_waitcnt lgkmcnt(9)\n"
"v_mfma_f32_16x16x32_bf16 v[124:127], v[92:95], v[148:151], v[124:127]\n"
"v_mfma_f32_16x16x32_bf16 v[128:131], v[96:99], v[148:151], v[128:131]\n"
"v_mfma_f32_16x16x32_bf16 v[132:135], v[100:103], v[148:151], v[132:135]\n"
"v_mfma_f32_16x16x32_bf16 v[136:139], v[104:107], v[148:151], v[136:139]\n"
"ds_read_b128 v[156:159], v6 offset:256\n"
"s_waitcnt vmcnt(16)\n"
"ds_read_b64_tr_b16 v[92:93], v5 offset:16896\n"
"ds_read_b64_tr_b16 v[94:95], v5 offset:17408\n"
"ds_read_b64_tr_b16 v[96:97], v5 offset:16928\n"
"ds_read_b64_tr_b16 v[98:99], v5 offset:17440\n"
"ds_read_b64_tr_b16 v[100:101], v5 offset:16960\n"
"ds_read_b64_tr_b16 v[102:103], v5 offset:17472\n"
"ds_read_b64_tr_b16 v[104:105], v5 offset:16992\n"
"ds_read_b64_tr_b16 v[106:107], v5 offset:17504\n"
"s_waitcnt lgkmcnt(9)\n"
"v_mfma_f32_16x16x32_bf16 v[124:127], v[108:111], v[152:155], v[124:127]\n"
"v_mfma_f32_16x16x32_bf16 v[128:131], v[112:115], v[152:155], v[128:131]\n"
"v_mfma_f32_16x16x32_bf16 v[132:135], v[116:119], v[152:155], v[132:135]\n"
"v_mfma_f32_16x16x32_bf16 v[136:139], v[120:123], v[152:155], v[136:139]\n"
"ds_read_b128 v[160:163], v6 offset:320\n"
"s_waitcnt vmcnt(12)\n"
"ds_read_b64_tr_b16 v[108:109], v5 offset:21120\n"
"ds_read_b64_tr_b16 v[110:111], v5 offset:21632\n"
"ds_read_b64_tr_b16 v[112:113], v5 offset:21152\n"
"ds_read_b64_tr_b16 v[114:115], v5 offset:21664\n"
"ds_read_b64_tr_b16 v[116:117], v5 offset:21184\n"
"ds_read_b64_tr_b16 v[118:119], v5 offset:21696\n"
"ds_read_b64_tr_b16 v[120:121], v5 offset:21216\n"
"ds_read_b64_tr_b16 v[122:123], v5 offset:21728\n"
"s_waitcnt lgkmcnt(9)\n"
"v_mfma_f32_16x16x32_bf16 v[124:127], v[92:95], v[156:159], v[124:127]\n"
"v_mfma_f32_16x16x32_bf16 v[128:131], v[96:99], v[156:159], v[128:131]\n"
"v_mfma_f32_16x16x32_bf16 v[132:135], v[100:103], v[156:159], v[132:135]\n"
"v_mfma_f32_16x16x32_bf16 v[136:139], v[104:107], v[156:159], v[136:139]\n"
"ds_read_b128 v[164:167], v6 offset:384\n"
"s_waitcnt vmcnt(8)\n"
"ds_read_b64_tr_b16 v[92:93], v5 offset:25344\n"
"ds_read_b64_tr_b16 v[94:95], v5 offset:25856\n"
"ds_read_b64_tr_b16 v[96:97], v5 offset:25376\n"
"ds_read_b64_tr_b16 v[98:99], v5 offset:25888\n"
"ds_read_b64_tr_b16 v[100:101], v5 offset:25408\n"
"ds_read_b64_tr_b16 v[102:103], v5 offset:25920\n"
"ds_read_b64_tr_b16 v[104:105], v5 offset:25440\n"
"ds_read_b64_tr_b16 v[106:107], v5 offset:25952\n"
"s_waitcnt lgkmcnt(9)\n"
"v_mfma_f32_16x16x32_bf16 v[124:127], v[108:111], v[160:163], v[124:127]\n"
"v_mfma_f32_16x16x32_bf16 v[128:131], v[112:115], v[160:163], v[128:131]\n"
"v_mfma_f32_16x16x32_bf16 v[132:135], v[116:119], v[160:163], v[132:135]\n"
"v_mfma_f32_16x16x32_bf16 v[136:139], v[120:123], v[160:163], v[136:139]\n"
"ds_read_b128 v[168:171], v6 offset:448\n"
"s_waitcnt vmcnt(4)\n"
"ds_read_b64_tr_b16 v[108:109], v5 offset:29568\n"
"ds_read_b64_tr_b16 v[110:111], v5 offset:30080\n"
"ds_read_b64_tr_b16 v[112:113], v5 offset:29600\n"
"ds_read_b64_tr_b16 v[114:115], v5 offset:30112\n"
"ds_read_b64_tr_b16 v[116:117], v5 offset:29632\n"
"ds_read_b64_tr_b16 v[118:119], v5 offset:30144\n"
"ds_read_b64_tr_b16 v[120:121], v5 offset:29664\n"
"ds_read_b64_tr_b16 v[122:123], v5 offset:30176\n"
"s_waitcnt lgkmcnt(9)\n"
"v_mfma_f32_16x16x32_bf16 v[124:127], v[92:95], v[164:167], v[124:127]\n"
"v_mfma_f32_16x16x32_bf16 v[128:131], v[96:99], v[164:167], v[128:131]\n"
"v_mfma_f32_16x16x32_bf16 v[132:135], v[100:103], v[164:167], v[132:135]\n"
"v_mfma_f32_16x16x32_bf16 v[136:139], v[104:107], v[164:167], v[136:139]\n"
"ds_read_b128 v[172:175], v6 offset:512\n"
"s_waitcnt vmcnt(0)\n"
"ds_read_b64_tr_b16 v[92:93], v5 offset:33792\n"
"ds_read_b64_tr_b16 v[94:95], v5 offset:34304\n"
"ds_read_b64_tr_b16 v[96:97], v5 offset:33824\n"
"ds_read_b64_tr_b16 v[98:99], v5 offset:34336\n"
"ds_read_b64_tr_b16 v[100:101], v5 offset:33856\n"
"ds_read_b64_tr_b16 v[102:103], v5 offset:34368\n"
"ds_read_b64_tr_b16 v[104:105], v5 offset:33888\n"
"ds_read_b64_tr_b16 v[106:107], v5 offset:34400\n"
"s_waitcnt lgkmcnt(9)\n"
"v_mfma_f32_16x16x32_bf16 v[124:127], v[108:111], v[168:171], v[124:127]\n"
"v_mfma_f32_16x16x32_bf16 v[128:131], v[112:115], v[168:171], v[128:131]\n"
"v_mfma_f32_16x16x32_bf16 v[132:135], v[116:119], v[168:171], v[132:135]\n"
"v_mfma_f32_16x16x32_bf16 v[136:139], v[120:123], v[168:171], v[136:139]\n"
"s_waitcnt lgkmcnt(0)\n"
"v_mfma_f32_16x16x32_bf16 v[124:127], v[92:95], v[172:175], v[124:127]\n"
"v_mfma_f32_16x16x32_bf16 v[128:131], v[96:99], v[172:175], v[128:131]\n"
"v_mfma_f32_16x16x32_bf16 v[132:135], v[100:103], v[172:175], v[132:135]\n"
"v_mfma_f32_16x16x32_bf16 v[136:139], v[104:107], v[172:175], v[136:139]\n"
"s_mov_b32 s56, 0x400000\n"
"s_cmp_eq_u32 s30, 4\n"
"s_nop 2\n"
"ds_write_b128 v6, v[124:127] offset:0\n"
"ds_write_b128 v6, v[128:131] offset:64\n"
"ds_write_b128 v6, v[132:135] offset:128\n"
"ds_write_b128 v6, v[136:139] offset:192\n"
"s_cbranch_scc1 .LK1_us\n"
"ds_write_b32 v7, v6 offset:41988\n"
"s_sleep 10\n"
"s_branch .LK1_ue\n"
".LK1_us:\n"
"ds_read_b32 v5, v7 offset:41988\n"
"ds_read_b128 v[96:99], v17 offset:1280\n"
"ds_read_b128 v[92:95], v17\n"
"s_sub_u32 s56, s56, 1\n"
"s_cbranch_scc1 .LK1_uo\n"
"s_waitcnt lgkmcnt(0)\n"
"v_cmp_eq_u32_e32 vcc, 0, v5\n"
"s_cbranch_vccnz .LK1_us\n"
".LK1_uo:\n"
"s_cmp_lt_u32 s2, 18\n"
"v_add_f32_e32 v92, v92, v96\n"
"v_add_f32_e32 v93, v93, v97\n"
"v_add_f32_e32 v94, v94, v98\n"
"v_add_f32_e32 v95, v95, v99\n"
"v_cvt_pk_bf16_f32 v92, 0, v92\n"
"v_cvt_pk_bf16_f32 v93, 0, v93\n"
"v_cvt_pk_bf16_f32 v94, 0, v94\n"
"v_cvt_pk_bf16_f32 v95, 0, v95\n"
"v_add_f32_e32 v92, s51, v92\n"
"v_add_f32_e32 v93, s51, v93\n"
"v_add_f32_e32 v94, s51, v94\n"
"v_add_f32_e32 v95, s51, v95\n"
"s_cbranch_scc0 .LK1_mo\n"
"v_cvt_pk_bf16_f32 v12, v92, v93\n"
"v_cvt_pk_bf16_f32 v14, v94, v95\n"
"buffer_store_dwordx4 v[12:15], v16, s[40:43], s52 offen sc1\n"
"buffer_store_dwordx4 v[12:15], v16, s[40:43], s53 offen sc1\n"
".LK1_eU:\n"
".LK1_ue:\n"
"s_setprio 0\n"
"s_branch .LK1_im\n"
".LK1_mo:\n"
"s_mov_b32 s51, 1.0\n"
"s_bitcmp1_b32 24, s48\n"
"s_cmov_b32 s51, 0x3fb8aa3b\n"
"v_cvt_pk_bf16_f32 v92, 0, v92\n"
"v_cvt_pk_bf16_f32 v93, 0, v93\n"
"v_cvt_pk_bf16_f32 v94, 0, v94\n"
"v_cvt_pk_bf16_f32 v95, 0, v95\n"
"v_mul_f32_e32 v92, s51, v92\n"
"v_mul_f32_e32 v93, s51, v93\n"
"v_mul_f32_e32 v94, s51, v94\n"
"v_mul_f32_e32 v95, s51, v95\n"
"s_mov_b64 exec, 0xffff\n"
"buffer_store_dwordx4 v[92:95], v4, s[40:43], s49 offen\n"
"s_mov_b64 exec, -1\n"
"s_branch .LK1_ue\n"
".LK1_kt:\n"
"v_and_b32_e32 v16, 0x23, v1\n"
"v_bfe_u32 v6, v1, 2, 2\n"
"v_lshl_add_u32 v16, v6, 3, v16\n"
"v_bfe_u32 v6, v1, 4, 1\n"
"v_lshl_add_u32 v16, v6, 2, v16\n"
"v_mul_u32_u24_e32 v16, 36, v16\n"
"v_add_u32_e32 v16, 20, v16\n"
"s_movk_i32 s34, 0x5100\n"
"s_movk_i32 s46, 0x5100\n"
"s_mov_b32 s48, 0\n"
"s_mov_b32 m0, 0x1d740\n"
"s_barrier\n"
"s_waitcnt lgkmcnt(0)\n"
"s_mov_b64 s[32:33], s[14:15]\n"
"s_mov_b64 s[44:45], s[16:17]\n"
".LK1_kl:\n"
"buffer_load_dwordx4 v16, s[32:35], s48 offen lds\n"
"s_addk_i32 m0, 0x480\n"
"s_nop 0\n"
"buffer_load_dwordx4 v16, s[44:47], s48 offen lds\n"
"s_addk_i32 m0, 0x480\n"
"s_addk_i32 s48, 0x900\n"
"s_cmp_lt_u32 s48, s34\n"
"s_cbranch_scc1 .LK1_kl\n"
"v_and_b32_e32 v17, 60, v1\n"
"v_lshlrev_b32_e32 v17, 2, v17\n"
"v_and_b32_e32 v6, 2, v1\n"
"v_mul_u32_u24_e32 v6, 0x240, v6\n"
"v_sub_u32_e32 v17, v17, v6\n"
"v_add_u32_e32 v17, 0x1dbc8, v17\n"
"v_and_b32_e32 v6, 15, v1\n"
"v_add_u32_e32 v19, 19, v6\n"
"v_lshlrev_b32_e32 v7, 2, v6\n"
"v_subrev_u32_e32 v7, 13, v7\n"
"v_cmp_gt_u32_e32 vcc, 4, v6\n"
"v_cndmask_b32_e32 v19, v7, v19, vcc\n"
"s_waitcnt vmcnt(0)\n"
"ds_read_b64_tr_b16 v[20:21], v17 offset:0\n"
"ds_read_b64_tr_b16 v[22:23], v17 offset:256\n"
"ds_read_b64_tr_b16 v[24:25], v17 offset:512\n"
"ds_read_b64_tr_b16 v[26:27], v17 offset:768\n"
"ds_read_b64_tr_b16 v[28:29], v17 offset:2304\n"
"ds_read_b64_tr_b16 v[30:31], v17 offset:2560\n"
"ds_read_b64_tr_b16 v[32:33], v17 offset:2816\n"
"ds_read_b64_tr_b16 v[34:35], v17 offset:3072\n"
"ds_read_b64_tr_b16 v[36:37], v17 offset:4608\n"
"ds_read_b64_tr_b16 v[38:39], v17 offset:4864\n"
"ds_read_b64_tr_b16 v[40:41], v17 offset:5120\n"
"ds_read_b64_tr_b16 v[42:43], v17 offset:5376\n"
"s_waitcnt lgkmcnt(10)\n"
"ds_read_b64_tr_b16 v[44:45], v17 offset:6912\n"
"ds_read_b64_tr_b16 v[46:47], v17 offset:7168\n"
"s_waitcnt lgkmcnt(10)\n"
"ds_read_b64_tr_b16 v[48:49], v17 offset:7424\n"
"ds_read_b64_tr_b16 v[50:51], v17 offset:7680\n"
"s_waitcnt lgkmcnt(10)\n"
"ds_read_b64_tr_b16 v[52:53], v17 offset:9216\n"
"ds_read_b64_tr_b16 v[54:55], v17 offset:9472\n"
"s_waitcnt lgkmcnt(10)\n"
"ds_read_b64_tr_b16 v[56:57], v17 offset:9728\n"
"ds_read_b64_tr_b16 v[58:59], v17 offset:9984\n"
"s_waitcnt lgkmcnt(10)\n"
"ds_read_b64_tr_b16 v[60:61], v17 offset:11520\n"
"ds_read_b64_tr_b16 v[62:63], v17 offset:11776\n"
"s_waitcnt lgkmcnt(10)\n"
"ds_read_b64_tr_b16 v[64:65], v17 offset:12032\n"
"ds_read_b64_tr_b16 v[66:67], v17 offset:12288\n"
"s_waitcnt lgkmcnt(10)\n"
"ds_read_b64_tr_b16 v[68:69], v17 offset:13824\n"
"ds_read_b64_tr_b16 v[70:71], v17 offset:14080\n"
"s_waitcnt lgkmcnt(10)\n"
"ds_read_b64_tr_b16 v[72:73], v17 offset:14336\n"
"ds_read_b64_tr_b16 v[74:75], v17 offset:14592\n"
"s_waitcnt lgkmcnt(10)\n"
"ds_read_b64_tr_b16 v[76:77], v17 offset:16128\n"
"ds_read_b64_tr_b16 v[78:79], v17 offset:16384\n"
"s_waitcnt lgkmcnt(10)\n"
"ds_read_b64_tr_b16 v[80:81], v17 offset:16640\n"
"ds_read_b64_tr_b16 v[82:83], v17 offset:16896\n"
"s_waitcnt lgkmcnt(10)\n"
"ds_read_b64_tr_b16 v[84:85], v17 offset:18432\n"
"ds_read_b64_tr_b16 v[86:87], v17 offset:18688\n"
"s_waitcnt lgkmcnt(10)\n"
"ds_read_b64_tr_b16 v[88:89], v17 offset:18944\n"
"ds_read_b64_tr_b16 v[90:91], v17 offset:19200\n"
"s_waitcnt lgkmcnt(0)\n"
"s_branch .LK1_kv\n"
".LK1_ib:\n"
"s_barrier\n"
"s_cmp_lt_u32 s2, 54\n"
"s_cbranch_scc0 .LK1_im\n"
"s_sleep 0\n"
"s_branch .LK1_u\n"
".LK1_im:\n"
"v_bfe_u32 v16, v1, 1, 2\n"
"v_and_b32_e32 v6, 24, v1\n"
"v_add_u32_e32 v16, v16, v6\n"
"v_lshrrev_b32_e32 v6, 5, v1\n"
"v_lshl_add_u32 v16, v6, 2, v16\n"
"v_mul_u32_u24_e32 v16, 36, v16\n"
"v_and_b32_e32 v6, 16, v4\n"
"v_add_u32_e32 v16, v16, v6\n"
"s_movk_i32 s34, 0x5100\n"
"s_mov_b32 s48, 0\n"
"s_mov_b32 m0, s50\n"
"s_cmp_eq_u32 s30, 4\n"
"s_waitcnt lgkmcnt(0)\n"
"s_cselect_b64 s[32:33], s[14:15], s[16:17]\n"
".LK1_il:\n"
"buffer_load_dwordx4 v16, s[32:35], s48 offen lds\n"
"buffer_load_dwordx4 v16, s[32:35], s48 offen offset:1152 lds\n"
"buffer_load_dwordx4 v16, s[32:35], s48 offen offset:2304 lds\n"
"s_addk_i32 m0, 0xd80\n"
"s_addk_i32 s48, 0xd80\n"
"s_cmp_lt_u32 s48, s34\n"
"s_cbranch_scc1 .LK1_il\n"
"v_lshl_add_u32 v17, v1, 3, s50\n"
"v_and_b32_e32 v6, 15, v1\n"
"v_mul_u32_u24_e32 v19, 11, v6\n"
"v_lshrrev_b32_e32 v19, 5, v19\n"
"v_add_u32_e32 v19, v19, v6\n"
"s_waitcnt vmcnt(0)\n"
"ds_read_b64_tr_b16 v[20:21], v17 offset:0\n"
"ds_read_b64_tr_b16 v[22:23], v17 offset:512\n"
"ds_read_b64_tr_b16 v[24:25], v17 offset:1152\n"
"ds_read_b64_tr_b16 v[26:27], v17 offset:1664\n"
"ds_read_b64_tr_b16 v[28:29], v17 offset:2304\n"
"ds_read_b64_tr_b16 v[30:31], v17 offset:2816\n"
"ds_read_b64_tr_b16 v[32:33], v17 offset:3456\n"
"ds_read_b64_tr_b16 v[34:35], v17 offset:3968\n"
"ds_read_b64_tr_b16 v[36:37], v17 offset:4608\n"
"ds_read_b64_tr_b16 v[38:39], v17 offset:5120\n"
"ds_read_b64_tr_b16 v[40:41], v17 offset:5760\n"
"ds_read_b64_tr_b16 v[42:43], v17 offset:6272\n"
"s_waitcnt lgkmcnt(10)\n"
"ds_read_b64_tr_b16 v[44:45], v17 offset:6912\n"
"ds_read_b64_tr_b16 v[46:47], v17 offset:7424\n"
"s_waitcnt lgkmcnt(10)\n"
"ds_read_b64_tr_b16 v[48:49], v17 offset:8064\n"
"ds_read_b64_tr_b16 v[50:51], v17 offset:8576\n"
"s_waitcnt lgkmcnt(10)\n"
"ds_read_b64_tr_b16 v[52:53], v17 offset:9216\n"
"ds_read_b64_tr_b16 v[54:55], v17 offset:9728\n"
"s_waitcnt lgkmcnt(10)\n"
"ds_read_b64_tr_b16 v[56:57], v17 offset:10368\n"
"ds_read_b64_tr_b16 v[58:59], v17 offset:10880\n"
"s_waitcnt lgkmcnt(10)\n"
"ds_read_b64_tr_b16 v[60:61], v17 offset:11520\n"
"ds_read_b64_tr_b16 v[62:63], v17 offset:12032\n"
"s_waitcnt lgkmcnt(10)\n"
"ds_read_b64_tr_b16 v[64:65], v17 offset:12672\n"
"ds_read_b64_tr_b16 v[66:67], v17 offset:13184\n"
"s_waitcnt lgkmcnt(10)\n"
"ds_read_b64_tr_b16 v[68:69], v17 offset:13824\n"
"ds_read_b64_tr_b16 v[70:71], v17 offset:14336\n"
"s_waitcnt lgkmcnt(10)\n"
"ds_read_b64_tr_b16 v[72:73], v17 offset:14976\n"
"ds_read_b64_tr_b16 v[74:75], v17 offset:15488\n"
"s_waitcnt lgkmcnt(10)\n"
"ds_read_b64_tr_b16 v[76:77], v17 offset:16128\n"
"ds_read_b64_tr_b16 v[78:79], v17 offset:16640\n"
"s_waitcnt lgkmcnt(10)\n"
"ds_read_b64_tr_b16 v[80:81], v17 offset:17280\n"
"ds_read_b64_tr_b16 v[82:83], v17 offset:17792\n"
"s_waitcnt lgkmcnt(10)\n"
"ds_read_b64_tr_b16 v[84:85], v17 offset:18432\n"
"ds_read_b64_tr_b16 v[86:87], v17 offset:18944\n"
"s_waitcnt lgkmcnt(10)\n"
"ds_read_b64_tr_b16 v[88:89], v17 offset:19584\n"
"ds_read_b64_tr_b16 v[90:91], v17 offset:20096\n"
"s_waitcnt lgkmcnt(0)\n"
".LK1_kv:\n"
"s_mov_b32 s42, 0x28e0000\n"
"s_mov_b32 s43, s35\n"
"v_lshrrev_b32_e32 v7, 4, v1\n"
"v_mul_u32_u24_e32 v7, 24, v7\n"
"v_add_lshl_u32 v19, v19, v7, 4\n"
"v_and_b32_e32 v7, 15, v1\n"
"v_mul_u32_u24_e32 v18, 0x490, v7\n"
"v_and_b32_e32 v7, 48, v1\n"
"v_add_u32_e32 v18, v18, v7\n"
".LK1_bK:\n"
"s_barrier\n"
"ds_read_b128 v[140:143], v18 offset:0\n"
"ds_read_b128 v[144:147], v18 offset:18688\n"
"ds_read_b128 v[148:151], v18 offset:64\n"
"ds_read_b128 v[152:155], v18 offset:18752\n"
"s_barrier\n"
"ds_read_b128 v[156:159], v18 offset:128\n"
"ds_read_b128 v[160:163], v18 offset:18816\n"
"ds_read_b128 v[164:167], v18 offset:192\n"
"ds_read_b128 v[168:171], v18 offset:18880\n"
"ds_read_b128 v[172:175], v18 offset:256\n"
"ds_read_b128 v[176:179], v18 offset:18944\n"
"s_waitcnt lgkmcnt(9)\n"
"v_mfma_f32_16x16x32_bf16 v[188:191], v[20:23], v[140:143], 0\n"
"v_mfma_f32_16x16x32_bf16 v[196:199], v[140:143], v[20:23], 0\n"
"s_waitcnt lgkmcnt(8)\n"
"v_mfma_f32_16x16x32_bf16 v[192:195], v[20:23], v[144:147], 0\n"
"v_mfma_f32_16x16x32_bf16 v[200:203], v[144:147], v[20:23], 0\n"
"ds_read_b128 v[180:183], v18 offset:320\n"
"ds_read_b128 v[184:187], v18 offset:19008\n"
"s_waitcnt lgkmcnt(9)\n"
"v_mfma_f32_16x16x32_bf16 v[188:191], v[24:27], v[148:151], v[188:191]\n"
"v_mfma_f32_16x16x32_bf16 v[196:199], v[148:151], v[24:27], v[196:199]\n"
"s_waitcnt lgkmcnt(8)\n"
"v_mfma_f32_16x16x32_bf16 v[192:195], v[24:27], v[152:155], v[192:195]\n"
"v_mfma_f32_16x16x32_bf16 v[200:203], v[152:155], v[24:27], v[200:203]\n"
"s_waitcnt lgkmcnt(7)\n"
"v_mfma_f32_16x16x32_bf16 v[188:191], v[28:31], v[156:159], v[188:191]\n"
"v_mfma_f32_16x16x32_bf16 v[196:199], v[156:159], v[28:31], v[196:199]\n"
"s_waitcnt lgkmcnt(6)\n"
"v_mfma_f32_16x16x32_bf16 v[192:195], v[28:31], v[160:163], v[192:195]\n"
"v_mfma_f32_16x16x32_bf16 v[200:203], v[160:163], v[28:31], v[200:203]\n"
"s_waitcnt lgkmcnt(5)\n"
"v_mfma_f32_16x16x32_bf16 v[188:191], v[32:35], v[164:167], v[188:191]\n"
"v_mfma_f32_16x16x32_bf16 v[196:199], v[164:167], v[32:35], v[196:199]\n"
"s_waitcnt lgkmcnt(4)\n"
"v_mfma_f32_16x16x32_bf16 v[192:195], v[32:35], v[168:171], v[192:195]\n"
"v_mfma_f32_16x16x32_bf16 v[200:203], v[168:171], v[32:35], v[200:203]\n"
"s_barrier\n"
"ds_read_b128 v[140:143], v18 offset:384\n"
"ds_read_b128 v[144:147], v18 offset:19072\n"
"ds_read_b128 v[148:151], v18 offset:448\n"
"ds_read_b128 v[152:155], v18 offset:19136\n"
"ds_read_b128 v[156:159], v18 offset:512\n"
"ds_read_b128 v[160:163], v18 offset:19200\n"
"s_waitcnt lgkmcnt(9)\n"
"v_mfma_f32_16x16x32_bf16 v[188:191], v[36:39], v[172:175], v[188:191]\n"
"v_mfma_f32_16x16x32_bf16 v[196:199], v[172:175], v[36:39], v[196:199]\n"
"s_waitcnt lgkmcnt(8)\n"
"v_mfma_f32_16x16x32_bf16 v[192:195], v[36:39], v[176:179], v[192:195]\n"
"v_mfma_f32_16x16x32_bf16 v[200:203], v[176:179], v[36:39], v[200:203]\n"
"ds_read_b128 v[164:167], v18 offset:576\n"
"ds_read_b128 v[168:171], v18 offset:19264\n"
"s_waitcnt lgkmcnt(9)\n"
"v_mfma_f32_16x16x32_bf16 v[188:191], v[40:43], v[180:183], v[188:191]\n"
"v_mfma_f32_16x16x32_bf16 v[196:199], v[180:183], v[40:43], v[196:199]\n"
"s_waitcnt lgkmcnt(8)\n"
"v_mfma_f32_16x16x32_bf16 v[192:195], v[40:43], v[184:187], v[192:195]\n"
"v_mfma_f32_16x16x32_bf16 v[200:203], v[184:187], v[40:43], v[200:203]\n"
"s_waitcnt lgkmcnt(7)\n"
"v_mfma_f32_16x16x32_bf16 v[188:191], v[44:47], v[140:143], v[188:191]\n"
"v_mfma_f32_16x16x32_bf16 v[196:199], v[140:143], v[44:47], v[196:199]\n"
"s_waitcnt lgkmcnt(6)\n"
"v_mfma_f32_16x16x32_bf16 v[192:195], v[44:47], v[144:147], v[192:195]\n"
"v_mfma_f32_16x16x32_bf16 v[200:203], v[144:147], v[44:47], v[200:203]\n"
"s_waitcnt lgkmcnt(5)\n"
"v_mfma_f32_16x16x32_bf16 v[188:191], v[48:51], v[148:151], v[188:191]\n"
"v_mfma_f32_16x16x32_bf16 v[196:199], v[148:151], v[48:51], v[196:199]\n"
"s_waitcnt lgkmcnt(4)\n"
"v_mfma_f32_16x16x32_bf16 v[192:195], v[48:51], v[152:155], v[192:195]\n"
"v_mfma_f32_16x16x32_bf16 v[200:203], v[152:155], v[48:51], v[200:203]\n"
"s_barrier\n"
"ds_read_b128 v[172:175], v18 offset:640\n"
"ds_read_b128 v[176:179], v18 offset:19328\n"
"ds_read_b128 v[180:183], v18 offset:704\n"
"ds_read_b128 v[184:187], v18 offset:19392\n"
"ds_read_b128 v[140:143], v18 offset:768\n"
"ds_read_b128 v[144:147], v18 offset:19456\n"
"s_waitcnt lgkmcnt(9)\n"
"v_mfma_f32_16x16x32_bf16 v[188:191], v[52:55], v[156:159], v[188:191]\n"
"v_mfma_f32_16x16x32_bf16 v[196:199], v[156:159], v[52:55], v[196:199]\n"
"s_waitcnt lgkmcnt(8)\n"
"v_mfma_f32_16x16x32_bf16 v[192:195], v[52:55], v[160:163], v[192:195]\n"
"v_mfma_f32_16x16x32_bf16 v[200:203], v[160:163], v[52:55], v[200:203]\n"
"ds_read_b128 v[148:151], v18 offset:832\n"
"ds_read_b128 v[152:155], v18 offset:19520\n"
"s_waitcnt lgkmcnt(9)\n"
"v_mfma_f32_16x16x32_bf16 v[188:191], v[56:59], v[164:167], v[188:191]\n"
"v_mfma_f32_16x16x32_bf16 v[196:199], v[164:167], v[56:59], v[196:199]\n"
"s_waitcnt lgkmcnt(8)\n"
"v_mfma_f32_16x16x32_bf16 v[192:195], v[56:59], v[168:171], v[192:195]\n"
"v_mfma_f32_16x16x32_bf16 v[200:203], v[168:171], v[56:59], v[200:203]\n"
"s_waitcnt lgkmcnt(7)\n"
"v_mfma_f32_16x16x32_bf16 v[188:191], v[60:63], v[172:175], v[188:191]\n"
"v_mfma_f32_16x16x32_bf16 v[196:199], v[172:175], v[60:63], v[196:199]\n"
"s_waitcnt lgkmcnt(6)\n"
"v_mfma_f32_16x16x32_bf16 v[192:195], v[60:63], v[176:179], v[192:195]\n"
"v_mfma_f32_16x16x32_bf16 v[200:203], v[176:179], v[60:63], v[200:203]\n"
"s_waitcnt lgkmcnt(5)\n"
"v_mfma_f32_16x16x32_bf16 v[188:191], v[64:67], v[180:183], v[188:191]\n"
"v_mfma_f32_16x16x32_bf16 v[196:199], v[180:183], v[64:67], v[196:199]\n"
"s_waitcnt lgkmcnt(4)\n"
"v_mfma_f32_16x16x32_bf16 v[192:195], v[64:67], v[184:187], v[192:195]\n"
"v_mfma_f32_16x16x32_bf16 v[200:203], v[184:187], v[64:67], v[200:203]\n"
"s_barrier\n"
"ds_read_b128 v[156:159], v18 offset:896\n"
"ds_read_b128 v[160:163], v18 offset:19584\n"
"ds_read_b128 v[164:167], v18 offset:960\n"
"ds_read_b128 v[168:171], v18 offset:19648\n"
"ds_read_b128 v[172:175], v18 offset:1024\n"
"ds_read_b128 v[176:179], v18 offset:19712\n"
"s_waitcnt lgkmcnt(9)\n"
"v_mfma_f32_16x16x32_bf16 v[188:191], v[68:71], v[140:143], v[188:191]\n"
"v_mfma_f32_16x16x32_bf16 v[196:199], v[140:143], v[68:71], v[196:199]\n"
"s_waitcnt lgkmcnt(8)\n"
"v_mfma_f32_16x16x32_bf16 v[192:195], v[68:71], v[144:147], v[192:195]\n"
"v_mfma_f32_16x16x32_bf16 v[200:203], v[144:147], v[68:71], v[200:203]\n"
"ds_read_b128 v[180:183], v18 offset:1088\n"
"ds_read_b128 v[184:187], v18 offset:19776\n"
"s_waitcnt lgkmcnt(9)\n"
"v_mfma_f32_16x16x32_bf16 v[188:191], v[72:75], v[148:151], v[188:191]\n"
"v_mfma_f32_16x16x32_bf16 v[196:199], v[148:151], v[72:75], v[196:199]\n"
"s_waitcnt lgkmcnt(8)\n"
"v_mfma_f32_16x16x32_bf16 v[192:195], v[72:75], v[152:155], v[192:195]\n"
"v_mfma_f32_16x16x32_bf16 v[200:203], v[152:155], v[72:75], v[200:203]\n"
"s_waitcnt lgkmcnt(7)\n"
"v_mfma_f32_16x16x32_bf16 v[188:191], v[76:79], v[156:159], v[188:191]\n"
"v_mfma_f32_16x16x32_bf16 v[196:199], v[156:159], v[76:79], v[196:199]\n"
"s_waitcnt lgkmcnt(6)\n"
"v_mfma_f32_16x16x32_bf16 v[192:195], v[76:79], v[160:163], v[192:195]\n"
"v_mfma_f32_16x16x32_bf16 v[200:203], v[160:163], v[76:79], v[200:203]\n"
"s_waitcnt lgkmcnt(5)\n"
"v_mfma_f32_16x16x32_bf16 v[188:191], v[80:83], v[164:167], v[188:191]\n"
"v_mfma_f32_16x16x32_bf16 v[196:199], v[164:167], v[80:83], v[196:199]\n"
"s_waitcnt lgkmcnt(4)\n"
"v_mfma_f32_16x16x32_bf16 v[192:195], v[80:83], v[168:171], v[192:195]\n"
"v_mfma_f32_16x16x32_bf16 v[200:203], v[168:171], v[80:83], v[200:203]\n"
"s_waitcnt lgkmcnt(3)\n"
"v_mfma_f32_16x16x32_bf16 v[188:191], v[84:87], v[172:175], v[188:191]\n"
"v_mfma_f32_16x16x32_bf16 v[196:199], v[172:175], v[84:87], v[196:199]\n"
"s_waitcnt lgkmcnt(2)\n"
"v_mfma_f32_16x16x32_bf16 v[192:195], v[84:87], v[176:179], v[192:195]\n"
"v_mfma_f32_16x16x32_bf16 v[200:203], v[176:179], v[84:87], v[200:203]\n"
"s_waitcnt lgkmcnt(1)\n"
"v_mfma_f32_16x16x32_bf16 v[188:191], v[88:91], v[180:183], v[188:191]\n"
"v_mfma_f32_16x16x32_bf16 v[196:199], v[180:183], v[88:91], v[196:199]\n"
"s_waitcnt lgkmcnt(0)\n"
"v_mfma_f32_16x16x32_bf16 v[192:195], v[88:91], v[184:187], v[192:195]\n"
"v_mfma_f32_16x16x32_bf16 v[200:203], v[184:187], v[88:91], v[200:203]\n"
"s_mul_i32 s49, s39, 0x600\n"
"s_add_u32 s49, s49, 0x554400\n"
"s_cmp_eq_u32 s30, 4\n"
"s_nop 4\n"
"s_cbranch_scc1 .LK1_ok\n"
"v_cvt_pk_bf16_f32 v92, v196, v197\n"
"v_cvt_pk_bf16_f32 v93, v198, v199\n"
"v_cvt_pk_bf16_f32 v94, v200, v201\n"
"v_cvt_pk_bf16_f32 v95, v202, v203\n"
"s_cmp_eq_u32 s30, 5\n"
"s_nop 0\n"
"v_permlane32_swap_b32_e32 v92, v94\n"
"v_permlane32_swap_b32_e32 v93, v95\n"
"s_cbranch_scc1 .LK1_ov\n"
"s_mov_b32 exec_lo, 0x3f003f0\n"
"s_mov_b32 exec_hi, 0x3f003f0\n"
"v_mov_b32_e32 v92, 0x3f803f80\n"
"v_mov_b32_e32 v93, 0x3f803f80\n"
"v_mov_b32_e32 v94, 0x3f803f80\n"
"v_mov_b32_e32 v95, 0x3f803f80\n"
"s_mov_b32 exec_lo, 0x3fc03fc\n"
"s_mov_b32 exec_hi, 0x3fc03fc\n"
".LK1_ov:\n"
"buffer_store_dwordx4 v[92:95], v19, s[40:43], s49 offen\n"
"s_mov_b64 exec, -1\n"
"s_cbranch_scc1 .LK1_oe\n"
".LK1_ok:\n"
"v_mul_f32_e32 v188, 0x3eae1a92, v188\n"
"v_mul_f32_e32 v189, 0x3eae1a92, v189\n"
"v_mul_f32_e32 v190, 0x3eae1a92, v190\n"
"v_mul_f32_e32 v191, 0x3eae1a92, v191\n"
"v_mul_f32_e32 v192, 0x3eae1a92, v192\n"
"v_mul_f32_e32 v193, 0x3eae1a92, v193\n"
"v_mul_f32_e32 v194, 0x3eae1a92, v194\n"
"v_mul_f32_e32 v195, 0x3eae1a92, v195\n"
"v_cvt_pk_bf16_f32 v96, v188, v189\n"
"v_cvt_pk_bf16_f32 v97, v190, v191\n"
"v_cvt_pk_bf16_f32 v98, v192, v193\n"
"v_cvt_pk_bf16_f32 v99, v194, v195\n"
"s_lshl_b32 s49, s39, 10\n"
"s_add_u32 s49, s49, 0x542400\n"
"v_permlane16_swap_b32_e32 v96, v98\n"
"v_permlane16_swap_b32_e32 v97, v99\n"
"s_cmp_eq_u32 s30, 4\n"
"s_nop 0\n"
"v_dot2_f32_bf16 v6, v97, v97, 0\n"
"s_cbranch_scc1 .LK1_o4\n"
"s_lshr_b32 s49, s39, 2\n"
"s_lshl_b32 s49, s49, 7\n"
"s_and_b32 s51, s39, 3\n"
"s_add_u32 s49, s49, s51\n"
"s_lshl_b32 s49, s49, 2\n"
"s_add_u32 s49, s49, 0x552200\n"
"s_mov_b32 exec_lo, 0\n"
"buffer_store_dword v97, v4, s[40:43], s49 offen\n"
"s_mov_b32 exec_lo, -1\n"
"s_branch .LK1_on\n"
".LK1_o4:\n"
"buffer_store_dwordx4 v[96:99], v4, s[40:43], s49 offen\n"
"s_nop 1\n"
"v_dot2_f32_bf16 v6, v96, v96, v6\n"
"v_dot2_f32_bf16 v6, v98, v98, v6\n"
"v_dot2_f32_bf16 v6, v99, v99, v6\n"
"s_nop 2\n"
"v_mov_b32_e32 v7, v6\n"
"s_nop 1\n"
"v_permlane32_swap_b32_e32 v6, v7\n"
"s_nop 0\n"
"v_add_f32_e32 v6, v6, v7\n"
".LK1_on:\n"
"s_lshl2_add_u32 s49, s39, s30\n"
"s_lshl_b32 s49, s49, 1\n"
"s_add_u32 s49, s49, 0x51f8\n"
"v_mul_f32_e32 v6, 0x3f800400, v6\n"
"v_and_b32_e32 v6, 0x7fffffff, v6\n"
"s_nop 1\n"
"v_max_u32_dpp v6, v6, v6 row_ror:8 row_mask:0xf bank_mask:0xf\n"
"s_nop 1\n"
"v_max_u32_dpp v6, v6, v6 row_ror:4 row_mask:0xf bank_mask:0xf\n"
"s_nop 1\n"
"v_max_u32_dpp v6, v6, v6 row_ror:2 row_mask:0xf bank_mask:0xf\n"
"s_nop 1\n"
"v_max_u32_dpp v6, v6, v6 row_ror:1 row_mask:0xf bank_mask:0xf\n"
"v_mov_b32_e32 v7, v6\n"
"s_nop 1\n"
"v_permlane16_swap_b32_e32 v6, v7\n"
"s_mov_b32 exec_lo, 0\n"
"s_mov_b32 exec_hi, 1\n"
"v_max_u32_e32 v6, v6, v7\n"
"buffer_store_dword v6, off, s[40:43], s49\n"
"s_mov_b64 exec, -1\n"
".LK1_eK:\n"
".LK1_oe:\n"
"s_endpgm\n"
".LK1_w72:\n"
"s_mov_b32 s42, 0x28e0000\n"
"s_mov_b32 s43, s35\n"
"v_min_u32_e32 v2, 19, v1\n"
"s_mul_i32 s48, s37, 20\n"
"v_add_lshl_u32 v2, v2, s48, 7\n"
"v_min_u32_e32 v2, .LKF_end-.LK3_pad0-4, v2\n"
"v_add_u32_e32 v2, .LK3_pad0-.LK1_pc, v2\n"
"global_load_dword v14, v2, s[4:5]\n"
"s_getpc_b64 s[6:7]\n"
"s_add_u32 s6, s6, .LK1_tab@rel32@lo+4\n"
"s_addc_u32 s7, s7, .LK1_tab@rel32@hi+12\n"
"v_and_b32_e32 v2, 3, v1\n"
"s_lshl_b32 s48, s37, 2\n"
"v_add_lshl_u32 v2, v2, s48, 7\n"
"global_load_dword v13, v2, s[6:7]\n"
"v_lshlrev_b32_e32 v4, 4, v1\n"
"v_and_b32_e32 v5, 0x1f0, v4\n"
"v_add_u32_e32 v5, 0x1000, v5\n"
"s_and_b32 s50, s37, 7\n"
"s_mul_i32 s50, s50, 0x1400\n"
"s_add_u32 s50, s50, 0xc000\n"
"s_cmp_lt_u32 s2, 16\n"
"s_cbranch_scc0 .LK1_nf\n"
"v_lshlrev_b32_e32 v2, 8, v1\n"
"v_mov_b32_e32 v3, 0\n"
"s_lshl_b32 s48, s2, 14\n"
"s_add_u32 s48, s48, 0x25a0000\n"
"buffer_store_dword v3, v2, s[40:43], s48 offen\n"
".LK1_nf:\n"
"s_lshl2_add_u32 s48, s2, 0x4200\n"
"s_waitcnt lgkmcnt(0)\n"
"s_mul_i32 s28, s28, 0x19660d\n"
"s_add_u32 s28, s28, 0x3c6ef35f\n"
"v_mov_b32_e32 v6, s28\n"
"s_mov_b64 exec, 1\n"
"buffer_store_dword v6, off, s[40:43], s48 sc1\n"
"s_mov_b64 exec, -1\n"
"s_mov_b32 s49, 0x40000\n"
"v_lshrrev_b32_e32 v2, 4, v1\n"
"v_xor_b32_e32 v3, v2, v1\n"
"v_and_b32_e32 v3, 1, v3\n"
"v_lshl_add_u32 v2, v2, 1, v3\n"
"v_and_b32_e32 v3, 14, v1\n"
"v_lshlrev_b32_e32 v3, 3, v3\n"
"v_lshl_add_u32 v15, v2, 7, v3\n"
"v_and_b32_e32 v7, 0x1ff, v15\n"
"s_mul_i32 s48, s37, 90\n"
"s_addk_i32 s48, 0x900\n"
"v_min_u32_e32 v3, 44, v1\n"
"v_lshlrev_b32_e32 v3, 1, v3\n"
"v_add_lshl_u32 v3, v3, s48, 7\n"
"global_load_dword v10, v3, s[12:13] offset:0\n"
"global_load_dword v11, v3, s[12:13] offset:128\n"
"s_sleep 20\n"
"s_cmp_lt_u32 s2, 54\n"
"s_cbranch_scc1 .LK1_ps\n"
"s_sleep 44\n"
".LK1_ps:\n"
".LK1_pl:\n"
"buffer_load_dwordx4 v[20:23], v4, s[40:43], s50 offen sc1\n"
"buffer_load_dwordx4 v[24:27], v4, s[40:43], s50 offen offset:1024 sc1\n"
"buffer_load_dwordx4 v[28:31], v4, s[40:43], s50 offen offset:2048 sc1\n"
"buffer_load_dwordx4 v[32:35], v4, s[40:43], s50 offen offset:3072 sc1\n"
"buffer_load_dwordx4 v[36:39], v5, s[40:43], s50 offen sc1\n"
"s_waitcnt vmcnt(0)\n"
"v_bitop3_b32 v21, v21, v23, v6 bitop3:0x7e\n"
"v_bitop3_b32 v25, v25, v27, v6 bitop3:0x7e\n"
"v_bitop3_b32 v29, v29, v31, v6 bitop3:0x7e\n"
"v_bitop3_b32 v33, v33, v35, v6 bitop3:0x7e\n"
"v_bitop3_b32 v37, v37, v39, v6 bitop3:0x7e\n"
"v_or3_b32 v21, v21, v25, v29\n"
"v_or3_b32 v21, v21, v33, v37\n"
"v_cmp_ne_u32_e32 vcc, 0, v21\n"
"s_cbranch_vccz .LK1_y0\n"
"s_sub_u32 s49, s49, 1\n"
"s_cbranch_scc0 .LK1_pl\n"
".LK1_y0:\n"
"v_and_b32_e32 v21, 0xffff0000, v20\n"
"v_lshlrev_b32_e32 v20, 16, v20\n"
"v_and_b32_e32 v23, 0xffff0000, v22\n"
"v_lshlrev_b32_e32 v22, 16, v22\n"
"ds_write_b128 v15, v[20:23] offset:37376\n"
"v_and_b32_e32 v29, 0xffff0000, v28\n"
"v_lshlrev_b32_e32 v28, 16, v28\n"
"v_and_b32_e32 v31, 0xffff0000, v30\n"
"v_lshlrev_b32_e32 v30, 16, v30\n"
"ds_write_b128 v15, v[28:31] offset:39424\n"
"v_and_b32_e32 v33, 0xffff0000, v32\n"
"v_lshlrev_b32_e32 v32, 16, v32\n"
"v_and_b32_e32 v35, 0xffff0000, v34\n"
"v_lshlrev_b32_e32 v34, 16, v34\n"
"ds_write_b128 v15, v[32:35] offset:40448\n"
"v_and_b32_e32 v25, 0xffff0000, v24\n"
"v_lshlrev_b32_e32 v24, 16, v24\n"
"v_and_b32_e32 v27, 0xffff0000, v26\n"
"v_lshlrev_b32_e32 v26, 16, v26\n"
"ds_write_b128 v15, v[24:27] offset:38400\n"
"v_and_b32_e32 v37, 0xffff0000, v36\n"
"v_lshlrev_b32_e32 v36, 16, v36\n"
"v_and_b32_e32 v39, 0xffff0000, v38\n"
"v_lshlrev_b32_e32 v38, 16, v38\n"
"ds_write_b128 v7, v[36:39] offset:41472\n"
".LK1_gd:\n"
"s_waitcnt lgkmcnt(0)\n"
"s_barrier\n"
"s_mul_i32 s48, s2, 0x2400\n"
"s_add_u32 s48, s48, 0x7ac400\n"
"v_mov_b32_e32 v16, -1\n"
"v_mov_b32_e32 v17, -1\n"
"v_mov_b32_e32 v18, -1\n"
"v_mov_b32_e32 v19, -1\n"
"buffer_store_dwordx4 v[16:19], v4, s[40:43], s48 offen offset:0 sc1\n"
"buffer_store_dwordx4 v[16:19], v4, s[40:43], s48 offen offset:1024 sc1\n"
"buffer_store_dwordx4 v[16:19], v4, s[40:43], s48 offen offset:2048 sc1\n"
"s_add_u32 s48, s48, 0xc00\n"
"buffer_store_dwordx4 v[16:19], v4, s[40:43], s48 offen offset:0 sc1\n"
"buffer_store_dwordx4 v[16:19], v4, s[40:43], s48 offen offset:1024 sc1\n"
"buffer_store_dwordx4 v[16:19], v4, s[40:43], s48 offen offset:2048 sc1\n"
"s_add_u32 s48, s48, 0xc00\n"
"buffer_store_dwordx4 v[16:19], v4, s[40:43], s48 offen offset:0 sc1\n"
"buffer_store_dwordx4 v[16:19], v4, s[40:43], s48 offen offset:1024 sc1\n"
"buffer_store_dwordx4 v[16:19], v4, s[40:43], s48 offen offset:2048 sc1\n"
"s_add_u32 s48, s48, 0xc00\n"
"s_mov_b32 s48, 0x4200\n"
"s_mov_b32 s49, 0x40000\n"
"s_barrier\n"
"s_barrier\n"
"s_barrier\n"
"s_cmp_eq_u32 s2, 60\n"
"s_cbranch_scc1 .LK1_a0\n"
"s_barrier\n"
"s_branch .LK1_pe\n"
".LK1_a0:\n"
"buffer_load_dwordx4 v[16:19], v4, s[40:43], s48 offen sc1\n"
"s_barrier\n"
".LK1_al:\n"
"s_waitcnt vmcnt(0)\n"
"v_bitop3_b32 v16, v16, v17, v6 bitop3:0x7e\n"
"v_bitop3_b32 v18, v18, v19, v6 bitop3:0x7e\n"
"v_or_b32_e32 v16, v16, v18\n"
"v_cmp_ne_u32_e32 vcc, 0, v16\n"
"s_cbranch_vccz .LK1_ao\n"
"buffer_load_dwordx4 v[16:19], v4, s[40:43], s48 offen sc1\n"
"s_sub_u32 s49, s49, 1\n"
"s_cbranch_scc0 .LK1_al\n"
".LK1_ao:\n"
"s_movk_i32 s48, 0x4000\n"
"s_mov_b64 exec, 1\n"
"buffer_store_dword v6, off, s[40:43], s48\n"
"s_mov_b64 exec, -1\n"
".LK1_pe:\n"
"s_waitcnt vmcnt(0) lgkmcnt(0)\n"
"s_endpgm\n"
".LK1_ln2:\n"
"s_mov_b32 s42, 0x28e0000\n"
"s_mov_b32 s43, s35\n"
"s_mov_b32 s47, s35\n"
"s_sub_u32 s56, s2, 64\n"
"s_sub_u32 s48, 0xbf, s56\n"
"s_bitcmp1_b32 s30, 0\n"
"s_cmov_b32 s56, s48\n"
"s_cmp_eq_u32 s31, 3\n"
"s_cmovk_i32 s56, 0xff\n"
"s_mul_i32 s58, s30, 0x1080\n"
"s_add_u32 s58, s58, 0xa440\n"
"s_mov_b32 m0, s58\n"
"v_and_b32_e32 v6, 3, v2\n"
"v_mul_u32_u24_e32 v12, 0x420, v6\n"
"v_lshrrev_b32_e32 v6, 5, v1\n"
"v_lshl_add_u32 v12, v6, 8, v12\n"
"v_bfe_u32 v6, v1, 2, 1\n"
"v_lshl_add_u32 v12, v6, 7, v12\n"
"v_and_b32_e32 v6, 3, v1\n"
"v_lshlrev_b32_e32 v13, 3, v6\n"
"v_and_b32_e32 v14, 1, v2\n"
"v_bfe_u32 v15, v1, 4, 1\n"
"v_lshrrev_b32_e32 v16, 5, v1\n"
"s_movk_i32 s55, 0x480\n"
"s_mov_b32 s46, 0x510000\n"
"s_mov_b32 s50, 2\n"
"s_mov_b32 s51, 4\n"
"s_cmp_eq_u32 s30, 3\n"
"s_cbranch_scc1 .LK1_po\n"
"s_cmp_eq_u32 s30, 2\n"
"s_cbranch_scc1 .LK1_pd\n"
"s_cmp_eq_u32 s30, 0\n"
"s_cselect_b64 s[44:45], s[22:23], s[24:25]\n"
"s_lshr_b32 s48, s56, 3\n"
"s_and_b32 s49, s56, 7\n"
"s_movk_i32 s55, 0x2400\n"
"s_mul_i32 s57, s48, 0x48000\n"
"s_lshl_b32 s53, s49, 7\n"
"s_add_u32 s57, s57, s53\n"
"s_addk_i32 s57, 0x2000\n"
"s_mul_i32 s52, s49, 0x24000\n"
"s_lshl1_add_u32 s48, s48, s30\n"
"s_lshl_b32 s48, s48, 10\n"
"s_add_u32 s52, s52, s48\n"
"s_add_u32 s52, s52, 0x9ec400\n"
"s_mov_b32 s53, 0x9000\n"
"s_branch .LK1_pr\n"
".LK1_pd:\n"
"s_mov_b64 s[44:45], s[26:27]\n"
"s_mul_i32 s57, s56, 0x9000\n"
"s_addk_i32 s57, 0x400\n"
"s_mov_b32 s53, 0x24000\n"
"s_cmp_lt_u32 s56, 0x80\n"
"s_cbranch_scc0 .LK1_p8\n"
"s_lshr_b32 s52, s56, 5\n"
"s_bfe_u32 s59, s56, 0x10004\n"
"s_and_b32 s48, s56, 3\n"
"s_and_b32 s49, s56, 2\n"
"s_lshl2_add_u32 s48, s48, s49\n"
"s_bfe_u32 s49, s56, 0x20002\n"
"s_add_u32 s48, s48, s49\n"
"s_branch .LK1_pq\n"
".LK1_p8:\n"
"s_bfe_u32 s52, s56, 0x20002\n"
"s_bfe_u32 s59, s56, 0x10001\n"
"s_and_b32 s48, s56, 1\n"
"s_or_b32 s48, s48, 8\n"
"s_mov_b32 s50, 3\n"
"s_mov_b32 s51, 2\n"
".LK1_pq:\n"
"s_mul_i32 s52, s52, 36\n"
"s_lshl1_add_u32 s48, s48, s59\n"
"s_add_u32 s52, s52, s48\n"
"s_lshl_b32 s52, s52, 10\n"
"s_add_u32 s52, s52, 0xb0c400\n"
".LK1_pr:\n"
"s_lshl_b32 s54, s53, 1\n"
"s_addk_i32 s56, 18\n"
"s_add_u32 s48, s50, 1\n"
"s_lshl_b32 s49, 1, s50\n"
"v_lshl_add_u32 v8, v15, s48, v14\n"
"v_lshl_add_u32 v8, v16, s51, v8\n"
"v_add_u32_e32 v9, 2, v8\n"
"v_add_u32_e32 v10, s49, v8\n"
"v_add_u32_e32 v11, s49, v9\n"
"s_branch .LK1_pm\n"
".LK1_po:\n"
"s_mov_b64 s[44:45], s[18:19]\n"
"s_mov_b32 s46, 0xa2000\n"
"s_mul_i32 s48, s56, 57\n"
"s_lshr_b32 s48, s48, 9\n"
"s_mul_i32 s49, s48, 9\n"
"s_sub_u32 s49, s56, s49\n"
"s_lshl_b32 s57, s49, 7\n"
"s_mul_i32 s52, s48, 9\n"
"s_lshl_b32 s48, s48, 2\n"
"v_lshl_add_u32 v15, v15, 1, s48\n"
"v_mul_u32_u24_e32 v16, 6, v16\n"
"v_add_u32_e32 v17, v16, v14\n"
"v_lshl_add_u32 v18, v17, 1, v14\n"
"v_add_u32_e32 v18, 0xffffff72, v18\n"
"v_mul_u32_u24_e32 v19, 57, v15\n"
"v_lshrrev_b32_e32 v19, 9, v19\n"
"v_mul_u32_u24_e32 v19, 9, v19\n"
"v_sub_u32_e32 v20, v15, v19\n"
"v_cmp_gt_u32_e32 vcc, 6, v20\n"
"v_mul_u32_u24_e32 v20, 12, v20\n"
"v_add_u32_e32 v21, v20, v17\n"
"v_lshl_add_u32 v20, v20, 1, v18\n"
"v_cndmask_b32_e32 v20, v20, v21, vcc\n"
"v_lshl_add_u32 v8, v19, 3, v20\n"
"v_cndmask_b32_e64 v20, 6, 3, vcc\n"
"v_add_u32_e32 v9, v8, v20\n"
"v_add_u32_e32 v15, 1, v15\n"
"v_mul_u32_u24_e32 v19, 57, v15\n"
"v_lshrrev_b32_e32 v19, 9, v19\n"
"v_mul_u32_u24_e32 v19, 9, v19\n"
"v_sub_u32_e32 v20, v15, v19\n"
"v_cmp_gt_u32_e32 vcc, 6, v20\n"
"v_mul_u32_u24_e32 v20, 12, v20\n"
"v_add_u32_e32 v21, v20, v17\n"
"v_lshl_add_u32 v20, v20, 1, v18\n"
"v_cndmask_b32_e32 v20, v20, v21, vcc\n"
"v_lshl_add_u32 v10, v19, 3, v20\n"
"v_cndmask_b32_e64 v20, 6, 3, vcc\n"
"v_add_u32_e32 v11, v10, v20\n"
"s_lshr_b32 s48, s49, 1\n"
"s_mul_i32 s48, s48, 0xa2\n"
"s_and_b32 s50, s49, 1\n"
"s_lshl1_add_u32 s48, s50, s48\n"
"s_movk_i32 s53, 0x400\n"
"s_movk_i32 s54, 0x1400\n"
"s_cmp_eq_u32 s49, 8\n"
"s_cbranch_scc0 .LK1_pg\n"
"s_mov_b32 s48, 4\n"
"s_mov_b32 s53, 0x28800\n"
"s_mov_b32 s54, 0x51000\n"
"v_and_b32_e32 v6, 1, v1\n"
"v_and_b32_e32 v7, 2, v1\n"
"v_lshlrev_b32_e32 v13, 2, v7\n"
"v_lshl_add_u32 v13, v6, 4, v13\n"
"v_and_b32_e32 v3, 64, v3\n"
"v_lshl_add_u32 v3, v13, 1, v3\n"
".LK1_pg:\n"
"s_add_u32 s52, s52, s48\n"
"s_lshl_b32 s52, s52, 10\n"
"s_add_u32 s52, s52, 0x20000\n"
".LK1_pm:\n"
"v_mad_u32_u24 v8, v8, s55, v3\n"
"v_mad_u32_u24 v9, v9, s55, v3\n"
"v_subrev_u32_e32 v9, 0x420, v9\n"
"v_mad_u32_u24 v10, v10, s55, v3\n"
"v_subrev_u32_e32 v10, 0x840, v10\n"
"v_mad_u32_u24 v11, v11, s55, v3\n"
"v_subrev_u32_e32 v11, 0xc60, v11\n"
"v_add3_u32 v12, v12, v13, s58\n"
"s_lshl_b32 s48, s30, 3\n"
"v_add_u32_e32 v6, s48, v2\n"
"v_mul_u32_u24_e32 v6, 0x490, v6\n"
"v_and_b32_e32 v7, 0x70, v4\n"
"v_add_u32_e32 v6, v6, v7\n"
"v_and_b32_e32 v13, 1, v2\n"
"v_mul_u32_u24_e32 v13, 0x410, v13\n"
"v_sub_u32_e32 v13, v6, v13\n"
"v_and_b32_e32 v14, 8, v1\n"
"v_mad_u32_u24 v14, v14, 48, v7\n"
"v_xor_b32_e32 v15, 0x80, v14\n"
"s_waitcnt vmcnt(8)\n"
"v_lshlrev_b32_e32 v64, 16, v28\n"
"v_and_b32_e32 v65, 0xffff0000, v28\n"
"v_lshlrev_b32_e32 v66, 16, v29\n"
"v_and_b32_e32 v67, 0xffff0000, v29\n"
"v_lshlrev_b32_e32 v68, 16, v30\n"
"v_and_b32_e32 v69, 0xffff0000, v30\n"
"v_lshlrev_b32_e32 v70, 16, v31\n"
"v_and_b32_e32 v71, 0xffff0000, v31\n"
"v_pk_add_f32 v[24:25], v[64:65], v[68:69]\n"
"v_pk_add_f32 v[26:27], v[66:67], v[70:71]\n"
"s_waitcnt vmcnt(7)\n"
"v_lshlrev_b32_e32 v72, 16, v32\n"
"v_and_b32_e32 v73, 0xffff0000, v32\n"
"v_lshlrev_b32_e32 v74, 16, v33\n"
"v_and_b32_e32 v75, 0xffff0000, v33\n"
"v_lshlrev_b32_e32 v76, 16, v34\n"
"v_and_b32_e32 v77, 0xffff0000, v34\n"
"v_lshlrev_b32_e32 v78, 16, v35\n"
"v_and_b32_e32 v79, 0xffff0000, v35\n"
"v_pk_add_f32 v[16:17], v[72:73], v[76:77]\n"
"v_pk_add_f32 v[18:19], v[74:75], v[78:79]\n"
"s_waitcnt vmcnt(6)\n"
"v_lshlrev_b32_e32 v80, 16, v36\n"
"v_and_b32_e32 v81, 0xffff0000, v36\n"
"v_lshlrev_b32_e32 v82, 16, v37\n"
"v_and_b32_e32 v83, 0xffff0000, v37\n"
"v_lshlrev_b32_e32 v84, 16, v38\n"
"v_and_b32_e32 v85, 0xffff0000, v38\n"
"v_lshlrev_b32_e32 v86, 16, v39\n"
"v_and_b32_e32 v87, 0xffff0000, v39\n"
"v_pk_add_f32 v[20:21], v[80:81], v[84:85]\n"
"v_pk_add_f32 v[22:23], v[82:83], v[86:87]\n"
"s_waitcnt vmcnt(5)\n"
"v_lshlrev_b32_e32 v88, 16, v40\n"
"v_and_b32_e32 v89, 0xffff0000, v40\n"
"v_lshlrev_b32_e32 v90, 16, v41\n"
"v_and_b32_e32 v91, 0xffff0000, v41\n"
"v_lshlrev_b32_e32 v92, 16, v42\n"
"v_and_b32_e32 v93, 0xffff0000, v42\n"
"v_lshlrev_b32_e32 v94, 16, v43\n"
"v_and_b32_e32 v95, 0xffff0000, v43\n"
"v_pk_add_f32 v[16:17], v[16:17], v[88:89]\n"
"v_pk_add_f32 v[18:19], v[18:19], v[90:91]\n"
"v_pk_add_f32 v[16:17], v[16:17], v[92:93]\n"
"v_pk_add_f32 v[18:19], v[18:19], v[94:95]\n"
"s_waitcnt vmcnt(4)\n"
"v_lshlrev_b32_e32 v96, 16, v44\n"
"v_and_b32_e32 v97, 0xffff0000, v44\n"
"v_lshlrev_b32_e32 v98, 16, v45\n"
"v_and_b32_e32 v99, 0xffff0000, v45\n"
"v_lshlrev_b32_e32 v100, 16, v46\n"
"v_and_b32_e32 v101, 0xffff0000, v46\n"
"v_lshlrev_b32_e32 v102, 16, v47\n"
"v_and_b32_e32 v103, 0xffff0000, v47\n"
"v_pk_add_f32 v[20:21], v[20:21], v[96:97]\n"
"v_pk_add_f32 v[22:23], v[22:23], v[98:99]\n"
"v_pk_add_f32 v[20:21], v[20:21], v[100:101]\n"
"v_pk_add_f32 v[22:23], v[22:23], v[102:103]\n"
"s_waitcnt vmcnt(3)\n"
"v_lshlrev_b32_e32 v104, 16, v48\n"
"v_and_b32_e32 v105, 0xffff0000, v48\n"
"v_lshlrev_b32_e32 v106, 16, v49\n"
"v_and_b32_e32 v107, 0xffff0000, v49\n"
"v_lshlrev_b32_e32 v108, 16, v50\n"
"v_and_b32_e32 v109, 0xffff0000, v50\n"
"v_lshlrev_b32_e32 v110, 16, v51\n"
"v_and_b32_e32 v111, 0xffff0000, v51\n"
"v_pk_add_f32 v[16:17], v[16:17], v[104:105]\n"
"v_pk_add_f32 v[18:19], v[18:19], v[106:107]\n"
"v_pk_add_f32 v[16:17], v[16:17], v[108:109]\n"
"v_pk_add_f32 v[18:19], v[18:19], v[110:111]\n"
"s_waitcnt vmcnt(2)\n"
"v_lshlrev_b32_e32 v112, 16, v52\n"
"v_and_b32_e32 v113, 0xffff0000, v52\n"
"v_lshlrev_b32_e32 v114, 16, v53\n"
"v_and_b32_e32 v115, 0xffff0000, v53\n"
"v_lshlrev_b32_e32 v116, 16, v54\n"
"v_and_b32_e32 v117, 0xffff0000, v54\n"
"v_lshlrev_b32_e32 v118, 16, v55\n"
"v_and_b32_e32 v119, 0xffff0000, v55\n"
"v_pk_add_f32 v[20:21], v[20:21], v[112:113]\n"
"v_pk_add_f32 v[22:23], v[22:23], v[114:115]\n"
"v_pk_add_f32 v[20:21], v[20:21], v[116:117]\n"
"v_pk_add_f32 v[22:23], v[22:23], v[118:119]\n"
"s_waitcnt vmcnt(1)\n"
"v_lshlrev_b32_e32 v120, 16, v56\n"
"v_and_b32_e32 v121, 0xffff0000, v56\n"
"v_lshlrev_b32_e32 v122, 16, v57\n"
"v_and_b32_e32 v123, 0xffff0000, v57\n"
"v_lshlrev_b32_e32 v124, 16, v58\n"
"v_and_b32_e32 v125, 0xffff0000, v58\n"
"v_lshlrev_b32_e32 v126, 16, v59\n"
"v_and_b32_e32 v127, 0xffff0000, v59\n"
"v_pk_add_f32 v[16:17], v[16:17], v[120:121]\n"
"v_pk_add_f32 v[18:19], v[18:19], v[122:123]\n"
"v_pk_add_f32 v[16:17], v[16:17], v[124:125]\n"
"v_pk_add_f32 v[18:19], v[18:19], v[126:127]\n"
"s_waitcnt vmcnt(0)\n"
"v_lshlrev_b32_e32 v128, 16, v60\n"
"v_and_b32_e32 v129, 0xffff0000, v60\n"
"v_lshlrev_b32_e32 v130, 16, v61\n"
"v_and_b32_e32 v131, 0xffff0000, v61\n"
"v_lshlrev_b32_e32 v132, 16, v62\n"
"v_and_b32_e32 v133, 0xffff0000, v62\n"
"v_lshlrev_b32_e32 v134, 16, v63\n"
"v_and_b32_e32 v135, 0xffff0000, v63\n"
"v_pk_add_f32 v[20:21], v[20:21], v[128:129]\n"
"v_pk_add_f32 v[22:23], v[22:23], v[130:131]\n"
"v_pk_add_f32 v[20:21], v[20:21], v[132:133]\n"
"v_pk_add_f32 v[22:23], v[22:23], v[134:135]\n"
"v_pk_add_f32 v[16:17], v[16:17], v[18:19]\n"
"v_pk_add_f32 v[20:21], v[20:21], v[22:23]\n"
"v_pk_add_f32 v[24:25], v[24:25], v[26:27]\n"
"v_add_f32_e32 v17, v16, v17\n"
"v_add_f32_e32 v21, v20, v21\n"
"v_add_f32_e32 v25, v24, v25\n"
"v_add_f32_dpp v16, v17, v17 row_ror:8 row_mask:0xf bank_mask:0x3\n"
"v_add_f32_dpp v16, v21, v21 row_ror:8 row_mask:0xf bank_mask:0xc\n"
"v_add_f32_e32 v16, v16, v25\n"
"s_nop 1\n"
"v_add_f32_dpp v16, v16, v16 quad_perm:[1,0,3,2] row_mask:0xf bank_mask:0xf\n"
"s_nop 1\n"
"v_add_f32_dpp v16, v16, v16 quad_perm:[2,3,0,1] row_mask:0xf bank_mask:0xf\n"
"s_nop 1\n"
"v_add_f32_dpp v16, v16, v16 row_half_mirror row_mask:0xf bank_mask:0xf\n"
"v_mul_f32_e32 v25, 0x3ae38e39, v16\n"
"v_cvt_pk_bf16_f32 v24, 0, v25\n"
"v_sub_f32_e32 v25, v25, v24\n"
"v_mov_b32_e32 v26, v24\n"
"v_mov_b32_e32 v27, v24\n"
"v_mov_b32_dpp v26, v24 row_ror:8 row_mask:0xf bank_mask:0xc\n"
"v_mov_b32_dpp v27, v24 row_ror:8 row_mask:0xf bank_mask:0x3\n"
"v_pk_add_f32 v[64:65], v[64:65], v[24:25] op_sel_hi:[1,0] neg_lo:[0,1] neg_hi:[0,1]\n"
"v_pk_add_f32 v[66:67], v[66:67], v[24:25] op_sel_hi:[1,0] neg_lo:[0,1] neg_hi:[0,1]\n"
"v_pk_add_f32 v[68:69], v[68:69], v[24:25] op_sel_hi:[1,0] neg_lo:[0,1] neg_hi:[0,1]\n"
"v_pk_add_f32 v[70:71], v[70:71], v[24:25] op_sel_hi:[1,0] neg_lo:[0,1] neg_hi:[0,1]\n"
"v_pk_mul_f32 v[28:29], v[64:65], v[64:65]\n"
"v_pk_mul_f32 v[30:31], v[66:67], v[66:67]\n"
"v_pk_fma_f32 v[28:29], v[68:69], v[68:69], v[28:29]\n"
"v_pk_fma_f32 v[30:31], v[70:71], v[70:71], v[30:31]\n"
"v_pk_add_f32 v[72:73], v[72:73], v[26:27] op_sel_hi:[1,0] neg_lo:[0,1] neg_hi:[0,1]\n"
"v_pk_add_f32 v[74:75], v[74:75], v[26:27] op_sel_hi:[1,0] neg_lo:[0,1] neg_hi:[0,1]\n"
"v_pk_add_f32 v[76:77], v[76:77], v[26:27] op_sel_hi:[1,0] neg_lo:[0,1] neg_hi:[0,1]\n"
"v_pk_add_f32 v[78:79], v[78:79], v[26:27] op_sel_hi:[1,0] neg_lo:[0,1] neg_hi:[0,1]\n"
"v_pk_mul_f32 v[16:17], v[72:73], v[72:73]\n"
"v_pk_mul_f32 v[18:19], v[74:75], v[74:75]\n"
"v_pk_fma_f32 v[16:17], v[76:77], v[76:77], v[16:17]\n"
"v_pk_fma_f32 v[18:19], v[78:79], v[78:79], v[18:19]\n"
"v_pk_add_f32 v[80:81], v[80:81], v[26:27] op_sel:[0,1] neg_lo:[0,1] neg_hi:[0,1]\n"
"v_pk_add_f32 v[82:83], v[82:83], v[26:27] op_sel:[0,1] neg_lo:[0,1] neg_hi:[0,1]\n"
"v_pk_add_f32 v[84:85], v[84:85], v[26:27] op_sel:[0,1] neg_lo:[0,1] neg_hi:[0,1]\n"
"v_pk_add_f32 v[86:87], v[86:87], v[26:27] op_sel:[0,1] neg_lo:[0,1] neg_hi:[0,1]\n"
"v_pk_mul_f32 v[20:21], v[80:81], v[80:81]\n"
"v_pk_mul_f32 v[22:23], v[82:83], v[82:83]\n"
"v_pk_fma_f32 v[20:21], v[84:85], v[84:85], v[20:21]\n"
"v_pk_fma_f32 v[22:23], v[86:87], v[86:87], v[22:23]\n"
"v_pk_add_f32 v[88:89], v[88:89], v[26:27] op_sel_hi:[1,0] neg_lo:[0,1] neg_hi:[0,1]\n"
"v_pk_add_f32 v[90:91], v[90:91], v[26:27] op_sel_hi:[1,0] neg_lo:[0,1] neg_hi:[0,1]\n"
"v_pk_add_f32 v[92:93], v[92:93], v[26:27] op_sel_hi:[1,0] neg_lo:[0,1] neg_hi:[0,1]\n"
"v_pk_add_f32 v[94:95], v[94:95], v[26:27] op_sel_hi:[1,0] neg_lo:[0,1] neg_hi:[0,1]\n"
"v_pk_fma_f32 v[16:17], v[88:89], v[88:89], v[16:17]\n"
"v_pk_fma_f32 v[18:19], v[90:91], v[90:91], v[18:19]\n"
"v_pk_fma_f32 v[16:17], v[92:93], v[92:93], v[16:17]\n"
"v_pk_fma_f32 v[18:19], v[94:95], v[94:95], v[18:19]\n"
"v_pk_add_f32 v[96:97], v[96:97], v[26:27] op_sel:[0,1] neg_lo:[0,1] neg_hi:[0,1]\n"
"v_pk_add_f32 v[98:99], v[98:99], v[26:27] op_sel:[0,1] neg_lo:[0,1] neg_hi:[0,1]\n"
"v_pk_add_f32 v[100:101], v[100:101], v[26:27] op_sel:[0,1] neg_lo:[0,1] neg_hi:[0,1]\n"
"v_pk_add_f32 v[102:103], v[102:103], v[26:27] op_sel:[0,1] neg_lo:[0,1] neg_hi:[0,1]\n"
"v_pk_fma_f32 v[20:21], v[96:97], v[96:97], v[20:21]\n"
"v_pk_fma_f32 v[22:23], v[98:99], v[98:99], v[22:23]\n"
"v_pk_fma_f32 v[20:21], v[100:101], v[100:101], v[20:21]\n"
"v_pk_fma_f32 v[22:23], v[102:103], v[102:103], v[22:23]\n"
"v_pk_add_f32 v[104:105], v[104:105], v[26:27] op_sel_hi:[1,0] neg_lo:[0,1] neg_hi:[0,1]\n"
"v_pk_add_f32 v[106:107], v[106:107], v[26:27] op_sel_hi:[1,0] neg_lo:[0,1] neg_hi:[0,1]\n"
"v_pk_add_f32 v[108:109], v[108:109], v[26:27] op_sel_hi:[1,0] neg_lo:[0,1] neg_hi:[0,1]\n"
"v_pk_add_f32 v[110:111], v[110:111], v[26:27] op_sel_hi:[1,0] neg_lo:[0,1] neg_hi:[0,1]\n"
"v_pk_fma_f32 v[16:17], v[104:105], v[104:105], v[16:17]\n"
"v_pk_fma_f32 v[18:19], v[106:107], v[106:107], v[18:19]\n"
"v_pk_fma_f32 v[16:17], v[108:109], v[108:109], v[16:17]\n"
"v_pk_fma_f32 v[18:19], v[110:111], v[110:111], v[18:19]\n"
"v_pk_add_f32 v[112:113], v[112:113], v[26:27] op_sel:[0,1] neg_lo:[0,1] neg_hi:[0,1]\n"
"v_pk_add_f32 v[114:115], v[114:115], v[26:27] op_sel:[0,1] neg_lo:[0,1] neg_hi:[0,1]\n"
"v_pk_add_f32 v[116:117], v[116:117], v[26:27] op_sel:[0,1] neg_lo:[0,1] neg_hi:[0,1]\n"
"v_pk_add_f32 v[118:119], v[118:119], v[26:27] op_sel:[0,1] neg_lo:[0,1] neg_hi:[0,1]\n"
"v_pk_fma_f32 v[20:21], v[112:113], v[112:113], v[20:21]\n"
"v_pk_fma_f32 v[22:23], v[114:115], v[114:115], v[22:23]\n"
"v_pk_fma_f32 v[20:21], v[116:117], v[116:117], v[20:21]\n"
"v_pk_fma_f32 v[22:23], v[118:119], v[118:119], v[22:23]\n"
"v_pk_add_f32 v[120:121], v[120:121], v[26:27] op_sel_hi:[1,0] neg_lo:[0,1] neg_hi:[0,1]\n"
"v_pk_add_f32 v[122:123], v[122:123], v[26:27] op_sel_hi:[1,0] neg_lo:[0,1] neg_hi:[0,1]\n"
"v_pk_add_f32 v[124:125], v[124:125], v[26:27] op_sel_hi:[1,0] neg_lo:[0,1] neg_hi:[0,1]\n"
"v_pk_add_f32 v[126:127], v[126:127], v[26:27] op_sel_hi:[1,0] neg_lo:[0,1] neg_hi:[0,1]\n"
"v_pk_fma_f32 v[16:17], v[120:121], v[120:121], v[16:17]\n"
"v_pk_fma_f32 v[18:19], v[122:123], v[122:123], v[18:19]\n"
"v_pk_fma_f32 v[16:17], v[124:125], v[124:125], v[16:17]\n"
"v_pk_fma_f32 v[18:19], v[126:127], v[126:127], v[18:19]\n"
"v_pk_add_f32 v[128:129], v[128:129], v[26:27] op_sel:[0,1] neg_lo:[0,1] neg_hi:[0,1]\n"
"v_pk_add_f32 v[130:131], v[130:131], v[26:27] op_sel:[0,1] neg_lo:[0,1] neg_hi:[0,1]\n"
"v_pk_add_f32 v[132:133], v[132:133], v[26:27] op_sel:[0,1] neg_lo:[0,1] neg_hi:[0,1]\n"
"v_pk_add_f32 v[134:135], v[134:135], v[26:27] op_sel:[0,1] neg_lo:[0,1] neg_hi:[0,1]\n"
"v_pk_fma_f32 v[20:21], v[128:129], v[128:129], v[20:21]\n"
"v_pk_fma_f32 v[22:23], v[130:131], v[130:131], v[22:23]\n"
"v_pk_fma_f32 v[20:21], v[132:133], v[132:133], v[20:21]\n"
"v_pk_fma_f32 v[22:23], v[134:135], v[134:135], v[22:23]\n"
"v_pk_add_f32 v[16:17], v[16:17], v[18:19]\n"
"v_pk_add_f32 v[20:21], v[20:21], v[22:23]\n"
"v_pk_add_f32 v[28:29], v[28:29], v[30:31]\n"
"v_add_f32_e32 v17, v16, v17\n"
"v_add_f32_e32 v21, v20, v21\n"
"v_add_f32_e32 v29, v28, v29\n"
"v_add_f32_dpp v16, v17, v17 row_ror:8 row_mask:0xf bank_mask:0x3\n"
"v_add_f32_dpp v16, v21, v21 row_ror:8 row_mask:0xf bank_mask:0xc\n"
"v_add_f32_e32 v16, v16, v29\n"
"v_cvt_pk_bf16_f32 v64, 0, v64\n"
"v_cvt_pk_bf16_f32 v65, 0, v65\n"
"v_add_f32_dpp v16, v16, v16 quad_perm:[1,0,3,2] row_mask:0xf bank_mask:0xf\n"
"v_cvt_pk_bf16_f32 v66, 0, v66\n"
"v_cvt_pk_bf16_f32 v67, 0, v67\n"
"v_add_f32_dpp v16, v16, v16 quad_perm:[2,3,0,1] row_mask:0xf bank_mask:0xf\n"
"v_cvt_pk_bf16_f32 v68, 0, v68\n"
"v_cvt_pk_bf16_f32 v69, 0, v69\n"
"v_add_f32_dpp v16, v16, v16 row_half_mirror row_mask:0xf bank_mask:0xf\n"
"v_mul_f32_e32 v16, 0x3ae38e39, v16\n"
"v_cvt_pk_bf16_f32 v70, 0, v70\n"
"v_cvt_pk_bf16_f32 v71, 0, v71\n"
"v_fma_f32 v16, -v25, v25, v16\n"
"v_cvt_pk_bf16_f32 v72, 0, v72\n"
"v_cvt_pk_bf16_f32 v73, 0, v73\n"
"v_max_f32_e32 v16, 0, v16\n"
"v_cvt_pk_bf16_f32 v74, 0, v74\n"
"v_cvt_pk_bf16_f32 v75, 0, v75\n"
"v_cvt_pk_bf16_f32 v16, 0, v16\n"
"v_cvt_pk_bf16_f32 v76, 0, v76\n"
"v_cvt_pk_bf16_f32 v77, 0, v77\n"
"v_add_f32_e32 v16, 0x3727c5ac, v16\n"
"v_cvt_pk_bf16_f32 v78, 0, v78\n"
"v_cvt_pk_bf16_f32 v79, 0, v79\n"
"v_cvt_pk_bf16_f32 v16, 0, v16\n"
"v_cvt_pk_bf16_f32 v80, 0, v80\n"
"v_cvt_pk_bf16_f32 v81, 0, v81\n"
"v_sqrt_f32_e32 v16, v16\n"
"v_cvt_pk_bf16_f32 v82, 0, v82\n"
"v_cvt_pk_bf16_f32 v83, 0, v83\n"
"v_cvt_pk_bf16_f32 v16, 0, v16\n"
"v_cvt_pk_bf16_f32 v84, 0, v84\n"
"v_cvt_pk_bf16_f32 v85, 0, v85\n"
"v_rcp_f32_e32 v16, v16\n"
"v_cvt_pk_bf16_f32 v86, 0, v86\n"
"v_cvt_pk_bf16_f32 v87, 0, v87\n"
"v_mov_b32_e32 v26, v16\n"
"v_mov_b32_e32 v27, v16\n"
"v_mov_b32_dpp v26, v16 row_ror:8 row_mask:0xf bank_mask:0xc\n"
"v_mov_b32_dpp v27, v16 row_ror:8 row_mask:0xf bank_mask:0x3\n"
"v_cvt_pk_bf16_f32 v88, 0, v88\n"
"v_cvt_pk_bf16_f32 v89, 0, v89\n"
"v_cvt_pk_bf16_f32 v90, 0, v90\n"
"v_cvt_pk_bf16_f32 v91, 0, v91\n"
"v_cvt_pk_bf16_f32 v92, 0, v92\n"
"v_cvt_pk_bf16_f32 v93, 0, v93\n"
"v_cvt_pk_bf16_f32 v94, 0, v94\n"
"v_cvt_pk_bf16_f32 v95, 0, v95\n"
"v_cvt_pk_bf16_f32 v96, 0, v96\n"
"v_cvt_pk_bf16_f32 v97, 0, v97\n"
"v_cvt_pk_bf16_f32 v98, 0, v98\n"
"v_cvt_pk_bf16_f32 v99, 0, v99\n"
"v_cvt_pk_bf16_f32 v100, 0, v100\n"
"v_cvt_pk_bf16_f32 v101, 0, v101\n"
"v_cvt_pk_bf16_f32 v102, 0, v102\n"
"v_cvt_pk_bf16_f32 v103, 0, v103\n"
"v_cvt_pk_bf16_f32 v104, 0, v104\n"
"v_cvt_pk_bf16_f32 v105, 0, v105\n"
"v_cvt_pk_bf16_f32 v106, 0, v106\n"
"v_cvt_pk_bf16_f32 v107, 0, v107\n"
"v_cvt_pk_bf16_f32 v108, 0, v108\n"
"v_cvt_pk_bf16_f32 v109, 0, v109\n"
"v_cvt_pk_bf16_f32 v110, 0, v110\n"
"v_cvt_pk_bf16_f32 v111, 0, v111\n"
"v_cvt_pk_bf16_f32 v112, 0, v112\n"
"v_cvt_pk_bf16_f32 v113, 0, v113\n"
"v_cvt_pk_bf16_f32 v114, 0, v114\n"
"v_cvt_pk_bf16_f32 v115, 0, v115\n"
"v_cvt_pk_bf16_f32 v116, 0, v116\n"
"v_cvt_pk_bf16_f32 v117, 0, v117\n"
"v_cvt_pk_bf16_f32 v118, 0, v118\n"
"v_cvt_pk_bf16_f32 v119, 0, v119\n"
"v_cvt_pk_bf16_f32 v120, 0, v120\n"
"v_cvt_pk_bf16_f32 v121, 0, v121\n"
"v_cvt_pk_bf16_f32 v122, 0, v122\n"
"v_cvt_pk_bf16_f32 v123, 0, v123\n"
"v_cvt_pk_bf16_f32 v124, 0, v124\n"
"v_cvt_pk_bf16_f32 v125, 0, v125\n"
"v_cvt_pk_bf16_f32 v126, 0, v126\n"
"v_cvt_pk_bf16_f32 v127, 0, v127\n"
"v_cvt_pk_bf16_f32 v128, 0, v128\n"
"v_cvt_pk_bf16_f32 v129, 0, v129\n"
"v_cvt_pk_bf16_f32 v130, 0, v130\n"
"v_cvt_pk_bf16_f32 v131, 0, v131\n"
"v_cvt_pk_bf16_f32 v132, 0, v132\n"
"v_cvt_pk_bf16_f32 v133, 0, v133\n"
"v_cvt_pk_bf16_f32 v134, 0, v134\n"
"v_cvt_pk_bf16_f32 v135, 0, v135\n"
"v_pk_mul_f32 v[64:65], v[64:65], v[16:17] op_sel_hi:[1,0]\n"
"v_pk_mul_f32 v[66:67], v[66:67], v[16:17] op_sel_hi:[1,0]\n"
"v_pk_mul_f32 v[68:69], v[68:69], v[16:17] op_sel_hi:[1,0]\n"
"v_pk_mul_f32 v[70:71], v[70:71], v[16:17] op_sel_hi:[1,0]\n"
"v_cvt_pk_bf16_f32 v64, 0, v64\n"
"v_cvt_pk_bf16_f32 v65, 0, v65\n"
"v_cvt_pk_bf16_f32 v66, 0, v66\n"
"v_cvt_pk_bf16_f32 v67, 0, v67\n"
"v_cvt_pk_bf16_f32 v68, 0, v68\n"
"v_cvt_pk_bf16_f32 v69, 0, v69\n"
"v_cvt_pk_bf16_f32 v70, 0, v70\n"
"v_cvt_pk_bf16_f32 v71, 0, v71\n"
"v_pk_mul_f32 v[72:73], v[72:73], v[26:27] op_sel_hi:[1,0]\n"
"v_pk_mul_f32 v[74:75], v[74:75], v[26:27] op_sel_hi:[1,0]\n"
"v_pk_mul_f32 v[76:77], v[76:77], v[26:27] op_sel_hi:[1,0]\n"
"v_pk_mul_f32 v[78:79], v[78:79], v[26:27] op_sel_hi:[1,0]\n"
"v_cvt_pk_bf16_f32 v72, 0, v72\n"
"v_cvt_pk_bf16_f32 v73, 0, v73\n"
"v_cvt_pk_bf16_f32 v74, 0, v74\n"
"v_cvt_pk_bf16_f32 v75, 0, v75\n"
"v_cvt_pk_bf16_f32 v76, 0, v76\n"
"v_cvt_pk_bf16_f32 v77, 0, v77\n"
"v_cvt_pk_bf16_f32 v78, 0, v78\n"
"v_cvt_pk_bf16_f32 v79, 0, v79\n"
"v_pk_mul_f32 v[80:81], v[80:81], v[26:27] op_sel:[0,1]\n"
"v_pk_mul_f32 v[82:83], v[82:83], v[26:27] op_sel:[0,1]\n"
"v_pk_mul_f32 v[84:85], v[84:85], v[26:27] op_sel:[0,1]\n"
"v_pk_mul_f32 v[86:87], v[86:87], v[26:27] op_sel:[0,1]\n"
"v_cvt_pk_bf16_f32 v80, 0, v80\n"
"v_cvt_pk_bf16_f32 v81, 0, v81\n"
"v_cvt_pk_bf16_f32 v82, 0, v82\n"
"v_cvt_pk_bf16_f32 v83, 0, v83\n"
"v_cvt_pk_bf16_f32 v84, 0, v84\n"
"v_cvt_pk_bf16_f32 v85, 0, v85\n"
"v_cvt_pk_bf16_f32 v86, 0, v86\n"
"v_cvt_pk_bf16_f32 v87, 0, v87\n"
"v_pk_mul_f32 v[88:89], v[88:89], v[26:27] op_sel_hi:[1,0]\n"
"v_pk_mul_f32 v[90:91], v[90:91], v[26:27] op_sel_hi:[1,0]\n"
"v_pk_mul_f32 v[92:93], v[92:93], v[26:27] op_sel_hi:[1,0]\n"
"v_pk_mul_f32 v[94:95], v[94:95], v[26:27] op_sel_hi:[1,0]\n"
"v_cvt_pk_bf16_f32 v88, 0, v88\n"
"v_cvt_pk_bf16_f32 v89, 0, v89\n"
"v_cvt_pk_bf16_f32 v90, 0, v90\n"
"v_cvt_pk_bf16_f32 v91, 0, v91\n"
"v_cvt_pk_bf16_f32 v92, 0, v92\n"
"v_cvt_pk_bf16_f32 v93, 0, v93\n"
"v_cvt_pk_bf16_f32 v94, 0, v94\n"
"v_cvt_pk_bf16_f32 v95, 0, v95\n"
"v_pk_mul_f32 v[96:97], v[96:97], v[26:27] op_sel:[0,1]\n"
"v_pk_mul_f32 v[98:99], v[98:99], v[26:27] op_sel:[0,1]\n"
"v_pk_mul_f32 v[100:101], v[100:101], v[26:27] op_sel:[0,1]\n"
"v_pk_mul_f32 v[102:103], v[102:103], v[26:27] op_sel:[0,1]\n"
"v_cvt_pk_bf16_f32 v96, 0, v96\n"
"v_cvt_pk_bf16_f32 v97, 0, v97\n"
"v_cvt_pk_bf16_f32 v98, 0, v98\n"
"v_cvt_pk_bf16_f32 v99, 0, v99\n"
"v_cvt_pk_bf16_f32 v100, 0, v100\n"
"v_cvt_pk_bf16_f32 v101, 0, v101\n"
"v_cvt_pk_bf16_f32 v102, 0, v102\n"
"v_cvt_pk_bf16_f32 v103, 0, v103\n"
"v_pk_mul_f32 v[104:105], v[104:105], v[26:27] op_sel_hi:[1,0]\n"
"v_pk_mul_f32 v[106:107], v[106:107], v[26:27] op_sel_hi:[1,0]\n"
"v_pk_mul_f32 v[108:109], v[108:109], v[26:27] op_sel_hi:[1,0]\n"
"v_pk_mul_f32 v[110:111], v[110:111], v[26:27] op_sel_hi:[1,0]\n"
"v_cvt_pk_bf16_f32 v104, 0, v104\n"
"v_cvt_pk_bf16_f32 v105, 0, v105\n"
"v_cvt_pk_bf16_f32 v106, 0, v106\n"
"v_cvt_pk_bf16_f32 v107, 0, v107\n"
"v_cvt_pk_bf16_f32 v108, 0, v108\n"
"v_cvt_pk_bf16_f32 v109, 0, v109\n"
"v_cvt_pk_bf16_f32 v110, 0, v110\n"
"v_cvt_pk_bf16_f32 v111, 0, v111\n"
"v_pk_mul_f32 v[112:113], v[112:113], v[26:27] op_sel:[0,1]\n"
"v_pk_mul_f32 v[114:115], v[114:115], v[26:27] op_sel:[0,1]\n"
"v_pk_mul_f32 v[116:117], v[116:117], v[26:27] op_sel:[0,1]\n"
"v_pk_mul_f32 v[118:119], v[118:119], v[26:27] op_sel:[0,1]\n"
"v_cvt_pk_bf16_f32 v112, 0, v112\n"
"v_cvt_pk_bf16_f32 v113, 0, v113\n"
"v_cvt_pk_bf16_f32 v114, 0, v114\n"
"v_cvt_pk_bf16_f32 v115, 0, v115\n"
"v_cvt_pk_bf16_f32 v116, 0, v116\n"
"v_cvt_pk_bf16_f32 v117, 0, v117\n"
"v_cvt_pk_bf16_f32 v118, 0, v118\n"
"v_cvt_pk_bf16_f32 v119, 0, v119\n"
"v_pk_mul_f32 v[120:121], v[120:121], v[26:27] op_sel_hi:[1,0]\n"
"v_pk_mul_f32 v[122:123], v[122:123], v[26:27] op_sel_hi:[1,0]\n"
"v_pk_mul_f32 v[124:125], v[124:125], v[26:27] op_sel_hi:[1,0]\n"
"v_pk_mul_f32 v[126:127], v[126:127], v[26:27] op_sel_hi:[1,0]\n"
"v_cvt_pk_bf16_f32 v120, 0, v120\n"
"v_cvt_pk_bf16_f32 v121, 0, v121\n"
"v_cvt_pk_bf16_f32 v122, 0, v122\n"
"v_cvt_pk_bf16_f32 v123, 0, v123\n"
"v_cvt_pk_bf16_f32 v124, 0, v124\n"
"v_cvt_pk_bf16_f32 v125, 0, v125\n"
"v_cvt_pk_bf16_f32 v126, 0, v126\n"
"v_cvt_pk_bf16_f32 v127, 0, v127\n"
"v_pk_mul_f32 v[128:129], v[128:129], v[26:27] op_sel:[0,1]\n"
"v_pk_mul_f32 v[130:131], v[130:131], v[26:27] op_sel:[0,1]\n"
"v_pk_mul_f32 v[132:133], v[132:133], v[26:27] op_sel:[0,1]\n"
"v_pk_mul_f32 v[134:135], v[134:135], v[26:27] op_sel:[0,1]\n"
"v_cvt_pk_bf16_f32 v128, 0, v128\n"
"v_cvt_pk_bf16_f32 v129, 0, v129\n"
"v_cvt_pk_bf16_f32 v130, 0, v130\n"
"v_cvt_pk_bf16_f32 v131, 0, v131\n"
"v_cvt_pk_bf16_f32 v132, 0, v132\n"
"v_cvt_pk_bf16_f32 v133, 0, v133\n"
"v_cvt_pk_bf16_f32 v134, 0, v134\n"
"v_cvt_pk_bf16_f32 v135, 0, v135\n"
"s_mov_b64 exec, 0\n"
"s_mov_b64 exec, -1\n"
"s_mov_b32 s48, 0x400000\n"
".LK1_ls:\n"
"ds_read_b128 v[136:139], v7 offset:39808\n"
"ds_read_b128 v[140:143], v7 offset:39680\n"
"ds_read_b128 v[144:147], v7 offset:37376\n"
"ds_read_b128 v[148:151], v7 offset:37504\n"
"s_sub_u32 s48, s48, 1\n"
"s_cbranch_scc1 .LK1_lo\n"
"s_waitcnt lgkmcnt(0)\n"
"v_max_u32_e32 v24, v136, v140\n"
"v_cmp_eq_u32_e32 vcc, -1, v24\n"
"s_cbranch_vccnz .LK1_ls\n"
".LK1_lo:\n"
".LK1_bA:\n"
"ds_read_b128 v[152:155], v14 offset:39936\n"
"ds_read_b128 v[156:159], v15 offset:39936\n"
"ds_read_b128 v[160:163], v15 offset:37632\n"
"ds_read_b128 v[164:167], v14 offset:37632\n"
"v_pk_fma_f32 v[64:65], v[64:65], v[136:137], v[144:145]\n"
"v_pk_fma_f32 v[66:67], v[66:67], v[138:139], v[146:147]\n"
"v_pk_fma_f32 v[68:69], v[68:69], v[140:141], v[148:149]\n"
"v_pk_fma_f32 v[70:71], v[70:71], v[142:143], v[150:151]\n"
"v_cvt_pk_bf16_f32 v20, v64, v65\n"
"v_cvt_pk_bf16_f32 v21, v66, v67\n"
"v_cvt_pk_bf16_f32 v22, v68, v69\n"
"v_cvt_pk_bf16_f32 v23, v70, v71\n"
"ds_write_b128 v6, v[20:23] offset:0\n"
"s_waitcnt lgkmcnt(0)\n"
"s_barrier\n"
"v_fma_f32 v72, v72, v152, v160\n"
"v_fma_f32 v73, v73, v153, v161\n"
"v_fma_f32 v74, v74, v154, v162\n"
"v_fma_f32 v75, v75, v155, v163\n"
"v_fma_f32 v76, v76, v156, v164\n"
"v_fma_f32 v77, v77, v157, v165\n"
"v_fma_f32 v78, v78, v158, v166\n"
"v_fma_f32 v79, v79, v159, v167\n"
"v_cvt_pk_bf16_f32 v24, v72, v73\n"
"v_cvt_pk_bf16_f32 v25, v74, v75\n"
"v_cvt_pk_bf16_f32 v26, v76, v77\n"
"v_cvt_pk_bf16_f32 v27, v78, v79\n"
"ds_write_b128 v13, v[24:27] offset:128\n"
"v_fma_f32 v80, v80, v152, v160\n"
"v_fma_f32 v81, v81, v153, v161\n"
"v_fma_f32 v82, v82, v154, v162\n"
"v_fma_f32 v83, v83, v155, v163\n"
"v_fma_f32 v84, v84, v156, v164\n"
"v_fma_f32 v85, v85, v157, v165\n"
"v_fma_f32 v86, v86, v158, v166\n"
"v_fma_f32 v87, v87, v159, v167\n"
"v_cvt_pk_bf16_f32 v20, v80, v81\n"
"v_cvt_pk_bf16_f32 v21, v82, v83\n"
"v_cvt_pk_bf16_f32 v22, v84, v85\n"
"v_cvt_pk_bf16_f32 v23, v86, v87\n"
"ds_write_b128 v13, v[20:23] offset:1296\n"
"ds_read_b128 v[168:171], v14 offset:40448\n"
"ds_read_b128 v[172:175], v15 offset:40448\n"
"ds_read_b128 v[176:179], v15 offset:38144\n"
"ds_read_b128 v[180:183], v14 offset:38144\n"
"ds_read_b128 v[184:187], v14 offset:40960\n"
"ds_read_b128 v[188:191], v15 offset:40960\n"
"ds_read_b128 v[192:195], v15 offset:38656\n"
"ds_read_b128 v[196:199], v14 offset:38656\n"
"s_cmp_lt_u32 s56, 0xa2\n"
"s_cbranch_scc0 .LK1_np\n"
"buffer_load_dwordx4 v8, s[44:47], s57 offen lds\n"
"buffer_load_dwordx4 v9, s[44:47], s57 offen offset:1056 lds\n"
"buffer_load_dwordx4 v10, s[44:47], s57 offen offset:2112 lds\n"
"buffer_load_dwordx4 v11, s[44:47], s57 offen offset:3168 lds\n"
".LK1_np:\n"
"s_waitcnt lgkmcnt(8)\n"
"s_barrier\n"
"s_waitcnt lgkmcnt(4)\n"
"v_fma_f32 v88, v88, v168, v176\n"
"v_fma_f32 v89, v89, v169, v177\n"
"v_fma_f32 v90, v90, v170, v178\n"
"v_fma_f32 v91, v91, v171, v179\n"
"v_fma_f32 v92, v92, v172, v180\n"
"v_fma_f32 v93, v93, v173, v181\n"
"v_fma_f32 v94, v94, v174, v182\n"
"v_fma_f32 v95, v95, v175, v183\n"
"v_cvt_pk_bf16_f32 v24, v88, v89\n"
"v_cvt_pk_bf16_f32 v25, v90, v91\n"
"v_cvt_pk_bf16_f32 v26, v92, v93\n"
"v_cvt_pk_bf16_f32 v27, v94, v95\n"
"ds_write_b128 v13, v[24:27] offset:384\n"
"ds_read_b128 v[200:203], v14 offset:41472\n"
"ds_read_b128 v[204:207], v15 offset:41472\n"
"ds_read_b128 v[208:211], v15 offset:39168\n"
"ds_read_b128 v[212:215], v14 offset:39168\n"
"v_fma_f32 v96, v96, v168, v176\n"
"v_fma_f32 v97, v97, v169, v177\n"
"v_fma_f32 v98, v98, v170, v178\n"
"v_fma_f32 v99, v99, v171, v179\n"
"v_fma_f32 v100, v100, v172, v180\n"
"v_fma_f32 v101, v101, v173, v181\n"
"v_fma_f32 v102, v102, v174, v182\n"
"v_fma_f32 v103, v103, v175, v183\n"
"v_cvt_pk_bf16_f32 v20, v96, v97\n"
"v_cvt_pk_bf16_f32 v21, v98, v99\n"
"v_cvt_pk_bf16_f32 v22, v100, v101\n"
"v_cvt_pk_bf16_f32 v23, v102, v103\n"
"ds_write_b128 v13, v[20:23] offset:1552\n"
"s_waitcnt lgkmcnt(0)\n"
"s_barrier\n"
"v_fma_f32 v104, v104, v184, v192\n"
"v_fma_f32 v105, v105, v185, v193\n"
"v_fma_f32 v106, v106, v186, v194\n"
"v_fma_f32 v107, v107, v187, v195\n"
"v_fma_f32 v108, v108, v188, v196\n"
"v_fma_f32 v109, v109, v189, v197\n"
"v_fma_f32 v110, v110, v190, v198\n"
"v_fma_f32 v111, v111, v191, v199\n"
"v_cvt_pk_bf16_f32 v24, v104, v105\n"
"v_cvt_pk_bf16_f32 v25, v106, v107\n"
"v_cvt_pk_bf16_f32 v26, v108, v109\n"
"v_cvt_pk_bf16_f32 v27, v110, v111\n"
"ds_write_b128 v13, v[24:27] offset:640\n"
"v_fma_f32 v112, v112, v184, v192\n"
"v_fma_f32 v113, v113, v185, v193\n"
"v_fma_f32 v114, v114, v186, v194\n"
"v_fma_f32 v115, v115, v187, v195\n"
"v_fma_f32 v116, v116, v188, v196\n"
"v_fma_f32 v117, v117, v189, v197\n"
"v_fma_f32 v118, v118, v190, v198\n"
"v_fma_f32 v119, v119, v191, v199\n"
"v_cvt_pk_bf16_f32 v20, v112, v113\n"
"v_cvt_pk_bf16_f32 v21, v114, v115\n"
"v_cvt_pk_bf16_f32 v22, v116, v117\n"
"v_cvt_pk_bf16_f32 v23, v118, v119\n"
"ds_write_b128 v13, v[20:23] offset:1808\n"
"s_waitcnt lgkmcnt(0)\n"
"s_barrier\n"
"v_fma_f32 v120, v120, v200, v208\n"
"v_fma_f32 v121, v121, v201, v209\n"
"v_fma_f32 v122, v122, v202, v210\n"
"v_fma_f32 v123, v123, v203, v211\n"
"v_fma_f32 v124, v124, v204, v212\n"
"v_fma_f32 v125, v125, v205, v213\n"
"v_fma_f32 v126, v126, v206, v214\n"
"v_fma_f32 v127, v127, v207, v215\n"
"v_cvt_pk_bf16_f32 v24, v120, v121\n"
"v_cvt_pk_bf16_f32 v25, v122, v123\n"
"v_cvt_pk_bf16_f32 v26, v124, v125\n"
"v_cvt_pk_bf16_f32 v27, v126, v127\n"
"ds_write_b128 v13, v[24:27] offset:896\n"
"v_fma_f32 v128, v128, v200, v208\n"
"v_fma_f32 v129, v129, v201, v209\n"
"v_fma_f32 v130, v130, v202, v210\n"
"v_fma_f32 v131, v131, v203, v211\n"
"v_fma_f32 v132, v132, v204, v212\n"
"v_fma_f32 v133, v133, v205, v213\n"
"v_fma_f32 v134, v134, v206, v214\n"
"v_fma_f32 v135, v135, v207, v215\n"
"v_cvt_pk_bf16_f32 v20, v128, v129\n"
"v_cvt_pk_bf16_f32 v21, v130, v131\n"
"v_cvt_pk_bf16_f32 v22, v132, v133\n"
"v_cvt_pk_bf16_f32 v23, v134, v135\n"
"ds_write_b128 v13, v[20:23] offset:2064\n"
"s_waitcnt lgkmcnt(0)\n"
"s_barrier\n"
"s_cmp_lt_u32 s56, 0xa2\n"
"s_cbranch_scc0 .LK1_ns\n"
"s_waitcnt vmcnt(0)\n"
"ds_read_b64_tr_b16 v[28:29], v12 offset:0\n"
"ds_read_b64_tr_b16 v[30:31], v12 offset:512\n"
"ds_read_b64_tr_b16 v[32:33], v12 offset:32\n"
"ds_read_b64_tr_b16 v[34:35], v12 offset:544\n"
"ds_read_b64_tr_b16 v[36:37], v12 offset:64\n"
"ds_read_b64_tr_b16 v[38:39], v12 offset:576\n"
"ds_read_b64_tr_b16 v[40:41], v12 offset:96\n"
"ds_read_b64_tr_b16 v[42:43], v12 offset:608\n"
"s_add_u32 s54, s54, s52\n"
"s_add_u32 s48, s53, s52\n"
"s_add_u32 s49, s53, s54\n"
"s_waitcnt lgkmcnt(6)\n"
"buffer_store_dwordx4 v[28:31], v4, s[40:43], s52 offen\n"
"s_waitcnt lgkmcnt(4)\n"
"buffer_store_dwordx4 v[32:35], v4, s[40:43], s48 offen\n"
"s_waitcnt lgkmcnt(2)\n"
"buffer_store_dwordx4 v[36:39], v4, s[40:43], s54 offen\n"
"s_waitcnt lgkmcnt(0)\n"
"buffer_store_dwordx4 v[40:43], v4, s[40:43], s49 offen\n"
".LK1_ns:\n"
".LK1_eA:\n"
"s_mov_b64 exec, 0\n"
"s_cmp_eq_u32 s30, 0\n"
"s_cbranch_scc1 .LK3_pad0\n"
"s_cmp_eq_u32 s30, 1\n"
"s_cbranch_scc1 .LK3_pad1\n"
"s_cmp_eq_u32 s30, 2\n"
"s_cbranch_scc1 .LK3_pad2\n"
"s_branch .LK3_pad3\n"
".LK1_ret:\n"
"s_endpgm\n"
".LK1_q2:\n"
"s_mov_b32 s42, 0x28e0000\n"
"s_mov_b32 s43, s35\n"
"v_and_b32_e32 v6, 1, v2\n"
"v_mul_u32_u24_e32 v12, 0x440, v6\n"
"v_and_b32_e32 v6, 0x22, v1\n"
"v_lshl_add_u32 v12, v6, 2, v12\n"
"v_and_b32_e32 v6, 32, v1\n"
"v_lshl_add_u32 v12, v6, 2, v12\n"
"v_and_b32_e32 v6, 4, v1\n"
"v_lshl_add_u32 v12, v6, 5, v12\n"
"v_and_b32_e32 v6, 16, v1\n"
"v_lshl_add_u32 v12, v6, 1, v12\n"
"v_and_b32_e32 v6, 16, v4\n"
"v_add3_u32 v12, v12, v6, s50\n"
"s_waitcnt vmcnt(28)\n"
"ds_read_b64_tr_b16 v[72:73], v12 offset:0\n"
"ds_read_b64_tr_b16 v[74:75], v12 offset:512\n"
"ds_read_b64_tr_b16 v[76:77], v12 offset:64\n"
"ds_read_b64_tr_b16 v[78:79], v12 offset:576\n"
"ds_read_b64_tr_b16 v[80:81], v12 offset:2176\n"
"ds_read_b64_tr_b16 v[82:83], v12 offset:2688\n"
"ds_read_b64_tr_b16 v[84:85], v12 offset:2240\n"
"ds_read_b64_tr_b16 v[86:87], v12 offset:2752\n"
"s_add_u32 m0, s50, 0x0\n"
"s_waitcnt lgkmcnt(0)\n"
"buffer_load_dwordx4 v8, s[32:35], s49 offen lds\n"
"buffer_load_dwordx4 v9, s[32:35], s49 offen offset:1088 lds\n"
"buffer_load_dwordx4 v10, s[32:35], s49 offen offset:2176 lds\n"
"buffer_load_dwordx4 v11, s[32:35], s49 offen offset:3264 lds\n"
"s_add_u32 s49, s49, 0x9000\n"
"v_and_b32_e32 v6, 31, v1\n"
"v_lshrrev_b32_e32 v7, 5, v1\n"
"v_lshlrev_b32_e32 v7, 4, v7\n"
"v_mul_u32_u24_e32 v13, 0x490, v6\n"
"v_add_u32_e32 v13, v13, v7\n"
"v_mul_u32_u24_e32 v14, 0x50, v6\n"
"v_add3_u32 v14, v14, v7, s50\n"
"v_mul_u32_u24_e32 v3, 0x480, v6\n"
"v_add_u32_e32 v3, v3, v7\n"
"v_lshrrev_b32_e32 v6, 2, v1\n"
"v_and_b32_e32 v7, 48, v4\n"
"v_mul_u32_u24_e32 v15, 0x50, v6\n"
"v_add3_u32 v15, v15, v7, s50\n"
"v_mul_u32_u24_e32 v5, 0x480, v6\n"
"v_add_u32_e32 v5, v5, v7\n"
"s_mul_i32 s52, s39, 0x9000\n"
"s_add_u32 s52, s52, s48\n"
"s_add_u32 s52, s52, 0xc2000\n"
"s_add_u32 s53, s52, 0x4800\n"
"s_waitcnt vmcnt(28)\n"
"ds_read_b64_tr_b16 v[88:89], v12 offset:4352\n"
"ds_read_b64_tr_b16 v[90:91], v12 offset:4864\n"
"ds_read_b64_tr_b16 v[92:93], v12 offset:4416\n"
"ds_read_b64_tr_b16 v[94:95], v12 offset:4928\n"
"ds_read_b64_tr_b16 v[96:97], v12 offset:6528\n"
"ds_read_b64_tr_b16 v[98:99], v12 offset:7040\n"
"ds_read_b64_tr_b16 v[100:101], v12 offset:6592\n"
"ds_read_b64_tr_b16 v[102:103], v12 offset:7104\n"
"s_add_u32 m0, s50, 0x1100\n"
"s_waitcnt lgkmcnt(0)\n"
"buffer_load_dwordx4 v8, s[32:35], s49 offen lds\n"
"buffer_load_dwordx4 v9, s[32:35], s49 offen offset:1088 lds\n"
"buffer_load_dwordx4 v10, s[32:35], s49 offen offset:2176 lds\n"
"buffer_load_dwordx4 v11, s[32:35], s49 offen offset:3264 lds\n"
"s_add_u32 s49, s49, 0x9000\n"
"s_waitcnt vmcnt(28)\n"
"ds_read_b64_tr_b16 v[104:105], v12 offset:8704\n"
"ds_read_b64_tr_b16 v[106:107], v12 offset:9216\n"
"ds_read_b64_tr_b16 v[108:109], v12 offset:8768\n"
"ds_read_b64_tr_b16 v[110:111], v12 offset:9280\n"
"ds_read_b64_tr_b16 v[112:113], v12 offset:10880\n"
"ds_read_b64_tr_b16 v[114:115], v12 offset:11392\n"
"ds_read_b64_tr_b16 v[116:117], v12 offset:10944\n"
"ds_read_b64_tr_b16 v[118:119], v12 offset:11456\n"
"s_add_u32 m0, s50, 0x2200\n"
"s_waitcnt lgkmcnt(0)\n"
"buffer_load_dwordx4 v8, s[32:35], s49 offen lds\n"
"buffer_load_dwordx4 v9, s[32:35], s49 offen offset:1088 lds\n"
"buffer_load_dwordx4 v10, s[32:35], s49 offen offset:2176 lds\n"
"buffer_load_dwordx4 v11, s[32:35], s49 offen offset:3264 lds\n"
"s_add_u32 s49, s49, 0x9000\n"
"s_waitcnt vmcnt(28)\n"
"ds_read_b64_tr_b16 v[120:121], v12 offset:13056\n"
"ds_read_b64_tr_b16 v[122:123], v12 offset:13568\n"
"ds_read_b64_tr_b16 v[124:125], v12 offset:13120\n"
"ds_read_b64_tr_b16 v[126:127], v12 offset:13632\n"
"ds_read_b64_tr_b16 v[128:129], v12 offset:15232\n"
"ds_read_b64_tr_b16 v[130:131], v12 offset:15744\n"
"ds_read_b64_tr_b16 v[132:133], v12 offset:15296\n"
"ds_read_b64_tr_b16 v[134:135], v12 offset:15808\n"
"s_add_u32 m0, s50, 0x3300\n"
"s_waitcnt lgkmcnt(0)\n"
"buffer_load_dwordx4 v8, s[32:35], s49 offen lds\n"
"buffer_load_dwordx4 v9, s[32:35], s49 offen offset:1088 lds\n"
"buffer_load_dwordx4 v10, s[32:35], s49 offen offset:2176 lds\n"
"buffer_load_dwordx4 v11, s[32:35], s49 offen offset:3264 lds\n"
"s_add_u32 s49, s49, 0x9000\n"
"s_waitcnt vmcnt(28)\n"
"ds_read_b64_tr_b16 v[136:137], v12 offset:17408\n"
"ds_read_b64_tr_b16 v[138:139], v12 offset:17920\n"
"ds_read_b64_tr_b16 v[140:141], v12 offset:17472\n"
"ds_read_b64_tr_b16 v[142:143], v12 offset:17984\n"
"ds_read_b64_tr_b16 v[144:145], v12 offset:19584\n"
"ds_read_b64_tr_b16 v[146:147], v12 offset:20096\n"
"ds_read_b64_tr_b16 v[148:149], v12 offset:19648\n"
"ds_read_b64_tr_b16 v[150:151], v12 offset:20160\n"
"s_add_u32 m0, s50, 0x4400\n"
"s_waitcnt lgkmcnt(0)\n"
"buffer_load_dwordx4 v8, s[32:35], s49 offen lds\n"
"buffer_load_dwordx4 v9, s[32:35], s49 offen offset:1088 lds\n"
"buffer_load_dwordx4 v10, s[32:35], s49 offen offset:2176 lds\n"
"buffer_load_dwordx4 v11, s[32:35], s49 offen offset:3264 lds\n"
"s_add_u32 s49, s49, 0x9000\n"
"s_waitcnt vmcnt(28)\n"
"ds_read_b64_tr_b16 v[152:153], v12 offset:21760\n"
"ds_read_b64_tr_b16 v[154:155], v12 offset:22272\n"
"ds_read_b64_tr_b16 v[156:157], v12 offset:21824\n"
"ds_read_b64_tr_b16 v[158:159], v12 offset:22336\n"
"ds_read_b64_tr_b16 v[160:161], v12 offset:23936\n"
"ds_read_b64_tr_b16 v[162:163], v12 offset:24448\n"
"ds_read_b64_tr_b16 v[164:165], v12 offset:24000\n"
"ds_read_b64_tr_b16 v[166:167], v12 offset:24512\n"
"s_add_u32 m0, s50, 0x5500\n"
"s_waitcnt lgkmcnt(0)\n"
"buffer_load_dwordx4 v8, s[32:35], s49 offen lds\n"
"buffer_load_dwordx4 v9, s[32:35], s49 offen offset:1088 lds\n"
"buffer_load_dwordx4 v10, s[32:35], s49 offen offset:2176 lds\n"
"buffer_load_dwordx4 v11, s[32:35], s49 offen offset:3264 lds\n"
"s_add_u32 s49, s49, 0x9000\n"
"s_waitcnt vmcnt(28)\n"
"ds_read_b64_tr_b16 v[168:169], v12 offset:26112\n"
"ds_read_b64_tr_b16 v[170:171], v12 offset:26624\n"
"ds_read_b64_tr_b16 v[172:173], v12 offset:26176\n"
"ds_read_b64_tr_b16 v[174:175], v12 offset:26688\n"
"ds_read_b64_tr_b16 v[176:177], v12 offset:28288\n"
"ds_read_b64_tr_b16 v[178:179], v12 offset:28800\n"
"ds_read_b64_tr_b16 v[180:181], v12 offset:28352\n"
"ds_read_b64_tr_b16 v[182:183], v12 offset:28864\n"
"s_add_u32 m0, s50, 0x6600\n"
"s_waitcnt lgkmcnt(0)\n"
"buffer_load_dwordx4 v8, s[32:35], s49 offen lds\n"
"buffer_load_dwordx4 v9, s[32:35], s49 offen offset:1088 lds\n"
"buffer_load_dwordx4 v10, s[32:35], s49 offen offset:2176 lds\n"
"buffer_load_dwordx4 v11, s[32:35], s49 offen offset:3264 lds\n"
"s_add_u32 s49, s49, 0x9000\n"
"s_waitcnt vmcnt(28)\n"
"ds_read_b64_tr_b16 v[184:185], v12 offset:30464\n"
"ds_read_b64_tr_b16 v[186:187], v12 offset:30976\n"
"ds_read_b64_tr_b16 v[188:189], v12 offset:30528\n"
"ds_read_b64_tr_b16 v[190:191], v12 offset:31040\n"
"ds_read_b64_tr_b16 v[192:193], v12 offset:32640\n"
"ds_read_b64_tr_b16 v[194:195], v12 offset:33152\n"
"ds_read_b64_tr_b16 v[196:197], v12 offset:32704\n"
"ds_read_b64_tr_b16 v[198:199], v12 offset:33216\n"
"s_add_u32 m0, s50, 0x7700\n"
"s_waitcnt lgkmcnt(0)\n"
"buffer_load_dwordx4 v8, s[32:35], s49 offen lds\n"
"buffer_load_dwordx4 v9, s[32:35], s49 offen offset:1088 lds\n"
"buffer_load_dwordx4 v10, s[32:35], s49 offen offset:2176 lds\n"
"buffer_load_dwordx4 v11, s[32:35], s49 offen offset:3264 lds\n"
"s_add_u32 s49, s49, 0x9000\n"
"s_waitcnt vmcnt(28)\n"
"ds_read_b64_tr_b16 v[200:201], v12 offset:0\n"
"ds_read_b64_tr_b16 v[202:203], v12 offset:512\n"
"ds_read_b64_tr_b16 v[204:205], v12 offset:64\n"
"ds_read_b64_tr_b16 v[206:207], v12 offset:576\n"
"ds_read_b64_tr_b16 v[208:209], v12 offset:2176\n"
"ds_read_b64_tr_b16 v[210:211], v12 offset:2688\n"
"ds_read_b64_tr_b16 v[212:213], v12 offset:2240\n"
"ds_read_b64_tr_b16 v[214:215], v12 offset:2752\n"
"s_add_u32 m0, s50, 0x0\n"
"s_waitcnt lgkmcnt(0)\n"
"buffer_load_dwordx4 v8, s[32:35], s49 offen lds\n"
"buffer_load_dwordx4 v9, s[32:35], s49 offen offset:1088 lds\n"
"buffer_load_dwordx4 v10, s[32:35], s49 offen offset:2176 lds\n"
"buffer_load_dwordx4 v11, s[32:35], s49 offen offset:3264 lds\n"
"s_add_u32 s49, s49, 0x9000\n"
"s_waitcnt vmcnt(28)\n"
"ds_read_b64_tr_b16 v[216:217], v12 offset:4352\n"
"ds_read_b64_tr_b16 v[218:219], v12 offset:4864\n"
"ds_read_b64_tr_b16 v[220:221], v12 offset:4416\n"
"ds_read_b64_tr_b16 v[222:223], v12 offset:4928\n"
"ds_read_b64_tr_b16 v[224:225], v12 offset:6528\n"
"ds_read_b64_tr_b16 v[226:227], v12 offset:7040\n"
"ds_read_b64_tr_b16 v[228:229], v12 offset:6592\n"
"ds_read_b64_tr_b16 v[230:231], v12 offset:7104\n"
"s_add_u32 m0, s50, 0x1100\n"
"s_waitcnt lgkmcnt(0)\n"
"buffer_load_dwordx4 v8, s[32:35], s49 offen lds\n"
"buffer_load_dwordx4 v9, s[32:35], s49 offen offset:1088 lds\n"
"buffer_load_dwordx4 v10, s[32:35], s49 offen offset:2176 lds\n"
"buffer_load_dwordx4 v11, s[32:35], s49 offen offset:3264 lds\n"
"s_add_u32 s49, s49, 0x9000\n"
"s_setprio 2\n"
".LK1_bQ:\n"
"s_barrier\n"
"ds_read_b128 v[48:51], v13 offset:0\n"
"ds_read_b128 v[52:55], v13 offset:32\n"
"ds_read_b128 v[56:59], v13 offset:64\n"
"ds_read_b128 v[60:63], v13 offset:96\n"
"s_waitcnt lgkmcnt(3)\n"
"v_mfma_f32_32x32x16_bf16 v[16:31], v[72:75], v[48:51], 0\n"
"v_mfma_f32_32x32x16_bf16 v[32:47], v[76:79], v[48:51], 0\n"
"s_waitcnt lgkmcnt(2)\n"
"v_mfma_f32_32x32x16_bf16 v[16:31], v[80:83], v[52:55], v[16:31]\n"
"s_waitcnt vmcnt(28)\n"
"ds_read_b64_tr_b16 v[232:233], v12 offset:8704\n"
"ds_read_b64_tr_b16 v[234:235], v12 offset:9216\n"
"v_mfma_f32_32x32x16_bf16 v[32:47], v[84:87], v[52:55], v[32:47]\n"
"ds_read_b64_tr_b16 v[236:237], v12 offset:8768\n"
"ds_read_b64_tr_b16 v[238:239], v12 offset:9280\n"
"s_waitcnt lgkmcnt(5)\n"
"v_mfma_f32_32x32x16_bf16 v[16:31], v[88:91], v[56:59], v[16:31]\n"
"v_mfma_f32_32x32x16_bf16 v[32:47], v[92:95], v[56:59], v[32:47]\n"
"s_barrier\n"
"ds_read_b128 v[64:67], v13 offset:128\n"
"ds_read_b128 v[68:71], v13 offset:160\n"
"ds_read_b128 v[48:51], v13 offset:192\n"
"ds_read_b128 v[52:55], v13 offset:224\n"
"s_waitcnt lgkmcnt(8)\n"
"v_mfma_f32_32x32x16_bf16 v[16:31], v[96:99], v[60:63], v[16:31]\n"
"ds_read_b64_tr_b16 v[240:241], v12 offset:10880\n"
"ds_read_b64_tr_b16 v[242:243], v12 offset:11392\n"
"v_mfma_f32_32x32x16_bf16 v[32:47], v[100:103], v[60:63], v[32:47]\n"
"ds_read_b64_tr_b16 v[244:245], v12 offset:10944\n"
"ds_read_b64_tr_b16 v[246:247], v12 offset:11456\n"
"s_waitcnt lgkmcnt(7)\n"
"v_mfma_f32_32x32x16_bf16 v[16:31], v[104:107], v[64:67], v[16:31]\n"
"ds_read_b128 v[56:59], v13 offset:256\n"
"ds_read_b128 v[60:63], v13 offset:288\n"
"v_mfma_f32_32x32x16_bf16 v[32:47], v[108:111], v[64:67], v[32:47]\n"
"s_waitcnt lgkmcnt(8)\n"
"v_mfma_f32_32x32x16_bf16 v[16:31], v[112:115], v[68:71], v[16:31]\n"
"s_waitcnt vmcnt(24)\n"
"ds_read_b64_tr_b16 v[72:73], v12 offset:13056\n"
"ds_read_b64_tr_b16 v[74:75], v12 offset:13568\n"
"v_mfma_f32_32x32x16_bf16 v[32:47], v[116:119], v[68:71], v[32:47]\n"
"ds_read_b64_tr_b16 v[76:77], v12 offset:13120\n"
"ds_read_b64_tr_b16 v[78:79], v12 offset:13632\n"
"s_waitcnt lgkmcnt(11)\n"
"v_mfma_f32_32x32x16_bf16 v[16:31], v[120:123], v[48:51], v[16:31]\n"
"ds_read_b128 v[64:67], v13 offset:320\n"
"ds_read_b128 v[68:71], v13 offset:352\n"
"v_mfma_f32_32x32x16_bf16 v[32:47], v[124:127], v[48:51], v[32:47]\n"
"s_waitcnt lgkmcnt(12)\n"
"v_mfma_f32_32x32x16_bf16 v[16:31], v[128:131], v[52:55], v[16:31]\n"
"ds_read_b64_tr_b16 v[80:81], v12 offset:15232\n"
"ds_read_b64_tr_b16 v[82:83], v12 offset:15744\n"
"v_mfma_f32_32x32x16_bf16 v[32:47], v[132:135], v[52:55], v[32:47]\n"
"ds_read_b64_tr_b16 v[84:85], v12 offset:15296\n"
"ds_read_b64_tr_b16 v[86:87], v12 offset:15808\n"
"s_waitcnt lgkmcnt(11)\n"
"v_mfma_f32_32x32x16_bf16 v[16:31], v[136:139], v[56:59], v[16:31]\n"
"v_mfma_f32_32x32x16_bf16 v[32:47], v[140:143], v[56:59], v[32:47]\n"
"s_waitcnt lgkmcnt(10)\n"
"v_mfma_f32_32x32x16_bf16 v[16:31], v[144:147], v[60:63], v[16:31]\n"
"s_waitcnt vmcnt(20)\n"
"ds_read_b64_tr_b16 v[88:89], v12 offset:17408\n"
"ds_read_b64_tr_b16 v[90:91], v12 offset:17920\n"
"v_mfma_f32_32x32x16_bf16 v[32:47], v[148:151], v[60:63], v[32:47]\n"
"ds_read_b64_tr_b16 v[92:93], v12 offset:17472\n"
"ds_read_b64_tr_b16 v[94:95], v12 offset:17984\n"
"s_waitcnt lgkmcnt(9)\n"
"v_mfma_f32_32x32x16_bf16 v[16:31], v[152:155], v[64:67], v[16:31]\n"
"s_barrier\n"
"ds_read_b128 v[48:51], v13 offset:384\n"
"ds_read_b128 v[52:55], v13 offset:416\n"
"ds_read_b128 v[56:59], v13 offset:448\n"
"ds_read_b128 v[60:63], v13 offset:480\n"
"v_mfma_f32_32x32x16_bf16 v[32:47], v[156:159], v[64:67], v[32:47]\n"
"s_waitcnt lgkmcnt(12)\n"
"v_mfma_f32_32x32x16_bf16 v[16:31], v[160:163], v[68:71], v[16:31]\n"
"ds_read_b64_tr_b16 v[96:97], v12 offset:19584\n"
"ds_read_b64_tr_b16 v[98:99], v12 offset:20096\n"
"v_mfma_f32_32x32x16_bf16 v[32:47], v[164:167], v[68:71], v[32:47]\n"
"ds_read_b64_tr_b16 v[100:101], v12 offset:19648\n"
"ds_read_b64_tr_b16 v[102:103], v12 offset:20160\n"
"s_waitcnt lgkmcnt(7)\n"
"v_mfma_f32_32x32x16_bf16 v[16:31], v[168:171], v[48:51], v[16:31]\n"
"ds_read_b128 v[64:67], v13 offset:512\n"
"ds_read_b128 v[68:71], v13 offset:544\n"
"v_mfma_f32_32x32x16_bf16 v[32:47], v[172:175], v[48:51], v[32:47]\n"
"s_waitcnt lgkmcnt(8)\n"
"v_mfma_f32_32x32x16_bf16 v[16:31], v[176:179], v[52:55], v[16:31]\n"
"s_waitcnt vmcnt(16)\n"
"ds_read_b64_tr_b16 v[104:105], v12 offset:21760\n"
"ds_read_b64_tr_b16 v[106:107], v12 offset:22272\n"
"v_mfma_f32_32x32x16_bf16 v[32:47], v[180:183], v[52:55], v[32:47]\n"
"ds_read_b64_tr_b16 v[108:109], v12 offset:21824\n"
"ds_read_b64_tr_b16 v[110:111], v12 offset:22336\n"
"s_waitcnt lgkmcnt(11)\n"
"v_mfma_f32_32x32x16_bf16 v[16:31], v[184:187], v[56:59], v[16:31]\n"
"ds_read_b128 v[48:51], v13 offset:576\n"
"ds_read_b128 v[52:55], v13 offset:608\n"
"v_mfma_f32_32x32x16_bf16 v[32:47], v[188:191], v[56:59], v[32:47]\n"
"s_waitcnt lgkmcnt(12)\n"
"v_mfma_f32_32x32x16_bf16 v[16:31], v[192:195], v[60:63], v[16:31]\n"
"ds_read_b64_tr_b16 v[112:113], v12 offset:23936\n"
"ds_read_b64_tr_b16 v[114:115], v12 offset:24448\n"
"v_mfma_f32_32x32x16_bf16 v[32:47], v[196:199], v[60:63], v[32:47]\n"
"ds_read_b64_tr_b16 v[116:117], v12 offset:24000\n"
"ds_read_b64_tr_b16 v[118:119], v12 offset:24512\n"
"s_waitcnt lgkmcnt(11)\n"
"v_mfma_f32_32x32x16_bf16 v[16:31], v[200:203], v[64:67], v[16:31]\n"
"v_mfma_f32_32x32x16_bf16 v[32:47], v[204:207], v[64:67], v[32:47]\n"
"s_waitcnt lgkmcnt(10)\n"
"v_mfma_f32_32x32x16_bf16 v[16:31], v[208:211], v[68:71], v[16:31]\n"
"s_waitcnt vmcnt(12)\n"
"ds_read_b64_tr_b16 v[120:121], v12 offset:26112\n"
"ds_read_b64_tr_b16 v[122:123], v12 offset:26624\n"
"v_mfma_f32_32x32x16_bf16 v[32:47], v[212:215], v[68:71], v[32:47]\n"
"s_barrier\n"
"ds_read_b128 v[56:59], v13 offset:640\n"
"ds_read_b128 v[60:63], v13 offset:672\n"
"ds_read_b64_tr_b16 v[124:125], v12 offset:26176\n"
"ds_read_b64_tr_b16 v[126:127], v12 offset:26688\n"
"s_waitcnt lgkmcnt(11)\n"
"v_mfma_f32_32x32x16_bf16 v[16:31], v[216:219], v[48:51], v[16:31]\n"
"ds_read_b128 v[64:67], v13 offset:704\n"
"ds_read_b128 v[68:71], v13 offset:736\n"
"v_mfma_f32_32x32x16_bf16 v[32:47], v[220:223], v[48:51], v[32:47]\n"
"s_waitcnt lgkmcnt(12)\n"
"v_mfma_f32_32x32x16_bf16 v[16:31], v[224:227], v[52:55], v[16:31]\n"
"ds_read_b64_tr_b16 v[128:129], v12 offset:28288\n"
"ds_read_b64_tr_b16 v[130:131], v12 offset:28800\n"
"v_mfma_f32_32x32x16_bf16 v[32:47], v[228:231], v[52:55], v[32:47]\n"
"ds_read_b64_tr_b16 v[132:133], v12 offset:28352\n"
"ds_read_b64_tr_b16 v[134:135], v12 offset:28864\n"
"s_waitcnt lgkmcnt(9)\n"
"v_mfma_f32_32x32x16_bf16 v[16:31], v[232:235], v[56:59], v[16:31]\n"
"ds_read_b128 v[48:51], v13 offset:768\n"
"ds_read_b128 v[52:55], v13 offset:800\n"
"v_mfma_f32_32x32x16_bf16 v[32:47], v[236:239], v[56:59], v[32:47]\n"
"s_waitcnt lgkmcnt(10)\n"
"v_mfma_f32_32x32x16_bf16 v[16:31], v[240:243], v[60:63], v[16:31]\n"
"s_waitcnt vmcnt(8)\n"
"ds_read_b64_tr_b16 v[136:137], v12 offset:30464\n"
"ds_read_b64_tr_b16 v[138:139], v12 offset:30976\n"
"v_mfma_f32_32x32x16_bf16 v[32:47], v[244:247], v[60:63], v[32:47]\n"
"ds_read_b64_tr_b16 v[140:141], v12 offset:30528\n"
"ds_read_b64_tr_b16 v[142:143], v12 offset:31040\n"
"s_waitcnt lgkmcnt(11)\n"
"v_mfma_f32_32x32x16_bf16 v[16:31], v[72:75], v[64:67], v[16:31]\n"
"ds_read_b128 v[56:59], v13 offset:832\n"
"ds_read_b128 v[60:63], v13 offset:864\n"
"v_mfma_f32_32x32x16_bf16 v[32:47], v[76:79], v[64:67], v[32:47]\n"
"s_waitcnt lgkmcnt(12)\n"
"v_mfma_f32_32x32x16_bf16 v[16:31], v[80:83], v[68:71], v[16:31]\n"
"ds_read_b64_tr_b16 v[144:145], v12 offset:32640\n"
"ds_read_b64_tr_b16 v[146:147], v12 offset:33152\n"
"v_mfma_f32_32x32x16_bf16 v[32:47], v[84:87], v[68:71], v[32:47]\n"
"ds_read_b64_tr_b16 v[148:149], v12 offset:32704\n"
"ds_read_b64_tr_b16 v[150:151], v12 offset:33216\n"
"s_waitcnt lgkmcnt(11)\n"
"v_mfma_f32_32x32x16_bf16 v[16:31], v[88:91], v[48:51], v[16:31]\n"
"s_barrier\n"
"ds_read_b128 v[64:67], v13 offset:896\n"
"ds_read_b128 v[68:71], v13 offset:928\n"
"v_mfma_f32_32x32x16_bf16 v[32:47], v[92:95], v[48:51], v[32:47]\n"
"s_waitcnt lgkmcnt(12)\n"
"v_mfma_f32_32x32x16_bf16 v[16:31], v[96:99], v[52:55], v[16:31]\n"
"s_waitcnt vmcnt(4)\n"
"ds_read_b64_tr_b16 v[152:153], v12 offset:0\n"
"ds_read_b64_tr_b16 v[154:155], v12 offset:512\n"
"v_mfma_f32_32x32x16_bf16 v[32:47], v[100:103], v[52:55], v[32:47]\n"
"ds_read_b64_tr_b16 v[156:157], v12 offset:64\n"
"ds_read_b64_tr_b16 v[158:159], v12 offset:576\n"
"s_waitcnt lgkmcnt(11)\n"
"v_mfma_f32_32x32x16_bf16 v[16:31], v[104:107], v[56:59], v[16:31]\n"
"ds_read_b128 v[48:51], v13 offset:960\n"
"ds_read_b128 v[52:55], v13 offset:992\n"
"v_mfma_f32_32x32x16_bf16 v[32:47], v[108:111], v[56:59], v[32:47]\n"
"s_waitcnt lgkmcnt(12)\n"
"v_mfma_f32_32x32x16_bf16 v[16:31], v[112:115], v[60:63], v[16:31]\n"
"ds_read_b64_tr_b16 v[160:161], v12 offset:2176\n"
"ds_read_b64_tr_b16 v[162:163], v12 offset:2688\n"
"v_mfma_f32_32x32x16_bf16 v[32:47], v[116:119], v[60:63], v[32:47]\n"
"ds_read_b64_tr_b16 v[164:165], v12 offset:2240\n"
"ds_read_b64_tr_b16 v[166:167], v12 offset:2752\n"
"s_waitcnt lgkmcnt(11)\n"
"v_mfma_f32_32x32x16_bf16 v[16:31], v[120:123], v[64:67], v[16:31]\n"
"ds_read_b128 v[56:59], v13 offset:1024\n"
"ds_read_b128 v[60:63], v13 offset:1056\n"
"v_mfma_f32_32x32x16_bf16 v[32:47], v[124:127], v[64:67], v[32:47]\n"
"s_waitcnt lgkmcnt(12)\n"
"v_mfma_f32_32x32x16_bf16 v[16:31], v[128:131], v[68:71], v[16:31]\n"
"s_waitcnt vmcnt(0)\n"
"ds_read_b64_tr_b16 v[168:169], v12 offset:4352\n"
"ds_read_b64_tr_b16 v[170:171], v12 offset:4864\n"
"v_mfma_f32_32x32x16_bf16 v[32:47], v[132:135], v[68:71], v[32:47]\n"
"ds_read_b64_tr_b16 v[172:173], v12 offset:4416\n"
"ds_read_b64_tr_b16 v[174:175], v12 offset:4928\n"
"s_waitcnt lgkmcnt(11)\n"
"v_mfma_f32_32x32x16_bf16 v[16:31], v[136:139], v[48:51], v[16:31]\n"
"ds_read_b128 v[64:67], v13 offset:1088\n"
"ds_read_b128 v[68:71], v13 offset:1120\n"
"v_mfma_f32_32x32x16_bf16 v[32:47], v[140:143], v[48:51], v[32:47]\n"
"s_waitcnt lgkmcnt(12)\n"
"v_mfma_f32_32x32x16_bf16 v[16:31], v[144:147], v[52:55], v[16:31]\n"
"ds_read_b64_tr_b16 v[176:177], v12 offset:6528\n"
"ds_read_b64_tr_b16 v[178:179], v12 offset:7040\n"
"v_mfma_f32_32x32x16_bf16 v[32:47], v[148:151], v[52:55], v[32:47]\n"
"ds_read_b64_tr_b16 v[180:181], v12 offset:6592\n"
"ds_read_b64_tr_b16 v[182:183], v12 offset:7104\n"
"s_waitcnt lgkmcnt(11)\n"
"v_mfma_f32_32x32x16_bf16 v[16:31], v[152:155], v[56:59], v[16:31]\n"
"s_waitcnt lgkmcnt(10)\n"
"v_mfma_f32_32x32x16_bf16 v[16:31], v[160:163], v[60:63], v[16:31]\n"
"s_waitcnt lgkmcnt(0)\n"
"v_mfma_f32_32x32x16_bf16 v[16:31], v[168:171], v[64:67], v[16:31]\n"
"v_mfma_f32_32x32x16_bf16 v[16:31], v[176:179], v[68:71], v[16:31]\n"
"v_mfma_f32_32x32x16_bf16 v[32:47], v[156:159], v[56:59], v[32:47]\n"
"s_nop 4\n"
"v_mfma_f32_32x32x16_bf16 v[32:47], v[164:167], v[60:63], v[32:47]\n"
"s_nop 4\n"
"v_cvt_pk_bf16_f32 v16, v16, v17\n"
"v_cvt_pk_bf16_f32 v17, v18, v19\n"
"v_cvt_pk_bf16_f32 v18, v20, v21\n"
"v_cvt_pk_bf16_f32 v19, v22, v23\n"
"ds_write_b128 v14, v[16:19] offset:8704\n"
"v_mfma_f32_32x32x16_bf16 v[32:47], v[172:175], v[64:67], v[32:47]\n"
"v_cvt_pk_bf16_f32 v24, v24, v25\n"
"v_cvt_pk_bf16_f32 v25, v26, v27\n"
"v_cvt_pk_bf16_f32 v26, v28, v29\n"
"v_cvt_pk_bf16_f32 v27, v30, v31\n"
"ds_write_b128 v14, v[24:27] offset:8736\n"
"v_mfma_f32_32x32x16_bf16 v[32:47], v[180:183], v[68:71], v[32:47]\n"
"ds_read_b128 v[104:107], v15 offset:8704\n"
"ds_read_b128 v[108:111], v15 offset:9984\n"
"s_nop 7\n"
"s_nop 3\n"
"v_cvt_pk_bf16_f32 v32, v32, v33\n"
"v_cvt_pk_bf16_f32 v33, v34, v35\n"
"v_cvt_pk_bf16_f32 v34, v36, v37\n"
"v_cvt_pk_bf16_f32 v35, v38, v39\n"
"ds_write_b128 v14, v[32:35] offset:11264\n"
"v_cvt_pk_bf16_f32 v40, v40, v41\n"
"v_cvt_pk_bf16_f32 v41, v42, v43\n"
"v_cvt_pk_bf16_f32 v42, v44, v45\n"
"v_cvt_pk_bf16_f32 v43, v46, v47\n"
"ds_write_b128 v14, v[40:43] offset:11296\n"
"ds_read_b128 v[112:115], v15 offset:11264\n"
"ds_read_b128 v[116:119], v15 offset:12544\n"
"s_waitcnt lgkmcnt(5)\n"
"buffer_store_dwordx4 v[104:107], v5, s[40:43], s52 offen\n"
"s_waitcnt lgkmcnt(4)\n"
"buffer_store_dwordx4 v[108:111], v5, s[40:43], s53 offen\n"
"s_waitcnt lgkmcnt(1)\n"
"buffer_store_dwordx4 v[112:115], v5, s[40:43], s52 offen offset:64\n"
"s_waitcnt lgkmcnt(0)\n"
"buffer_store_dwordx4 v[116:119], v5, s[40:43], s53 offen offset:64\n"
".LK1_eQ:\n"
"s_endpgm\n"
".pushsection .rodata\n"
".p2align 7\n"
".LK1_tab:\n"
".long 0,0,24576,24672,16,16,25744,25840,32,32,26912,27008,48,48,28080,28176,64,64,29248,29344,80,80,30416,30512,96,96,31584,31680,112,112,32752,32848\n"
".long 128,512,24580,24674,144,528,25748,25842,160,544,26916,27010,176,560,28084,28178,192,576,29252,29346,208,592,30420,30514,224,608,31588,31682,240,624,32756,32850\n"
".long 256,1024,24584,24676,272,1040,25752,25844,288,1056,26920,27012,304,1072,28088,28180,320,1088,29256,29348,336,1104,30424,30516,352,1120,31592,31684,368,1136,32760,32852\n"
".long 384,1536,24588,24678,400,1552,25756,25846,416,1568,26924,27014,432,1584,28092,28182,448,1600,29260,29350,464,1616,30428,30518,480,1632,31596,31686,496,1648,32764,32854\n"
".long 512,2048,24592,24680,528,2064,25760,25848,544,2080,26928,27016,560,2096,28096,28184,576,2112,29264,29352,592,2128,30432,30520,608,2144,31600,31688,624,2160,32768,32856\n"
".long 640,2560,24596,24682,656,2576,25764,25850,672,2592,26932,27018,688,2608,28100,28186,704,2624,29268,29354,720,2640,30436,30522,736,2656,31604,31690,752,2672,32772,32858\n"
".long 768,2560,24596,24682,784,2576,25764,25850,800,2592,26932,27018,816,2608,28100,28186,832,2624,29268,29354,848,2640,30436,30522,864,2656,31604,31690,880,2672,32772,32858\n"
".long 896,2560,24596,24682,912,2576,25764,25850,928,2592,26932,27018,944,2608,28100,28186,960,2624,29268,29354,976,2640,30436,30522,992,2656,31604,31690,1008,2672,32772,32858\n"
".long 24576,33920,36864,37888,25744,33920,38016,39040,26912,33920,39168,40192,28080,33920,40320,41344,29248,33920,41472,42496,30416,33920,42624,43648,31584,33920,43776,44800,32752,33920,44928,45952\n"
".long 33920,24576,36928,33920,33920,25744,38080,33920,33920,26912,39232,33920,33920,28080,40384,33920,33920,29248,41536,33920,33920,30416,42688,33920,33920,31584,43840,33920,33920,32752,44992,33920\n"
".long 24592,33936,36872,37920,25760,33936,38024,39072,26928,33936,39176,40224,28096,33936,40328,41376,29264,33936,41480,42528,30432,33936,42632,43680,31600,33936,43784,44832,32768,33936,44936,45984\n"
".long 33936,24592,36936,33920,33936,25760,38088,33920,33936,26928,39240,33920,33936,28096,40392,33920,33936,29264,41544,33920,33936,30432,42696,33920,33936,31600,43848,33920,33936,32768,45000,33920\n"
".long 24608,33952,36880,37896,25776,33952,38032,39048,26944,33952,39184,40200,28112,33952,40336,41352,29280,33952,41488,42504,30448,33952,42640,43656,31616,33952,43792,44808,32784,33952,44944,45960\n"
".long 33952,24608,36944,33920,33952,25776,38096,33920,33952,26944,39248,33920,33952,28112,40400,33920,33952,29280,41552,33920,33952,30448,42704,33920,33952,31616,43856,33920,33952,32784,45008,33920\n"
".long 24624,33968,36888,37928,25792,33968,38040,39080,26960,33968,39192,40232,28128,33968,40344,41384,29296,33968,41496,42536,30464,33968,42648,43688,31632,33968,43800,44840,32800,33968,44952,45992\n"
".long 33968,24624,36952,33920,33968,25792,38104,33920,33968,26960,39256,33920,33968,28128,40408,33920,33968,29296,41560,33920,33968,30464,42712,33920,33968,31632,43864,33920,33968,32800,45016,33920\n"
".long 46080,48128,0,0,46080,48128,16,16,46080,48128,32,32,46080,48128,48,48,46080,48128,64,64,46080,48128,80,80,46080,48128,96,96,46080,48128,112,112\n"
".long 46208,48128,4096,2147483648,46208,48128,4112,2147483648,46208,48128,4128,2147483648,46208,48128,4144,2147483648,46208,48128,4160,2147483648,46208,48128,4176,2147483648,46208,48128,4192,2147483648,46208,48128,4208,2147483648\n"
".long 46096,48192,256,8,46096,48192,272,24,46096,48192,288,40,46096,48192,304,56,46096,48192,320,72,46096,48192,336,88,46096,48192,352,104,46096,48192,368,120\n"
".long 46224,48192,4352,2147483648,46224,48192,4368,2147483648,46224,48192,4384,2147483648,46224,48192,4400,2147483648,46224,48192,4416,2147483648,46224,48192,4432,2147483648,46224,48192,4448,2147483648,46224,48192,4464,2147483648\n"
".long 46112,48144,512,256,46112,48144,528,272,46112,48144,544,288,46112,48144,560,304,46112,48144,576,320,46112,48144,592,336,46112,48144,608,352,46112,48144,624,368\n"
".long 46240,48144,4608,2147483648,46240,48144,4624,2147483648,46240,48144,4640,2147483648,46240,48144,4656,2147483648,46240,48144,4672,2147483648,46240,48144,4688,2147483648,46240,48144,4704,2147483648,46240,48144,4720,2147483648\n"
".long 46128,48208,768,264,46128,48208,784,280,46128,48208,800,296,46128,48208,816,312,46128,48208,832,328,46128,48208,848,344,46128,48208,864,360,46128,48208,880,376\n"
".long 46256,48208,4864,2147483648,46256,48208,4880,2147483648,46256,48208,4896,2147483648,46256,48208,4912,2147483648,46256,48208,4928,2147483648,46256,48208,4944,2147483648,46256,48208,4960,2147483648,46256,48208,4976,2147483648\n"
".long 35104,35104,0,33920,35136,35136,128,33936,35168,35168,256,33952,35200,35200,384,33968,35232,35232,512,33984,35264,35264,640,34000,35296,35296,768,34016,35328,35328,896,34032\n"
".long 35104,35104,1024,34048,35136,35136,1152,34064,35168,35168,1280,34080,35200,35200,1408,34096,35232,35232,1536,34112,35264,35264,1664,34128,35296,35296,1792,34144,35328,35328,1920,34160\n"
".long 35104,35104,2147483648,34176,35136,35136,2147483648,34192,35168,35168,2147483648,33920,35200,35200,2147483648,33920,35232,35232,2147483648,33920,35264,35264,2147483648,33920,35296,35296,2147483648,33920,35328,35328,2147483648,33920\n"
".long 35104,35104,2147483648,33920,35136,35136,2147483648,33920,35168,35168,2147483648,33920,35200,35200,2147483648,33920,35232,35232,2147483648,33920,35264,35264,2147483648,33920,35296,35296,2147483648,33920,35328,35328,2147483648,33920\n"
".long 35120,35104,2147483648,33920,35152,35136,2147483648,33920,35184,35168,2147483648,33920,35216,35200,2147483648,33920,35248,35232,2147483648,33920,35280,35264,2147483648,33920,35312,35296,2147483648,33920,35344,35328,2147483648,33920\n"
".long 35120,35104,2147483648,33920,35152,35136,2147483648,33920,35184,35168,2147483648,33920,35216,35200,2147483648,33920,35248,35232,2147483648,33920,35280,35264,2147483648,33920,35312,35296,2147483648,33920,35344,35328,2147483648,33920\n"
".long 35120,35104,2147483648,33920,35152,35136,2147483648,33920,35184,35168,2147483648,33920,35216,35200,2147483648,33920,35248,35232,2147483648,33920,35280,35264,2147483648,33920,35312,35296,2147483648,33920,35344,35328,2147483648,33920\n"
".long 35120,35104,2147483648,33920,35152,35136,2147483648,33920,35184,35168,2147483648,33920,35216,35200,2147483648,33920,35248,35232,2147483648,33920,35280,35264,2147483648,33920,35312,35296,2147483648,33920,35344,35328,2147483648,33920\n"
".long 3072,128,24600,24684,3088,144,25768,25852,3104,160,26936,27020,3120,176,28104,28188,3136,192,29272,29356,3152,208,30440,30524,3168,224,31608,31692,3184,240,32776,32860\n"
".long 3200,640,24604,24686,3216,656,25772,25854,3232,672,26940,27022,3248,688,28108,28190,3264,704,29276,29358,3280,720,30444,30526,3296,736,31612,31694,3312,752,32780,32862\n"
".long 3328,1152,24608,24688,3344,1168,25776,25856,3360,1184,26944,27024,3376,1200,28112,28192,3392,1216,29280,29360,3408,1232,30448,30528,3424,1248,31616,31696,3440,1264,32784,32864\n"
".long 3456,1664,24612,24690,3472,1680,25780,25858,3488,1696,26948,27026,3504,1712,28116,28194,3520,1728,29284,29362,3536,1744,30452,30530,3552,1760,31620,31698,3568,1776,32788,32866\n"
".long 3584,2176,24616,24692,3600,2192,25784,25860,3616,2208,26952,27028,3632,2224,28120,28196,3648,2240,29288,29364,3664,2256,30456,30532,3680,2272,31624,31700,3696,2288,32792,32868\n"
".long 3712,2688,24620,24694,3728,2704,25788,25862,3744,2720,26956,27030,3760,2736,28124,28198,3776,2752,29292,29366,3792,2768,30460,30534,3808,2784,31628,31702,3824,2800,32796,32870\n"
".long 3840,2688,24620,24694,3856,2704,25788,25862,3872,2720,26956,27030,3888,2736,28124,28198,3904,2752,29292,29366,3920,2768,30460,30534,3936,2784,31628,31702,3952,2800,32796,32870\n"
".long 3968,2688,24620,24694,3984,2704,25788,25862,4000,2720,26956,27030,4016,2736,28124,28198,4032,2752,29292,29366,4048,2768,30460,30534,4064,2784,31628,31702,4080,2800,32796,32870\n"
".long 24576,33920,37120,37904,25744,33920,38272,39056,26912,33920,39424,40208,28080,33920,40576,41360,29248,33920,41728,42512,30416,33920,42880,43664,31584,33920,44032,44816,32752,33920,45184,45968\n"
".long 33920,24576,37184,33920,33920,25744,38336,33920,33920,26912,39488,33920,33920,28080,40640,33920,33920,29248,41792,33920,33920,30416,42944,33920,33920,31584,44096,33920,33920,32752,45248,33920\n"
".long 24592,33936,37128,37936,25760,33936,38280,39088,26928,33936,39432,40240,28096,33936,40584,41392,29264,33936,41736,42544,30432,33936,42888,43696,31600,33936,44040,44848,32768,33936,45192,46000\n"
".long 33936,24592,37192,33920,33936,25760,38344,33920,33936,26928,39496,33920,33936,28096,40648,33920,33936,29264,41800,33920,33936,30432,42952,33920,33936,31600,44104,33920,33936,32768,45256,33920\n"
".long 24608,33952,37136,37912,25776,33952,38288,39064,26944,33952,39440,40216,28112,33952,40592,41368,29280,33952,41744,42520,30448,33952,42896,43672,31616,33952,44048,44824,32784,33952,45200,45976\n"
".long 33952,24608,37200,33920,33952,25776,38352,33920,33952,26944,39504,33920,33952,28112,40656,33920,33952,29280,41808,33920,33952,30448,42960,33920,33952,31616,44112,33920,33952,32784,45264,33920\n"
".long 24624,33968,37144,37944,25792,33968,38296,39096,26960,33968,39448,40248,28128,33968,40600,41400,29296,33968,41752,42552,30464,33968,42904,43704,31632,33968,44056,44856,32800,33968,45208,46008\n"
".long 33968,24624,37208,33920,33968,25792,38360,33920,33968,26960,39512,33920,33968,28128,40664,33920,33968,29296,41816,33920,33968,30464,42968,33920,33968,31632,44120,33920,33968,32800,45272,33920\n"
".long 46592,48160,0,0,46592,48160,16,16,46592,48160,32,32,46592,48160,48,48,46592,48160,64,64,46592,48160,80,80,46592,48160,96,96,46592,48160,112,112\n"
".long 46720,48160,4096,2147483648,46720,48160,4112,2147483648,46720,48160,4128,2147483648,46720,48160,4144,2147483648,46720,48160,4160,2147483648,46720,48160,4176,2147483648,46720,48160,4192,2147483648,46720,48160,4208,2147483648\n"
".long 46608,48224,256,8,46608,48224,272,24,46608,48224,288,40,46608,48224,304,56,46608,48224,320,72,46608,48224,336,88,46608,48224,352,104,46608,48224,368,120\n"
".long 46736,48224,4352,2147483648,46736,48224,4368,2147483648,46736,48224,4384,2147483648,46736,48224,4400,2147483648,46736,48224,4416,2147483648,46736,48224,4432,2147483648,46736,48224,4448,2147483648,46736,48224,4464,2147483648\n"
".long 46624,48176,512,256,46624,48176,528,272,46624,48176,544,288,46624,48176,560,304,46624,48176,576,320,46624,48176,592,336,46624,48176,608,352,46624,48176,624,368\n"
".long 46752,48176,4608,2147483648,46752,48176,4624,2147483648,46752,48176,4640,2147483648,46752,48176,4656,2147483648,46752,48176,4672,2147483648,46752,48176,4688,2147483648,46752,48176,4704,2147483648,46752,48176,4720,2147483648\n"
".long 46640,48240,768,264,46640,48240,784,280,46640,48240,800,296,46640,48240,816,312,46640,48240,832,328,46640,48240,848,344,46640,48240,864,360,46640,48240,880,376\n"
".long 46768,48240,4864,2147483648,46768,48240,4880,2147483648,46768,48240,4896,2147483648,46768,48240,4912,2147483648,46768,48240,4928,2147483648,46768,48240,4944,2147483648,46768,48240,4960,2147483648,46768,48240,4976,2147483648\n"
".long 35108,35104,0,34208,35140,35136,128,34224,35172,35168,256,34240,35204,35200,384,34256,35236,35232,512,34272,35268,35264,640,34288,35300,35296,768,34304,35332,35328,896,34320\n"
".long 35108,35104,1024,34336,35140,35136,1152,34352,35172,35168,1280,34368,35204,35200,1408,34384,35236,35232,1536,34400,35268,35264,1664,34416,35300,35296,1792,34432,35332,35328,1920,34448\n"
".long 35108,35104,2147483648,34464,35140,35136,2147483648,34480,35172,35168,2147483648,34208,35204,35200,2147483648,34208,35236,35232,2147483648,34208,35268,35264,2147483648,34208,35300,35296,2147483648,34208,35332,35328,2147483648,34208\n"
".long 35108,35104,2147483648,34208,35140,35136,2147483648,34208,35172,35168,2147483648,34208,35204,35200,2147483648,34208,35236,35232,2147483648,34208,35268,35264,2147483648,34208,35300,35296,2147483648,34208,35332,35328,2147483648,34208\n"
".long 35124,35104,2147483648,34208,35156,35136,2147483648,34208,35188,35168,2147483648,34208,35220,35200,2147483648,34208,35252,35232,2147483648,34208,35284,35264,2147483648,34208,35316,35296,2147483648,34208,35348,35328,2147483648,34208\n"
".long 35124,35104,2147483648,34208,35156,35136,2147483648,34208,35188,35168,2147483648,34208,35220,35200,2147483648,34208,35252,35232,2147483648,34208,35284,35264,2147483648,34208,35316,35296,2147483648,34208,35348,35328,2147483648,34208\n"
".long 35124,35104,2147483648,34208,35156,35136,2147483648,34208,35188,35168,2147483648,34208,35220,35200,2147483648,34208,35252,35232,2147483648,34208,35284,35264,2147483648,34208,35316,35296,2147483648,34208,35348,35328,2147483648,34208\n"
".long 35124,35104,2147483648,34208,35156,35136,2147483648,34208,35188,35168,2147483648,34208,35220,35200,2147483648,34208,35252,35232,2147483648,34208,35284,35264,2147483648,34208,35316,35296,2147483648,34208,35348,35328,2147483648,34208\n"
".long 6144,256,24624,24696,6160,272,25792,25864,6176,288,26960,27032,6192,304,28128,28200,6208,320,29296,29368,6224,336,30464,30536,6240,352,31632,31704,6256,368,32800,32872\n"
".long 6272,768,24628,24698,6288,784,25796,25866,6304,800,26964,27034,6320,816,28132,28202,6336,832,29300,29370,6352,848,30468,30538,6368,864,31636,31706,6384,880,32804,32874\n"
".long 6400,1280,24632,24700,6416,1296,25800,25868,6432,1312,26968,27036,6448,1328,28136,28204,6464,1344,29304,29372,6480,1360,30472,30540,6496,1376,31640,31708,6512,1392,32808,32876\n"
".long 6528,1792,24636,24702,6544,1808,25804,25870,6560,1824,26972,27038,6576,1840,28140,28206,6592,1856,29308,29374,6608,1872,30476,30542,6624,1888,31644,31710,6640,1904,32812,32878\n"
".long 6656,2304,24640,24704,6672,2320,25808,25872,6688,2336,26976,27040,6704,2352,28144,28208,6720,2368,29312,29376,6736,2384,30480,30544,6752,2400,31648,31712,6768,2416,32816,32880\n"
".long 6784,2816,24644,24706,6800,2832,25812,25874,6816,2848,26980,27042,6832,2864,28148,28210,6848,2880,29316,29378,6864,2896,30484,30546,6880,2912,31652,31714,6896,2928,32820,32882\n"
".long 6912,2816,24644,24706,6928,2832,25812,25874,6944,2848,26980,27042,6960,2864,28148,28210,6976,2880,29316,29378,6992,2896,30484,30546,7008,2912,31652,31714,7024,2928,32820,32882\n"
".long 7040,2816,24644,24706,7056,2832,25812,25874,7072,2848,26980,27042,7088,2864,28148,28210,7104,2880,29316,29378,7120,2896,30484,30546,7136,2912,31652,31714,7152,2928,32820,32882\n"
".long 24576,33920,37376,37952,25744,33920,38528,39104,26912,33920,39680,40256,28080,33920,40832,41408,29248,33920,41984,42560,30416,33920,43136,43712,31584,33920,44288,44864,32752,33920,45440,46016\n"
".long 33920,24576,37440,33920,33920,25744,38592,33920,33920,26912,39744,33920,33920,28080,40896,33920,33920,29248,42048,33920,33920,30416,43200,33920,33920,31584,44352,33920,33920,32752,45504,33920\n"
".long 24592,33936,37384,37984,25760,33936,38536,39136,26928,33936,39688,40288,28096,33936,40840,41440,29264,33936,41992,42592,30432,33936,43144,43744,31600,33936,44296,44896,32768,33936,45448,46048\n"
".long 33936,24592,37448,33920,33936,25760,38600,33920,33936,26928,39752,33920,33936,28096,40904,33920,33936,29264,42056,33920,33936,30432,43208,33920,33936,31600,44360,33920,33936,32768,45512,33920\n"
".long 24608,33952,37392,37960,25776,33952,38544,39112,26944,33952,39696,40264,28112,33952,40848,41416,29280,33952,42000,42568,30448,33952,43152,43720,31616,33952,44304,44872,32784,33952,45456,46024\n"
".long 33952,24608,37456,33920,33952,25776,38608,33920,33952,26944,39760,33920,33952,28112,40912,33920,33952,29280,42064,33920,33952,30448,43216,33920,33952,31616,44368,33920,33952,32784,45520,33920\n"
".long 24624,33968,37400,37992,25792,33968,38552,39144,26960,33968,39704,40296,28128,33968,40856,41448,29296,33968,42008,42600,30464,33968,43160,43752,31632,33968,44312,44904,32800,33968,45464,46056\n"
".long 33968,24624,37464,33920,33968,25792,38616,33920,33968,26960,39768,33920,33968,28128,40920,33920,33968,29296,42072,33920,33968,30464,43224,33920,33968,31632,44376,33920,33968,32800,45528,33920\n"
".long 47104,48256,0,0,47104,48256,16,16,47104,48256,32,32,47104,48256,48,48,47104,48256,64,64,47104,48256,80,80,47104,48256,96,96,47104,48256,112,112\n"
".long 47232,48256,4096,2147483648,47232,48256,4112,2147483648,47232,48256,4128,2147483648,47232,48256,4144,2147483648,47232,48256,4160,2147483648,47232,48256,4176,2147483648,47232,48256,4192,2147483648,47232,48256,4208,2147483648\n"
".long 47120,48320,256,8,47120,48320,272,24,47120,48320,288,40,47120,48320,304,56,47120,48320,320,72,47120,48320,336,88,47120,48320,352,104,47120,48320,368,120\n"
".long 47248,48320,4352,2147483648,47248,48320,4368,2147483648,47248,48320,4384,2147483648,47248,48320,4400,2147483648,47248,48320,4416,2147483648,47248,48320,4432,2147483648,47248,48320,4448,2147483648,47248,48320,4464,2147483648\n"
".long 47136,48272,512,256,47136,48272,528,272,47136,48272,544,288,47136,48272,560,304,47136,48272,576,320,47136,48272,592,336,47136,48272,608,352,47136,48272,624,368\n"
".long 47264,48272,4608,2147483648,47264,48272,4624,2147483648,47264,48272,4640,2147483648,47264,48272,4656,2147483648,47264,48272,4672,2147483648,47264,48272,4688,2147483648,47264,48272,4704,2147483648,47264,48272,4720,2147483648\n"
".long 47152,48336,768,264,47152,48336,784,280,47152,48336,800,296,47152,48336,816,312,47152,48336,832,328,47152,48336,848,344,47152,48336,864,360,47152,48336,880,376\n"
".long 47280,48336,4864,2147483648,47280,48336,4880,2147483648,47280,48336,4896,2147483648,47280,48336,4912,2147483648,47280,48336,4928,2147483648,47280,48336,4944,2147483648,47280,48336,4960,2147483648,47280,48336,4976,2147483648\n"
".long 35112,35104,0,34496,35144,35136,128,34512,35176,35168,256,34528,35208,35200,384,34544,35240,35232,512,34560,35272,35264,640,34576,35304,35296,768,34592,35336,35328,896,34608\n"
".long 35112,35104,1024,34624,35144,35136,1152,34640,35176,35168,1280,34656,35208,35200,1408,34672,35240,35232,1536,34688,35272,35264,1664,34704,35304,35296,1792,34720,35336,35328,1920,34736\n"
".long 35112,35104,2147483648,34752,35144,35136,2147483648,34768,35176,35168,2147483648,34496,35208,35200,2147483648,34496,35240,35232,2147483648,34496,35272,35264,2147483648,34496,35304,35296,2147483648,34496,35336,35328,2147483648,34496\n"
".long 35112,35104,2147483648,34496,35144,35136,2147483648,34496,35176,35168,2147483648,34496,35208,35200,2147483648,34496,35240,35232,2147483648,34496,35272,35264,2147483648,34496,35304,35296,2147483648,34496,35336,35328,2147483648,34496\n"
".long 35128,35104,2147483648,34496,35160,35136,2147483648,34496,35192,35168,2147483648,34496,35224,35200,2147483648,34496,35256,35232,2147483648,34496,35288,35264,2147483648,34496,35320,35296,2147483648,34496,35352,35328,2147483648,34496\n"
".long 35128,35104,2147483648,34496,35160,35136,2147483648,34496,35192,35168,2147483648,34496,35224,35200,2147483648,34496,35256,35232,2147483648,34496,35288,35264,2147483648,34496,35320,35296,2147483648,34496,35352,35328,2147483648,34496\n"
".long 35128,35104,2147483648,34496,35160,35136,2147483648,34496,35192,35168,2147483648,34496,35224,35200,2147483648,34496,35256,35232,2147483648,34496,35288,35264,2147483648,34496,35320,35296,2147483648,34496,35352,35328,2147483648,34496\n"
".long 35128,35104,2147483648,34496,35160,35136,2147483648,34496,35192,35168,2147483648,34496,35224,35200,2147483648,34496,35256,35232,2147483648,34496,35288,35264,2147483648,34496,35320,35296,2147483648,34496,35352,35328,2147483648,34496\n"
".long 9216,384,24648,24708,9232,400,25816,25876,9248,416,26984,27044,9264,432,28152,28212,9280,448,29320,29380,9296,464,30488,30548,9312,480,31656,31716,9328,496,32824,32884\n"
".long 9344,896,24652,24710,9360,912,25820,25878,9376,928,26988,27046,9392,944,28156,28214,9408,960,29324,29382,9424,976,30492,30550,9440,992,31660,31718,9456,1008,32828,32886\n"
".long 9472,1408,24656,24712,9488,1424,25824,25880,9504,1440,26992,27048,9520,1456,28160,28216,9536,1472,29328,29384,9552,1488,30496,30552,9568,1504,31664,31720,9584,1520,32832,32888\n"
".long 9600,1920,24660,24714,9616,1936,25828,25882,9632,1952,26996,27050,9648,1968,28164,28218,9664,1984,29332,29386,9680,2000,30500,30554,9696,2016,31668,31722,9712,2032,32836,32890\n"
".long 9728,2432,24664,24716,9744,2448,25832,25884,9760,2464,27000,27052,9776,2480,28168,28220,9792,2496,29336,29388,9808,2512,30504,30556,9824,2528,31672,31724,9840,2544,32840,32892\n"
".long 9856,2944,24668,24718,9872,2960,25836,25886,9888,2976,27004,27054,9904,2992,28172,28222,9920,3008,29340,29390,9936,3024,30508,30558,9952,3040,31676,31726,9968,3056,32844,32894\n"
".long 9984,2944,24668,24718,10000,2960,25836,25886,10016,2976,27004,27054,10032,2992,28172,28222,10048,3008,29340,29390,10064,3024,30508,30558,10080,3040,31676,31726,10096,3056,32844,32894\n"
".long 10112,2944,24668,24718,10128,2960,25836,25886,10144,2976,27004,27054,10160,2992,28172,28222,10176,3008,29340,29390,10192,3024,30508,30558,10208,3040,31676,31726,10224,3056,32844,32894\n"
".long 24576,33920,37632,37968,25744,33920,38784,39120,26912,33920,39936,40272,28080,33920,41088,41424,29248,33920,42240,42576,30416,33920,43392,43728,31584,33920,44544,44880,32752,33920,45696,46032\n"
".long 33920,24576,37696,33920,33920,25744,38848,33920,33920,26912,40000,33920,33920,28080,41152,33920,33920,29248,42304,33920,33920,30416,43456,33920,33920,31584,44608,33920,33920,32752,45760,33920\n"
".long 24592,33936,37640,38000,25760,33936,38792,39152,26928,33936,39944,40304,28096,33936,41096,41456,29264,33936,42248,42608,30432,33936,43400,43760,31600,33936,44552,44912,32768,33936,45704,46064\n"
".long 33936,24592,37704,33920,33936,25760,38856,33920,33936,26928,40008,33920,33936,28096,41160,33920,33936,29264,42312,33920,33936,30432,43464,33920,33936,31600,44616,33920,33936,32768,45768,33920\n"
".long 24608,33952,37648,37976,25776,33952,38800,39128,26944,33952,39952,40280,28112,33952,41104,41432,29280,33952,42256,42584,30448,33952,43408,43736,31616,33952,44560,44888,32784,33952,45712,46040\n"
".long 33952,24608,37712,33920,33952,25776,38864,33920,33952,26944,40016,33920,33952,28112,41168,33920,33952,29280,42320,33920,33952,30448,43472,33920,33952,31616,44624,33920,33952,32784,45776,33920\n"
".long 24624,33968,37656,38008,25792,33968,38808,39160,26960,33968,39960,40312,28128,33968,41112,41464,29296,33968,42264,42616,30464,33968,43416,43768,31632,33968,44568,44920,32800,33968,45720,46072\n"
".long 33968,24624,37720,33920,33968,25792,38872,33920,33968,26960,40024,33920,33968,28128,41176,33920,33968,29296,42328,33920,33968,30464,43480,33920,33968,31632,44632,33920,33968,32800,45784,33920\n"
".long 47616,48288,0,0,47616,48288,16,16,47616,48288,32,32,47616,48288,48,48,47616,48288,64,64,47616,48288,80,80,47616,48288,96,96,47616,48288,112,112\n"
".long 47744,48288,4096,2147483648,47744,48288,4112,2147483648,47744,48288,4128,2147483648,47744,48288,4144,2147483648,47744,48288,4160,2147483648,47744,48288,4176,2147483648,47744,48288,4192,2147483648,47744,48288,4208,2147483648\n"
".long 47632,48352,256,8,47632,48352,272,24,47632,48352,288,40,47632,48352,304,56,47632,48352,320,72,47632,48352,336,88,47632,48352,352,104,47632,48352,368,120\n"
".long 47760,48352,4352,2147483648,47760,48352,4368,2147483648,47760,48352,4384,2147483648,47760,48352,4400,2147483648,47760,48352,4416,2147483648,47760,48352,4432,2147483648,47760,48352,4448,2147483648,47760,48352,4464,2147483648\n"
".long 47648,48304,512,256,47648,48304,528,272,47648,48304,544,288,47648,48304,560,304,47648,48304,576,320,47648,48304,592,336,47648,48304,608,352,47648,48304,624,368\n"
".long 47776,48304,4608,2147483648,47776,48304,4624,2147483648,47776,48304,4640,2147483648,47776,48304,4656,2147483648,47776,48304,4672,2147483648,47776,48304,4688,2147483648,47776,48304,4704,2147483648,47776,48304,4720,2147483648\n"
".long 47664,48368,768,264,47664,48368,784,280,47664,48368,800,296,47664,48368,816,312,47664,48368,832,328,47664,48368,848,344,47664,48368,864,360,47664,48368,880,376\n"
".long 47792,48368,4864,2147483648,47792,48368,4880,2147483648,47792,48368,4896,2147483648,47792,48368,4912,2147483648,47792,48368,4928,2147483648,47792,48368,4944,2147483648,47792,48368,4960,2147483648,47792,48368,4976,2147483648\n"
".long 35116,35104,0,34784,35148,35136,128,34800,35180,35168,256,34816,35212,35200,384,34832,35244,35232,512,34848,35276,35264,640,34864,35308,35296,768,34880,35340,35328,896,34896\n"
".long 35116,35104,1024,34912,35148,35136,1152,34928,35180,35168,1280,34944,35212,35200,1408,34960,35244,35232,1536,34976,35276,35264,1664,34992,35308,35296,1792,35008,35340,35328,1920,35024\n"
".long 35116,35104,2147483648,35040,35148,35136,2147483648,35056,35180,35168,2147483648,34784,35212,35200,2147483648,34784,35244,35232,2147483648,34784,35276,35264,2147483648,34784,35308,35296,2147483648,34784,35340,35328,2147483648,34784\n"
".long 35116,35104,2147483648,34784,35148,35136,2147483648,34784,35180,35168,2147483648,34784,35212,35200,2147483648,34784,35244,35232,2147483648,34784,35276,35264,2147483648,34784,35308,35296,2147483648,34784,35340,35328,2147483648,34784\n"
".long 35132,35104,2147483648,34784,35164,35136,2147483648,34784,35196,35168,2147483648,34784,35228,35200,2147483648,34784,35260,35232,2147483648,34784,35292,35264,2147483648,34784,35324,35296,2147483648,34784,35356,35328,2147483648,34784\n"
".long 35132,35104,2147483648,34784,35164,35136,2147483648,34784,35196,35168,2147483648,34784,35228,35200,2147483648,34784,35260,35232,2147483648,34784,35292,35264,2147483648,34784,35324,35296,2147483648,34784,35356,35328,2147483648,34784\n"
".long 35132,35104,2147483648,34784,35164,35136,2147483648,34784,35196,35168,2147483648,34784,35228,35200,2147483648,34784,35260,35232,2147483648,34784,35292,35264,2147483648,34784,35324,35296,2147483648,34784,35356,35328,2147483648,34784\n"
".long 35132,35104,2147483648,34784,35164,35136,2147483648,34784,35196,35168,2147483648,34784,35228,35200,2147483648,34784,35260,35232,2147483648,34784,35292,35264,2147483648,34784,35324,35296,2147483648,34784,35356,35328,2147483648,34784\n"
".popsection\n"
".LK1_end:\n"
    :
    : "{s[0:1]}"(__builtin_amdgcn_kernarg_segment_ptr()), "{s2}"(blockIdx.x), "{v0}"(threadIdx.x)
    : "v1", "v2", "v3", "v4", "v5", "v6", "v7", "v8", "v9", "v10", "v11", "v12", "v13", "v14", "v15", "v16", "v17", "v18", "v19", "v20", "v21", "v22", "v23", "v24", "v25", "v26", "v27", "v28", "v29", "v30", "v31", "v32", "v33", "v34", "v35", "v36", "v37", "v38", "v39", "v40", "v41", "v42", "v43", "v44", "v45", "v46", "v47", "v48", "v49", "v50", "v51", "v52", "v53", "v54", "v55", "v56", "v57", "v58", "v59", "v60", "v61", "v62", "v63", "v64", "v65", "v66", "v67", "v68", "v69", "v70", "v71", "v72", "v73", "v74", "v75", "v76", "v77", "v78", "v79", "v80", "v81", "v82", "v83", "v84", "v85", "v86", "v87", "v88", "v89", "v90", "v91", "v92", "v93", "v94", "v95", "v96", "v97", "v98", "v99", "v100", "v101", "v102", "v103", "v104", "v105", "v106", "v107", "v108", "v109", "v110", "v111", "v112", "v113", "v114", "v115", "v116", "v117", "v118", "v119", "v120", "v121", "v122", "v123", "v124", "v125", "v126", "v127", "v128", "v129", "v130", "v131", "v132", "v133", "v134", "v135", "v136", "v137", "v138", "v139", "v140", "v141", "v142", "v143", "v144", "v145", "v146", "v147", "v148", "v149", "v150", "v151", "v152", "v153", "v154", "v155", "v156", "v157", "v158", "v159", "v160", "v161", "v162", "v163", "v164", "v165", "v166", "v167", "v168", "v169", "v170", "v171", "v172", "v173", "v174", "v175", "v176", "v177", "v178", "v179", "v180", "v181", "v182", "v183", "v184", "v185", "v186", "v187", "v188", "v189", "v190", "v191", "v192", "v193", "v194", "v195", "v196", "v197", "v198", "v199", "v200", "v201", "v202", "v203", "v204", "v205", "v206", "v207", "v208", "v209", "v210", "v211", "v212", "v213", "v214", "v215", "v216", "v217", "v218", "v219", "v220", "v221", "v222", "v223", "v224", "v225", "v226", "v227", "v228", "v229", "v230", "v231", "v232", "v233", "v234", "v235", "v236", "v237", "v238", "v239", "v240", "v241", "v242", "v243", "v244", "v245", "v246", "v247", "v248", "v249", "v250", "v251", "v252", "v253", "v254", "v255", "s3", "s4", "s5", "s6", "s7", "s8", "s9", "s10", "s11", "s12", "s13", "s14", "s15", "s16", "s17", "s18", "s19", "s20", "s21", "s22", "s23", "s24", "s25", "s26", "s27", "s28", "s29", "s30", "s31", "s32", "s33", "s34", "s35", "s36", "s37", "s38", "s39", "s40", "s41", "s42", "s43", "s44", "s45", "s46", "s47", "s48", "s49", "s50", "s51", "s52", "s53", "s54", "s55", "s56", "s57", "s58", "s59", "s60", "s61", "s62", "s63", "s64", "s65", "s66", "s67", "s68", "s69", "s70", "s71", "s72", "s73", "s74", "s75", "s76", "s77", "s78", "s79", "s80", "s81", "s82", "s83", "s84", "s85", "s86", "s87", "s88", "s89", "s90", "s91", "s92", "s93", "s94", "s95", "s96", "s97", "s98", "s99", "vcc", "scc", "memory"
  );
}

__global__ __launch_bounds__(256, 1) __attribute__((aligned(4096))) void k3f(const u16* __restrict__ x, const u16* __restrict__ wg, const u16* __restrict__ wu, char* __restrict__ ws,
    const u16* __restrict__ wd, u16* __restrict__ out) {
  unsigned b_ = blockIdx.x; auto ka_ = __builtin_amdgcn_kernarg_segment_ptr();
  asm volatile(
".LK3_pad0:\n"
"s_cbranch_execz .LK3_pad4\n"
"s_load_dwordx2 s[48:49], s[86:87], 0x18\n"
"s_load_dwordx2 s[44:45], s[86:87], 0x0\n"
"s_load_dwordx4 s[40:43], s[86:87], 0x8\n"
"v_readfirstlane_b32 s80, v0\n"
"v_and_b32_e32 v6, 63, v0\n"
"v_and_b32_e32 v7, 31, v0\n"
"s_mov_b32 s50, 0x28e0000\n"
"s_mov_b32 s51, 0x20000\n"
"s_mov_b64 s[38:39], -1\n"
"v_lshlrev_b32_e32 v4, 7, v6\n"
"v_lshlrev_b32_e32 v2, 3, v6\n"
"s_lshr_b32 s80, s80, 6\n"
"s_lshr_b32 s79, s85, 3\n"
"s_lshl2_add_u32 s81, s79, s80\n"
"s_bfe_u32 s84, s85, 0x10002\n"
"s_bfe_u32 s90, s85, 0x20003\n"
"s_lshl2_add_u32 s84, s84, s90\n"
"s_and_b32 s90, s85, 3\n"
"s_lshl2_add_u32 s84, s84, s90\n"
"s_lshr_b32 s90, s85, 5\n"
"s_lshl3_add_u32 s84, s84, s90\n"
"s_mul_i32 s57, s81, 0x580\n"
"s_add_u32 s57, s57, 0x542400\n"
"s_mul_i32 s55, s84, 0x2400\n"
"s_add_u32 s55, s55, 0xc2000\n"
"s_lshl_b32 s52, s80, 14\n"
"s_add_u32 s52, s52, 0x542400\n"
"v_lshlrev_b32_e32 v3, 2, v0\n"
"v_lshlrev_b32_e32 v0, 4, v6\n"
"v_cmp_gt_u32_e32 vcc, 24, v7\n"
"v_and_b32_e32 v8, 0x80, v3\n"
"v_bfrev_b32_e32 v9, 1\n"
"v_sub_u32_e32 v10, v0, v8\n"
"v_cndmask_b32_e32 v1, v9, v10, vcc\n"
"s_mov_b32 s36, 0\n"
"s_mov_b32 s37, -1\n"
"v_lshrrev_b32_e32 v7, 3, v7\n"
"v_and_b32_e32 v10, 7, v6\n"
"v_lshl_add_u32 v7, v10, 5, v7\n"
"v_lshrrev_b32_e32 v8, 3, v8\n"
"v_mad_u32_u24 v3, v7, 36, v8\n"
"v_add_u32_e32 v7, 32, v3\n"
"v_cndmask_b32_e64 v7, v7, v9, s[36:37]\n"
"s_lshl_b32 s53, s80, 11\n"
"s_add_u32 s53, s53, 0x552400\n"
"s_mul_i32 s58, s80, 0x900\n"
"s_add_u32 s58, s58, s55\n"
"s_mul_i32 s54, s80, 0x6000\n"
"s_add_u32 s54, s54, 0x554400\n"
"s_mov_b32 s72, 0x5200\n"
"v_mov_b32_e32 v9, 0x3f803f80\n"
"v_accvgpr_write_b32 a32, v9\n"
"v_accvgpr_write_b32 a33, v9\n"
"v_accvgpr_write_b32 a34, v9\n"
"v_accvgpr_write_b32 a35, v9\n"
"v_accvgpr_write_b32 a36, v9\n"
"v_accvgpr_write_b32 a37, v9\n"
"v_accvgpr_write_b32 a38, v9\n"
"v_accvgpr_write_b32 a39, v9\n"
"v_accvgpr_write_b32 a40, v9\n"
"v_accvgpr_write_b32 a41, v9\n"
"v_accvgpr_write_b32 a42, v9\n"
"v_accvgpr_write_b32 a43, v9\n"
"v_accvgpr_write_b32 a44, v9\n"
"v_accvgpr_write_b32 a45, v9\n"
"v_accvgpr_write_b32 a46, v9\n"
"v_accvgpr_write_b32 a47, v9\n"
"s_waitcnt lgkmcnt(0)\n"
".LK3_top_%=:\n"
"s_mov_b32 exec_lo, 0x7ff\n"
"s_mov_b32 exec_hi, 0x0\n"
"buffer_load_dword v6, v4, s[48:51], s57 offen\n"
"s_mov_b64 exec, -1\n"
"buffer_load_dwordx4 v[76:79], v3, s[48:51], s55 offen\n"
"buffer_load_dwordx2 v[80:81], v7, s[48:51], s55 offen\n"
"buffer_load_dwordx4 v[152:155], v0, s[48:51], s52 offen\n"
"s_mov_b32 exec_hi, 0\n"
"buffer_load_dwordx4 a[32:35], v0, s[48:51], s53 offen\n"
"buffer_load_dwordx4 a[36:39], v0, s[48:51], s53 offen offset:512\n"
"buffer_load_dwordx4 a[40:43], v0, s[48:51], s53 offen offset:1024\n"
"buffer_load_dwordx4 a[44:47], v0, s[48:51], s53 offen offset:1536\n"
"s_mov_b64 exec, -1\n"
"buffer_load_dwordx4 v[164:167], v0, s[48:51], s52 offen offset:1024\n"
"buffer_load_dwordx4 v[156:159], v1, s[48:51], s54 offen\n"
"buffer_load_dwordx4 v[176:179], v0, s[48:51], s52 offen offset:2048\n"
"buffer_load_dwordx4 v[160:163], v1, s[48:51], s54 offen offset:768\n"
"buffer_load_dwordx4 v[168:171], v1, s[48:51], s54 offen offset:1536\n"
"buffer_load_dwordx4 v[188:191], v0, s[48:51], s52 offen offset:3072\n"
"buffer_load_dwordx4 v[172:175], v1, s[48:51], s54 offen offset:2304\n"
"buffer_load_dwordx4 v[180:183], v1, s[48:51], s54 offen offset:3072\n"
"v_lshrrev_b32_e32 v145, 1, v2\n"
"v_mul_u32_u24_e32 v145, 9, v145\n"
"buffer_load_dwordx2 v[112:113], v2, s[48:51], s72 offen\n"
"v_mov_b32_e32 v132, -1\n"
"v_mov_b32_e32 v133, -1\n"
"v_mov_b32_e32 v134, -1\n"
"v_mov_b32_e32 v135, -1\n"
"v_mov_b32_e32 v128, 0\n"
"v_mov_b32_e32 v129, 0\n"
"v_mov_b32_e32 v130, 0\n"
"v_mov_b32_e32 v131, 0\n"
"s_getpc_b64 s[34:35]\n"
"s_add_u32 s34, s34, .LK1_tab@rel32@lo+4\n"
"s_addc_u32 s35, s35, .LK1_tab@rel32@hi+12\n"
"s_lshl_b32 s90, s80, 12\n"
"s_add_u32 s34, s34, s90\n"
"s_addc_u32 s35, s35, 0\n"
"s_lshr_b32 s82, s80, 1\n"
"s_and_b32 s83, s80, 1\n"
"s_cmp_eq_u32 s82, 0\n"
"s_cselect_b64 s[76:77], s[40:41], s[42:43]\n"
"s_mov_b32 s78, 0x510000\n"
"s_mov_b32 s46, 0x240000\n"
"s_mov_b32 s47, 0x20000\n"
"s_mul_i32 s59, s80, 0x28800\n"
"s_add_u32 s59, s59, 0x20000\n"
"s_mul_i32 s60, s81, 0x1480\n"
"s_add_u32 s60, s60, 0x20000\n"
"s_mul_i32 s62, s84, 0x2400\n"
"s_lshl_b32 s61, s80, 11\n"
"s_add_u32 s61, s61, s62\n"
"s_lshl_b32 s63, s80, 11\n"
"s_add_u32 s63, s63, 0x1200\n"
"s_lshl_b32 s64, s80, 11\n"
"s_add_u32 s64, s64, 0x9000\n"
"s_bfe_u32 s92, s84, 0x20001\n"
"s_and_b32 s93, s84, 1\n"
"s_lshr_b32 s94, s84, 3\n"
"s_mul_i32 s75, s94, 0x12000\n"
"s_lshl_b32 s90, s92, 10\n"
"s_add_u32 s75, s75, s90\n"
"s_lshl_b32 s90, s93, 7\n"
"s_add_u32 s75, s75, s90\n"
"s_add_u32 s75, s75, 0x56c400\n"
"s_lshl_b32 s65, s80, 14\n"
"s_add_u32 s65, s65, s75\n"
"s_lshl_b32 s66, s82, 12\n"
"s_lshl_b32 s90, s83, 9\n"
"s_add_u32 s66, s66, s90\n"
"s_add_u32 s66, s66, s75\n"
"s_and_b32 s92, s85, 3\n"
"s_mul_i32 s69, s79, 0x28800\n"
"s_mul_i32 s90, s83, 0x14400\n"
"s_add_u32 s69, s69, s90\n"
"s_lshl_b32 s90, s92, 11\n"
"s_add_u32 s69, s69, s90\n"
"s_mul_i32 s70, s92, 0x48000\n"
"s_mul_i32 s90, s81, 0x900\n"
"s_add_u32 s70, s70, s90\n"
"s_add_u32 s70, s70, 0x9ec400\n"
"s_bfe_u32 s93, s85, 0x10002\n"
"s_bfe_u32 s94, s85, 0x30003\n"
"s_lshr_b32 s95, s85, 6\n"
"s_lshl_b32 s71, s93, 22\n"
"s_lshl_b32 s90, s94, 19\n"
"s_add_u32 s71, s71, s90\n"
"s_lshl_b32 s90, s95, 17\n"
"s_add_u32 s71, s71, s90\n"
"s_lshl_b32 s90, s80, 15\n"
"s_add_u32 s71, s71, s90\n"
"s_lshl_b32 s90, s92, 13\n"
"s_add_u32 s71, s71, s90\n"
"s_add_u32 s71, s71, 0x1da0000\n"
"s_mov_b64 exec, 0\n"
"s_cmp_eq_u32 s80, 0\n"
"s_cbranch_scc1 .LKF_pad0\n"
"s_cmp_eq_u32 s80, 1\n"
"s_cbranch_scc1 .LKF_pad1\n"
"s_cmp_eq_u32 s80, 2\n"
"s_cbranch_scc1 .LKF_pad2\n"
"s_branch .LKF_pad3\n"
".LK3_ret:\n"
"s_mov_b64 exec, -1\n"
"s_waitcnt vmcnt(14)\n"
"v_cndmask_b32_e64 v82, 0, v81, s[36:37]\n"
"v_mov_b32_e32 v83, v80\n"
"v_cndmask_b32_e64 v81, 0, v81, s[36:37]\n"
"s_waitcnt vmcnt(13)\n"
"v_mfma_f32_32x32x16_bf16 v[24:39], v[152:155], v[76:79], 0\n"
"s_waitcnt vmcnt(12)\n"
"v_mfma_f32_32x32x8bf16_1k v[24:39], a[32:33], v[80:81], v[24:39]\n"
"s_nop 7\n"
"s_waitcnt vmcnt(8)\n"
"v_mfma_f32_32x32x16_bf16 v[40:55], v[164:167], v[76:79], 0\n"
"s_add_u32 s88, s52, 0x1000\n"
"buffer_load_dwordx4 v[200:203], v0, s[48:51], s88 offen\n"
"buffer_load_dwordx4 v[184:187], v1, s[48:51], s54 offen offset:3840\n"
"s_nop 1\n"
"v_exp_f32_e32 v56, v24\n"
"v_exp_f32_e32 v57, v25\n"
"v_exp_f32_e32 v58, v26\n"
"v_exp_f32_e32 v59, v27\n"
"s_nop 0\n"
"s_add_u32 s89, s54, 0x1200\n"
"buffer_load_dwordx4 v[192:195], v1, s[48:51], s89 offen\n"
"buffer_load_dwordx4 v[212:215], v0, s[48:51], s88 offen offset:1024\n"
"s_waitcnt vmcnt(11)\n"
"v_cvt_pk_bf16_f32 v68, v56, v57\n"
"v_cvt_pk_bf16_f32 v69, v58, v59\n"
"v_exp_f32_e32 v60, v28\n"
"v_exp_f32_e32 v61, v29\n"
"v_exp_f32_e32 v62, v30\n"
"v_exp_f32_e32 v63, v31\n"
"v_mfma_f32_32x32x8bf16_1k v[40:55], a[32:33], v[82:83], v[40:55]\n"
"v_cvt_pk_bf16_f32 v70, v60, v61\n"
"v_cvt_pk_bf16_f32 v71, v62, v63\n"
"v_exp_f32_e32 v56, v32\n"
"v_exp_f32_e32 v57, v33\n"
"v_exp_f32_e32 v58, v34\n"
"v_exp_f32_e32 v59, v35\n"
"v_mfma_f32_32x32x16_bf16 a[0:15], v[156:159], v[68:71], 0\n"
"buffer_load_dwordx4 v[196:199], v1, s[48:51], s89 offen offset:768\n"
"buffer_load_dwordx4 v[204:207], v1, s[48:51], s89 offen offset:1536\n"
"s_waitcnt vmcnt(12)\n"
"v_cvt_pk_bf16_f32 v72, v56, v57\n"
"v_cvt_pk_bf16_f32 v73, v58, v59\n"
"v_exp_f32_e32 v64, v36\n"
"v_exp_f32_e32 v65, v37\n"
"v_exp_f32_e32 v66, v38\n"
"v_exp_f32_e32 v67, v39\n"
"v_mfma_f32_32x32x16_bf16 v[24:39], v[176:179], v[76:79], 0\n"
"buffer_load_dwordx4 v[136:139], v145, s[48:51], s58 offen\n"
"buffer_load_dwordx4 v[140:143], v145, s[48:51], s58 offen offset:16\n"
"s_waitcnt vmcnt(13)\n"
"v_cvt_pk_bf16_f32 v74, v64, v65\n"
"v_cvt_pk_bf16_f32 v75, v66, v67\n"
"v_exp_f32_e32 v56, v40\n"
"v_exp_f32_e32 v57, v41\n"
"v_exp_f32_e32 v58, v42\n"
"v_exp_f32_e32 v59, v43\n"
"v_mfma_f32_32x32x16_bf16 a[0:15], v[160:163], v[72:75], a[0:15]\n"
"buffer_load_dword v144, v145, s[48:51], s58 offen offset:32\n"
"buffer_load_dwordx4 v[224:227], v0, s[48:51], s88 offen offset:2048\n"
"s_waitcnt vmcnt(14)\n"
"v_cvt_pk_bf16_f32 v68, v56, v57\n"
"v_cvt_pk_bf16_f32 v69, v58, v59\n"
"v_exp_f32_e32 v60, v44\n"
"v_exp_f32_e32 v61, v45\n"
"v_exp_f32_e32 v62, v46\n"
"v_exp_f32_e32 v63, v47\n"
"v_mfma_f32_32x32x8bf16_1k v[24:39], a[34:35], v[80:81], v[24:39]\n"
"v_cvt_pk_bf16_f32 v70, v60, v61\n"
"v_cvt_pk_bf16_f32 v71, v62, v63\n"
"v_exp_f32_e32 v56, v48\n"
"v_exp_f32_e32 v57, v49\n"
"v_exp_f32_e32 v58, v50\n"
"v_exp_f32_e32 v59, v51\n"
"v_mfma_f32_32x32x16_bf16 a[0:15], v[168:171], v[68:71], a[0:15]\n"
"buffer_load_dwordx4 v[208:211], v1, s[48:51], s89 offen offset:2304\n"
"buffer_load_dwordx4 v[216:219], v1, s[48:51], s89 offen offset:3072\n"
"s_waitcnt vmcnt(15)\n"
"v_cvt_pk_bf16_f32 v72, v56, v57\n"
"v_cvt_pk_bf16_f32 v73, v58, v59\n"
"v_exp_f32_e32 v64, v52\n"
"v_exp_f32_e32 v65, v53\n"
"v_exp_f32_e32 v66, v54\n"
"v_exp_f32_e32 v67, v55\n"
"v_mfma_f32_32x32x16_bf16 v[40:55], v[188:191], v[76:79], 0\n"
"s_waitcnt vmcnt(12)\n"
"buffer_load_dwordx4 v[236:239], v0, s[48:51], s88 offen offset:3072\n"
"buffer_load_dwordx4 v[220:223], v1, s[48:51], s89 offen offset:3840\n"
"v_cvt_pk_bf16_f32 v74, v64, v65\n"
"v_cvt_pk_bf16_f32 v75, v66, v67\n"
"v_exp_f32_e32 v56, v24\n"
"v_exp_f32_e32 v57, v25\n"
"v_add_f32_e32 v112, v112, v113\n"
"v_exp_f32_e32 v58, v26\n"
"v_exp_f32_e32 v59, v27\n"
"v_mfma_f32_32x32x16_bf16 a[0:15], v[172:175], v[72:75], a[0:15]\n"
"s_add_u32 s89, s54, 0x2400\n"
"buffer_load_dwordx4 v[228:231], v1, s[48:51], s89 offen\n"
"s_add_u32 s88, s52, 0x2000\n"
"buffer_load_dwordx4 a[48:51], v0, s[48:51], s88 offen\n"
"v_cvt_pk_bf16_f32 v68, v56, v57\n"
"v_cvt_pk_bf16_f32 v69, v58, v59\n"
"v_exp_f32_e32 v60, v28\n"
"v_exp_f32_e32 v61, v29\n"
"v_max_u32_dpp v112, v112, v112 row_ror:8 row_mask:0xf bank_mask:0xf\n"
"v_exp_f32_e32 v62, v30\n"
"v_exp_f32_e32 v63, v31\n"
"v_mfma_f32_32x32x8bf16_1k v[40:55], a[34:35], v[82:83], v[40:55]\n"
"v_cvt_pk_bf16_f32 v70, v60, v61\n"
"v_cvt_pk_bf16_f32 v71, v62, v63\n"
"v_exp_f32_e32 v56, v32\n"
"v_exp_f32_e32 v57, v33\n"
"v_max_u32_dpp v112, v112, v112 row_ror:4 row_mask:0xf bank_mask:0xf\n"
"v_exp_f32_e32 v58, v34\n"
"v_exp_f32_e32 v59, v35\n"
"v_mfma_f32_32x32x16_bf16 a[0:15], v[180:183], v[68:71], a[0:15]\n"
"buffer_load_dwordx4 v[232:235], v1, s[48:51], s89 offen offset:768\n"
"buffer_load_dwordx4 v[240:243], v1, s[48:51], s89 offen offset:1536\n"
"s_waitcnt vmcnt(17)\n"
"v_cvt_pk_bf16_f32 v72, v56, v57\n"
"v_cvt_pk_bf16_f32 v73, v58, v59\n"
"v_exp_f32_e32 v64, v36\n"
"v_exp_f32_e32 v65, v37\n"
"v_max_u32_dpp v112, v112, v112 row_ror:2 row_mask:0xf bank_mask:0xf\n"
"v_exp_f32_e32 v66, v38\n"
"v_exp_f32_e32 v67, v39\n"
"v_mfma_f32_32x32x16_bf16 v[24:39], v[200:203], v[76:79], 0\n"
"buffer_load_dwordx4 a[60:63], v0, s[48:51], s88 offen offset:1024\n"
"buffer_load_dwordx4 v[244:247], v1, s[48:51], s89 offen offset:2304\n"
"s_waitcnt vmcnt(18)\n"
"v_cvt_pk_bf16_f32 v74, v64, v65\n"
"v_cvt_pk_bf16_f32 v75, v66, v67\n"
"v_exp_f32_e32 v56, v40\n"
"v_exp_f32_e32 v57, v41\n"
"v_max_u32_dpp v112, v112, v112 row_ror:1 row_mask:0xf bank_mask:0xf\n"
"v_exp_f32_e32 v58, v42\n"
"v_exp_f32_e32 v59, v43\n"
"v_mfma_f32_32x32x16_bf16 a[0:15], v[184:187], v[72:75], a[0:15]\n"
"buffer_load_dwordx4 a[52:55], v1, s[48:51], s89 offen offset:3072\n"
"buffer_load_dwordx4 a[72:75], v0, s[48:51], s88 offen offset:2048\n"
"s_waitcnt vmcnt(19)\n"
"v_cvt_pk_bf16_f32 v68, v56, v57\n"
"v_cvt_pk_bf16_f32 v69, v58, v59\n"
"v_exp_f32_e32 v60, v44\n"
"v_exp_f32_e32 v61, v45\n"
"v_mov_b32_e32 v113, v112\n"
"v_exp_f32_e32 v62, v46\n"
"v_exp_f32_e32 v63, v47\n"
"v_mfma_f32_32x32x8bf16_1k v[24:39], a[36:37], v[80:81], v[24:39]\n"
"v_cvt_pk_bf16_f32 v70, v60, v61\n"
"v_cvt_pk_bf16_f32 v71, v62, v63\n"
"v_exp_f32_e32 v56, v48\n"
"v_exp_f32_e32 v57, v49\n"
"v_permlane16_swap_b32_e32 v112, v113\n"
"v_exp_f32_e32 v58, v50\n"
"v_exp_f32_e32 v59, v51\n"
"v_mfma_f32_32x32x16_bf16 a[0:15], v[192:195], v[68:71], a[0:15]\n"
"buffer_load_dwordx4 a[56:59], v1, s[48:51], s89 offen offset:3840\n"
"s_add_u32 s89, s54, 0x3600\n"
"buffer_load_dwordx4 a[64:67], v1, s[48:51], s89 offen\n"
"s_waitcnt vmcnt(20)\n"
"v_cvt_pk_bf16_f32 v72, v56, v57\n"
"v_cvt_pk_bf16_f32 v73, v58, v59\n"
"v_exp_f32_e32 v64, v52\n"
"v_exp_f32_e32 v65, v53\n"
"v_max_u32_e32 v112, v112, v113\n"
"v_mov_b32_e32 v113, v112\n"
"v_exp_f32_e32 v66, v54\n"
"v_exp_f32_e32 v67, v55\n"
"v_mfma_f32_32x32x16_bf16 v[40:55], v[212:215], v[76:79], 0\n"
"buffer_load_dwordx4 a[84:87], v0, s[48:51], s88 offen offset:3072\n"
"buffer_load_dwordx4 a[68:71], v1, s[48:51], s89 offen offset:768\n"
"s_waitcnt vmcnt(21)\n"
"v_cvt_pk_bf16_f32 v74, v64, v65\n"
"v_cvt_pk_bf16_f32 v75, v66, v67\n"
"v_exp_f32_e32 v56, v24\n"
"v_exp_f32_e32 v57, v25\n"
"v_permlane32_swap_b32_e32 v112, v113\n"
"v_exp_f32_e32 v58, v26\n"
"v_exp_f32_e32 v59, v27\n"
"v_mfma_f32_32x32x16_bf16 a[0:15], v[196:199], v[72:75], a[0:15]\n"
"buffer_load_dwordx4 a[76:79], v1, s[48:51], s89 offen offset:1536\n"
"s_add_u32 s88, s52, 0x3000\n"
"buffer_load_dwordx4 a[96:99], v0, s[48:51], s88 offen\n"
"s_waitcnt vmcnt(22)\n"
"v_cvt_pk_bf16_f32 v68, v56, v57\n"
"v_cvt_pk_bf16_f32 v69, v58, v59\n"
"v_exp_f32_e32 v60, v28\n"
"v_exp_f32_e32 v61, v29\n"
"v_max_u32_e32 v112, v112, v113\n"
"v_exp_f32_e32 v62, v30\n"
"v_exp_f32_e32 v63, v31\n"
"v_mfma_f32_32x32x8bf16_1k v[40:55], a[36:37], v[82:83], v[40:55]\n"
"v_cvt_pk_bf16_f32 v70, v60, v61\n"
"v_cvt_pk_bf16_f32 v71, v62, v63\n"
"v_exp_f32_e32 v56, v32\n"
"v_exp_f32_e32 v57, v33\n"
"v_readfirstlane_b32 s56, v112\n"
"v_exp_f32_e32 v58, v34\n"
"v_exp_f32_e32 v59, v35\n"
"v_mfma_f32_32x32x16_bf16 a[0:15], v[204:207], v[68:71], a[0:15]\n"
"buffer_load_dwordx4 a[80:83], v1, s[48:51], s89 offen offset:2304\n"
"buffer_load_dwordx4 a[88:91], v1, s[48:51], s89 offen offset:3072\n"
"s_waitcnt vmcnt(20)\n"
"v_cvt_pk_bf16_f32 v72, v56, v57\n"
"v_cvt_pk_bf16_f32 v73, v58, v59\n"
"v_exp_f32_e32 v64, v36\n"
"v_exp_f32_e32 v65, v37\n"
"v_exp_f32_e32 v66, v38\n"
"v_exp_f32_e32 v67, v39\n"
"v_mfma_f32_32x32x16_bf16 v[24:39], v[224:227], v[76:79], 0\n"
"buffer_load_dwordx4 a[108:111], v0, s[48:51], s88 offen offset:1024\n"
"buffer_load_dwordx4 a[92:95], v1, s[48:51], s89 offen offset:3840\n"
"s_waitcnt vmcnt(21)\n"
"v_cvt_pk_bf16_f32 v74, v64, v65\n"
"v_cvt_pk_bf16_f32 v75, v66, v67\n"
"v_exp_f32_e32 v56, v40\n"
"v_exp_f32_e32 v57, v41\n"
"v_exp_f32_e32 v58, v42\n"
"v_exp_f32_e32 v59, v43\n"
"v_mfma_f32_32x32x16_bf16 a[0:15], v[208:211], v[72:75], a[0:15]\n"
"s_add_u32 s89, s54, 0x4800\n"
"buffer_load_dwordx4 a[100:103], v1, s[48:51], s89 offen\n"
"buffer_load_dwordx4 a[120:123], v0, s[48:51], s88 offen offset:2048\n"
"s_waitcnt vmcnt(22)\n"
"v_cvt_pk_bf16_f32 v68, v56, v57\n"
"v_cvt_pk_bf16_f32 v69, v58, v59\n"
"v_exp_f32_e32 v60, v44\n"
"v_exp_f32_e32 v61, v45\n"
"v_exp_f32_e32 v62, v46\n"
"v_exp_f32_e32 v63, v47\n"
"v_mfma_f32_32x32x8bf16_1k v[24:39], a[38:39], v[80:81], v[24:39]\n"
"v_cvt_pk_bf16_f32 v70, v60, v61\n"
"v_cvt_pk_bf16_f32 v71, v62, v63\n"
"v_exp_f32_e32 v56, v48\n"
"v_exp_f32_e32 v57, v49\n"
"v_exp_f32_e32 v58, v50\n"
"v_exp_f32_e32 v59, v51\n"
"v_mfma_f32_32x32x16_bf16 a[0:15], v[216:219], v[68:71], a[0:15]\n"
"buffer_load_dwordx4 a[104:107], v1, s[48:51], s89 offen offset:768\n"
"buffer_load_dwordx4 a[112:115], v1, s[48:51], s89 offen offset:1536\n"
"s_waitcnt vmcnt(23)\n"
"v_cvt_pk_bf16_f32 v72, v56, v57\n"
"v_cvt_pk_bf16_f32 v73, v58, v59\n"
"v_exp_f32_e32 v64, v52\n"
"v_exp_f32_e32 v65, v53\n"
"v_exp_f32_e32 v66, v54\n"
"v_exp_f32_e32 v67, v55\n"
"v_mfma_f32_32x32x16_bf16 v[40:55], v[236:239], v[76:79], 0\n"
"buffer_load_dwordx4 a[132:135], v0, s[48:51], s88 offen offset:3072\n"
"buffer_load_dwordx4 a[116:119], v1, s[48:51], s89 offen offset:2304\n"
"s_waitcnt vmcnt(24)\n"
"v_cvt_pk_bf16_f32 v74, v64, v65\n"
"v_cvt_pk_bf16_f32 v75, v66, v67\n"
"v_exp_f32_e32 v56, v24\n"
"v_exp_f32_e32 v57, v25\n"
"v_exp_f32_e32 v58, v26\n"
"v_exp_f32_e32 v59, v27\n"
"v_mfma_f32_32x32x16_bf16 a[0:15], v[220:223], v[72:75], a[0:15]\n"
"buffer_load_dwordx4 a[124:127], v1, s[48:51], s89 offen offset:3072\n"
"buffer_load_dwordx4 a[128:131], v1, s[48:51], s89 offen offset:3840\n"
"s_waitcnt vmcnt(25)\n"
"v_cvt_pk_bf16_f32 v68, v56, v57\n"
"v_cvt_pk_bf16_f32 v69, v58, v59\n"
"v_exp_f32_e32 v60, v28\n"
"v_exp_f32_e32 v61, v29\n"
"v_exp_f32_e32 v62, v30\n"
"v_exp_f32_e32 v63, v31\n"
"v_mfma_f32_32x32x8bf16_1k v[40:55], a[38:39], v[82:83], v[40:55]\n"
"v_cvt_pk_bf16_f32 v70, v60, v61\n"
"v_cvt_pk_bf16_f32 v71, v62, v63\n"
"v_exp_f32_e32 v56, v32\n"
"v_exp_f32_e32 v57, v33\n"
"v_exp_f32_e32 v58, v34\n"
"v_exp_f32_e32 v59, v35\n"
"v_mfma_f32_32x32x16_bf16 a[0:15], v[228:231], v[68:71], a[0:15]\n"
"s_add_u32 s89, s54, 0x5a00\n"
"buffer_load_dwordx4 a[136:139], v1, s[48:51], s89 offen\n"
"buffer_load_dwordx4 a[140:143], v1, s[48:51], s89 offen offset:768\n"
"s_waitcnt vmcnt(26)\n"
"v_cvt_pk_bf16_f32 v72, v56, v57\n"
"v_cvt_pk_bf16_f32 v73, v58, v59\n"
"v_exp_f32_e32 v64, v36\n"
"v_exp_f32_e32 v65, v37\n"
"v_exp_f32_e32 v66, v38\n"
"v_exp_f32_e32 v67, v39\n"
"v_mfma_f32_32x32x16_bf16 v[24:39], a[48:51], v[76:79], 0\n"
"global_load_dwordx4 v[8:11], v0, s[34:35] offset:0\n"
"global_load_dwordx4 v[12:15], v0, s[34:35] offset:1024\n"
"s_waitcnt vmcnt(27)\n"
"v_cvt_pk_bf16_f32 v74, v64, v65\n"
"v_cvt_pk_bf16_f32 v75, v66, v67\n"
"v_exp_f32_e32 v56, v40\n"
"v_exp_f32_e32 v57, v41\n"
"v_exp_f32_e32 v58, v42\n"
"v_exp_f32_e32 v59, v43\n"
"v_mfma_f32_32x32x16_bf16 a[0:15], v[232:235], v[72:75], a[0:15]\n"
"global_load_dwordx4 v[16:19], v0, s[34:35] offset:2048\n"
"global_load_dwordx4 v[20:23], v0, s[34:35] offset:3072\n"
"s_waitcnt vmcnt(28)\n"
"v_cvt_pk_bf16_f32 v68, v56, v57\n"
"v_cvt_pk_bf16_f32 v69, v58, v59\n"
"v_exp_f32_e32 v60, v44\n"
"v_exp_f32_e32 v61, v45\n"
"v_exp_f32_e32 v62, v46\n"
"v_exp_f32_e32 v63, v47\n"
"v_mfma_f32_32x32x8bf16_1k v[24:39], a[40:41], v[80:81], v[24:39]\n"
"v_cvt_pk_bf16_f32 v70, v60, v61\n"
"v_cvt_pk_bf16_f32 v71, v62, v63\n"
"v_exp_f32_e32 v56, v48\n"
"v_exp_f32_e32 v57, v49\n"
"v_exp_f32_e32 v58, v50\n"
"v_exp_f32_e32 v59, v51\n"
"v_mfma_f32_32x32x16_bf16 a[0:15], v[240:243], v[68:71], a[0:15]\n"
"s_waitcnt vmcnt(27)\n"
"v_cvt_pk_bf16_f32 v72, v56, v57\n"
"v_cvt_pk_bf16_f32 v73, v58, v59\n"
"v_exp_f32_e32 v64, v52\n"
"v_exp_f32_e32 v65, v53\n"
"v_exp_f32_e32 v66, v54\n"
"v_exp_f32_e32 v67, v55\n"
"v_mfma_f32_32x32x16_bf16 v[40:55], a[60:63], v[76:79], 0\n"
"s_waitcnt vmcnt(26)\n"
"v_cvt_pk_bf16_f32 v74, v64, v65\n"
"v_cvt_pk_bf16_f32 v75, v66, v67\n"
"v_exp_f32_e32 v56, v24\n"
"v_exp_f32_e32 v57, v25\n"
"v_dot2_f32_bf16 v146, v136, v136, 0\n"
"v_exp_f32_e32 v58, v26\n"
"v_exp_f32_e32 v59, v27\n"
"v_mfma_f32_32x32x16_bf16 a[0:15], v[244:247], v[72:75], a[0:15]\n"
"s_waitcnt vmcnt(25)\n"
"v_cvt_pk_bf16_f32 v68, v56, v57\n"
"v_cvt_pk_bf16_f32 v69, v58, v59\n"
"v_exp_f32_e32 v60, v28\n"
"v_exp_f32_e32 v61, v29\n"
"v_dot2c_f32_bf16_e32 v146, v137, v137\n"
"v_exp_f32_e32 v62, v30\n"
"v_exp_f32_e32 v63, v31\n"
"v_mfma_f32_32x32x8bf16_1k v[40:55], a[40:41], v[82:83], v[40:55]\n"
"v_cvt_pk_bf16_f32 v70, v60, v61\n"
"v_cvt_pk_bf16_f32 v71, v62, v63\n"
"v_exp_f32_e32 v56, v32\n"
"v_exp_f32_e32 v57, v33\n"
"v_dot2c_f32_bf16_e32 v146, v138, v138\n"
"v_exp_f32_e32 v58, v34\n"
"v_exp_f32_e32 v59, v35\n"
"v_mfma_f32_32x32x16_bf16 a[0:15], a[52:55], v[68:71], a[0:15]\n"
"s_waitcnt vmcnt(24)\n"
"v_cvt_pk_bf16_f32 v72, v56, v57\n"
"v_cvt_pk_bf16_f32 v73, v58, v59\n"
"v_exp_f32_e32 v64, v36\n"
"v_exp_f32_e32 v65, v37\n"
"v_dot2c_f32_bf16_e32 v146, v139, v139\n"
"v_exp_f32_e32 v66, v38\n"
"v_exp_f32_e32 v67, v39\n"
"v_mfma_f32_32x32x16_bf16 v[24:39], a[72:75], v[76:79], 0\n"
"buffer_load_dwordx4 v[84:87], v3, s[48:51], s55 offen offset:144\n"
"buffer_load_dwordx2 v[88:89], v7, s[48:51], s55 offen offset:144\n"
"s_waitcnt vmcnt(25)\n"
"v_cvt_pk_bf16_f32 v74, v64, v65\n"
"v_cvt_pk_bf16_f32 v75, v66, v67\n"
"v_exp_f32_e32 v56, v40\n"
"v_exp_f32_e32 v57, v41\n"
"v_dot2c_f32_bf16_e32 v146, v140, v140\n"
"v_exp_f32_e32 v58, v42\n"
"v_exp_f32_e32 v59, v43\n"
"v_mfma_f32_32x32x16_bf16 a[0:15], a[56:59], v[72:75], a[0:15]\n"
"s_waitcnt vmcnt(24)\n"
"v_cvt_pk_bf16_f32 v68, v56, v57\n"
"v_cvt_pk_bf16_f32 v69, v58, v59\n"
"v_exp_f32_e32 v60, v44\n"
"v_exp_f32_e32 v61, v45\n"
"v_dot2c_f32_bf16_e32 v146, v141, v141\n"
"v_exp_f32_e32 v62, v46\n"
"v_exp_f32_e32 v63, v47\n"
"v_mfma_f32_32x32x8bf16_1k v[24:39], a[42:43], v[80:81], v[24:39]\n"
"v_cvt_pk_bf16_f32 v70, v60, v61\n"
"v_cvt_pk_bf16_f32 v71, v62, v63\n"
"v_exp_f32_e32 v56, v48\n"
"v_exp_f32_e32 v57, v49\n"
"v_dot2c_f32_bf16_e32 v146, v142, v142\n"
"v_exp_f32_e32 v58, v50\n"
"v_exp_f32_e32 v59, v51\n"
"v_mfma_f32_32x32x16_bf16 a[0:15], a[64:67], v[68:71], a[0:15]\n"
"s_waitcnt vmcnt(23)\n"
"v_cvt_pk_bf16_f32 v72, v56, v57\n"
"v_cvt_pk_bf16_f32 v73, v58, v59\n"
"v_exp_f32_e32 v64, v52\n"
"v_exp_f32_e32 v65, v53\n"
"v_dot2c_f32_bf16_e32 v146, v143, v143\n"
"v_exp_f32_e32 v66, v54\n"
"v_exp_f32_e32 v67, v55\n"
"v_mfma_f32_32x32x16_bf16 v[40:55], a[84:87], v[76:79], 0\n"
"s_waitcnt vmcnt(22)\n"
"v_cvt_pk_bf16_f32 v74, v64, v65\n"
"v_cvt_pk_bf16_f32 v75, v66, v67\n"
"v_exp_f32_e32 v56, v24\n"
"v_exp_f32_e32 v57, v25\n"
"v_dot2c_f32_bf16_e32 v146, v144, v144\n"
"v_exp_f32_e32 v58, v26\n"
"v_exp_f32_e32 v59, v27\n"
"v_mfma_f32_32x32x16_bf16 a[0:15], a[68:71], v[72:75], a[0:15]\n"
"s_waitcnt vmcnt(21)\n"
"v_cvt_pk_bf16_f32 v68, v56, v57\n"
"v_cvt_pk_bf16_f32 v69, v58, v59\n"
"v_exp_f32_e32 v60, v28\n"
"v_exp_f32_e32 v61, v29\n"
"v_lshl_add_u32 v145, s80, 2, v0\n"
"v_exp_f32_e32 v62, v30\n"
"v_exp_f32_e32 v63, v31\n"
"v_mfma_f32_32x32x8bf16_1k v[40:55], a[42:43], v[82:83], v[40:55]\n"
"v_cvt_pk_bf16_f32 v70, v60, v61\n"
"v_cvt_pk_bf16_f32 v71, v62, v63\n"
"v_exp_f32_e32 v56, v32\n"
"v_exp_f32_e32 v57, v33\n"
"v_exp_f32_e32 v58, v34\n"
"v_exp_f32_e32 v59, v35\n"
"v_mfma_f32_32x32x16_bf16 a[0:15], a[76:79], v[68:71], a[0:15]\n"
"s_waitcnt vmcnt(20)\n"
"v_cvt_pk_bf16_f32 v72, v56, v57\n"
"v_cvt_pk_bf16_f32 v73, v58, v59\n"
"v_exp_f32_e32 v64, v36\n"
"v_exp_f32_e32 v65, v37\n"
"v_exp_f32_e32 v66, v38\n"
"v_exp_f32_e32 v67, v39\n"
"v_mfma_f32_32x32x16_bf16 v[24:39], a[96:99], v[76:79], 0\n"
"ds_write_b32 v145, v146 offset:36864\n"
"s_waitcnt vmcnt(19)\n"
"v_cvt_pk_bf16_f32 v74, v64, v65\n"
"v_cvt_pk_bf16_f32 v75, v66, v67\n"
"v_exp_f32_e32 v56, v40\n"
"v_exp_f32_e32 v57, v41\n"
"v_exp_f32_e32 v58, v42\n"
"v_exp_f32_e32 v59, v43\n"
"v_mfma_f32_32x32x16_bf16 a[0:15], a[80:83], v[72:75], a[0:15]\n"
"s_waitcnt vmcnt(18)\n"
"v_cvt_pk_bf16_f32 v68, v56, v57\n"
"v_cvt_pk_bf16_f32 v69, v58, v59\n"
"v_exp_f32_e32 v60, v44\n"
"v_exp_f32_e32 v61, v45\n"
"v_exp_f32_e32 v62, v46\n"
"v_exp_f32_e32 v63, v47\n"
"v_mfma_f32_32x32x8bf16_1k v[24:39], a[44:45], v[80:81], v[24:39]\n"
"v_cvt_pk_bf16_f32 v70, v60, v61\n"
"v_cvt_pk_bf16_f32 v71, v62, v63\n"
"v_exp_f32_e32 v56, v48\n"
"v_exp_f32_e32 v57, v49\n"
"v_exp_f32_e32 v58, v50\n"
"v_exp_f32_e32 v59, v51\n"
"v_mfma_f32_32x32x16_bf16 a[0:15], a[88:91], v[68:71], a[0:15]\n"
"s_waitcnt vmcnt(17)\n"
"v_cvt_pk_bf16_f32 v72, v56, v57\n"
"v_cvt_pk_bf16_f32 v73, v58, v59\n"
"v_exp_f32_e32 v64, v52\n"
"v_exp_f32_e32 v65, v53\n"
"v_exp_f32_e32 v66, v54\n"
"v_exp_f32_e32 v67, v55\n"
"v_mfma_f32_32x32x16_bf16 v[40:55], a[108:111], v[76:79], 0\n"
"buffer_load_dwordx4 a[144:147], v0, s[48:51], s59 offen\n"
"buffer_load_dwordx4 a[148:151], v0, s[48:51], s59 offen offset:1024\n"
"s_waitcnt vmcnt(18)\n"
"v_cvt_pk_bf16_f32 v74, v64, v65\n"
"v_cvt_pk_bf16_f32 v75, v66, v67\n"
"v_exp_f32_e32 v56, v24\n"
"v_exp_f32_e32 v57, v25\n"
"v_exp_f32_e32 v58, v26\n"
"v_exp_f32_e32 v59, v27\n"
"v_mfma_f32_32x32x16_bf16 a[0:15], a[92:95], v[72:75], a[0:15]\n"
"s_waitcnt vmcnt(17)\n"
"v_cvt_pk_bf16_f32 v68, v56, v57\n"
"v_cvt_pk_bf16_f32 v69, v58, v59\n"
"v_exp_f32_e32 v60, v28\n"
"v_exp_f32_e32 v61, v29\n"
"v_exp_f32_e32 v62, v30\n"
"v_exp_f32_e32 v63, v31\n"
"v_mfma_f32_32x32x8bf16_1k v[40:55], a[44:45], v[82:83], v[40:55]\n"
"v_cvt_pk_bf16_f32 v70, v60, v61\n"
"v_cvt_pk_bf16_f32 v71, v62, v63\n"
"v_exp_f32_e32 v56, v32\n"
"v_exp_f32_e32 v57, v33\n"
"v_exp_f32_e32 v58, v34\n"
"v_exp_f32_e32 v59, v35\n"
"v_mfma_f32_32x32x16_bf16 a[0:15], a[100:103], v[68:71], a[0:15]\n"
"s_waitcnt vmcnt(16)\n"
"v_cvt_pk_bf16_f32 v72, v56, v57\n"
"v_cvt_pk_bf16_f32 v73, v58, v59\n"
"v_exp_f32_e32 v64, v36\n"
"v_exp_f32_e32 v65, v37\n"
"v_exp_f32_e32 v66, v38\n"
"v_exp_f32_e32 v67, v39\n"
"v_mfma_f32_32x32x16_bf16 v[24:39], a[120:123], v[76:79], 0\n"
"buffer_load_dwordx4 a[152:155], v0, s[48:51], s59 offen offset:2048\n"
"buffer_load_dwordx4 a[156:159], v0, s[48:51], s59 offen offset:3072\n"
"s_waitcnt vmcnt(17)\n"
"v_cvt_pk_bf16_f32 v74, v64, v65\n"
"v_cvt_pk_bf16_f32 v75, v66, v67\n"
"v_exp_f32_e32 v56, v40\n"
"v_exp_f32_e32 v57, v41\n"
"v_exp_f32_e32 v58, v42\n"
"v_exp_f32_e32 v59, v43\n"
"v_mfma_f32_32x32x16_bf16 a[0:15], a[104:107], v[72:75], a[0:15]\n"
"s_add_u32 s88, s59, 0x1000\n"
"s_waitcnt vmcnt(16)\n"
"v_cvt_pk_bf16_f32 v68, v56, v57\n"
"v_cvt_pk_bf16_f32 v69, v58, v59\n"
"v_exp_f32_e32 v60, v44\n"
"v_exp_f32_e32 v61, v45\n"
"v_exp_f32_e32 v62, v46\n"
"v_exp_f32_e32 v63, v47\n"
"v_mfma_f32_32x32x8bf16_1k v[24:39], a[46:47], v[80:81], v[24:39]\n"
"v_cvt_pk_bf16_f32 v70, v60, v61\n"
"v_cvt_pk_bf16_f32 v71, v62, v63\n"
"v_exp_f32_e32 v56, v48\n"
"v_exp_f32_e32 v57, v49\n"
"v_exp_f32_e32 v58, v50\n"
"v_exp_f32_e32 v59, v51\n"
"v_mfma_f32_32x32x16_bf16 a[0:15], a[112:115], v[68:71], a[0:15]\n"
"s_waitcnt vmcnt(15)\n"
"v_cvt_pk_bf16_f32 v72, v56, v57\n"
"v_cvt_pk_bf16_f32 v73, v58, v59\n"
"v_exp_f32_e32 v64, v52\n"
"v_exp_f32_e32 v65, v53\n"
"v_exp_f32_e32 v66, v54\n"
"v_exp_f32_e32 v67, v55\n"
"v_mfma_f32_32x32x16_bf16 v[40:55], a[132:135], v[76:79], 0\n"
"buffer_load_dwordx4 a[160:163], v0, s[48:51], s88 offen\n"
"buffer_load_dwordx4 a[164:167], v0, s[48:51], s88 offen offset:1024\n"
"s_waitcnt vmcnt(16)\n"
"v_cvt_pk_bf16_f32 v74, v64, v65\n"
".LK3_pad1:\n"
"s_cbranch_execz .LK3_pad5\n"
"v_cvt_pk_bf16_f32 v75, v66, v67\n"
"v_exp_f32_e32 v56, v24\n"
"v_exp_f32_e32 v57, v25\n"
"v_exp_f32_e32 v58, v26\n"
"v_exp_f32_e32 v59, v27\n"
"v_mfma_f32_32x32x16_bf16 a[0:15], a[116:119], v[72:75], a[0:15]\n"
"s_waitcnt vmcnt(6)\n"
"v_cvt_pk_bf16_f32 v68, v56, v57\n"
"v_cvt_pk_bf16_f32 v69, v58, v59\n"
"v_exp_f32_e32 v60, v28\n"
"v_exp_f32_e32 v61, v29\n"
"v_cndmask_b32_e64 v90, 0, v89, s[36:37]\n"
"v_mov_b32_e32 v91, v88\n"
"v_cndmask_b32_e64 v89, 0, v89, s[36:37]\n"
"v_exp_f32_e32 v62, v30\n"
"v_exp_f32_e32 v63, v31\n"
"v_mfma_f32_32x32x8bf16_1k v[40:55], a[46:47], v[82:83], v[40:55]\n"
"v_cvt_pk_bf16_f32 v70, v60, v61\n"
"v_cvt_pk_bf16_f32 v71, v62, v63\n"
"v_exp_f32_e32 v56, v32\n"
"v_exp_f32_e32 v57, v33\n"
"v_exp_f32_e32 v58, v34\n"
"v_exp_f32_e32 v59, v35\n"
"v_mfma_f32_32x32x16_bf16 a[0:15], a[124:127], v[68:71], a[0:15]\n"
"v_cvt_pk_bf16_f32 v72, v56, v57\n"
"v_cvt_pk_bf16_f32 v73, v58, v59\n"
"v_exp_f32_e32 v64, v36\n"
"v_exp_f32_e32 v65, v37\n"
"v_exp_f32_e32 v66, v38\n"
"v_exp_f32_e32 v67, v39\n"
"v_mfma_f32_32x32x16_bf16 v[24:39], v[152:155], v[84:87], 0\n"
"ds_write_b128 v23, v[128:131]\n"
"v_cvt_pk_bf16_f32 v74, v64, v65\n"
"v_cvt_pk_bf16_f32 v75, v66, v67\n"
"v_exp_f32_e32 v56, v40\n"
"v_exp_f32_e32 v57, v41\n"
"v_exp_f32_e32 v58, v42\n"
"v_exp_f32_e32 v59, v43\n"
"v_mfma_f32_32x32x16_bf16 a[0:15], a[128:131], v[72:75], a[0:15]\n"
"buffer_load_dwordx4 a[168:171], v0, s[48:51], s88 offen offset:2048\n"
"buffer_load_dwordx4 a[172:175], v0, s[48:51], s88 offen offset:3072\n"
"v_cvt_pk_bf16_f32 v68, v56, v57\n"
"v_cvt_pk_bf16_f32 v69, v58, v59\n"
"v_exp_f32_e32 v60, v44\n"
"v_exp_f32_e32 v61, v45\n"
"v_exp_f32_e32 v62, v46\n"
"v_exp_f32_e32 v63, v47\n"
"v_mfma_f32_32x32x8bf16_1k v[24:39], a[32:33], v[88:89], v[24:39]\n"
"v_cvt_pk_bf16_f32 v70, v60, v61\n"
"v_cvt_pk_bf16_f32 v71, v62, v63\n"
"v_exp_f32_e32 v56, v48\n"
"v_exp_f32_e32 v57, v49\n"
"v_exp_f32_e32 v58, v50\n"
"v_exp_f32_e32 v59, v51\n"
"v_mfma_f32_32x32x16_bf16 a[0:15], a[136:139], v[68:71], a[0:15]\n"
"s_add_u32 s88, s59, 0x2000\n"
"v_cvt_pk_bf16_f32 v72, v56, v57\n"
"v_cvt_pk_bf16_f32 v73, v58, v59\n"
"v_exp_f32_e32 v64, v52\n"
"v_exp_f32_e32 v65, v53\n"
"v_exp_f32_e32 v66, v54\n"
"v_exp_f32_e32 v67, v55\n"
"v_mfma_f32_32x32x16_bf16 v[40:55], v[164:167], v[84:87], 0\n"
"buffer_load_dwordx4 a[176:179], v0, s[48:51], s88 offen\n"
"buffer_load_dwordx4 a[180:183], v0, s[48:51], s88 offen offset:1024\n"
"v_cvt_pk_bf16_f32 v74, v64, v65\n"
"v_cvt_pk_bf16_f32 v75, v66, v67\n"
"v_exp_f32_e32 v56, v24\n"
"v_exp_f32_e32 v57, v25\n"
"v_exp_f32_e32 v58, v26\n"
"v_exp_f32_e32 v59, v27\n"
"v_mfma_f32_32x32x16_bf16 a[0:15], a[140:143], v[72:75], a[0:15]\n"
"v_cvt_pk_bf16_f32 v68, v56, v57\n"
"v_cvt_pk_bf16_f32 v69, v58, v59\n"
"v_exp_f32_e32 v60, v28\n"
"v_exp_f32_e32 v61, v29\n"
"v_exp_f32_e32 v62, v30\n"
"v_exp_f32_e32 v63, v31\n"
"v_mfma_f32_32x32x8bf16_1k v[40:55], a[32:33], v[90:91], v[40:55]\n"
"v_cvt_pk_bf16_f32 v70, v60, v61\n"
"v_cvt_pk_bf16_f32 v71, v62, v63\n"
"v_exp_f32_e32 v56, v32\n"
"v_exp_f32_e32 v57, v33\n"
"v_exp_f32_e32 v58, v34\n"
"v_exp_f32_e32 v59, v35\n"
"v_mfma_f32_32x32x16_bf16 a[16:31], v[156:159], v[68:71], 0\n"
"ds_write_b128 v8, a[0:3] offset:0\n"
"v_cvt_pk_bf16_f32 v72, v56, v57\n"
"v_cvt_pk_bf16_f32 v73, v58, v59\n"
"v_exp_f32_e32 v64, v36\n"
"v_exp_f32_e32 v65, v37\n"
"v_exp_f32_e32 v66, v38\n"
"v_exp_f32_e32 v67, v39\n"
"v_mfma_f32_32x32x16_bf16 v[24:39], v[176:179], v[84:87], 0\n"
"ds_write_b128 v8, a[4:7] offset:1024\n"
"v_cvt_pk_bf16_f32 v74, v64, v65\n"
"v_cvt_pk_bf16_f32 v75, v66, v67\n"
"v_exp_f32_e32 v56, v40\n"
"v_exp_f32_e32 v57, v41\n"
"v_exp_f32_e32 v58, v42\n"
"v_exp_f32_e32 v59, v43\n"
"v_mfma_f32_32x32x16_bf16 a[16:31], v[160:163], v[72:75], a[16:31]\n"
"ds_write_b128 v8, a[8:11] offset:2048\n"
"v_cvt_pk_bf16_f32 v68, v56, v57\n"
"v_cvt_pk_bf16_f32 v69, v58, v59\n"
"v_exp_f32_e32 v60, v44\n"
"v_exp_f32_e32 v61, v45\n"
"v_exp_f32_e32 v62, v46\n"
"v_exp_f32_e32 v63, v47\n"
"v_mfma_f32_32x32x8bf16_1k v[24:39], a[34:35], v[88:89], v[24:39]\n"
"v_cvt_pk_bf16_f32 v70, v60, v61\n"
"v_cvt_pk_bf16_f32 v71, v62, v63\n"
"v_exp_f32_e32 v56, v48\n"
"v_exp_f32_e32 v57, v49\n"
"v_exp_f32_e32 v58, v50\n"
"v_exp_f32_e32 v59, v51\n"
"v_mfma_f32_32x32x16_bf16 a[16:31], v[168:171], v[68:71], a[16:31]\n"
"s_waitcnt lgkmcnt(0)\n"
"s_barrier\n"
"v_cvt_pk_bf16_f32 v72, v56, v57\n"
"v_cvt_pk_bf16_f32 v73, v58, v59\n"
"v_exp_f32_e32 v64, v52\n"
"v_exp_f32_e32 v65, v53\n"
"v_exp_f32_e32 v66, v54\n"
"v_exp_f32_e32 v67, v55\n"
"v_mfma_f32_32x32x16_bf16 v[40:55], v[188:191], v[84:87], 0\n"
"ds_read_b128 v[112:115], v9 offset:0\n"
"ds_read_b128 v[116:119], v9 offset:3072\n"
"v_cvt_pk_bf16_f32 v74, v64, v65\n"
"v_cvt_pk_bf16_f32 v75, v66, v67\n"
"v_exp_f32_e32 v56, v24\n"
"v_exp_f32_e32 v57, v25\n"
"v_exp_f32_e32 v58, v26\n"
"v_exp_f32_e32 v59, v27\n"
"v_mfma_f32_32x32x16_bf16 a[16:31], v[172:175], v[72:75], a[16:31]\n"
"ds_read_b128 v[120:123], v9 offset:6144\n"
"ds_read_b128 v[124:127], v9 offset:9216\n"
"v_cvt_pk_bf16_f32 v68, v56, v57\n"
"v_cvt_pk_bf16_f32 v69, v58, v59\n"
"v_exp_f32_e32 v60, v28\n"
"v_exp_f32_e32 v61, v29\n"
"v_exp_f32_e32 v62, v30\n"
"v_exp_f32_e32 v63, v31\n"
"v_mfma_f32_32x32x8bf16_1k v[40:55], a[34:35], v[90:91], v[40:55]\n"
"v_cvt_pk_bf16_f32 v70, v60, v61\n"
"v_cvt_pk_bf16_f32 v71, v62, v63\n"
"v_exp_f32_e32 v56, v32\n"
"v_exp_f32_e32 v57, v33\n"
"v_exp_f32_e32 v58, v34\n"
"v_exp_f32_e32 v59, v35\n"
"v_mfma_f32_32x32x16_bf16 a[16:31], v[180:183], v[68:71], a[16:31]\n"
"s_waitcnt lgkmcnt(0)\n"
"v_cvt_pk_bf16_f32 v72, v56, v57\n"
"v_cvt_pk_bf16_f32 v73, v58, v59\n"
"v_exp_f32_e32 v64, v36\n"
"v_exp_f32_e32 v65, v37\n"
"v_add_f32_e32 v115, v115, v119\n"
"v_add_f32_e32 v112, v112, v116\n"
"v_add_f32_e32 v113, v113, v117\n"
"v_add_f32_e32 v114, v114, v118\n"
"v_add_f32_e32 v123, v123, v127\n"
"v_add_f32_e32 v120, v120, v124\n"
"v_exp_f32_e32 v66, v38\n"
"v_exp_f32_e32 v67, v39\n"
"v_mfma_f32_32x32x16_bf16 v[24:39], v[200:203], v[84:87], 0\n"
"buffer_load_dwordx4 a[184:187], v0, s[48:51], s88 offen offset:2048\n"
"buffer_load_dwordx4 a[188:191], v0, s[48:51], s88 offen offset:3072\n"
"v_cvt_pk_bf16_f32 v74, v64, v65\n"
"v_cvt_pk_bf16_f32 v75, v66, v67\n"
"v_exp_f32_e32 v56, v40\n"
"v_exp_f32_e32 v57, v41\n"
"v_add_f32_e32 v121, v121, v125\n"
"v_add_f32_e32 v122, v122, v126\n"
"v_add_f32_e32 v115, v115, v123\n"
"v_rcp_f32_e32 v115, v115\n"
"v_add_f32_e32 v112, v112, v120\n"
"v_add_f32_e32 v113, v113, v121\n"
"v_exp_f32_e32 v58, v42\n"
"v_exp_f32_e32 v59, v43\n"
"v_mfma_f32_32x32x16_bf16 a[16:31], v[184:187], v[72:75], a[16:31]\n"
"s_add_u32 s88, s59, 0x3000\n"
"v_cvt_pk_bf16_f32 v68, v56, v57\n"
"v_cvt_pk_bf16_f32 v69, v58, v59\n"
"v_exp_f32_e32 v60, v44\n"
"v_exp_f32_e32 v61, v45\n"
"v_add_f32_e32 v114, v114, v122\n"
"v_mul_f32_e32 v112, v112, v115\n"
"v_mul_f32_e32 v113, v113, v115\n"
"v_mul_f32_e32 v114, v114, v115\n"
"v_cvt_pk_bf16_f32 v112, v112, v113\n"
"v_cvt_pk_bf16_f32 v114, v114, v114\n"
"v_exp_f32_e32 v62, v46\n"
"v_exp_f32_e32 v63, v47\n"
"v_mfma_f32_32x32x8bf16_1k v[24:39], a[36:37], v[88:89], v[24:39]\n"
"v_cvt_pk_bf16_f32 v70, v60, v61\n"
"v_cvt_pk_bf16_f32 v71, v62, v63\n"
"v_exp_f32_e32 v56, v48\n"
"v_exp_f32_e32 v57, v49\n"
"v_exp_f32_e32 v58, v50\n"
"v_exp_f32_e32 v59, v51\n"
"v_mfma_f32_32x32x16_bf16 a[16:31], v[192:195], v[68:71], a[16:31]\n"
"ds_write_b32 v10, v112 offset:0\n"
"ds_write_b16 v11, v114 offset:0\n"
"v_cvt_pk_bf16_f32 v72, v56, v57\n"
"v_cvt_pk_bf16_f32 v73, v58, v59\n"
"v_exp_f32_e32 v64, v52\n"
"v_exp_f32_e32 v65, v53\n"
"v_exp_f32_e32 v66, v54\n"
"v_exp_f32_e32 v67, v55\n"
"v_mfma_f32_32x32x16_bf16 v[40:55], v[212:215], v[84:87], 0\n"
"ds_read_b128 v[136:139], v0 offset:36864\n"
"v_cvt_pk_bf16_f32 v74, v64, v65\n"
"v_cvt_pk_bf16_f32 v75, v66, v67\n"
"v_exp_f32_e32 v56, v24\n"
"v_exp_f32_e32 v57, v25\n"
"v_exp_f32_e32 v58, v26\n"
"v_exp_f32_e32 v59, v27\n"
"v_mfma_f32_32x32x16_bf16 a[16:31], v[196:199], v[72:75], a[16:31]\n"
"s_waitcnt lgkmcnt(1)\n"
"s_barrier\n"
"v_cvt_pk_bf16_f32 v68, v56, v57\n"
"v_cvt_pk_bf16_f32 v69, v58, v59\n"
"v_exp_f32_e32 v60, v28\n"
"v_exp_f32_e32 v61, v29\n"
"v_exp_f32_e32 v62, v30\n"
"v_exp_f32_e32 v63, v31\n"
"v_mfma_f32_32x32x8bf16_1k v[40:55], a[36:37], v[90:91], v[40:55]\n"
"v_cvt_pk_bf16_f32 v70, v60, v61\n"
"v_cvt_pk_bf16_f32 v71, v62, v63\n"
"v_exp_f32_e32 v56, v32\n"
"v_exp_f32_e32 v57, v33\n"
"v_exp_f32_e32 v58, v34\n"
"v_exp_f32_e32 v59, v35\n"
"v_mfma_f32_32x32x16_bf16 a[16:31], v[204:207], v[68:71], a[16:31]\n"
"s_waitcnt lgkmcnt(0)\n"
"v_cvt_pk_bf16_f32 v72, v56, v57\n"
"v_cvt_pk_bf16_f32 v73, v58, v59\n"
"v_exp_f32_e32 v64, v36\n"
"v_exp_f32_e32 v65, v37\n"
"v_exp_f32_e32 v66, v38\n"
"v_exp_f32_e32 v67, v39\n"
"v_mfma_f32_32x32x16_bf16 v[24:39], v[224:227], v[84:87], 0\n"
"buffer_load_dwordx4 v[76:79], v3, s[48:51], s55 offen offset:288\n"
"buffer_load_dwordx2 v[80:81], v7, s[48:51], s55 offen offset:288\n"
"v_cvt_pk_bf16_f32 v74, v64, v65\n"
"v_cvt_pk_bf16_f32 v75, v66, v67\n"
"v_exp_f32_e32 v56, v40\n"
"v_exp_f32_e32 v57, v41\n"
"v_max3_u32 v136, v136, v137, v138\n"
"v_exp_f32_e32 v58, v42\n"
"v_exp_f32_e32 v59, v43\n"
"v_mfma_f32_32x32x16_bf16 a[16:31], v[208:211], v[72:75], a[16:31]\n"
"buffer_load_dwordx4 a[192:195], v0, s[48:51], s88 offen\n"
"buffer_load_dwordx4 a[196:199], v0, s[48:51], s88 offen offset:1024\n"
"v_cvt_pk_bf16_f32 v68, v56, v57\n"
"v_cvt_pk_bf16_f32 v69, v58, v59\n"
"v_exp_f32_e32 v60, v44\n"
"v_exp_f32_e32 v61, v45\n"
"v_max_u32_e32 v136, v136, v139\n"
"v_exp_f32_e32 v62, v46\n"
"v_exp_f32_e32 v63, v47\n"
"v_mfma_f32_32x32x8bf16_1k v[24:39], a[38:39], v[88:89], v[24:39]\n"
"v_cvt_pk_bf16_f32 v70, v60, v61\n"
"v_cvt_pk_bf16_f32 v71, v62, v63\n"
"v_exp_f32_e32 v56, v48\n"
"v_exp_f32_e32 v57, v49\n"
"v_mul_f32_e32 v136, s56, v136\n"
"v_cmp_nge_f32_e32 vcc, 0x45610000, v136\n"
"v_exp_f32_e32 v58, v50\n"
"v_exp_f32_e32 v59, v51\n"
"v_mfma_f32_32x32x16_bf16 a[16:31], v[216:219], v[68:71], a[16:31]\n"
"v_cvt_pk_bf16_f32 v72, v56, v57\n"
"v_cvt_pk_bf16_f32 v73, v58, v59\n"
"v_exp_f32_e32 v64, v52\n"
"v_exp_f32_e32 v65, v53\n"
"s_and_b64 vcc, vcc, s[38:39]\n"
"s_cbranch_scc1 .LK3_bad_%=\n"
"s_mov_b32 s79, 0x20000\n"
"v_exp_f32_e32 v66, v54\n"
"v_exp_f32_e32 v67, v55\n"
"v_mfma_f32_32x32x16_bf16 v[40:55], v[236:239], v[84:87], 0\n"
"buffer_load_dwordx4 a[200:203], v0, s[48:51], s88 offen offset:2048\n"
"buffer_load_dwordx4 a[204:207], v0, s[48:51], s88 offen offset:3072\n"
"v_cvt_pk_bf16_f32 v74, v64, v65\n"
"v_cvt_pk_bf16_f32 v75, v66, v67\n"
"v_exp_f32_e32 v56, v24\n"
"v_exp_f32_e32 v57, v25\n"
"v_exp_f32_e32 v58, v26\n"
"v_exp_f32_e32 v59, v27\n"
"v_mfma_f32_32x32x16_bf16 a[16:31], v[220:223], v[72:75], a[16:31]\n"
"s_add_u32 s88, s59, 0x4000\n"
"v_cvt_pk_bf16_f32 v68, v56, v57\n"
"v_cvt_pk_bf16_f32 v69, v58, v59\n"
"v_exp_f32_e32 v60, v28\n"
"v_exp_f32_e32 v61, v29\n"
"v_exp_f32_e32 v62, v30\n"
"v_exp_f32_e32 v63, v31\n"
"v_mfma_f32_32x32x8bf16_1k v[40:55], a[38:39], v[90:91], v[40:55]\n"
"v_cvt_pk_bf16_f32 v70, v60, v61\n"
"v_cvt_pk_bf16_f32 v71, v62, v63\n"
"v_exp_f32_e32 v56, v32\n"
"v_exp_f32_e32 v57, v33\n"
"v_exp_f32_e32 v58, v34\n"
"v_exp_f32_e32 v59, v35\n"
"v_mfma_f32_32x32x16_bf16 a[16:31], v[228:231], v[68:71], a[16:31]\n"
"buffer_load_dword v6, v22, s[76:79], s69 offen\n"
"s_add_u32 s69, s69, 0x2400\n"
"v_cvt_pk_bf16_f32 v72, v56, v57\n"
"v_cvt_pk_bf16_f32 v73, v58, v59\n"
"v_exp_f32_e32 v64, v36\n"
"v_exp_f32_e32 v65, v37\n"
"v_exp_f32_e32 v66, v38\n"
"v_exp_f32_e32 v67, v39\n"
"v_mfma_f32_32x32x16_bf16 v[24:39], a[48:51], v[84:87], 0\n"
"buffer_load_dwordx4 a[208:211], v0, s[48:51], s88 offen\n"
"buffer_load_dwordx4 a[212:215], v0, s[48:51], s88 offen offset:1024\n"
"v_cvt_pk_bf16_f32 v74, v64, v65\n"
"v_cvt_pk_bf16_f32 v75, v66, v67\n"
"v_exp_f32_e32 v56, v40\n"
"v_exp_f32_e32 v57, v41\n"
"v_exp_f32_e32 v58, v42\n"
"v_exp_f32_e32 v59, v43\n"
"v_mfma_f32_32x32x16_bf16 a[16:31], v[232:235], v[72:75], a[16:31]\n"
"ds_read_b128 v[136:139], v12 offset:0\n"
"ds_read_b128 v[140:143], v13 offset:0\n"
"v_cvt_pk_bf16_f32 v68, v56, v57\n"
"v_cvt_pk_bf16_f32 v69, v58, v59\n"
"v_exp_f32_e32 v60, v44\n"
"v_exp_f32_e32 v61, v45\n"
"v_exp_f32_e32 v62, v46\n"
"v_exp_f32_e32 v63, v47\n"
"v_mfma_f32_32x32x8bf16_1k v[24:39], a[40:41], v[88:89], v[24:39]\n"
"v_cvt_pk_bf16_f32 v70, v60, v61\n"
"v_cvt_pk_bf16_f32 v71, v62, v63\n"
"v_exp_f32_e32 v56, v48\n"
"v_exp_f32_e32 v57, v49\n"
"v_exp_f32_e32 v58, v50\n"
"v_exp_f32_e32 v59, v51\n"
"v_mfma_f32_32x32x16_bf16 a[16:31], v[240:243], v[68:71], a[16:31]\n"
"v_cvt_pk_bf16_f32 v72, v56, v57\n"
"v_cvt_pk_bf16_f32 v73, v58, v59\n"
"v_exp_f32_e32 v64, v52\n"
"v_exp_f32_e32 v65, v53\n"
"v_exp_f32_e32 v66, v54\n"
"v_exp_f32_e32 v67, v55\n"
"v_mfma_f32_32x32x16_bf16 v[40:55], a[60:63], v[84:87], 0\n"
"s_waitcnt vmcnt(12) lgkmcnt(0)\n"
"v_cvt_pk_bf16_f32 v74, v64, v65\n"
"v_cvt_pk_bf16_f32 v75, v66, v67\n"
"v_exp_f32_e32 v56, v24\n"
"v_exp_f32_e32 v57, v25\n"
"v_mfma_f32_16x16x32_bf16 v[92:95], a[144:147], v[136:139], 0\n"
"v_exp_f32_e32 v58, v26\n"
"v_exp_f32_e32 v59, v27\n"
"v_mfma_f32_32x32x16_bf16 a[16:31], v[244:247], v[72:75], a[16:31]\n"
"buffer_load_dwordx4 a[216:219], v0, s[48:51], s88 offen offset:2048\n"
"buffer_load_dwordx4 a[220:223], v0, s[48:51], s88 offen offset:3072\n"
"v_cvt_pk_bf16_f32 v68, v56, v57\n"
"v_cvt_pk_bf16_f32 v69, v58, v59\n"
"v_exp_f32_e32 v60, v28\n"
"v_exp_f32_e32 v61, v29\n"
"v_exp_f32_e32 v62, v30\n"
"v_exp_f32_e32 v63, v31\n"
"v_mfma_f32_32x32x8bf16_1k v[40:55], a[40:41], v[90:91], v[40:55]\n"
"v_cvt_pk_bf16_f32 v70, v60, v61\n"
"v_cvt_pk_bf16_f32 v71, v62, v63\n"
"v_exp_f32_e32 v56, v32\n"
"v_exp_f32_e32 v57, v33\n"
"v_exp_f32_e32 v58, v34\n"
"v_exp_f32_e32 v59, v35\n"
"v_mfma_f32_32x32x16_bf16 a[16:31], a[52:55], v[68:71], a[16:31]\n"
"s_add_u32 s88, s59, 0x5000\n"
"v_cvt_pk_bf16_f32 v72, v56, v57\n"
"v_cvt_pk_bf16_f32 v73, v58, v59\n"
"v_exp_f32_e32 v64, v36\n"
"v_exp_f32_e32 v65, v37\n"
"v_mfma_f32_16x16x32_bf16 v[96:99], a[148:151], v[136:139], 0\n"
"v_exp_f32_e32 v66, v38\n"
"v_exp_f32_e32 v67, v39\n"
"v_mfma_f32_32x32x16_bf16 v[24:39], a[72:75], v[84:87], 0\n"
"buffer_load_dwordx4 a[224:227], v0, s[48:51], s88 offen\n"
"buffer_load_dwordx4 a[228:231], v0, s[48:51], s88 offen offset:1024\n"
"v_cvt_pk_bf16_f32 v74, v64, v65\n"
"v_cvt_pk_bf16_f32 v75, v66, v67\n"
"v_exp_f32_e32 v56, v40\n"
"v_exp_f32_e32 v57, v41\n"
"v_mfma_f32_16x16x32_bf16 v[100:103], a[152:155], v[136:139], 0\n"
"v_exp_f32_e32 v58, v42\n"
"v_exp_f32_e32 v59, v43\n"
"v_mfma_f32_32x32x16_bf16 a[16:31], a[56:59], v[72:75], a[16:31]\n"
"v_cvt_pk_bf16_f32 v68, v56, v57\n"
"v_cvt_pk_bf16_f32 v69, v58, v59\n"
"v_exp_f32_e32 v60, v44\n"
"v_exp_f32_e32 v61, v45\n"
"v_exp_f32_e32 v62, v46\n"
"v_exp_f32_e32 v63, v47\n"
"v_mfma_f32_32x32x8bf16_1k v[24:39], a[42:43], v[88:89], v[24:39]\n"
"v_cvt_pk_bf16_f32 v70, v60, v61\n"
"v_cvt_pk_bf16_f32 v71, v62, v63\n"
"v_exp_f32_e32 v56, v48\n"
"v_exp_f32_e32 v57, v49\n"
"v_exp_f32_e32 v58, v50\n"
"v_exp_f32_e32 v59, v51\n"
"v_mfma_f32_32x32x16_bf16 a[16:31], a[64:67], v[68:71], a[16:31]\n"
"v_cvt_pk_bf16_f32 v72, v56, v57\n"
"v_cvt_pk_bf16_f32 v73, v58, v59\n"
"v_exp_f32_e32 v64, v52\n"
"v_exp_f32_e32 v65, v53\n"
"v_mfma_f32_16x16x32_bf16 v[104:107], a[156:159], v[136:139], 0\n"
"v_exp_f32_e32 v66, v54\n"
"v_exp_f32_e32 v67, v55\n"
"v_mfma_f32_32x32x16_bf16 v[40:55], a[84:87], v[84:87], 0\n"
"buffer_load_dwordx4 a[232:235], v0, s[48:51], s88 offen offset:2048\n"
"buffer_load_dwordx4 a[236:239], v0, s[48:51], s88 offen offset:3072\n"
"v_cvt_pk_bf16_f32 v74, v64, v65\n"
"v_cvt_pk_bf16_f32 v75, v66, v67\n"
"v_exp_f32_e32 v56, v24\n"
"v_exp_f32_e32 v57, v25\n"
"v_mfma_f32_16x16x32_bf16 v[108:111], a[160:163], v[136:139], 0\n"
"v_exp_f32_e32 v58, v26\n"
"v_exp_f32_e32 v59, v27\n"
"v_mfma_f32_32x32x16_bf16 a[16:31], a[68:71], v[72:75], a[16:31]\n"
"s_add_u32 s88, s59, 0x6000\n"
"v_cvt_pk_bf16_f32 v68, v56, v57\n"
"v_cvt_pk_bf16_f32 v69, v58, v59\n"
"v_exp_f32_e32 v60, v28\n"
"v_exp_f32_e32 v61, v29\n"
"v_exp_f32_e32 v62, v30\n"
"v_exp_f32_e32 v63, v31\n"
"v_mfma_f32_32x32x8bf16_1k v[40:55], a[42:43], v[90:91], v[40:55]\n"
"v_cvt_pk_bf16_f32 v70, v60, v61\n"
"v_cvt_pk_bf16_f32 v71, v62, v63\n"
"v_exp_f32_e32 v56, v32\n"
"v_exp_f32_e32 v57, v33\n"
"v_exp_f32_e32 v58, v34\n"
"v_exp_f32_e32 v59, v35\n"
"v_mfma_f32_32x32x16_bf16 a[16:31], a[76:79], v[68:71], a[16:31]\n"
"v_cvt_pk_bf16_f32 v72, v56, v57\n"
"v_cvt_pk_bf16_f32 v73, v58, v59\n"
"v_exp_f32_e32 v64, v36\n"
"v_exp_f32_e32 v65, v37\n"
"v_mfma_f32_16x16x32_bf16 v[92:95], a[164:167], v[140:143], v[92:95]\n"
"v_exp_f32_e32 v66, v38\n"
"v_exp_f32_e32 v67, v39\n"
"v_mfma_f32_32x32x16_bf16 v[24:39], a[96:99], v[84:87], 0\n"
"buffer_load_dwordx4 a[240:243], v0, s[48:51], s88 offen\n"
"buffer_load_dwordx4 a[244:247], v0, s[48:51], s88 offen offset:1024\n"
"v_cvt_pk_bf16_f32 v74, v64, v65\n"
"v_cvt_pk_bf16_f32 v75, v66, v67\n"
"v_exp_f32_e32 v56, v40\n"
"v_exp_f32_e32 v57, v41\n"
"v_mfma_f32_16x16x32_bf16 v[96:99], a[168:171], v[140:143], v[96:99]\n"
"v_exp_f32_e32 v58, v42\n"
"v_exp_f32_e32 v59, v43\n"
"v_mfma_f32_32x32x16_bf16 a[16:31], a[80:83], v[72:75], a[16:31]\n"
"v_cvt_pk_bf16_f32 v68, v56, v57\n"
"v_cvt_pk_bf16_f32 v69, v58, v59\n"
"v_exp_f32_e32 v60, v44\n"
"v_exp_f32_e32 v61, v45\n"
"v_exp_f32_e32 v62, v46\n"
"v_exp_f32_e32 v63, v47\n"
"v_mfma_f32_32x32x8bf16_1k v[24:39], a[44:45], v[88:89], v[24:39]\n"
"v_cvt_pk_bf16_f32 v70, v60, v61\n"
"v_cvt_pk_bf16_f32 v71, v62, v63\n"
"v_exp_f32_e32 v56, v48\n"
"v_exp_f32_e32 v57, v49\n"
"v_exp_f32_e32 v58, v50\n"
"v_exp_f32_e32 v59, v51\n"
"v_mfma_f32_32x32x16_bf16 a[16:31], a[88:91], v[68:71], a[16:31]\n"
"v_cvt_pk_bf16_f32 v72, v56, v57\n"
"v_cvt_pk_bf16_f32 v73, v58, v59\n"
"v_exp_f32_e32 v64, v52\n"
"v_exp_f32_e32 v65, v53\n"
"v_mfma_f32_16x16x32_bf16 v[100:103], a[172:175], v[140:143], v[100:103]\n"
"v_exp_f32_e32 v66, v54\n"
"v_exp_f32_e32 v67, v55\n"
"v_mfma_f32_32x32x16_bf16 v[40:55], a[108:111], v[84:87], 0\n"
"ds_read_b128 v[144:147], v12 offset:64\n"
"ds_read_b128 v[148:151], v13 offset:64\n"
"v_cvt_pk_bf16_f32 v74, v64, v65\n"
"v_cvt_pk_bf16_f32 v75, v66, v67\n"
"v_exp_f32_e32 v56, v24\n"
"v_exp_f32_e32 v57, v25\n"
"v_mfma_f32_16x16x32_bf16 v[104:107], a[176:179], v[140:143], v[104:107]\n"
"v_exp_f32_e32 v58, v26\n"
"v_exp_f32_e32 v59, v27\n"
"v_mfma_f32_32x32x16_bf16 a[16:31], a[92:95], v[72:75], a[16:31]\n"
"buffer_load_dwordx4 a[248:251], v0, s[48:51], s88 offen offset:2048\n"
"buffer_load_dwordx4 a[252:255], v0, s[48:51], s88 offen offset:3072\n"
"v_cvt_pk_bf16_f32 v68, v56, v57\n"
"v_cvt_pk_bf16_f32 v69, v58, v59\n"
"v_exp_f32_e32 v60, v28\n"
"v_exp_f32_e32 v61, v29\n"
"v_exp_f32_e32 v62, v30\n"
"v_exp_f32_e32 v63, v31\n"
"v_mfma_f32_32x32x8bf16_1k v[40:55], a[44:45], v[90:91], v[40:55]\n"
"v_cvt_pk_bf16_f32 v70, v60, v61\n"
"v_cvt_pk_bf16_f32 v71, v62, v63\n"
"v_exp_f32_e32 v56, v32\n"
"v_exp_f32_e32 v57, v33\n"
"v_exp_f32_e32 v58, v34\n"
"v_exp_f32_e32 v59, v35\n"
"v_mfma_f32_32x32x16_bf16 a[16:31], a[100:103], v[68:71], a[16:31]\n"
"s_waitcnt vmcnt(10) lgkmcnt(0)\n"
"v_cvt_pk_bf16_f32 v72, v56, v57\n"
"v_cvt_pk_bf16_f32 v73, v58, v59\n"
"v_exp_f32_e32 v64, v36\n"
"v_exp_f32_e32 v65, v37\n"
"v_mfma_f32_16x16x32_bf16 v[92:95], a[180:183], v[144:147], v[92:95]\n"
"v_exp_f32_e32 v66, v38\n"
"v_exp_f32_e32 v67, v39\n"
"v_mfma_f32_32x32x16_bf16 v[24:39], a[120:123], v[84:87], 0\n"
"s_add_u32 s88, s59, 0x7000\n"
"v_cvt_pk_bf16_f32 v74, v64, v65\n"
"v_cvt_pk_bf16_f32 v75, v66, v67\n"
"v_exp_f32_e32 v56, v40\n"
"v_exp_f32_e32 v57, v41\n"
"v_mfma_f32_16x16x32_bf16 v[96:99], a[184:187], v[144:147], v[96:99]\n"
"v_exp_f32_e32 v58, v42\n"
"v_exp_f32_e32 v59, v43\n"
"v_mfma_f32_32x32x16_bf16 a[16:31], a[104:107], v[72:75], a[16:31]\n"
"buffer_load_dwordx4 v[248:251], v0, s[48:51], s88 offen\n"
"buffer_load_dwordx4 v[252:255], v0, s[48:51], s88 offen offset:1024\n"
"v_cvt_pk_bf16_f32 v68, v56, v57\n"
"v_cvt_pk_bf16_f32 v69, v58, v59\n"
"v_exp_f32_e32 v60, v44\n"
"v_exp_f32_e32 v61, v45\n"
"v_exp_f32_e32 v62, v46\n"
"v_exp_f32_e32 v63, v47\n"
"v_mfma_f32_32x32x8bf16_1k v[24:39], a[46:47], v[88:89], v[24:39]\n"
"v_cvt_pk_bf16_f32 v70, v60, v61\n"
"v_cvt_pk_bf16_f32 v71, v62, v63\n"
"v_exp_f32_e32 v56, v48\n"
"v_exp_f32_e32 v57, v49\n"
"v_exp_f32_e32 v58, v50\n"
"v_exp_f32_e32 v59, v51\n"
"v_mfma_f32_32x32x16_bf16 a[16:31], a[112:115], v[68:71], a[16:31]\n"
"v_cvt_pk_bf16_f32 v72, v56, v57\n"
"v_cvt_pk_bf16_f32 v73, v58, v59\n"
"v_exp_f32_e32 v64, v52\n"
"v_exp_f32_e32 v65, v53\n"
"v_mfma_f32_16x16x32_bf16 v[100:103], a[188:191], v[144:147], v[100:103]\n"
"v_exp_f32_e32 v66, v54\n"
"v_exp_f32_e32 v67, v55\n"
"v_mfma_f32_32x32x16_bf16 v[40:55], a[132:135], v[84:87], 0\n"
"buffer_load_dwordx4 a[144:147], v0, s[48:51], s88 offen offset:2048\n"
"buffer_load_dwordx4 a[148:151], v0, s[48:51], s88 offen offset:3072\n"
"v_cvt_pk_bf16_f32 v74, v64, v65\n"
"v_cvt_pk_bf16_f32 v75, v66, v67\n"
"v_exp_f32_e32 v56, v24\n"
"v_exp_f32_e32 v57, v25\n"
"v_mfma_f32_16x16x32_bf16 v[104:107], a[192:195], v[144:147], v[104:107]\n"
"v_lshrrev_b32_e32 v128, 5, v0\n"
"v_exp_f32_e32 v58, v26\n"
"v_exp_f32_e32 v59, v27\n"
"v_mfma_f32_32x32x16_bf16 a[16:31], a[116:119], v[72:75], a[16:31]\n"
"s_add_u32 s88, s59, 0x8000\n"
"v_cvt_pk_bf16_f32 v68, v56, v57\n"
"v_cvt_pk_bf16_f32 v69, v58, v59\n"
"v_exp_f32_e32 v60, v28\n"
"v_exp_f32_e32 v61, v29\n"
"v_cndmask_b32_e64 v82, 0, v81, s[36:37]\n"
"v_mov_b32_e32 v83, v80\n"
"v_cndmask_b32_e64 v81, 0, v81, s[36:37]\n"
"v_exp_f32_e32 v62, v30\n"
"v_exp_f32_e32 v63, v31\n"
"v_mfma_f32_32x32x8bf16_1k v[40:55], a[46:47], v[90:91], v[40:55]\n"
"s_and_b32 s16, s84, 31\n"
"s_lshl_b32 s16, s16, 8\n"
"v_cvt_pk_bf16_f32 v70, v60, v61\n"
"v_cvt_pk_bf16_f32 v71, v62, v63\n"
"v_exp_f32_e32 v56, v32\n"
"v_exp_f32_e32 v57, v33\n"
"v_and_b32_e32 v128, 24, v128\n"
"v_exp_f32_e32 v58, v34\n"
"v_exp_f32_e32 v59, v35\n"
"v_mfma_f32_32x32x16_bf16 a[16:31], a[124:127], v[68:71], a[16:31]\n"
"v_cvt_pk_bf16_f32 v72, v56, v57\n"
"v_cvt_pk_bf16_f32 v73, v58, v59\n"
"v_exp_f32_e32 v64, v36\n"
"v_exp_f32_e32 v65, v37\n"
"v_mfma_f32_16x16x32_bf16 v[108:111], a[196:199], v[144:147], v[108:111]\n"
"v_exp_f32_e32 v66, v38\n"
"v_exp_f32_e32 v67, v39\n"
"v_mfma_f32_32x32x16_bf16 v[24:39], v[152:155], v[76:79], 0\n"
"buffer_load_dwordx4 a[152:155], v0, s[48:51], s88 offen\n"
"buffer_load_dwordx4 a[156:159], v0, s[48:51], s88 offen offset:1024\n"
"v_cvt_pk_bf16_f32 v74, v64, v65\n"
"v_cvt_pk_bf16_f32 v75, v66, v67\n"
"v_exp_f32_e32 v56, v40\n"
"v_exp_f32_e32 v57, v41\n"
"v_mfma_f32_16x16x32_bf16 v[92:95], a[200:203], v[148:151], v[92:95]\n"
"v_bfe_u32 v129, v0, 7, 1\n"
"v_exp_f32_e32 v58, v42\n"
"v_exp_f32_e32 v59, v43\n"
"v_mfma_f32_32x32x16_bf16 a[16:31], a[128:131], v[72:75], a[16:31]\n"
"v_cvt_pk_bf16_f32 v68, v56, v57\n"
"v_cvt_pk_bf16_f32 v69, v58, v59\n"
"v_exp_f32_e32 v60, v44\n"
"v_exp_f32_e32 v61, v45\n"
"v_exp_f32_e32 v62, v46\n"
"v_exp_f32_e32 v63, v47\n"
"v_mfma_f32_32x32x8bf16_1k v[24:39], a[32:33], v[80:81], v[24:39]\n"
"s_mul_i32 s14, s83, 0x9000\n"
"s_add_u32 s16, s16, s14\n"
"v_cvt_pk_bf16_f32 v70, v60, v61\n"
"v_cvt_pk_bf16_f32 v71, v62, v63\n"
"v_exp_f32_e32 v56, v48\n"
"v_exp_f32_e32 v57, v49\n"
"v_or_b32_e32 v128, v128, v129\n"
"v_exp_f32_e32 v58, v50\n"
"v_exp_f32_e32 v59, v51\n"
"v_mfma_f32_32x32x16_bf16 a[16:31], a[136:139], v[68:71], a[16:31]\n"
"buffer_load_dword v6, v22, s[76:79], s69 offen\n"
"s_add_u32 s69, s69, 0x2400\n"
"v_cvt_pk_bf16_f32 v72, v56, v57\n"
"v_cvt_pk_bf16_f32 v73, v58, v59\n"
"v_exp_f32_e32 v64, v52\n"
"v_exp_f32_e32 v65, v53\n"
"v_mfma_f32_16x16x32_bf16 v[96:99], a[204:207], v[148:151], v[96:99]\n"
"v_exp_f32_e32 v66, v54\n"
"v_exp_f32_e32 v67, v55\n"
"v_mfma_f32_32x32x16_bf16 v[40:55], v[164:167], v[76:79], 0\n"
"buffer_load_dwordx4 a[160:163], v0, s[48:51], s88 offen offset:2048\n"
"buffer_load_dwordx4 a[164:167], v0, s[48:51], s88 offen offset:3072\n"
"v_cvt_pk_bf16_f32 v74, v64, v65\n"
"v_cvt_pk_bf16_f32 v75, v66, v67\n"
"v_exp_f32_e32 v56, v24\n"
"v_exp_f32_e32 v57, v25\n"
"v_mfma_f32_16x16x32_bf16 v[100:103], a[208:211], v[148:151], v[100:103]\n"
"v_and_b32_e32 v129, 0x70, v0\n"
"v_exp_f32_e32 v58, v26\n"
"v_exp_f32_e32 v59, v27\n"
"v_mfma_f32_32x32x16_bf16 a[16:31], a[140:143], v[72:75], a[16:31]\n"
"s_add_u32 s88, s59, 0x9000\n"
"v_cvt_pk_bf16_f32 v68, v56, v57\n"
"v_cvt_pk_bf16_f32 v69, v58, v59\n"
"v_exp_f32_e32 v60, v28\n"
"v_exp_f32_e32 v61, v29\n"
"v_exp_f32_e32 v62, v30\n"
"v_exp_f32_e32 v63, v31\n"
"v_mfma_f32_32x32x8bf16_1k v[40:55], a[32:33], v[82:83], v[40:55]\n"
"s_mul_i32 s17, s80, 0x840\n"
"s_lshr_b32 s15, s84, 5\n"
"v_cvt_pk_bf16_f32 v70, v60, v61\n"
"v_cvt_pk_bf16_f32 v71, v62, v63\n"
"v_exp_f32_e32 v56, v32\n"
"v_exp_f32_e32 v57, v33\n"
"v_mul_u32_u24_e32 v1, 0x2400, v128\n"
"v_exp_f32_e32 v58, v34\n"
"v_exp_f32_e32 v59, v35\n"
"v_mfma_f32_32x32x16_bf16 a[0:15], v[156:159], v[68:71], 0\n"
"ds_write_b128 v8, a[16:19] offset:12288\n"
"v_cvt_pk_bf16_f32 v72, v56, v57\n"
"v_cvt_pk_bf16_f32 v73, v58, v59\n"
"v_exp_f32_e32 v64, v36\n"
"v_exp_f32_e32 v65, v37\n"
"v_mfma_f32_16x16x32_bf16 v[104:107], a[212:215], v[148:151], v[104:107]\n"
"v_exp_f32_e32 v66, v38\n"
"v_exp_f32_e32 v67, v39\n"
"v_mfma_f32_32x32x16_bf16 v[24:39], v[176:179], v[76:79], 0\n"
"ds_write_b128 v8, a[20:23] offset:13312\n"
"v_cvt_pk_bf16_f32 v74, v64, v65\n"
"v_cvt_pk_bf16_f32 v75, v66, v67\n"
"v_exp_f32_e32 v56, v40\n"
"v_exp_f32_e32 v57, v41\n"
"v_add_u32_e32 v1, v1, v129\n"
"v_exp_f32_e32 v58, v42\n"
"v_exp_f32_e32 v59, v43\n"
"v_mfma_f32_32x32x16_bf16 a[0:15], v[160:163], v[72:75], a[0:15]\n"
"ds_write_b128 v8, a[24:27] offset:14336\n"
"v_cvt_pk_bf16_f32 v68, v56, v57\n"
"v_cvt_pk_bf16_f32 v69, v58, v59\n"
"v_exp_f32_e32 v60, v44\n"
"v_exp_f32_e32 v61, v45\n"
"v_exp_f32_e32 v62, v46\n"
"v_exp_f32_e32 v63, v47\n"
"v_mfma_f32_32x32x8bf16_1k v[24:39], a[34:35], v[80:81], v[24:39]\n"
"s_lshl2_add_u32 s15, s15, s80\n"
"s_mul_i32 s15, s15, 0x12000\n"
"v_cvt_pk_bf16_f32 v70, v60, v61\n"
"v_cvt_pk_bf16_f32 v71, v62, v63\n"
"v_exp_f32_e32 v56, v48\n"
"v_exp_f32_e32 v57, v49\n"
"v_add_u32_e32 v2, 0x43e0, v1\n"
"v_exp_f32_e32 v58, v50\n"
"v_exp_f32_e32 v59, v51\n"
"v_mfma_f32_32x32x16_bf16 a[0:15], v[168:171], v[68:71], a[0:15]\n"
"s_waitcnt lgkmcnt(0)\n"
"s_barrier\n"
"v_cvt_pk_bf16_f32 v72, v56, v57\n"
".LK3_pad2:\n"
"s_cbranch_execz .LK3_pad6\n"
"v_cvt_pk_bf16_f32 v73, v58, v59\n"
"v_exp_f32_e32 v64, v52\n"
"v_exp_f32_e32 v65, v53\n"
"v_exp_f32_e32 v66, v54\n"
"v_exp_f32_e32 v67, v55\n"
"v_mfma_f32_32x32x16_bf16 v[40:55], v[188:191], v[76:79], 0\n"
"ds_read_b128 v[112:115], v9 offset:12288\n"
"ds_read_b128 v[116:119], v9 offset:15360\n"
"v_cvt_pk_bf16_f32 v74, v64, v65\n"
"v_cvt_pk_bf16_f32 v75, v66, v67\n"
"v_exp_f32_e32 v56, v24\n"
"v_exp_f32_e32 v57, v25\n"
"v_exp_f32_e32 v58, v26\n"
"v_exp_f32_e32 v59, v27\n"
"v_mfma_f32_32x32x16_bf16 a[0:15], v[172:175], v[72:75], a[0:15]\n"
"ds_read_b128 v[120:123], v9 offset:18432\n"
"ds_read_b128 v[124:127], v9 offset:21504\n"
"v_cvt_pk_bf16_f32 v68, v56, v57\n"
"v_cvt_pk_bf16_f32 v69, v58, v59\n"
"v_exp_f32_e32 v60, v28\n"
"v_exp_f32_e32 v61, v29\n"
"v_exp_f32_e32 v62, v30\n"
"v_exp_f32_e32 v63, v31\n"
"v_mfma_f32_32x32x8bf16_1k v[40:55], a[34:35], v[82:83], v[40:55]\n"
"s_add_u32 s15, s15, 0x7ac400\n"
"v_cvt_pk_bf16_f32 v70, v60, v61\n"
"v_cvt_pk_bf16_f32 v71, v62, v63\n"
"v_exp_f32_e32 v56, v32\n"
"v_exp_f32_e32 v57, v33\n"
"v_exp_f32_e32 v58, v34\n"
"v_exp_f32_e32 v59, v35\n"
"v_mfma_f32_32x32x16_bf16 a[0:15], v[180:183], v[68:71], a[0:15]\n"
"s_waitcnt lgkmcnt(0)\n"
"v_cvt_pk_bf16_f32 v72, v56, v57\n"
"v_cvt_pk_bf16_f32 v73, v58, v59\n"
"v_exp_f32_e32 v64, v36\n"
"v_exp_f32_e32 v65, v37\n"
"v_add_f32_e32 v115, v115, v119\n"
"v_add_f32_e32 v112, v112, v116\n"
"v_add_f32_e32 v113, v113, v117\n"
"v_add_f32_e32 v114, v114, v118\n"
"v_add_f32_e32 v123, v123, v127\n"
"v_add_f32_e32 v120, v120, v124\n"
"v_exp_f32_e32 v66, v38\n"
"v_exp_f32_e32 v67, v39\n"
"v_mfma_f32_32x32x16_bf16 v[24:39], v[200:203], v[76:79], 0\n"
"buffer_load_dwordx4 a[168:171], v0, s[48:51], s88 offen\n"
"buffer_load_dwordx4 a[172:175], v0, s[48:51], s88 offen offset:1024\n"
"v_cvt_pk_bf16_f32 v74, v64, v65\n"
"v_cvt_pk_bf16_f32 v75, v66, v67\n"
"v_exp_f32_e32 v56, v40\n"
"v_exp_f32_e32 v57, v41\n"
"v_add_f32_e32 v121, v121, v125\n"
"v_add_f32_e32 v122, v122, v126\n"
"v_add_f32_e32 v115, v115, v123\n"
"v_rcp_f32_e32 v115, v115\n"
"v_add_f32_e32 v112, v112, v120\n"
"v_add_f32_e32 v113, v113, v121\n"
"v_exp_f32_e32 v58, v42\n"
"v_exp_f32_e32 v59, v43\n"
"v_mfma_f32_32x32x16_bf16 a[0:15], v[184:187], v[72:75], a[0:15]\n"
"v_cvt_pk_bf16_f32 v68, v56, v57\n"
"v_cvt_pk_bf16_f32 v69, v58, v59\n"
"v_exp_f32_e32 v60, v44\n"
"v_exp_f32_e32 v61, v45\n"
"v_add_f32_e32 v114, v114, v122\n"
"v_mul_f32_e32 v112, v112, v115\n"
"v_mul_f32_e32 v113, v113, v115\n"
"v_mul_f32_e32 v114, v114, v115\n"
"v_cvt_pk_bf16_f32 v112, v112, v113\n"
"v_cvt_pk_bf16_f32 v114, v114, v114\n"
"v_exp_f32_e32 v62, v46\n"
"v_exp_f32_e32 v63, v47\n"
"v_mfma_f32_32x32x8bf16_1k v[24:39], a[36:37], v[80:81], v[24:39]\n"
"v_cvt_pk_bf16_f32 v70, v60, v61\n"
"v_cvt_pk_bf16_f32 v71, v62, v63\n"
"v_exp_f32_e32 v56, v48\n"
"v_exp_f32_e32 v57, v49\n"
"v_exp_f32_e32 v58, v50\n"
"v_exp_f32_e32 v59, v51\n"
"v_mfma_f32_32x32x16_bf16 a[0:15], v[192:195], v[68:71], a[0:15]\n"
"ds_write_b32 v10, v112 offset:144\n"
"ds_write_b16 v11, v114 offset:144\n"
"v_cvt_pk_bf16_f32 v72, v56, v57\n"
"v_cvt_pk_bf16_f32 v73, v58, v59\n"
"v_exp_f32_e32 v64, v52\n"
"v_exp_f32_e32 v65, v53\n"
"v_exp_f32_e32 v66, v54\n"
"v_exp_f32_e32 v67, v55\n"
"v_mfma_f32_32x32x16_bf16 v[40:55], v[212:215], v[76:79], 0\n"
"buffer_load_dwordx4 a[176:179], v0, s[48:51], s88 offen offset:2048\n"
"buffer_load_dwordx4 a[180:183], v0, s[48:51], s88 offen offset:3072\n"
"v_cvt_pk_bf16_f32 v74, v64, v65\n"
"v_cvt_pk_bf16_f32 v75, v66, v67\n"
"v_exp_f32_e32 v56, v24\n"
"v_exp_f32_e32 v57, v25\n"
"v_exp_f32_e32 v58, v26\n"
"v_exp_f32_e32 v59, v27\n"
"v_mfma_f32_32x32x16_bf16 a[0:15], v[196:199], v[72:75], a[0:15]\n"
"s_waitcnt lgkmcnt(0)\n"
"s_barrier\n"
"v_cvt_pk_bf16_f32 v68, v56, v57\n"
"v_cvt_pk_bf16_f32 v69, v58, v59\n"
"v_exp_f32_e32 v60, v28\n"
"v_exp_f32_e32 v61, v29\n"
"v_exp_f32_e32 v62, v30\n"
"v_exp_f32_e32 v63, v31\n"
"v_mfma_f32_32x32x8bf16_1k v[40:55], a[36:37], v[82:83], v[40:55]\n"
"v_cvt_pk_bf16_f32 v70, v60, v61\n"
"v_cvt_pk_bf16_f32 v71, v62, v63\n"
"v_exp_f32_e32 v56, v32\n"
"v_exp_f32_e32 v57, v33\n"
"v_exp_f32_e32 v58, v34\n"
"v_exp_f32_e32 v59, v35\n"
"v_mfma_f32_32x32x16_bf16 a[0:15], v[204:207], v[68:71], a[0:15]\n"
"ds_read_b128 v[136:139], v12 offset:128\n"
"ds_read_b128 v[140:143], v13 offset:128\n"
"v_cvt_pk_bf16_f32 v72, v56, v57\n"
"v_cvt_pk_bf16_f32 v73, v58, v59\n"
"v_exp_f32_e32 v64, v36\n"
"v_exp_f32_e32 v65, v37\n"
"v_exp_f32_e32 v66, v38\n"
"v_exp_f32_e32 v67, v39\n"
"v_mfma_f32_32x32x16_bf16 v[24:39], v[224:227], v[76:79], 0\n"
"buffer_load_dwordx4 v[84:87], v3, s[48:51], s55 offen offset:432\n"
"buffer_load_dwordx2 v[88:89], v7, s[48:51], s55 offen offset:432\n"
"v_cvt_pk_bf16_f32 v74, v64, v65\n"
"v_cvt_pk_bf16_f32 v75, v66, v67\n"
"v_exp_f32_e32 v56, v40\n"
"v_exp_f32_e32 v57, v41\n"
"v_exp_f32_e32 v58, v42\n"
"v_exp_f32_e32 v59, v43\n"
"v_mfma_f32_32x32x16_bf16 a[0:15], v[208:211], v[72:75], a[0:15]\n"
"s_waitcnt vmcnt(16) lgkmcnt(0)\n"
"v_cvt_pk_bf16_f32 v68, v56, v57\n"
"v_cvt_pk_bf16_f32 v69, v58, v59\n"
"v_exp_f32_e32 v60, v44\n"
"v_exp_f32_e32 v61, v45\n"
"v_mfma_f32_16x16x32_bf16 v[92:95], a[216:219], v[136:139], v[92:95]\n"
"v_exp_f32_e32 v62, v46\n"
"v_exp_f32_e32 v63, v47\n"
"v_mfma_f32_32x32x8bf16_1k v[24:39], a[38:39], v[80:81], v[24:39]\n"
"v_cvt_pk_bf16_f32 v70, v60, v61\n"
"v_cvt_pk_bf16_f32 v71, v62, v63\n"
"v_exp_f32_e32 v56, v48\n"
"v_exp_f32_e32 v57, v49\n"
"v_exp_f32_e32 v58, v50\n"
"v_exp_f32_e32 v59, v51\n"
"v_mfma_f32_32x32x16_bf16 a[0:15], v[216:219], v[68:71], a[0:15]\n"
"s_add_u32 s88, s59, 0xa000\n"
"v_cvt_pk_bf16_f32 v72, v56, v57\n"
"v_cvt_pk_bf16_f32 v73, v58, v59\n"
"v_exp_f32_e32 v64, v52\n"
"v_exp_f32_e32 v65, v53\n"
"v_mfma_f32_16x16x32_bf16 v[96:99], a[220:223], v[136:139], v[96:99]\n"
"v_exp_f32_e32 v66, v54\n"
"v_exp_f32_e32 v67, v55\n"
"v_mfma_f32_32x32x16_bf16 v[40:55], v[236:239], v[76:79], 0\n"
"buffer_load_dwordx4 a[184:187], v0, s[48:51], s88 offen\n"
"buffer_load_dwordx4 a[188:191], v0, s[48:51], s88 offen offset:1024\n"
"v_cvt_pk_bf16_f32 v74, v64, v65\n"
"v_cvt_pk_bf16_f32 v75, v66, v67\n"
"v_exp_f32_e32 v56, v24\n"
"v_exp_f32_e32 v57, v25\n"
"v_mfma_f32_16x16x32_bf16 v[100:103], a[224:227], v[136:139], v[100:103]\n"
"v_exp_f32_e32 v58, v26\n"
"v_exp_f32_e32 v59, v27\n"
"v_mfma_f32_32x32x16_bf16 a[0:15], v[220:223], v[72:75], a[0:15]\n"
"v_cvt_pk_bf16_f32 v68, v56, v57\n"
"v_cvt_pk_bf16_f32 v69, v58, v59\n"
"v_exp_f32_e32 v60, v28\n"
"v_exp_f32_e32 v61, v29\n"
"v_exp_f32_e32 v62, v30\n"
"v_exp_f32_e32 v63, v31\n"
"v_mfma_f32_32x32x8bf16_1k v[40:55], a[38:39], v[82:83], v[40:55]\n"
"v_cvt_pk_bf16_f32 v70, v60, v61\n"
"v_cvt_pk_bf16_f32 v71, v62, v63\n"
"v_exp_f32_e32 v56, v32\n"
"v_exp_f32_e32 v57, v33\n"
"v_exp_f32_e32 v58, v34\n"
"v_exp_f32_e32 v59, v35\n"
"v_mfma_f32_32x32x16_bf16 a[0:15], v[228:231], v[68:71], a[0:15]\n"
"v_cvt_pk_bf16_f32 v72, v56, v57\n"
"v_cvt_pk_bf16_f32 v73, v58, v59\n"
"v_exp_f32_e32 v64, v36\n"
"v_exp_f32_e32 v65, v37\n"
"v_mfma_f32_16x16x32_bf16 v[104:107], a[228:231], v[136:139], v[104:107]\n"
"v_exp_f32_e32 v66, v38\n"
"v_exp_f32_e32 v67, v39\n"
"v_mfma_f32_32x32x16_bf16 v[24:39], a[48:51], v[76:79], 0\n"
"buffer_load_dwordx4 a[192:195], v0, s[48:51], s88 offen offset:2048\n"
"buffer_load_dwordx4 a[196:199], v0, s[48:51], s88 offen offset:3072\n"
"v_cvt_pk_bf16_f32 v74, v64, v65\n"
"v_cvt_pk_bf16_f32 v75, v66, v67\n"
"v_exp_f32_e32 v56, v40\n"
"v_exp_f32_e32 v57, v41\n"
"v_mfma_f32_16x16x32_bf16 v[108:111], a[232:235], v[136:139], v[108:111]\n"
"v_exp_f32_e32 v58, v42\n"
"v_exp_f32_e32 v59, v43\n"
"v_mfma_f32_32x32x16_bf16 a[0:15], v[232:235], v[72:75], a[0:15]\n"
"s_add_u32 s88, s59, 0xb000\n"
"v_cvt_pk_bf16_f32 v68, v56, v57\n"
"v_cvt_pk_bf16_f32 v69, v58, v59\n"
"v_exp_f32_e32 v60, v44\n"
"v_exp_f32_e32 v61, v45\n"
"v_exp_f32_e32 v62, v46\n"
"v_exp_f32_e32 v63, v47\n"
"v_mfma_f32_32x32x8bf16_1k v[24:39], a[40:41], v[80:81], v[24:39]\n"
"v_cvt_pk_bf16_f32 v70, v60, v61\n"
"v_cvt_pk_bf16_f32 v71, v62, v63\n"
"v_exp_f32_e32 v56, v48\n"
"v_exp_f32_e32 v57, v49\n"
"v_exp_f32_e32 v58, v50\n"
"v_exp_f32_e32 v59, v51\n"
"v_mfma_f32_32x32x16_bf16 a[0:15], v[240:243], v[68:71], a[0:15]\n"
"v_cvt_pk_bf16_f32 v72, v56, v57\n"
"v_cvt_pk_bf16_f32 v73, v58, v59\n"
"v_exp_f32_e32 v64, v52\n"
"v_exp_f32_e32 v65, v53\n"
"v_mfma_f32_16x16x32_bf16 v[92:95], a[236:239], v[140:143], v[92:95]\n"
"v_exp_f32_e32 v66, v54\n"
"v_exp_f32_e32 v67, v55\n"
"v_mfma_f32_32x32x16_bf16 v[40:55], a[60:63], v[76:79], 0\n"
"buffer_load_dwordx4 a[200:203], v0, s[48:51], s88 offen\n"
"buffer_load_dwordx4 a[204:207], v0, s[48:51], s88 offen offset:1024\n"
"v_cvt_pk_bf16_f32 v74, v64, v65\n"
"v_cvt_pk_bf16_f32 v75, v66, v67\n"
"v_exp_f32_e32 v56, v24\n"
"v_exp_f32_e32 v57, v25\n"
"v_mfma_f32_16x16x32_bf16 v[96:99], a[240:243], v[140:143], v[96:99]\n"
"v_exp_f32_e32 v58, v26\n"
"v_exp_f32_e32 v59, v27\n"
"v_mfma_f32_32x32x16_bf16 a[0:15], v[244:247], v[72:75], a[0:15]\n"
"v_cvt_pk_bf16_f32 v68, v56, v57\n"
"v_cvt_pk_bf16_f32 v69, v58, v59\n"
"v_exp_f32_e32 v60, v28\n"
"v_exp_f32_e32 v61, v29\n"
"v_exp_f32_e32 v62, v30\n"
"v_exp_f32_e32 v63, v31\n"
"v_mfma_f32_32x32x8bf16_1k v[40:55], a[40:41], v[82:83], v[40:55]\n"
"v_cvt_pk_bf16_f32 v70, v60, v61\n"
"v_cvt_pk_bf16_f32 v71, v62, v63\n"
"v_exp_f32_e32 v56, v32\n"
"v_exp_f32_e32 v57, v33\n"
"v_exp_f32_e32 v58, v34\n"
"v_exp_f32_e32 v59, v35\n"
"v_mfma_f32_32x32x16_bf16 a[0:15], a[52:55], v[68:71], a[0:15]\n"
"buffer_load_dword v6, v22, s[76:79], s69 offen\n"
"s_add_u32 s69, s69, 0x2400\n"
"v_cvt_pk_bf16_f32 v72, v56, v57\n"
"v_cvt_pk_bf16_f32 v73, v58, v59\n"
"v_exp_f32_e32 v64, v36\n"
"v_exp_f32_e32 v65, v37\n"
"v_mfma_f32_16x16x32_bf16 v[100:103], a[244:247], v[140:143], v[100:103]\n"
"v_exp_f32_e32 v66, v38\n"
"v_exp_f32_e32 v67, v39\n"
"v_mfma_f32_32x32x16_bf16 v[24:39], a[72:75], v[76:79], 0\n"
"ds_read_b128 v[144:147], v12 offset:192\n"
"ds_read_b128 v[148:151], v13 offset:192\n"
"v_cvt_pk_bf16_f32 v74, v64, v65\n"
"v_cvt_pk_bf16_f32 v75, v66, v67\n"
"v_exp_f32_e32 v56, v40\n"
"v_exp_f32_e32 v57, v41\n"
"v_mfma_f32_16x16x32_bf16 v[104:107], a[248:251], v[140:143], v[104:107]\n"
"v_exp_f32_e32 v58, v42\n"
"v_exp_f32_e32 v59, v43\n"
"v_mfma_f32_32x32x16_bf16 a[0:15], a[56:59], v[72:75], a[0:15]\n"
"buffer_load_dwordx4 a[208:211], v0, s[48:51], s88 offen offset:2048\n"
"buffer_load_dwordx4 a[212:215], v0, s[48:51], s88 offen offset:3072\n"
"v_cvt_pk_bf16_f32 v68, v56, v57\n"
"v_cvt_pk_bf16_f32 v69, v58, v59\n"
"v_exp_f32_e32 v60, v44\n"
"v_exp_f32_e32 v61, v45\n"
"v_exp_f32_e32 v62, v46\n"
"v_exp_f32_e32 v63, v47\n"
"v_mfma_f32_32x32x8bf16_1k v[24:39], a[42:43], v[80:81], v[24:39]\n"
"v_cvt_pk_bf16_f32 v70, v60, v61\n"
"v_cvt_pk_bf16_f32 v71, v62, v63\n"
"v_exp_f32_e32 v56, v48\n"
"v_exp_f32_e32 v57, v49\n"
"v_exp_f32_e32 v58, v50\n"
"v_exp_f32_e32 v59, v51\n"
"v_mfma_f32_32x32x16_bf16 a[0:15], a[64:67], v[68:71], a[0:15]\n"
"s_waitcnt vmcnt(15) lgkmcnt(0)\n"
"v_cvt_pk_bf16_f32 v72, v56, v57\n"
"v_cvt_pk_bf16_f32 v73, v58, v59\n"
"v_exp_f32_e32 v64, v52\n"
"v_exp_f32_e32 v65, v53\n"
"v_mfma_f32_16x16x32_bf16 v[92:95], a[252:255], v[144:147], v[92:95]\n"
"v_exp_f32_e32 v66, v54\n"
"v_exp_f32_e32 v67, v55\n"
"v_mfma_f32_32x32x16_bf16 v[40:55], a[84:87], v[76:79], 0\n"
"s_add_u32 s88, s59, 0xc000\n"
"v_cvt_pk_bf16_f32 v74, v64, v65\n"
"v_cvt_pk_bf16_f32 v75, v66, v67\n"
"v_exp_f32_e32 v56, v24\n"
"v_exp_f32_e32 v57, v25\n"
"v_mfma_f32_16x16x32_bf16 v[96:99], v[248:251], v[144:147], v[96:99]\n"
"v_exp_f32_e32 v58, v26\n"
"v_exp_f32_e32 v59, v27\n"
"v_mfma_f32_32x32x16_bf16 a[0:15], a[68:71], v[72:75], a[0:15]\n"
"buffer_load_dwordx4 a[216:219], v0, s[48:51], s88 offen\n"
"buffer_load_dwordx4 a[220:223], v0, s[48:51], s88 offen offset:1024\n"
"v_cvt_pk_bf16_f32 v68, v56, v57\n"
"v_cvt_pk_bf16_f32 v69, v58, v59\n"
"v_exp_f32_e32 v60, v28\n"
"v_exp_f32_e32 v61, v29\n"
"v_exp_f32_e32 v62, v30\n"
"v_exp_f32_e32 v63, v31\n"
"v_mfma_f32_32x32x8bf16_1k v[40:55], a[42:43], v[82:83], v[40:55]\n"
"v_cvt_pk_bf16_f32 v70, v60, v61\n"
"v_cvt_pk_bf16_f32 v71, v62, v63\n"
"v_exp_f32_e32 v56, v32\n"
"v_exp_f32_e32 v57, v33\n"
"v_exp_f32_e32 v58, v34\n"
"v_exp_f32_e32 v59, v35\n"
"v_mfma_f32_32x32x16_bf16 a[0:15], a[76:79], v[68:71], a[0:15]\n"
"v_cvt_pk_bf16_f32 v72, v56, v57\n"
"v_cvt_pk_bf16_f32 v73, v58, v59\n"
"v_exp_f32_e32 v64, v36\n"
"v_exp_f32_e32 v65, v37\n"
"v_mfma_f32_16x16x32_bf16 v[100:103], v[252:255], v[144:147], v[100:103]\n"
"v_exp_f32_e32 v66, v38\n"
"v_exp_f32_e32 v67, v39\n"
"v_mfma_f32_32x32x16_bf16 v[24:39], a[96:99], v[76:79], 0\n"
"buffer_load_dwordx4 a[224:227], v0, s[48:51], s88 offen offset:2048\n"
"buffer_load_dwordx4 a[228:231], v0, s[48:51], s88 offen offset:3072\n"
"v_cvt_pk_bf16_f32 v74, v64, v65\n"
"v_cvt_pk_bf16_f32 v75, v66, v67\n"
"v_exp_f32_e32 v56, v40\n"
"v_exp_f32_e32 v57, v41\n"
"v_mfma_f32_16x16x32_bf16 v[104:107], a[144:147], v[144:147], v[104:107]\n"
"v_exp_f32_e32 v58, v42\n"
"v_exp_f32_e32 v59, v43\n"
"v_mfma_f32_32x32x16_bf16 a[0:15], a[80:83], v[72:75], a[0:15]\n"
"s_add_u32 s88, s59, 0xd000\n"
"v_cvt_pk_bf16_f32 v68, v56, v57\n"
"v_cvt_pk_bf16_f32 v69, v58, v59\n"
"v_exp_f32_e32 v60, v44\n"
"v_exp_f32_e32 v61, v45\n"
"v_exp_f32_e32 v62, v46\n"
"v_exp_f32_e32 v63, v47\n"
"v_mfma_f32_32x32x8bf16_1k v[24:39], a[44:45], v[80:81], v[24:39]\n"
"v_cvt_pk_bf16_f32 v70, v60, v61\n"
"v_cvt_pk_bf16_f32 v71, v62, v63\n"
"v_exp_f32_e32 v56, v48\n"
"v_exp_f32_e32 v57, v49\n"
"v_exp_f32_e32 v58, v50\n"
"v_exp_f32_e32 v59, v51\n"
"v_mfma_f32_32x32x16_bf16 a[0:15], a[88:91], v[68:71], a[0:15]\n"
"v_cvt_pk_bf16_f32 v72, v56, v57\n"
"v_cvt_pk_bf16_f32 v73, v58, v59\n"
"v_exp_f32_e32 v64, v52\n"
"v_exp_f32_e32 v65, v53\n"
"v_mfma_f32_16x16x32_bf16 v[108:111], a[148:151], v[144:147], v[108:111]\n"
"v_exp_f32_e32 v66, v54\n"
"v_exp_f32_e32 v67, v55\n"
"v_mfma_f32_32x32x16_bf16 v[40:55], a[108:111], v[76:79], 0\n"
"buffer_load_dwordx4 a[232:235], v0, s[48:51], s88 offen\n"
"buffer_load_dwordx4 a[236:239], v0, s[48:51], s88 offen offset:1024\n"
"v_cvt_pk_bf16_f32 v74, v64, v65\n"
"v_cvt_pk_bf16_f32 v75, v66, v67\n"
"v_exp_f32_e32 v56, v24\n"
"v_exp_f32_e32 v57, v25\n"
"v_mfma_f32_16x16x32_bf16 v[92:95], a[152:155], v[148:151], v[92:95]\n"
"v_exp_f32_e32 v58, v26\n"
"v_exp_f32_e32 v59, v27\n"
"v_mfma_f32_32x32x16_bf16 a[0:15], a[92:95], v[72:75], a[0:15]\n"
"v_cvt_pk_bf16_f32 v68, v56, v57\n"
"v_cvt_pk_bf16_f32 v69, v58, v59\n"
"v_exp_f32_e32 v60, v28\n"
"v_exp_f32_e32 v61, v29\n"
"v_exp_f32_e32 v62, v30\n"
"v_exp_f32_e32 v63, v31\n"
"v_mfma_f32_32x32x8bf16_1k v[40:55], a[44:45], v[82:83], v[40:55]\n"
"v_cvt_pk_bf16_f32 v70, v60, v61\n"
"v_cvt_pk_bf16_f32 v71, v62, v63\n"
"v_exp_f32_e32 v56, v32\n"
"v_exp_f32_e32 v57, v33\n"
"v_exp_f32_e32 v58, v34\n"
"v_exp_f32_e32 v59, v35\n"
"v_mfma_f32_32x32x16_bf16 a[0:15], a[100:103], v[68:71], a[0:15]\n"
"v_cvt_pk_bf16_f32 v72, v56, v57\n"
"v_cvt_pk_bf16_f32 v73, v58, v59\n"
"v_exp_f32_e32 v64, v36\n"
"v_exp_f32_e32 v65, v37\n"
"v_mfma_f32_16x16x32_bf16 v[96:99], a[156:159], v[148:151], v[96:99]\n"
"v_exp_f32_e32 v66, v38\n"
"v_exp_f32_e32 v67, v39\n"
"v_mfma_f32_32x32x16_bf16 v[24:39], a[120:123], v[76:79], 0\n"
"buffer_load_dwordx4 a[240:243], v0, s[48:51], s88 offen offset:2048\n"
"buffer_load_dwordx4 a[244:247], v0, s[48:51], s88 offen offset:3072\n"
"v_cvt_pk_bf16_f32 v74, v64, v65\n"
"v_cvt_pk_bf16_f32 v75, v66, v67\n"
"v_exp_f32_e32 v56, v40\n"
"v_exp_f32_e32 v57, v41\n"
"v_mfma_f32_16x16x32_bf16 v[100:103], a[160:163], v[148:151], v[100:103]\n"
"v_exp_f32_e32 v58, v42\n"
"v_exp_f32_e32 v59, v43\n"
"v_mfma_f32_32x32x16_bf16 a[0:15], a[104:107], v[72:75], a[0:15]\n"
"s_add_u32 s88, s59, 0xe000\n"
"v_cvt_pk_bf16_f32 v68, v56, v57\n"
"v_cvt_pk_bf16_f32 v69, v58, v59\n"
"v_exp_f32_e32 v60, v44\n"
"v_exp_f32_e32 v61, v45\n"
"v_exp_f32_e32 v62, v46\n"
"v_exp_f32_e32 v63, v47\n"
"v_mfma_f32_32x32x8bf16_1k v[24:39], a[46:47], v[80:81], v[24:39]\n"
"v_cvt_pk_bf16_f32 v70, v60, v61\n"
"v_cvt_pk_bf16_f32 v71, v62, v63\n"
"v_exp_f32_e32 v56, v48\n"
"v_exp_f32_e32 v57, v49\n"
"v_exp_f32_e32 v58, v50\n"
"v_exp_f32_e32 v59, v51\n"
"v_mfma_f32_32x32x16_bf16 a[0:15], a[112:115], v[68:71], a[0:15]\n"
"s_add_u32 s90, s65, 0x0\n"
"buffer_store_dwordx4 v[132:135], v18, s[48:51], s90 offen sc1\n"
"v_cvt_pk_bf16_f32 v72, v56, v57\n"
"v_cvt_pk_bf16_f32 v73, v58, v59\n"
"v_exp_f32_e32 v64, v52\n"
"v_exp_f32_e32 v65, v53\n"
"v_mfma_f32_16x16x32_bf16 v[104:107], a[164:167], v[148:151], v[104:107]\n"
"v_exp_f32_e32 v66, v54\n"
"v_exp_f32_e32 v67, v55\n"
"v_mfma_f32_32x32x16_bf16 v[40:55], a[132:135], v[76:79], 0\n"
"buffer_load_dwordx4 a[248:251], v0, s[48:51], s88 offen\n"
"buffer_load_dwordx4 a[252:255], v0, s[48:51], s88 offen offset:1024\n"
"v_cvt_pk_bf16_f32 v74, v64, v65\n"
"v_cvt_pk_bf16_f32 v75, v66, v67\n"
"v_exp_f32_e32 v56, v24\n"
"v_exp_f32_e32 v57, v25\n"
"v_exp_f32_e32 v58, v26\n"
"v_exp_f32_e32 v59, v27\n"
"v_mfma_f32_32x32x16_bf16 a[0:15], a[116:119], v[72:75], a[0:15]\n"
"s_waitcnt vmcnt(20)\n"
"v_cvt_pk_bf16_f32 v68, v56, v57\n"
"v_cvt_pk_bf16_f32 v69, v58, v59\n"
"v_exp_f32_e32 v60, v28\n"
"v_exp_f32_e32 v61, v29\n"
"v_cndmask_b32_e64 v90, 0, v89, s[36:37]\n"
"v_mov_b32_e32 v91, v88\n"
"v_cndmask_b32_e64 v89, 0, v89, s[36:37]\n"
"v_exp_f32_e32 v62, v30\n"
"v_exp_f32_e32 v63, v31\n"
"v_mfma_f32_32x32x8bf16_1k v[40:55], a[46:47], v[82:83], v[40:55]\n"
"v_cvt_pk_bf16_f32 v70, v60, v61\n"
"v_cvt_pk_bf16_f32 v71, v62, v63\n"
"v_exp_f32_e32 v56, v32\n"
"v_exp_f32_e32 v57, v33\n"
"v_exp_f32_e32 v58, v34\n"
"v_exp_f32_e32 v59, v35\n"
"v_mfma_f32_32x32x16_bf16 a[0:15], a[124:127], v[68:71], a[0:15]\n"
"v_cvt_pk_bf16_f32 v72, v56, v57\n"
"v_cvt_pk_bf16_f32 v73, v58, v59\n"
"v_exp_f32_e32 v64, v36\n"
"v_exp_f32_e32 v65, v37\n"
"v_exp_f32_e32 v66, v38\n"
"v_exp_f32_e32 v67, v39\n"
"v_mfma_f32_32x32x16_bf16 v[24:39], v[152:155], v[84:87], 0\n"
"buffer_load_dwordx4 v[248:251], v0, s[48:51], s88 offen offset:2048\n"
"buffer_load_dwordx4 v[252:255], v0, s[48:51], s88 offen offset:3072\n"
"v_cvt_pk_bf16_f32 v74, v64, v65\n"
"v_cvt_pk_bf16_f32 v75, v66, v67\n"
"v_exp_f32_e32 v56, v40\n"
"v_exp_f32_e32 v57, v41\n"
"v_exp_f32_e32 v58, v42\n"
"v_exp_f32_e32 v59, v43\n"
"v_mfma_f32_32x32x16_bf16 a[0:15], a[128:131], v[72:75], a[0:15]\n"
"s_add_u32 s88, s59, 0xf000\n"
"v_cvt_pk_bf16_f32 v68, v56, v57\n"
"v_cvt_pk_bf16_f32 v69, v58, v59\n"
"v_exp_f32_e32 v60, v44\n"
"v_exp_f32_e32 v61, v45\n"
"v_exp_f32_e32 v62, v46\n"
"v_exp_f32_e32 v63, v47\n"
"v_mfma_f32_32x32x8bf16_1k v[24:39], a[32:33], v[88:89], v[24:39]\n"
"v_cvt_pk_bf16_f32 v70, v60, v61\n"
"v_cvt_pk_bf16_f32 v71, v62, v63\n"
"v_exp_f32_e32 v56, v48\n"
"v_exp_f32_e32 v57, v49\n"
"v_exp_f32_e32 v58, v50\n"
"v_exp_f32_e32 v59, v51\n"
"v_mfma_f32_32x32x16_bf16 a[0:15], a[136:139], v[68:71], a[0:15]\n"
"v_cvt_pk_bf16_f32 v72, v56, v57\n"
"v_cvt_pk_bf16_f32 v73, v58, v59\n"
"v_exp_f32_e32 v64, v52\n"
"v_exp_f32_e32 v65, v53\n"
"v_exp_f32_e32 v66, v54\n"
"v_exp_f32_e32 v67, v55\n"
"v_mfma_f32_32x32x16_bf16 v[40:55], v[164:167], v[84:87], 0\n"
"buffer_load_dwordx4 a[144:147], v0, s[48:51], s88 offen\n"
"buffer_load_dwordx4 a[148:151], v0, s[48:51], s88 offen offset:1024\n"
"v_cvt_pk_bf16_f32 v74, v64, v65\n"
"v_cvt_pk_bf16_f32 v75, v66, v67\n"
"v_exp_f32_e32 v56, v24\n"
"v_exp_f32_e32 v57, v25\n"
"v_exp_f32_e32 v58, v26\n"
"v_exp_f32_e32 v59, v27\n"
"v_mfma_f32_32x32x16_bf16 a[0:15], a[140:143], v[72:75], a[0:15]\n"
"v_cvt_pk_bf16_f32 v68, v56, v57\n"
"v_cvt_pk_bf16_f32 v69, v58, v59\n"
"v_exp_f32_e32 v60, v28\n"
"v_exp_f32_e32 v61, v29\n"
"v_exp_f32_e32 v62, v30\n"
"v_exp_f32_e32 v63, v31\n"
"v_mfma_f32_32x32x8bf16_1k v[40:55], a[32:33], v[90:91], v[40:55]\n"
"v_cvt_pk_bf16_f32 v70, v60, v61\n"
"v_cvt_pk_bf16_f32 v71, v62, v63\n"
"v_exp_f32_e32 v56, v32\n"
"v_exp_f32_e32 v57, v33\n"
"v_exp_f32_e32 v58, v34\n"
"v_exp_f32_e32 v59, v35\n"
"v_mfma_f32_32x32x16_bf16 a[16:31], v[156:159], v[68:71], 0\n"
"ds_write_b128 v8, a[0:3] offset:0\n"
"v_cvt_pk_bf16_f32 v72, v56, v57\n"
"v_cvt_pk_bf16_f32 v73, v58, v59\n"
"v_exp_f32_e32 v64, v36\n"
"v_exp_f32_e32 v65, v37\n"
"v_exp_f32_e32 v66, v38\n"
"v_exp_f32_e32 v67, v39\n"
"v_mfma_f32_32x32x16_bf16 v[24:39], v[176:179], v[84:87], 0\n"
"ds_write_b128 v8, a[4:7] offset:1024\n"
"v_cvt_pk_bf16_f32 v74, v64, v65\n"
"v_cvt_pk_bf16_f32 v75, v66, v67\n"
"v_exp_f32_e32 v56, v40\n"
"v_exp_f32_e32 v57, v41\n"
"v_exp_f32_e32 v58, v42\n"
"v_exp_f32_e32 v59, v43\n"
"v_mfma_f32_32x32x16_bf16 a[16:31], v[160:163], v[72:75], a[16:31]\n"
"ds_write_b128 v8, a[8:11] offset:2048\n"
"v_cvt_pk_bf16_f32 v68, v56, v57\n"
"v_cvt_pk_bf16_f32 v69, v58, v59\n"
"v_exp_f32_e32 v60, v44\n"
"v_exp_f32_e32 v61, v45\n"
"v_exp_f32_e32 v62, v46\n"
"v_exp_f32_e32 v63, v47\n"
"v_mfma_f32_32x32x8bf16_1k v[24:39], a[34:35], v[88:89], v[24:39]\n"
"v_cvt_pk_bf16_f32 v70, v60, v61\n"
"v_cvt_pk_bf16_f32 v71, v62, v63\n"
"v_exp_f32_e32 v56, v48\n"
"v_exp_f32_e32 v57, v49\n"
"v_exp_f32_e32 v58, v50\n"
"v_exp_f32_e32 v59, v51\n"
"v_mfma_f32_32x32x16_bf16 a[16:31], v[168:171], v[68:71], a[16:31]\n"
"s_waitcnt lgkmcnt(0)\n"
"s_barrier\n"
"buffer_load_dword v6, v22, s[76:79], s69 offen\n"
"s_add_u32 s69, s69, 0x2400\n"
"v_cvt_pk_bf16_f32 v72, v56, v57\n"
"v_cvt_pk_bf16_f32 v73, v58, v59\n"
"v_exp_f32_e32 v64, v52\n"
"v_exp_f32_e32 v65, v53\n"
"v_exp_f32_e32 v66, v54\n"
"v_exp_f32_e32 v67, v55\n"
"v_mfma_f32_32x32x16_bf16 v[40:55], v[188:191], v[84:87], 0\n"
"ds_read_b128 v[112:115], v9 offset:0\n"
"ds_read_b128 v[116:119], v9 offset:3072\n"
"v_cvt_pk_bf16_f32 v74, v64, v65\n"
"v_cvt_pk_bf16_f32 v75, v66, v67\n"
"v_exp_f32_e32 v56, v24\n"
"v_exp_f32_e32 v57, v25\n"
"v_exp_f32_e32 v58, v26\n"
"v_exp_f32_e32 v59, v27\n"
"v_mfma_f32_32x32x16_bf16 a[16:31], v[172:175], v[72:75], a[16:31]\n"
"ds_read_b128 v[120:123], v9 offset:6144\n"
"ds_read_b128 v[124:127], v9 offset:9216\n"
"v_cvt_pk_bf16_f32 v68, v56, v57\n"
"v_cvt_pk_bf16_f32 v69, v58, v59\n"
"v_exp_f32_e32 v60, v28\n"
"v_exp_f32_e32 v61, v29\n"
"v_exp_f32_e32 v62, v30\n"
"v_exp_f32_e32 v63, v31\n"
"v_mfma_f32_32x32x8bf16_1k v[40:55], a[34:35], v[90:91], v[40:55]\n"
"v_cvt_pk_bf16_f32 v70, v60, v61\n"
"v_cvt_pk_bf16_f32 v71, v62, v63\n"
"v_exp_f32_e32 v56, v32\n"
"v_exp_f32_e32 v57, v33\n"
"v_exp_f32_e32 v58, v34\n"
"v_exp_f32_e32 v59, v35\n"
"v_mfma_f32_32x32x16_bf16 a[16:31], v[180:183], v[68:71], a[16:31]\n"
"s_waitcnt lgkmcnt(0)\n"
"v_cvt_pk_bf16_f32 v72, v56, v57\n"
"v_cvt_pk_bf16_f32 v73, v58, v59\n"
"v_exp_f32_e32 v64, v36\n"
"v_exp_f32_e32 v65, v37\n"
"v_add_f32_e32 v115, v115, v119\n"
"v_add_f32_e32 v112, v112, v116\n"
"v_add_f32_e32 v113, v113, v117\n"
"v_add_f32_e32 v114, v114, v118\n"
"v_add_f32_e32 v123, v123, v127\n"
"v_add_f32_e32 v120, v120, v124\n"
"v_exp_f32_e32 v66, v38\n"
"v_exp_f32_e32 v67, v39\n"
"v_mfma_f32_32x32x16_bf16 v[24:39], v[200:203], v[84:87], 0\n"
"buffer_load_dwordx4 a[152:155], v0, s[48:51], s88 offen offset:2048\n"
"buffer_load_dwordx4 a[156:159], v0, s[48:51], s88 offen offset:3072\n"
"v_cvt_pk_bf16_f32 v74, v64, v65\n"
"v_cvt_pk_bf16_f32 v75, v66, v67\n"
"v_exp_f32_e32 v56, v40\n"
"v_exp_f32_e32 v57, v41\n"
"v_add_f32_e32 v121, v121, v125\n"
"v_add_f32_e32 v122, v122, v126\n"
"v_add_f32_e32 v115, v115, v123\n"
"v_rcp_f32_e32 v115, v115\n"
"v_add_f32_e32 v112, v112, v120\n"
"v_add_f32_e32 v113, v113, v121\n"
"v_exp_f32_e32 v58, v42\n"
"v_exp_f32_e32 v59, v43\n"
"v_mfma_f32_32x32x16_bf16 a[16:31], v[184:187], v[72:75], a[16:31]\n"
"s_add_u32 s88, s59, 0x10000\n"
"v_cvt_pk_bf16_f32 v68, v56, v57\n"
"v_cvt_pk_bf16_f32 v69, v58, v59\n"
"v_exp_f32_e32 v60, v44\n"
"v_exp_f32_e32 v61, v45\n"
"v_add_f32_e32 v114, v114, v122\n"
"v_mul_f32_e32 v112, v112, v115\n"
"v_mul_f32_e32 v113, v113, v115\n"
"v_mul_f32_e32 v114, v114, v115\n"
"v_cvt_pk_bf16_f32 v112, v112, v113\n"
"v_cvt_pk_bf16_f32 v114, v114, v114\n"
"v_exp_f32_e32 v62, v46\n"
"v_exp_f32_e32 v63, v47\n"
"v_mfma_f32_32x32x8bf16_1k v[24:39], a[36:37], v[88:89], v[24:39]\n"
"v_cvt_pk_bf16_f32 v70, v60, v61\n"
"v_cvt_pk_bf16_f32 v71, v62, v63\n"
"v_exp_f32_e32 v56, v48\n"
"v_exp_f32_e32 v57, v49\n"
"v_exp_f32_e32 v58, v50\n"
"v_exp_f32_e32 v59, v51\n"
"v_mfma_f32_32x32x16_bf16 a[16:31], v[192:195], v[68:71], a[16:31]\n"
"ds_write_b32 v10, v112 offset:288\n"
"ds_write_b16 v11, v114 offset:288\n"
"v_cvt_pk_bf16_f32 v72, v56, v57\n"
"v_cvt_pk_bf16_f32 v73, v58, v59\n"
"v_exp_f32_e32 v64, v52\n"
"v_exp_f32_e32 v65, v53\n"
"v_exp_f32_e32 v66, v54\n"
"v_exp_f32_e32 v67, v55\n"
"v_mfma_f32_32x32x16_bf16 v[40:55], v[212:215], v[84:87], 0\n"
"buffer_load_dwordx4 a[160:163], v0, s[48:51], s88 offen\n"
"buffer_load_dwordx4 a[164:167], v0, s[48:51], s88 offen offset:1024\n"
"v_cvt_pk_bf16_f32 v74, v64, v65\n"
"v_cvt_pk_bf16_f32 v75, v66, v67\n"
"v_exp_f32_e32 v56, v24\n"
"v_exp_f32_e32 v57, v25\n"
"v_exp_f32_e32 v58, v26\n"
"v_exp_f32_e32 v59, v27\n"
"v_mfma_f32_32x32x16_bf16 a[16:31], v[196:199], v[72:75], a[16:31]\n"
"s_waitcnt lgkmcnt(0)\n"
"s_barrier\n"
"v_cvt_pk_bf16_f32 v68, v56, v57\n"
"v_cvt_pk_bf16_f32 v69, v58, v59\n"
"v_exp_f32_e32 v60, v28\n"
"v_exp_f32_e32 v61, v29\n"
"v_exp_f32_e32 v62, v30\n"
"v_exp_f32_e32 v63, v31\n"
"v_mfma_f32_32x32x8bf16_1k v[40:55], a[36:37], v[90:91], v[40:55]\n"
"v_cvt_pk_bf16_f32 v70, v60, v61\n"
"v_cvt_pk_bf16_f32 v71, v62, v63\n"
"v_exp_f32_e32 v56, v32\n"
"v_exp_f32_e32 v57, v33\n"
"v_exp_f32_e32 v58, v34\n"
"v_exp_f32_e32 v59, v35\n"
"v_mfma_f32_32x32x16_bf16 a[16:31], v[204:207], v[68:71], a[16:31]\n"
"ds_read_b128 v[136:139], v12 offset:256\n"
"ds_read_b128 v[140:143], v13 offset:256\n"
"v_cvt_pk_bf16_f32 v72, v56, v57\n"
"v_cvt_pk_bf16_f32 v73, v58, v59\n"
"v_exp_f32_e32 v64, v36\n"
"v_exp_f32_e32 v65, v37\n"
"v_exp_f32_e32 v66, v38\n"
"v_exp_f32_e32 v67, v39\n"
"v_mfma_f32_32x32x16_bf16 v[24:39], v[224:227], v[84:87], 0\n"
"buffer_load_dwordx4 v[76:79], v3, s[48:51], s55 offen offset:576\n"
"buffer_load_dwordx2 v[80:81], v7, s[48:51], s55 offen offset:576\n"
"v_cvt_pk_bf16_f32 v74, v64, v65\n"
"v_cvt_pk_bf16_f32 v75, v66, v67\n"
"v_exp_f32_e32 v56, v40\n"
"v_exp_f32_e32 v57, v41\n"
"v_exp_f32_e32 v58, v42\n"
"v_exp_f32_e32 v59, v43\n"
"v_mfma_f32_32x32x16_bf16 a[16:31], v[208:211], v[72:75], a[16:31]\n"
"s_waitcnt vmcnt(26) lgkmcnt(0)\n"
"v_cvt_pk_bf16_f32 v68, v56, v57\n"
"v_cvt_pk_bf16_f32 v69, v58, v59\n"
"v_exp_f32_e32 v60, v44\n"
"v_exp_f32_e32 v61, v45\n"
"v_mfma_f32_16x16x32_bf16 v[92:95], a[168:171], v[136:139], v[92:95]\n"
"v_exp_f32_e32 v62, v46\n"
"v_exp_f32_e32 v63, v47\n"
"v_mfma_f32_32x32x8bf16_1k v[24:39], a[38:39], v[88:89], v[24:39]\n"
"v_cvt_pk_bf16_f32 v70, v60, v61\n"
".LK3_pad3:\n"
"s_cbranch_execz .LK3_pad7\n"
"v_cvt_pk_bf16_f32 v71, v62, v63\n"
"v_exp_f32_e32 v56, v48\n"
"v_exp_f32_e32 v57, v49\n"
"v_exp_f32_e32 v58, v50\n"
"v_exp_f32_e32 v59, v51\n"
"v_mfma_f32_32x32x16_bf16 a[16:31], v[216:219], v[68:71], a[16:31]\n"
"v_cvt_pk_bf16_f32 v72, v56, v57\n"
"v_cvt_pk_bf16_f32 v73, v58, v59\n"
"v_exp_f32_e32 v64, v52\n"
"v_exp_f32_e32 v65, v53\n"
"v_mfma_f32_16x16x32_bf16 v[96:99], a[172:175], v[136:139], v[96:99]\n"
"v_exp_f32_e32 v66, v54\n"
"v_exp_f32_e32 v67, v55\n"
"v_mfma_f32_32x32x16_bf16 v[40:55], v[236:239], v[84:87], 0\n"
"buffer_load_dwordx4 a[168:171], v0, s[48:51], s88 offen offset:2048\n"
"buffer_load_dwordx4 a[172:175], v0, s[48:51], s88 offen offset:3072\n"
"v_cvt_pk_bf16_f32 v74, v64, v65\n"
"v_cvt_pk_bf16_f32 v75, v66, v67\n"
"v_exp_f32_e32 v56, v24\n"
"v_exp_f32_e32 v57, v25\n"
"v_mfma_f32_16x16x32_bf16 v[100:103], a[176:179], v[136:139], v[100:103]\n"
"v_exp_f32_e32 v58, v26\n"
"v_exp_f32_e32 v59, v27\n"
"v_mfma_f32_32x32x16_bf16 a[16:31], v[220:223], v[72:75], a[16:31]\n"
"s_add_u32 s88, s59, 0x11000\n"
"v_cvt_pk_bf16_f32 v68, v56, v57\n"
"v_cvt_pk_bf16_f32 v69, v58, v59\n"
"v_exp_f32_e32 v60, v28\n"
"v_exp_f32_e32 v61, v29\n"
"v_exp_f32_e32 v62, v30\n"
"v_exp_f32_e32 v63, v31\n"
"v_mfma_f32_32x32x8bf16_1k v[40:55], a[38:39], v[90:91], v[40:55]\n"
"v_cvt_pk_bf16_f32 v70, v60, v61\n"
"v_cvt_pk_bf16_f32 v71, v62, v63\n"
"v_exp_f32_e32 v56, v32\n"
"v_exp_f32_e32 v57, v33\n"
"v_exp_f32_e32 v58, v34\n"
"v_exp_f32_e32 v59, v35\n"
"v_mfma_f32_32x32x16_bf16 a[16:31], v[228:231], v[68:71], a[16:31]\n"
"v_cvt_pk_bf16_f32 v72, v56, v57\n"
"v_cvt_pk_bf16_f32 v73, v58, v59\n"
"v_exp_f32_e32 v64, v36\n"
"v_exp_f32_e32 v65, v37\n"
"v_mfma_f32_16x16x32_bf16 v[104:107], a[180:183], v[136:139], v[104:107]\n"
"v_exp_f32_e32 v66, v38\n"
"v_exp_f32_e32 v67, v39\n"
"v_mfma_f32_32x32x16_bf16 v[24:39], a[48:51], v[84:87], 0\n"
"buffer_load_dwordx4 a[176:179], v0, s[48:51], s88 offen\n"
"buffer_load_dwordx4 a[180:183], v0, s[48:51], s88 offen offset:1024\n"
"v_cvt_pk_bf16_f32 v74, v64, v65\n"
"v_cvt_pk_bf16_f32 v75, v66, v67\n"
"v_exp_f32_e32 v56, v40\n"
"v_exp_f32_e32 v57, v41\n"
"v_mfma_f32_16x16x32_bf16 v[108:111], a[184:187], v[136:139], v[108:111]\n"
"v_exp_f32_e32 v58, v42\n"
"v_exp_f32_e32 v59, v43\n"
"v_mfma_f32_32x32x16_bf16 a[16:31], v[232:235], v[72:75], a[16:31]\n"
"v_cvt_pk_bf16_f32 v68, v56, v57\n"
"v_cvt_pk_bf16_f32 v69, v58, v59\n"
"v_exp_f32_e32 v60, v44\n"
"v_exp_f32_e32 v61, v45\n"
"v_exp_f32_e32 v62, v46\n"
"v_exp_f32_e32 v63, v47\n"
"v_mfma_f32_32x32x8bf16_1k v[24:39], a[40:41], v[88:89], v[24:39]\n"
"v_cvt_pk_bf16_f32 v70, v60, v61\n"
"v_cvt_pk_bf16_f32 v71, v62, v63\n"
"v_exp_f32_e32 v56, v48\n"
"v_exp_f32_e32 v57, v49\n"
"v_exp_f32_e32 v58, v50\n"
"v_exp_f32_e32 v59, v51\n"
"v_mfma_f32_32x32x16_bf16 a[16:31], v[240:243], v[68:71], a[16:31]\n"
"v_cvt_pk_bf16_f32 v72, v56, v57\n"
"v_cvt_pk_bf16_f32 v73, v58, v59\n"
"v_exp_f32_e32 v64, v52\n"
"v_exp_f32_e32 v65, v53\n"
"v_mfma_f32_16x16x32_bf16 v[92:95], a[188:191], v[140:143], v[92:95]\n"
"v_exp_f32_e32 v66, v54\n"
"v_exp_f32_e32 v67, v55\n"
"v_mfma_f32_32x32x16_bf16 v[40:55], a[60:63], v[84:87], 0\n"
"buffer_load_dwordx4 a[184:187], v0, s[48:51], s88 offen offset:2048\n"
"buffer_load_dwordx4 a[188:191], v0, s[48:51], s88 offen offset:3072\n"
"v_cvt_pk_bf16_f32 v74, v64, v65\n"
"v_cvt_pk_bf16_f32 v75, v66, v67\n"
"v_exp_f32_e32 v56, v24\n"
"v_exp_f32_e32 v57, v25\n"
"v_mfma_f32_16x16x32_bf16 v[96:99], a[192:195], v[140:143], v[96:99]\n"
"v_exp_f32_e32 v58, v26\n"
"v_exp_f32_e32 v59, v27\n"
"v_mfma_f32_32x32x16_bf16 a[16:31], v[244:247], v[72:75], a[16:31]\n"
"s_add_u32 s88, s59, 0x12000\n"
"v_cvt_pk_bf16_f32 v68, v56, v57\n"
"v_cvt_pk_bf16_f32 v69, v58, v59\n"
"v_exp_f32_e32 v60, v28\n"
"v_exp_f32_e32 v61, v29\n"
"v_exp_f32_e32 v62, v30\n"
"v_exp_f32_e32 v63, v31\n"
"v_mfma_f32_32x32x8bf16_1k v[40:55], a[40:41], v[90:91], v[40:55]\n"
"v_cvt_pk_bf16_f32 v70, v60, v61\n"
"v_cvt_pk_bf16_f32 v71, v62, v63\n"
"v_exp_f32_e32 v56, v32\n"
"v_exp_f32_e32 v57, v33\n"
"v_exp_f32_e32 v58, v34\n"
"v_exp_f32_e32 v59, v35\n"
"v_mfma_f32_32x32x16_bf16 a[16:31], a[52:55], v[68:71], a[16:31]\n"
"v_cvt_pk_bf16_f32 v72, v56, v57\n"
"v_cvt_pk_bf16_f32 v73, v58, v59\n"
"v_exp_f32_e32 v64, v36\n"
"v_exp_f32_e32 v65, v37\n"
"v_mfma_f32_16x16x32_bf16 v[100:103], a[196:199], v[140:143], v[100:103]\n"
"v_exp_f32_e32 v66, v38\n"
"v_exp_f32_e32 v67, v39\n"
"v_mfma_f32_32x32x16_bf16 v[24:39], a[72:75], v[84:87], 0\n"
"ds_read_b128 v[144:147], v12 offset:320\n"
"ds_read_b128 v[148:151], v13 offset:320\n"
"v_cvt_pk_bf16_f32 v74, v64, v65\n"
"v_cvt_pk_bf16_f32 v75, v66, v67\n"
"v_exp_f32_e32 v56, v40\n"
"v_exp_f32_e32 v57, v41\n"
"v_mfma_f32_16x16x32_bf16 v[104:107], a[200:203], v[140:143], v[104:107]\n"
"v_exp_f32_e32 v58, v42\n"
"v_exp_f32_e32 v59, v43\n"
"v_mfma_f32_32x32x16_bf16 a[16:31], a[56:59], v[72:75], a[16:31]\n"
"buffer_load_dwordx4 a[192:195], v0, s[48:51], s88 offen\n"
"buffer_load_dwordx4 a[196:199], v0, s[48:51], s88 offen offset:1024\n"
"v_cvt_pk_bf16_f32 v68, v56, v57\n"
"v_cvt_pk_bf16_f32 v69, v58, v59\n"
"v_exp_f32_e32 v60, v44\n"
"v_exp_f32_e32 v61, v45\n"
"v_exp_f32_e32 v62, v46\n"
"v_exp_f32_e32 v63, v47\n"
"v_mfma_f32_32x32x8bf16_1k v[24:39], a[42:43], v[88:89], v[24:39]\n"
"v_cvt_pk_bf16_f32 v70, v60, v61\n"
"v_cvt_pk_bf16_f32 v71, v62, v63\n"
"v_exp_f32_e32 v56, v48\n"
"v_exp_f32_e32 v57, v49\n"
"v_exp_f32_e32 v58, v50\n"
"v_exp_f32_e32 v59, v51\n"
"v_mfma_f32_32x32x16_bf16 a[16:31], a[64:67], v[68:71], a[16:31]\n"
"s_waitcnt vmcnt(24) lgkmcnt(0)\n"
"v_cvt_pk_bf16_f32 v72, v56, v57\n"
"v_cvt_pk_bf16_f32 v73, v58, v59\n"
"v_exp_f32_e32 v64, v52\n"
"v_exp_f32_e32 v65, v53\n"
"v_mfma_f32_16x16x32_bf16 v[92:95], a[204:207], v[144:147], v[92:95]\n"
"v_exp_f32_e32 v66, v54\n"
"v_exp_f32_e32 v67, v55\n"
"v_mfma_f32_32x32x16_bf16 v[40:55], a[84:87], v[84:87], 0\n"
"buffer_load_dwordx4 a[200:203], v0, s[48:51], s88 offen offset:2048\n"
"buffer_load_dwordx4 a[204:207], v0, s[48:51], s88 offen offset:3072\n"
"v_cvt_pk_bf16_f32 v74, v64, v65\n"
"v_cvt_pk_bf16_f32 v75, v66, v67\n"
"v_exp_f32_e32 v56, v24\n"
"v_exp_f32_e32 v57, v25\n"
"v_mfma_f32_16x16x32_bf16 v[96:99], a[208:211], v[144:147], v[96:99]\n"
"v_exp_f32_e32 v58, v26\n"
"v_exp_f32_e32 v59, v27\n"
"v_mfma_f32_32x32x16_bf16 a[16:31], a[68:71], v[72:75], a[16:31]\n"
"s_add_u32 s88, s59, 0x13000\n"
"v_cvt_pk_bf16_f32 v68, v56, v57\n"
"v_cvt_pk_bf16_f32 v69, v58, v59\n"
"v_exp_f32_e32 v60, v28\n"
"v_exp_f32_e32 v61, v29\n"
"v_exp_f32_e32 v62, v30\n"
"v_exp_f32_e32 v63, v31\n"
"v_mfma_f32_32x32x8bf16_1k v[40:55], a[42:43], v[90:91], v[40:55]\n"
"v_cvt_pk_bf16_f32 v70, v60, v61\n"
"v_cvt_pk_bf16_f32 v71, v62, v63\n"
"v_exp_f32_e32 v56, v32\n"
"v_exp_f32_e32 v57, v33\n"
"v_exp_f32_e32 v58, v34\n"
"v_exp_f32_e32 v59, v35\n"
"v_mfma_f32_32x32x16_bf16 a[16:31], a[76:79], v[68:71], a[16:31]\n"
"buffer_load_dword v6, v22, s[76:79], s69 offen\n"
"s_add_u32 s69, s69, 0x2400\n"
"v_cvt_pk_bf16_f32 v72, v56, v57\n"
"v_cvt_pk_bf16_f32 v73, v58, v59\n"
"v_exp_f32_e32 v64, v36\n"
"v_exp_f32_e32 v65, v37\n"
"v_mfma_f32_16x16x32_bf16 v[100:103], a[212:215], v[144:147], v[100:103]\n"
"v_exp_f32_e32 v66, v38\n"
"v_exp_f32_e32 v67, v39\n"
"v_mfma_f32_32x32x16_bf16 v[24:39], a[96:99], v[84:87], 0\n"
"buffer_load_dwordx4 a[208:211], v0, s[48:51], s88 offen\n"
"buffer_load_dwordx4 a[212:215], v0, s[48:51], s88 offen offset:1024\n"
"v_cvt_pk_bf16_f32 v74, v64, v65\n"
"v_cvt_pk_bf16_f32 v75, v66, v67\n"
"v_exp_f32_e32 v56, v40\n"
"v_exp_f32_e32 v57, v41\n"
"v_mfma_f32_16x16x32_bf16 v[104:107], a[216:219], v[144:147], v[104:107]\n"
"v_exp_f32_e32 v58, v42\n"
"v_exp_f32_e32 v59, v43\n"
"v_mfma_f32_32x32x16_bf16 a[16:31], a[80:83], v[72:75], a[16:31]\n"
"v_cvt_pk_bf16_f32 v68, v56, v57\n"
"v_cvt_pk_bf16_f32 v69, v58, v59\n"
"v_exp_f32_e32 v60, v44\n"
"v_exp_f32_e32 v61, v45\n"
"v_exp_f32_e32 v62, v46\n"
"v_exp_f32_e32 v63, v47\n"
"v_mfma_f32_32x32x8bf16_1k v[24:39], a[44:45], v[88:89], v[24:39]\n"
"v_cvt_pk_bf16_f32 v70, v60, v61\n"
"v_cvt_pk_bf16_f32 v71, v62, v63\n"
"v_exp_f32_e32 v56, v48\n"
"v_exp_f32_e32 v57, v49\n"
"v_exp_f32_e32 v58, v50\n"
"v_exp_f32_e32 v59, v51\n"
"v_mfma_f32_32x32x16_bf16 a[16:31], a[88:91], v[68:71], a[16:31]\n"
"v_cvt_pk_bf16_f32 v72, v56, v57\n"
"v_cvt_pk_bf16_f32 v73, v58, v59\n"
"v_exp_f32_e32 v64, v52\n"
"v_exp_f32_e32 v65, v53\n"
"v_mfma_f32_16x16x32_bf16 v[108:111], a[220:223], v[144:147], v[108:111]\n"
"v_exp_f32_e32 v66, v54\n"
"v_exp_f32_e32 v67, v55\n"
"v_mfma_f32_32x32x16_bf16 v[40:55], a[108:111], v[84:87], 0\n"
"buffer_load_dwordx4 a[216:219], v0, s[48:51], s88 offen offset:2048\n"
"buffer_load_dwordx4 a[220:223], v0, s[48:51], s88 offen offset:3072\n"
"v_cvt_pk_bf16_f32 v74, v64, v65\n"
"v_cvt_pk_bf16_f32 v75, v66, v67\n"
"v_exp_f32_e32 v56, v24\n"
"v_exp_f32_e32 v57, v25\n"
"v_mfma_f32_16x16x32_bf16 v[92:95], a[224:227], v[148:151], v[92:95]\n"
"v_exp_f32_e32 v58, v26\n"
"v_exp_f32_e32 v59, v27\n"
"v_mfma_f32_32x32x16_bf16 a[16:31], a[92:95], v[72:75], a[16:31]\n"
"s_add_u32 s88, s59, 0x14000\n"
"v_cvt_pk_bf16_f32 v68, v56, v57\n"
"v_cvt_pk_bf16_f32 v69, v58, v59\n"
"v_exp_f32_e32 v60, v28\n"
"v_exp_f32_e32 v61, v29\n"
"v_exp_f32_e32 v62, v30\n"
"v_exp_f32_e32 v63, v31\n"
"v_mfma_f32_32x32x8bf16_1k v[40:55], a[44:45], v[90:91], v[40:55]\n"
"v_cvt_pk_bf16_f32 v70, v60, v61\n"
"v_cvt_pk_bf16_f32 v71, v62, v63\n"
"v_exp_f32_e32 v56, v32\n"
"v_exp_f32_e32 v57, v33\n"
"v_exp_f32_e32 v58, v34\n"
"v_exp_f32_e32 v59, v35\n"
"v_mfma_f32_32x32x16_bf16 a[16:31], a[100:103], v[68:71], a[16:31]\n"
"v_cvt_pk_bf16_f32 v72, v56, v57\n"
"v_cvt_pk_bf16_f32 v73, v58, v59\n"
"v_exp_f32_e32 v64, v36\n"
"v_exp_f32_e32 v65, v37\n"
"v_mfma_f32_16x16x32_bf16 v[96:99], a[228:231], v[148:151], v[96:99]\n"
"v_exp_f32_e32 v66, v38\n"
"v_exp_f32_e32 v67, v39\n"
"v_mfma_f32_32x32x16_bf16 v[24:39], a[120:123], v[84:87], 0\n"
"buffer_load_dwordx4 a[224:227], v0, s[48:51], s88 offen\n"
"buffer_load_dwordx4 a[228:231], v0, s[48:51], s88 offen offset:1024\n"
"v_cvt_pk_bf16_f32 v74, v64, v65\n"
"v_cvt_pk_bf16_f32 v75, v66, v67\n"
"v_exp_f32_e32 v56, v40\n"
"v_exp_f32_e32 v57, v41\n"
"v_mfma_f32_16x16x32_bf16 v[100:103], a[232:235], v[148:151], v[100:103]\n"
"v_exp_f32_e32 v58, v42\n"
"v_exp_f32_e32 v59, v43\n"
"v_mfma_f32_32x32x16_bf16 a[16:31], a[104:107], v[72:75], a[16:31]\n"
"v_cvt_pk_bf16_f32 v68, v56, v57\n"
"v_cvt_pk_bf16_f32 v69, v58, v59\n"
"v_exp_f32_e32 v60, v44\n"
"v_exp_f32_e32 v61, v45\n"
"v_exp_f32_e32 v62, v46\n"
"v_exp_f32_e32 v63, v47\n"
"v_mfma_f32_32x32x8bf16_1k v[24:39], a[46:47], v[88:89], v[24:39]\n"
"v_cvt_pk_bf16_f32 v70, v60, v61\n"
"v_cvt_pk_bf16_f32 v71, v62, v63\n"
"v_exp_f32_e32 v56, v48\n"
"v_exp_f32_e32 v57, v49\n"
"v_exp_f32_e32 v58, v50\n"
"v_exp_f32_e32 v59, v51\n"
"v_mfma_f32_32x32x16_bf16 a[16:31], a[112:115], v[68:71], a[16:31]\n"
"v_cvt_pk_bf16_f32 v72, v56, v57\n"
"v_cvt_pk_bf16_f32 v73, v58, v59\n"
"v_exp_f32_e32 v64, v52\n"
"v_exp_f32_e32 v65, v53\n"
"v_mfma_f32_16x16x32_bf16 v[104:107], a[236:239], v[148:151], v[104:107]\n"
"v_exp_f32_e32 v66, v54\n"
"v_exp_f32_e32 v67, v55\n"
"v_mfma_f32_32x32x16_bf16 v[40:55], a[132:135], v[84:87], 0\n"
"buffer_load_dwordx4 a[232:235], v0, s[48:51], s88 offen offset:2048\n"
"buffer_load_dwordx4 a[236:239], v0, s[48:51], s88 offen offset:3072\n"
"v_cvt_pk_bf16_f32 v74, v64, v65\n"
"v_cvt_pk_bf16_f32 v75, v66, v67\n"
"v_exp_f32_e32 v56, v24\n"
"v_exp_f32_e32 v57, v25\n"
"v_exp_f32_e32 v58, v26\n"
"v_exp_f32_e32 v59, v27\n"
"v_mfma_f32_32x32x16_bf16 a[16:31], a[116:119], v[72:75], a[16:31]\n"
"s_waitcnt vmcnt(19)\n"
"v_cvt_pk_bf16_f32 v68, v56, v57\n"
"v_cvt_pk_bf16_f32 v69, v58, v59\n"
"v_exp_f32_e32 v60, v28\n"
"v_exp_f32_e32 v61, v29\n"
"v_cndmask_b32_e64 v82, 0, v81, s[36:37]\n"
"v_mov_b32_e32 v83, v80\n"
"v_cndmask_b32_e64 v81, 0, v81, s[36:37]\n"
"v_exp_f32_e32 v62, v30\n"
"v_exp_f32_e32 v63, v31\n"
"v_mfma_f32_32x32x8bf16_1k v[40:55], a[46:47], v[90:91], v[40:55]\n"
"v_cvt_pk_bf16_f32 v70, v60, v61\n"
"v_cvt_pk_bf16_f32 v71, v62, v63\n"
"v_exp_f32_e32 v56, v32\n"
"v_exp_f32_e32 v57, v33\n"
"v_exp_f32_e32 v58, v34\n"
"v_exp_f32_e32 v59, v35\n"
"v_mfma_f32_32x32x16_bf16 a[16:31], a[124:127], v[68:71], a[16:31]\n"
"s_add_u32 s88, s59, 0x15000\n"
"v_cvt_pk_bf16_f32 v72, v56, v57\n"
"v_cvt_pk_bf16_f32 v73, v58, v59\n"
"v_exp_f32_e32 v64, v36\n"
"v_exp_f32_e32 v65, v37\n"
"v_exp_f32_e32 v66, v38\n"
"v_exp_f32_e32 v67, v39\n"
"v_mfma_f32_32x32x16_bf16 v[24:39], v[152:155], v[76:79], 0\n"
"v_cvt_pk_bf16_f32 v74, v64, v65\n"
"v_cvt_pk_bf16_f32 v75, v66, v67\n"
"v_exp_f32_e32 v56, v40\n"
"v_exp_f32_e32 v57, v41\n"
"v_exp_f32_e32 v58, v42\n"
"v_exp_f32_e32 v59, v43\n"
"v_mfma_f32_32x32x16_bf16 a[16:31], a[128:131], v[72:75], a[16:31]\n"
"v_cvt_pk_bf16_f32 v68, v56, v57\n"
"v_cvt_pk_bf16_f32 v69, v58, v59\n"
"v_exp_f32_e32 v60, v44\n"
"v_exp_f32_e32 v61, v45\n"
"v_exp_f32_e32 v62, v46\n"
"v_exp_f32_e32 v63, v47\n"
"v_mfma_f32_32x32x8bf16_1k v[24:39], a[32:33], v[80:81], v[24:39]\n"
"v_cvt_pk_bf16_f32 v70, v60, v61\n"
"v_cvt_pk_bf16_f32 v71, v62, v63\n"
"v_exp_f32_e32 v56, v48\n"
"v_exp_f32_e32 v57, v49\n"
"v_exp_f32_e32 v58, v50\n"
"v_exp_f32_e32 v59, v51\n"
"v_mfma_f32_32x32x16_bf16 a[16:31], a[136:139], v[68:71], a[16:31]\n"
"s_add_u32 s90, s65, 0x2000\n"
"buffer_store_dwordx4 v[132:135], v18, s[48:51], s90 offen sc1\n"
"v_cvt_pk_bf16_f32 v72, v56, v57\n"
"v_cvt_pk_bf16_f32 v73, v58, v59\n"
"v_exp_f32_e32 v64, v52\n"
"v_exp_f32_e32 v65, v53\n"
"v_exp_f32_e32 v66, v54\n"
"v_exp_f32_e32 v67, v55\n"
"v_mfma_f32_32x32x16_bf16 v[40:55], v[164:167], v[76:79], 0\n"
"v_cvt_pk_bf16_f32 v74, v64, v65\n"
"v_cvt_pk_bf16_f32 v75, v66, v67\n"
"v_exp_f32_e32 v56, v24\n"
"v_exp_f32_e32 v57, v25\n"
"v_exp_f32_e32 v58, v26\n"
"v_exp_f32_e32 v59, v27\n"
"v_mfma_f32_32x32x16_bf16 a[16:31], a[140:143], v[72:75], a[16:31]\n"
"v_cvt_pk_bf16_f32 v68, v56, v57\n"
"v_cvt_pk_bf16_f32 v69, v58, v59\n"
"v_exp_f32_e32 v60, v28\n"
"v_exp_f32_e32 v61, v29\n"
"v_exp_f32_e32 v62, v30\n"
"v_exp_f32_e32 v63, v31\n"
"v_mfma_f32_32x32x8bf16_1k v[40:55], a[32:33], v[82:83], v[40:55]\n"
"v_cvt_pk_bf16_f32 v70, v60, v61\n"
"v_cvt_pk_bf16_f32 v71, v62, v63\n"
"v_exp_f32_e32 v56, v32\n"
"v_exp_f32_e32 v57, v33\n"
"v_exp_f32_e32 v58, v34\n"
"v_exp_f32_e32 v59, v35\n"
"v_mfma_f32_32x32x16_bf16 a[0:15], v[156:159], v[68:71], 0\n"
"ds_write_b128 v8, a[16:19] offset:12288\n"
"v_cvt_pk_bf16_f32 v72, v56, v57\n"
"v_cvt_pk_bf16_f32 v73, v58, v59\n"
"v_exp_f32_e32 v64, v36\n"
"v_exp_f32_e32 v65, v37\n"
"v_exp_f32_e32 v66, v38\n"
"v_exp_f32_e32 v67, v39\n"
"v_mfma_f32_32x32x16_bf16 v[24:39], v[176:179], v[76:79], 0\n"
"ds_write_b128 v8, a[20:23] offset:13312\n"
"v_cvt_pk_bf16_f32 v74, v64, v65\n"
"v_cvt_pk_bf16_f32 v75, v66, v67\n"
"v_exp_f32_e32 v56, v40\n"
"v_exp_f32_e32 v57, v41\n"
"v_exp_f32_e32 v58, v42\n"
"v_exp_f32_e32 v59, v43\n"
"v_mfma_f32_32x32x16_bf16 a[0:15], v[160:163], v[72:75], a[0:15]\n"
"ds_write_b128 v8, a[24:27] offset:14336\n"
"v_cvt_pk_bf16_f32 v68, v56, v57\n"
"v_cvt_pk_bf16_f32 v69, v58, v59\n"
"v_exp_f32_e32 v60, v44\n"
"v_exp_f32_e32 v61, v45\n"
"v_exp_f32_e32 v62, v46\n"
"v_exp_f32_e32 v63, v47\n"
"v_mfma_f32_32x32x8bf16_1k v[24:39], a[34:35], v[80:81], v[24:39]\n"
"v_cvt_pk_bf16_f32 v70, v60, v61\n"
"v_cvt_pk_bf16_f32 v71, v62, v63\n"
"v_exp_f32_e32 v56, v48\n"
"v_exp_f32_e32 v57, v49\n"
"v_exp_f32_e32 v58, v50\n"
"v_exp_f32_e32 v59, v51\n"
"v_mfma_f32_32x32x16_bf16 a[0:15], v[168:171], v[68:71], a[0:15]\n"
"s_waitcnt lgkmcnt(0)\n"
"s_barrier\n"
"v_cvt_pk_bf16_f32 v72, v56, v57\n"
"v_cvt_pk_bf16_f32 v73, v58, v59\n"
"v_exp_f32_e32 v64, v52\n"
"v_exp_f32_e32 v65, v53\n"
"v_exp_f32_e32 v66, v54\n"
"v_exp_f32_e32 v67, v55\n"
"v_mfma_f32_32x32x16_bf16 v[40:55], v[188:191], v[76:79], 0\n"
"ds_read_b128 v[112:115], v9 offset:12288\n"
"ds_read_b128 v[116:119], v9 offset:15360\n"
"v_cvt_pk_bf16_f32 v74, v64, v65\n"
"v_cvt_pk_bf16_f32 v75, v66, v67\n"
"v_exp_f32_e32 v56, v24\n"
"v_exp_f32_e32 v57, v25\n"
"v_exp_f32_e32 v58, v26\n"
"v_exp_f32_e32 v59, v27\n"
"v_mfma_f32_32x32x16_bf16 a[0:15], v[172:175], v[72:75], a[0:15]\n"
"ds_read_b128 v[120:123], v9 offset:18432\n"
"ds_read_b128 v[124:127], v9 offset:21504\n"
"v_cvt_pk_bf16_f32 v68, v56, v57\n"
"v_cvt_pk_bf16_f32 v69, v58, v59\n"
"v_exp_f32_e32 v60, v28\n"
"v_exp_f32_e32 v61, v29\n"
"v_exp_f32_e32 v62, v30\n"
"v_exp_f32_e32 v63, v31\n"
"v_mfma_f32_32x32x8bf16_1k v[40:55], a[34:35], v[82:83], v[40:55]\n"
"v_cvt_pk_bf16_f32 v70, v60, v61\n"
"v_cvt_pk_bf16_f32 v71, v62, v63\n"
"v_exp_f32_e32 v56, v32\n"
"v_exp_f32_e32 v57, v33\n"
"v_exp_f32_e32 v58, v34\n"
"v_exp_f32_e32 v59, v35\n"
"v_mfma_f32_32x32x16_bf16 a[0:15], v[180:183], v[68:71], a[0:15]\n"
"s_waitcnt lgkmcnt(0)\n"
"v_cvt_pk_bf16_f32 v72, v56, v57\n"
"v_cvt_pk_bf16_f32 v73, v58, v59\n"
"v_exp_f32_e32 v64, v36\n"
"v_exp_f32_e32 v65, v37\n"
"v_add_f32_e32 v115, v115, v119\n"
"v_add_f32_e32 v112, v112, v116\n"
"v_add_f32_e32 v113, v113, v117\n"
"v_add_f32_e32 v114, v114, v118\n"
"v_add_f32_e32 v123, v123, v127\n"
"v_add_f32_e32 v120, v120, v124\n"
"v_exp_f32_e32 v66, v38\n"
"v_exp_f32_e32 v67, v39\n"
"v_mfma_f32_32x32x16_bf16 v[24:39], v[200:203], v[76:79], 0\n"
"v_cvt_pk_bf16_f32 v74, v64, v65\n"
"v_cvt_pk_bf16_f32 v75, v66, v67\n"
"v_exp_f32_e32 v56, v40\n"
"v_exp_f32_e32 v57, v41\n"
"v_add_f32_e32 v121, v121, v125\n"
"v_add_f32_e32 v122, v122, v126\n"
"v_add_f32_e32 v115, v115, v123\n"
"v_rcp_f32_e32 v115, v115\n"
"v_add_f32_e32 v112, v112, v120\n"
"v_add_f32_e32 v113, v113, v121\n"
"v_exp_f32_e32 v58, v42\n"
"v_exp_f32_e32 v59, v43\n"
"v_mfma_f32_32x32x16_bf16 a[0:15], v[184:187], v[72:75], a[0:15]\n"
"v_cvt_pk_bf16_f32 v68, v56, v57\n"
"v_cvt_pk_bf16_f32 v69, v58, v59\n"
"v_exp_f32_e32 v60, v44\n"
"v_exp_f32_e32 v61, v45\n"
"v_add_f32_e32 v114, v114, v122\n"
"v_mul_f32_e32 v112, v112, v115\n"
"v_mul_f32_e32 v113, v113, v115\n"
"v_mul_f32_e32 v114, v114, v115\n"
"v_cvt_pk_bf16_f32 v112, v112, v113\n"
"v_cvt_pk_bf16_f32 v114, v114, v114\n"
"v_exp_f32_e32 v62, v46\n"
"v_exp_f32_e32 v63, v47\n"
"v_mfma_f32_32x32x8bf16_1k v[24:39], a[36:37], v[80:81], v[24:39]\n"
"v_cvt_pk_bf16_f32 v70, v60, v61\n"
"v_cvt_pk_bf16_f32 v71, v62, v63\n"
"v_exp_f32_e32 v56, v48\n"
"v_exp_f32_e32 v57, v49\n"
"v_exp_f32_e32 v58, v50\n"
"v_exp_f32_e32 v59, v51\n"
"v_mfma_f32_32x32x16_bf16 a[0:15], v[192:195], v[68:71], a[0:15]\n"
"ds_write_b32 v10, v112 offset:432\n"
"ds_write_b16 v11, v114 offset:432\n"
"buffer_load_dword v6, v22, s[76:79], s69 offen\n"
"s_add_u32 s69, s69, 0x2400\n"
"v_cvt_pk_bf16_f32 v72, v56, v57\n"
"v_cvt_pk_bf16_f32 v73, v58, v59\n"
"v_exp_f32_e32 v64, v52\n"
"v_exp_f32_e32 v65, v53\n"
"v_exp_f32_e32 v66, v54\n"
"v_exp_f32_e32 v67, v55\n"
"v_mfma_f32_32x32x16_bf16 v[40:55], v[212:215], v[76:79], 0\n"
"v_cvt_pk_bf16_f32 v74, v64, v65\n"
"v_cvt_pk_bf16_f32 v75, v66, v67\n"
"v_exp_f32_e32 v56, v24\n"
"v_exp_f32_e32 v57, v25\n"
"v_exp_f32_e32 v58, v26\n"
"v_exp_f32_e32 v59, v27\n"
"v_mfma_f32_32x32x16_bf16 a[0:15], v[196:199], v[72:75], a[0:15]\n"
"s_waitcnt lgkmcnt(0)\n"
"s_barrier\n"
"v_cvt_pk_bf16_f32 v68, v56, v57\n"
"v_cvt_pk_bf16_f32 v69, v58, v59\n"
"v_exp_f32_e32 v60, v28\n"
"v_exp_f32_e32 v61, v29\n"
"v_exp_f32_e32 v62, v30\n"
"v_exp_f32_e32 v63, v31\n"
"v_mfma_f32_32x32x8bf16_1k v[40:55], a[36:37], v[82:83], v[40:55]\n"
"v_cvt_pk_bf16_f32 v70, v60, v61\n"
"v_cvt_pk_bf16_f32 v71, v62, v63\n"
"v_exp_f32_e32 v56, v32\n"
"v_exp_f32_e32 v57, v33\n"
"v_exp_f32_e32 v58, v34\n"
"v_exp_f32_e32 v59, v35\n"
"v_mfma_f32_32x32x16_bf16 a[0:15], v[204:207], v[68:71], a[0:15]\n"
"ds_read_b128 v[136:139], v12 offset:384\n"
"ds_read_b128 v[140:143], v13 offset:384\n"
"v_cvt_pk_bf16_f32 v72, v56, v57\n"
"v_cvt_pk_bf16_f32 v73, v58, v59\n"
"v_exp_f32_e32 v64, v36\n"
"v_exp_f32_e32 v65, v37\n"
"v_exp_f32_e32 v66, v38\n"
"v_exp_f32_e32 v67, v39\n"
"v_mfma_f32_32x32x16_bf16 v[24:39], v[224:227], v[76:79], 0\n"
"buffer_load_dwordx4 v[84:87], v3, s[48:51], s55 offen offset:720\n"
"buffer_load_dwordx2 v[88:89], v7, s[48:51], s55 offen offset:720\n"
"v_cvt_pk_bf16_f32 v74, v64, v65\n"
"v_cvt_pk_bf16_f32 v75, v66, v67\n"
"v_exp_f32_e32 v56, v40\n"
"v_exp_f32_e32 v57, v41\n"
"v_exp_f32_e32 v58, v42\n"
"v_exp_f32_e32 v59, v43\n"
"v_mfma_f32_32x32x16_bf16 a[0:15], v[208:211], v[72:75], a[0:15]\n"
"s_waitcnt lgkmcnt(0)\n"
"v_cvt_pk_bf16_f32 v68, v56, v57\n"
"v_cvt_pk_bf16_f32 v69, v58, v59\n"
"v_exp_f32_e32 v60, v44\n"
"v_exp_f32_e32 v61, v45\n"
"v_mfma_f32_16x16x32_bf16 v[92:95], a[240:243], v[136:139], v[92:95]\n"
"v_exp_f32_e32 v62, v46\n"
"v_exp_f32_e32 v63, v47\n"
"v_mfma_f32_32x32x8bf16_1k v[24:39], a[38:39], v[80:81], v[24:39]\n"
"v_cvt_pk_bf16_f32 v70, v60, v61\n"
"v_cvt_pk_bf16_f32 v71, v62, v63\n"
"v_exp_f32_e32 v56, v48\n"
"v_exp_f32_e32 v57, v49\n"
"v_exp_f32_e32 v58, v50\n"
"v_exp_f32_e32 v59, v51\n"
"v_mfma_f32_32x32x16_bf16 a[0:15], v[216:219], v[68:71], a[0:15]\n"
"v_cvt_pk_bf16_f32 v72, v56, v57\n"
"v_cvt_pk_bf16_f32 v73, v58, v59\n"
"v_exp_f32_e32 v64, v52\n"
"v_exp_f32_e32 v65, v53\n"
"v_mfma_f32_16x16x32_bf16 v[96:99], a[244:247], v[136:139], v[96:99]\n"
"v_exp_f32_e32 v66, v54\n"
"v_exp_f32_e32 v67, v55\n"
"v_mfma_f32_32x32x16_bf16 v[40:55], v[236:239], v[76:79], 0\n"
"buffer_load_dwordx4 a[240:243], v0, s[48:51], s88 offen\n"
"buffer_load_dwordx4 a[244:247], v0, s[48:51], s88 offen offset:1024\n"
"v_cvt_pk_bf16_f32 v74, v64, v65\n"
"v_cvt_pk_bf16_f32 v75, v66, v67\n"
"v_exp_f32_e32 v56, v24\n"
"v_exp_f32_e32 v57, v25\n"
"v_mfma_f32_16x16x32_bf16 v[100:103], a[248:251], v[136:139], v[100:103]\n"
"v_exp_f32_e32 v58, v26\n"
"v_exp_f32_e32 v59, v27\n"
"v_mfma_f32_32x32x16_bf16 a[0:15], v[220:223], v[72:75], a[0:15]\n"
"v_cvt_pk_bf16_f32 v68, v56, v57\n"
"v_cvt_pk_bf16_f32 v69, v58, v59\n"
"v_exp_f32_e32 v60, v28\n"
"v_exp_f32_e32 v61, v29\n"
"v_exp_f32_e32 v62, v30\n"
"v_exp_f32_e32 v63, v31\n"
"v_mfma_f32_32x32x8bf16_1k v[40:55], a[38:39], v[82:83], v[40:55]\n"
"v_cvt_pk_bf16_f32 v70, v60, v61\n"
"v_cvt_pk_bf16_f32 v71, v62, v63\n"
"v_exp_f32_e32 v56, v32\n"
"v_exp_f32_e32 v57, v33\n"
"v_exp_f32_e32 v58, v34\n"
"v_exp_f32_e32 v59, v35\n"
"v_mfma_f32_32x32x16_bf16 a[0:15], v[228:231], v[68:71], a[0:15]\n"
"v_cvt_pk_bf16_f32 v72, v56, v57\n"
"v_cvt_pk_bf16_f32 v73, v58, v59\n"
"v_exp_f32_e32 v64, v36\n"
"v_exp_f32_e32 v65, v37\n"
"v_mfma_f32_16x16x32_bf16 v[104:107], a[252:255], v[136:139], v[104:107]\n"
"v_exp_f32_e32 v66, v38\n"
"v_exp_f32_e32 v67, v39\n"
"v_mfma_f32_32x32x16_bf16 v[24:39], a[48:51], v[76:79], 0\n"
"buffer_load_dwordx4 a[248:251], v0, s[48:51], s88 offen offset:2048\n"
"buffer_load_dwordx4 a[252:255], v0, s[48:51], s88 offen offset:3072\n"
"v_cvt_pk_bf16_f32 v74, v64, v65\n"
"v_cvt_pk_bf16_f32 v75, v66, v67\n"
"v_exp_f32_e32 v56, v40\n"
"v_exp_f32_e32 v57, v41\n"
"v_mfma_f32_16x16x32_bf16 v[108:111], v[248:251], v[136:139], v[108:111]\n"
"v_exp_f32_e32 v58, v42\n"
"v_exp_f32_e32 v59, v43\n"
"v_mfma_f32_32x32x16_bf16 a[0:15], v[232:235], v[72:75], a[0:15]\n"
"s_add_u32 s88, s59, 0x16000\n"
"v_cvt_pk_bf16_f32 v68, v56, v57\n"
"v_cvt_pk_bf16_f32 v69, v58, v59\n"
"v_exp_f32_e32 v60, v44\n"
"v_exp_f32_e32 v61, v45\n"
"v_exp_f32_e32 v62, v46\n"
"v_exp_f32_e32 v63, v47\n"
"v_mfma_f32_32x32x8bf16_1k v[24:39], a[40:41], v[80:81], v[24:39]\n"
"v_cvt_pk_bf16_f32 v70, v60, v61\n"
"v_cvt_pk_bf16_f32 v71, v62, v63\n"
"v_exp_f32_e32 v56, v48\n"
"v_exp_f32_e32 v57, v49\n"
"v_exp_f32_e32 v58, v50\n"
"v_exp_f32_e32 v59, v51\n"
"v_mfma_f32_32x32x16_bf16 a[0:15], v[240:243], v[68:71], a[0:15]\n"
"v_cvt_pk_bf16_f32 v72, v56, v57\n"
"v_cvt_pk_bf16_f32 v73, v58, v59\n"
"v_exp_f32_e32 v64, v52\n"
"v_exp_f32_e32 v65, v53\n"
"v_mfma_f32_16x16x32_bf16 v[92:95], v[252:255], v[140:143], v[92:95]\n"
"v_exp_f32_e32 v66, v54\n"
"v_exp_f32_e32 v67, v55\n"
"v_mfma_f32_32x32x16_bf16 v[40:55], a[60:63], v[76:79], 0\n"
"buffer_load_dwordx4 v[248:251], v0, s[48:51], s88 offen\n"
"buffer_load_dwordx4 v[252:255], v0, s[48:51], s88 offen offset:1024\n"
"v_cvt_pk_bf16_f32 v74, v64, v65\n"
"v_cvt_pk_bf16_f32 v75, v66, v67\n"
"v_exp_f32_e32 v56, v24\n"
"v_exp_f32_e32 v57, v25\n"
"v_mfma_f32_16x16x32_bf16 v[96:99], a[144:147], v[140:143], v[96:99]\n"
"v_exp_f32_e32 v58, v26\n"
"v_exp_f32_e32 v59, v27\n"
"v_mfma_f32_32x32x16_bf16 a[0:15], v[244:247], v[72:75], a[0:15]\n"
"v_cvt_pk_bf16_f32 v68, v56, v57\n"
"v_cvt_pk_bf16_f32 v69, v58, v59\n"
"v_exp_f32_e32 v60, v28\n"
"v_exp_f32_e32 v61, v29\n"
"v_exp_f32_e32 v62, v30\n"
"v_exp_f32_e32 v63, v31\n"
"v_mfma_f32_32x32x8bf16_1k v[40:55], a[40:41], v[82:83], v[40:55]\n"
"v_cvt_pk_bf16_f32 v70, v60, v61\n"
"v_cvt_pk_bf16_f32 v71, v62, v63\n"
"v_exp_f32_e32 v56, v32\n"
"v_exp_f32_e32 v57, v33\n"
"v_exp_f32_e32 v58, v34\n"
"v_exp_f32_e32 v59, v35\n"
"v_mfma_f32_32x32x16_bf16 a[0:15], a[52:55], v[68:71], a[0:15]\n"
"v_cvt_pk_bf16_f32 v72, v56, v57\n"
"v_cvt_pk_bf16_f32 v73, v58, v59\n"
"v_exp_f32_e32 v64, v36\n"
"v_exp_f32_e32 v65, v37\n"
"v_mfma_f32_16x16x32_bf16 v[100:103], a[148:151], v[140:143], v[100:103]\n"
"v_exp_f32_e32 v66, v38\n"
"v_exp_f32_e32 v67, v39\n"
"v_mfma_f32_32x32x16_bf16 v[24:39], a[72:75], v[76:79], 0\n"
"ds_read_b128 v[144:147], v12 offset:448\n"
"ds_read_b128 v[148:151], v13 offset:448\n"
"v_cvt_pk_bf16_f32 v74, v64, v65\n"
"v_cvt_pk_bf16_f32 v75, v66, v67\n"
"v_exp_f32_e32 v56, v40\n"
"v_exp_f32_e32 v57, v41\n"
"v_mfma_f32_16x16x32_bf16 v[104:107], a[152:155], v[140:143], v[104:107]\n"
"v_exp_f32_e32 v58, v42\n"
"v_exp_f32_e32 v59, v43\n"
"v_mfma_f32_32x32x16_bf16 a[0:15], a[56:59], v[72:75], a[0:15]\n"
"buffer_load_dwordx4 a[144:147], v0, s[48:51], s88 offen offset:2048\n"
"buffer_load_dwordx4 a[148:151], v0, s[48:51], s88 offen offset:3072\n"
"v_cvt_pk_bf16_f32 v68, v56, v57\n"
"v_cvt_pk_bf16_f32 v69, v58, v59\n"
"v_exp_f32_e32 v60, v44\n"
"v_exp_f32_e32 v61, v45\n"
"v_exp_f32_e32 v62, v46\n"
"v_exp_f32_e32 v63, v47\n"
"v_mfma_f32_32x32x8bf16_1k v[24:39], a[42:43], v[80:81], v[24:39]\n"
"v_cvt_pk_bf16_f32 v70, v60, v61\n"
"v_cvt_pk_bf16_f32 v71, v62, v63\n"
"v_exp_f32_e32 v56, v48\n"
"v_exp_f32_e32 v57, v49\n"
"v_exp_f32_e32 v58, v50\n"
"v_exp_f32_e32 v59, v51\n"
"v_mfma_f32_32x32x16_bf16 a[0:15], a[64:67], v[68:71], a[0:15]\n"
"s_waitcnt vmcnt(25) lgkmcnt(0)\n"
"v_cvt_pk_bf16_f32 v72, v56, v57\n"
"v_cvt_pk_bf16_f32 v73, v58, v59\n"
"v_exp_f32_e32 v64, v52\n"
"v_exp_f32_e32 v65, v53\n"
"v_mfma_f32_16x16x32_bf16 v[92:95], a[156:159], v[144:147], v[92:95]\n"
"v_exp_f32_e32 v66, v54\n"
"v_exp_f32_e32 v67, v55\n"
"v_mfma_f32_32x32x16_bf16 v[40:55], a[84:87], v[76:79], 0\n"
"s_add_u32 s88, s59, 0x17000\n"
"v_cvt_pk_bf16_f32 v74, v64, v65\n"
"v_cvt_pk_bf16_f32 v75, v66, v67\n"
"v_exp_f32_e32 v56, v24\n"
".LK3_pad4:\n"
"s_cbranch_execz .LK1_ret\n"
"v_exp_f32_e32 v57, v25\n"
"v_mfma_f32_16x16x32_bf16 v[96:99], a[160:163], v[144:147], v[96:99]\n"
"v_exp_f32_e32 v58, v26\n"
"v_exp_f32_e32 v59, v27\n"
"v_mfma_f32_32x32x16_bf16 a[0:15], a[68:71], v[72:75], a[0:15]\n"
"buffer_load_dwordx4 a[152:155], v0, s[48:51], s88 offen\n"
"buffer_load_dwordx4 a[156:159], v0, s[48:51], s88 offen offset:1024\n"
"v_cvt_pk_bf16_f32 v68, v56, v57\n"
"v_cvt_pk_bf16_f32 v69, v58, v59\n"
"v_exp_f32_e32 v60, v28\n"
"v_exp_f32_e32 v61, v29\n"
"v_exp_f32_e32 v62, v30\n"
"v_exp_f32_e32 v63, v31\n"
"v_mfma_f32_32x32x8bf16_1k v[40:55], a[42:43], v[82:83], v[40:55]\n"
"v_cvt_pk_bf16_f32 v70, v60, v61\n"
"v_cvt_pk_bf16_f32 v71, v62, v63\n"
"v_exp_f32_e32 v56, v32\n"
"v_exp_f32_e32 v57, v33\n"
"v_exp_f32_e32 v58, v34\n"
"v_exp_f32_e32 v59, v35\n"
"v_mfma_f32_32x32x16_bf16 a[0:15], a[76:79], v[68:71], a[0:15]\n"
"v_cvt_pk_bf16_f32 v72, v56, v57\n"
"v_cvt_pk_bf16_f32 v73, v58, v59\n"
"v_exp_f32_e32 v64, v36\n"
"v_exp_f32_e32 v65, v37\n"
"v_mfma_f32_16x16x32_bf16 v[100:103], a[164:167], v[144:147], v[100:103]\n"
"v_exp_f32_e32 v66, v38\n"
"v_exp_f32_e32 v67, v39\n"
"v_mfma_f32_32x32x16_bf16 v[24:39], a[96:99], v[76:79], 0\n"
"buffer_load_dwordx4 a[160:163], v0, s[48:51], s88 offen offset:2048\n"
"buffer_load_dwordx4 a[164:167], v0, s[48:51], s88 offen offset:3072\n"
"v_cvt_pk_bf16_f32 v74, v64, v65\n"
"v_cvt_pk_bf16_f32 v75, v66, v67\n"
"v_exp_f32_e32 v56, v40\n"
"v_exp_f32_e32 v57, v41\n"
"v_mfma_f32_16x16x32_bf16 v[104:107], a[168:171], v[144:147], v[104:107]\n"
"v_exp_f32_e32 v58, v42\n"
"v_exp_f32_e32 v59, v43\n"
"v_mfma_f32_32x32x16_bf16 a[0:15], a[80:83], v[72:75], a[0:15]\n"
"s_add_u32 s88, s59, 0x18000\n"
"v_cvt_pk_bf16_f32 v68, v56, v57\n"
"v_cvt_pk_bf16_f32 v69, v58, v59\n"
"v_exp_f32_e32 v60, v44\n"
"v_exp_f32_e32 v61, v45\n"
"v_exp_f32_e32 v62, v46\n"
"v_exp_f32_e32 v63, v47\n"
"v_mfma_f32_32x32x8bf16_1k v[24:39], a[44:45], v[80:81], v[24:39]\n"
"v_cvt_pk_bf16_f32 v70, v60, v61\n"
"v_cvt_pk_bf16_f32 v71, v62, v63\n"
"v_exp_f32_e32 v56, v48\n"
"v_exp_f32_e32 v57, v49\n"
"v_exp_f32_e32 v58, v50\n"
"v_exp_f32_e32 v59, v51\n"
"v_mfma_f32_32x32x16_bf16 a[0:15], a[88:91], v[68:71], a[0:15]\n"
"v_cvt_pk_bf16_f32 v72, v56, v57\n"
"v_cvt_pk_bf16_f32 v73, v58, v59\n"
"v_exp_f32_e32 v64, v52\n"
"v_exp_f32_e32 v65, v53\n"
"v_mfma_f32_16x16x32_bf16 v[108:111], a[172:175], v[144:147], v[108:111]\n"
"v_exp_f32_e32 v66, v54\n"
"v_exp_f32_e32 v67, v55\n"
"v_mfma_f32_32x32x16_bf16 v[40:55], a[108:111], v[76:79], 0\n"
"buffer_load_dwordx4 a[168:171], v0, s[48:51], s88 offen\n"
"buffer_load_dwordx4 a[172:175], v0, s[48:51], s88 offen offset:1024\n"
"v_cvt_pk_bf16_f32 v74, v64, v65\n"
"v_cvt_pk_bf16_f32 v75, v66, v67\n"
"v_exp_f32_e32 v56, v24\n"
"v_exp_f32_e32 v57, v25\n"
"v_mfma_f32_16x16x32_bf16 v[92:95], a[176:179], v[148:151], v[92:95]\n"
"v_exp_f32_e32 v58, v26\n"
"v_exp_f32_e32 v59, v27\n"
"v_mfma_f32_32x32x16_bf16 a[0:15], a[92:95], v[72:75], a[0:15]\n"
"v_cvt_pk_bf16_f32 v68, v56, v57\n"
"v_cvt_pk_bf16_f32 v69, v58, v59\n"
"v_exp_f32_e32 v60, v28\n"
"v_exp_f32_e32 v61, v29\n"
"v_exp_f32_e32 v62, v30\n"
"v_exp_f32_e32 v63, v31\n"
"v_mfma_f32_32x32x8bf16_1k v[40:55], a[44:45], v[82:83], v[40:55]\n"
"v_cvt_pk_bf16_f32 v70, v60, v61\n"
"v_cvt_pk_bf16_f32 v71, v62, v63\n"
"v_exp_f32_e32 v56, v32\n"
"v_exp_f32_e32 v57, v33\n"
"v_exp_f32_e32 v58, v34\n"
"v_exp_f32_e32 v59, v35\n"
"v_mfma_f32_32x32x16_bf16 a[0:15], a[100:103], v[68:71], a[0:15]\n"
"buffer_load_dword v6, v22, s[76:79], s69 offen\n"
"s_add_u32 s69, s69, 0x2400\n"
"v_cvt_pk_bf16_f32 v72, v56, v57\n"
"v_cvt_pk_bf16_f32 v73, v58, v59\n"
"v_exp_f32_e32 v64, v36\n"
"v_exp_f32_e32 v65, v37\n"
"v_mfma_f32_16x16x32_bf16 v[96:99], a[180:183], v[148:151], v[96:99]\n"
"v_exp_f32_e32 v66, v38\n"
"v_exp_f32_e32 v67, v39\n"
"v_mfma_f32_32x32x16_bf16 v[24:39], a[120:123], v[76:79], 0\n"
"buffer_load_dwordx4 a[176:179], v0, s[48:51], s88 offen offset:2048\n"
"buffer_load_dwordx4 a[180:183], v0, s[48:51], s88 offen offset:3072\n"
"v_cvt_pk_bf16_f32 v74, v64, v65\n"
"v_cvt_pk_bf16_f32 v75, v66, v67\n"
"v_exp_f32_e32 v56, v40\n"
"v_exp_f32_e32 v57, v41\n"
"v_mfma_f32_16x16x32_bf16 v[100:103], a[184:187], v[148:151], v[100:103]\n"
"v_exp_f32_e32 v58, v42\n"
"v_exp_f32_e32 v59, v43\n"
"v_mfma_f32_32x32x16_bf16 a[0:15], a[104:107], v[72:75], a[0:15]\n"
"ds_read_b128 v[136:139], v12 offset:512\n"
"ds_read_b128 v[140:143], v13 offset:512\n"
"v_cvt_pk_bf16_f32 v68, v56, v57\n"
"v_cvt_pk_bf16_f32 v69, v58, v59\n"
"v_exp_f32_e32 v60, v44\n"
"v_exp_f32_e32 v61, v45\n"
"v_exp_f32_e32 v62, v46\n"
"v_exp_f32_e32 v63, v47\n"
"v_mfma_f32_32x32x8bf16_1k v[24:39], a[46:47], v[80:81], v[24:39]\n"
"v_cvt_pk_bf16_f32 v70, v60, v61\n"
"v_cvt_pk_bf16_f32 v71, v62, v63\n"
"v_exp_f32_e32 v56, v48\n"
"v_exp_f32_e32 v57, v49\n"
"v_exp_f32_e32 v58, v50\n"
"v_exp_f32_e32 v59, v51\n"
"v_mfma_f32_32x32x16_bf16 a[0:15], a[112:115], v[68:71], a[0:15]\n"
"s_add_u32 s88, s59, 0x19000\n"
"v_cvt_pk_bf16_f32 v72, v56, v57\n"
"v_cvt_pk_bf16_f32 v73, v58, v59\n"
"v_exp_f32_e32 v64, v52\n"
"v_exp_f32_e32 v65, v53\n"
"v_mfma_f32_16x16x32_bf16 v[104:107], a[188:191], v[148:151], v[104:107]\n"
"v_exp_f32_e32 v66, v54\n"
"v_exp_f32_e32 v67, v55\n"
"v_mfma_f32_32x32x16_bf16 v[40:55], a[132:135], v[76:79], 0\n"
"s_waitcnt vmcnt(24) lgkmcnt(0)\n"
"v_cvt_pk_bf16_f32 v74, v64, v65\n"
"v_cvt_pk_bf16_f32 v75, v66, v67\n"
"v_exp_f32_e32 v56, v24\n"
"v_exp_f32_e32 v57, v25\n"
"v_mfma_f32_16x16x32_bf16 v[92:95], a[192:195], v[136:139], v[92:95]\n"
"v_exp_f32_e32 v58, v26\n"
"v_exp_f32_e32 v59, v27\n"
"v_mfma_f32_32x32x16_bf16 a[0:15], a[116:119], v[72:75], a[0:15]\n"
"s_waitcnt vmcnt(17)\n"
"v_cvt_pk_bf16_f32 v68, v56, v57\n"
"v_cvt_pk_bf16_f32 v69, v58, v59\n"
"v_exp_f32_e32 v60, v28\n"
"v_exp_f32_e32 v61, v29\n"
"v_cndmask_b32_e64 v90, 0, v89, s[36:37]\n"
"v_mov_b32_e32 v91, v88\n"
"v_cndmask_b32_e64 v89, 0, v89, s[36:37]\n"
"v_exp_f32_e32 v62, v30\n"
"v_exp_f32_e32 v63, v31\n"
"v_mfma_f32_32x32x8bf16_1k v[40:55], a[46:47], v[82:83], v[40:55]\n"
"v_cvt_pk_bf16_f32 v70, v60, v61\n"
"v_cvt_pk_bf16_f32 v71, v62, v63\n"
"v_exp_f32_e32 v56, v32\n"
"v_exp_f32_e32 v57, v33\n"
"v_exp_f32_e32 v58, v34\n"
"v_exp_f32_e32 v59, v35\n"
"v_mfma_f32_32x32x16_bf16 a[0:15], a[124:127], v[68:71], a[0:15]\n"
"buffer_load_dwordx4 a[184:187], v0, s[48:51], s88 offen\n"
"buffer_load_dwordx4 a[188:191], v0, s[48:51], s88 offen offset:1024\n"
"v_cvt_pk_bf16_f32 v72, v56, v57\n"
"v_cvt_pk_bf16_f32 v73, v58, v59\n"
"v_exp_f32_e32 v64, v36\n"
"v_exp_f32_e32 v65, v37\n"
"v_mfma_f32_16x16x32_bf16 v[96:99], a[196:199], v[136:139], v[96:99]\n"
"v_exp_f32_e32 v66, v38\n"
"v_exp_f32_e32 v67, v39\n"
"v_mfma_f32_32x32x16_bf16 v[24:39], v[152:155], v[84:87], 0\n"
"buffer_load_dwordx4 a[192:195], v0, s[48:51], s88 offen offset:2048\n"
"buffer_load_dwordx4 a[196:199], v0, s[48:51], s88 offen offset:3072\n"
"v_cvt_pk_bf16_f32 v74, v64, v65\n"
"v_cvt_pk_bf16_f32 v75, v66, v67\n"
"v_exp_f32_e32 v56, v40\n"
"v_exp_f32_e32 v57, v41\n"
"v_mfma_f32_16x16x32_bf16 v[100:103], a[200:203], v[136:139], v[100:103]\n"
"v_exp_f32_e32 v58, v42\n"
"v_exp_f32_e32 v59, v43\n"
"v_mfma_f32_32x32x16_bf16 a[0:15], a[128:131], v[72:75], a[0:15]\n"
"s_add_u32 s88, s59, 0x1a000\n"
"v_cvt_pk_bf16_f32 v68, v56, v57\n"
"v_cvt_pk_bf16_f32 v69, v58, v59\n"
"v_exp_f32_e32 v60, v44\n"
"v_exp_f32_e32 v61, v45\n"
"v_exp_f32_e32 v62, v46\n"
"v_exp_f32_e32 v63, v47\n"
"v_mfma_f32_32x32x8bf16_1k v[24:39], a[32:33], v[88:89], v[24:39]\n"
"v_cvt_pk_bf16_f32 v70, v60, v61\n"
"v_cvt_pk_bf16_f32 v71, v62, v63\n"
"v_exp_f32_e32 v56, v48\n"
"v_exp_f32_e32 v57, v49\n"
"v_exp_f32_e32 v58, v50\n"
"v_exp_f32_e32 v59, v51\n"
"v_mfma_f32_32x32x16_bf16 a[0:15], a[136:139], v[68:71], a[0:15]\n"
"v_cvt_pk_bf16_f32 v72, v56, v57\n"
"v_cvt_pk_bf16_f32 v73, v58, v59\n"
"v_exp_f32_e32 v64, v52\n"
"v_exp_f32_e32 v65, v53\n"
"v_mfma_f32_16x16x32_bf16 v[104:107], a[204:207], v[136:139], v[104:107]\n"
"v_exp_f32_e32 v66, v54\n"
"v_exp_f32_e32 v67, v55\n"
"v_mfma_f32_32x32x16_bf16 v[40:55], v[164:167], v[84:87], 0\n"
"buffer_load_dwordx4 a[200:203], v0, s[48:51], s88 offen\n"
"buffer_load_dwordx4 a[204:207], v0, s[48:51], s88 offen offset:1024\n"
"v_cvt_pk_bf16_f32 v74, v64, v65\n"
"v_cvt_pk_bf16_f32 v75, v66, v67\n"
"v_exp_f32_e32 v56, v24\n"
"v_exp_f32_e32 v57, v25\n"
"v_mfma_f32_16x16x32_bf16 v[108:111], a[208:211], v[136:139], v[108:111]\n"
"v_exp_f32_e32 v58, v26\n"
"v_exp_f32_e32 v59, v27\n"
"v_mfma_f32_32x32x16_bf16 a[0:15], a[140:143], v[72:75], a[0:15]\n"
"v_cvt_pk_bf16_f32 v68, v56, v57\n"
"v_cvt_pk_bf16_f32 v69, v58, v59\n"
"v_exp_f32_e32 v60, v28\n"
"v_exp_f32_e32 v61, v29\n"
"v_exp_f32_e32 v62, v30\n"
"v_exp_f32_e32 v63, v31\n"
"v_mfma_f32_32x32x8bf16_1k v[40:55], a[32:33], v[90:91], v[40:55]\n"
"v_cvt_pk_bf16_f32 v70, v60, v61\n"
"v_cvt_pk_bf16_f32 v71, v62, v63\n"
"v_exp_f32_e32 v56, v32\n"
"v_exp_f32_e32 v57, v33\n"
"v_exp_f32_e32 v58, v34\n"
"v_exp_f32_e32 v59, v35\n"
"v_mfma_f32_32x32x16_bf16 a[16:31], v[156:159], v[68:71], 0\n"
"ds_write_b128 v8, a[0:3] offset:0\n"
"v_cvt_pk_bf16_f32 v72, v56, v57\n"
"v_cvt_pk_bf16_f32 v73, v58, v59\n"
"v_exp_f32_e32 v64, v36\n"
"v_exp_f32_e32 v65, v37\n"
"v_mfma_f32_16x16x32_bf16 v[92:95], a[212:215], v[140:143], v[92:95]\n"
"v_exp_f32_e32 v66, v38\n"
"v_exp_f32_e32 v67, v39\n"
"v_mfma_f32_32x32x16_bf16 v[24:39], v[176:179], v[84:87], 0\n"
"ds_write_b128 v8, a[4:7] offset:1024\n"
"v_cvt_pk_bf16_f32 v74, v64, v65\n"
"v_cvt_pk_bf16_f32 v75, v66, v67\n"
"v_exp_f32_e32 v56, v40\n"
"v_exp_f32_e32 v57, v41\n"
"v_mfma_f32_16x16x32_bf16 v[96:99], a[216:219], v[140:143], v[96:99]\n"
"v_exp_f32_e32 v58, v42\n"
"v_exp_f32_e32 v59, v43\n"
"v_mfma_f32_32x32x16_bf16 a[16:31], v[160:163], v[72:75], a[16:31]\n"
"ds_write_b128 v8, a[8:11] offset:2048\n"
"v_cvt_pk_bf16_f32 v68, v56, v57\n"
"v_cvt_pk_bf16_f32 v69, v58, v59\n"
"v_exp_f32_e32 v60, v44\n"
"v_exp_f32_e32 v61, v45\n"
"v_exp_f32_e32 v62, v46\n"
"v_exp_f32_e32 v63, v47\n"
"v_mfma_f32_32x32x8bf16_1k v[24:39], a[34:35], v[88:89], v[24:39]\n"
"v_cvt_pk_bf16_f32 v70, v60, v61\n"
"v_cvt_pk_bf16_f32 v71, v62, v63\n"
"v_exp_f32_e32 v56, v48\n"
"v_exp_f32_e32 v57, v49\n"
"v_exp_f32_e32 v58, v50\n"
"v_exp_f32_e32 v59, v51\n"
"v_mfma_f32_32x32x16_bf16 a[16:31], v[168:171], v[68:71], a[16:31]\n"
"s_waitcnt lgkmcnt(0)\n"
"s_barrier\n"
"s_add_u32 s90, s66, 0x10000\n"
"buffer_store_dwordx2 v[132:133], v19, s[48:51], s90 offen sc1\n"
"v_cvt_pk_bf16_f32 v72, v56, v57\n"
"v_cvt_pk_bf16_f32 v73, v58, v59\n"
"v_exp_f32_e32 v64, v52\n"
"v_exp_f32_e32 v65, v53\n"
"v_mfma_f32_16x16x32_bf16 v[100:103], a[220:223], v[140:143], v[100:103]\n"
"v_exp_f32_e32 v66, v54\n"
"v_exp_f32_e32 v67, v55\n"
"v_mfma_f32_32x32x16_bf16 v[40:55], v[188:191], v[84:87], 0\n"
"ds_read_b128 v[112:115], v9 offset:0\n"
"ds_read_b128 v[116:119], v9 offset:3072\n"
"v_cvt_pk_bf16_f32 v74, v64, v65\n"
"v_cvt_pk_bf16_f32 v75, v66, v67\n"
"v_exp_f32_e32 v56, v24\n"
"v_exp_f32_e32 v57, v25\n"
"v_mfma_f32_16x16x32_bf16 v[104:107], a[224:227], v[140:143], v[104:107]\n"
"v_exp_f32_e32 v58, v26\n"
"v_exp_f32_e32 v59, v27\n"
"v_mfma_f32_32x32x16_bf16 a[16:31], v[172:175], v[72:75], a[16:31]\n"
"ds_read_b128 v[120:123], v9 offset:6144\n"
"ds_read_b128 v[124:127], v9 offset:9216\n"
"v_cvt_pk_bf16_f32 v68, v56, v57\n"
"v_cvt_pk_bf16_f32 v69, v58, v59\n"
"v_exp_f32_e32 v60, v28\n"
"v_exp_f32_e32 v61, v29\n"
"v_exp_f32_e32 v62, v30\n"
"v_exp_f32_e32 v63, v31\n"
"v_mfma_f32_32x32x8bf16_1k v[40:55], a[34:35], v[90:91], v[40:55]\n"
"v_cvt_pk_bf16_f32 v70, v60, v61\n"
"v_cvt_pk_bf16_f32 v71, v62, v63\n"
"v_exp_f32_e32 v56, v32\n"
"v_exp_f32_e32 v57, v33\n"
"v_exp_f32_e32 v58, v34\n"
"v_exp_f32_e32 v59, v35\n"
"v_mfma_f32_32x32x16_bf16 a[16:31], v[180:183], v[68:71], a[16:31]\n"
"s_waitcnt lgkmcnt(0)\n"
"v_cvt_pk_bf16_f32 v72, v56, v57\n"
"v_cvt_pk_bf16_f32 v73, v58, v59\n"
"v_exp_f32_e32 v64, v36\n"
"v_exp_f32_e32 v65, v37\n"
"v_add_f32_e32 v115, v115, v119\n"
"v_add_f32_e32 v112, v112, v116\n"
"v_add_f32_e32 v113, v113, v117\n"
"v_add_f32_e32 v114, v114, v118\n"
"v_add_f32_e32 v123, v123, v127\n"
"v_add_f32_e32 v120, v120, v124\n"
"v_exp_f32_e32 v66, v38\n"
"v_exp_f32_e32 v67, v39\n"
"v_mfma_f32_32x32x16_bf16 v[24:39], v[200:203], v[84:87], 0\n"
"buffer_load_dwordx4 a[208:211], v0, s[48:51], s88 offen offset:2048\n"
"buffer_load_dwordx4 a[212:215], v0, s[48:51], s88 offen offset:3072\n"
"v_cvt_pk_bf16_f32 v74, v64, v65\n"
"v_cvt_pk_bf16_f32 v75, v66, v67\n"
"v_exp_f32_e32 v56, v40\n"
"v_exp_f32_e32 v57, v41\n"
"v_add_f32_e32 v121, v121, v125\n"
"v_add_f32_e32 v122, v122, v126\n"
"v_add_f32_e32 v115, v115, v123\n"
"v_rcp_f32_e32 v115, v115\n"
"v_add_f32_e32 v112, v112, v120\n"
"v_add_f32_e32 v113, v113, v121\n"
"v_exp_f32_e32 v58, v42\n"
"v_exp_f32_e32 v59, v43\n"
"v_mfma_f32_32x32x16_bf16 a[16:31], v[184:187], v[72:75], a[16:31]\n"
"s_add_u32 s88, s59, 0x1b000\n"
"v_cvt_pk_bf16_f32 v68, v56, v57\n"
"v_cvt_pk_bf16_f32 v69, v58, v59\n"
"v_exp_f32_e32 v60, v44\n"
"v_exp_f32_e32 v61, v45\n"
"v_add_f32_e32 v114, v114, v122\n"
"v_mul_f32_e32 v112, v112, v115\n"
"v_mul_f32_e32 v113, v113, v115\n"
"v_mul_f32_e32 v114, v114, v115\n"
"v_cvt_pk_bf16_f32 v112, v112, v113\n"
"v_cvt_pk_bf16_f32 v114, v114, v114\n"
"v_exp_f32_e32 v62, v46\n"
"v_exp_f32_e32 v63, v47\n"
"v_mfma_f32_32x32x8bf16_1k v[24:39], a[36:37], v[88:89], v[24:39]\n"
"v_cvt_pk_bf16_f32 v70, v60, v61\n"
"v_cvt_pk_bf16_f32 v71, v62, v63\n"
"v_exp_f32_e32 v56, v48\n"
"v_exp_f32_e32 v57, v49\n"
"v_exp_f32_e32 v58, v50\n"
"v_exp_f32_e32 v59, v51\n"
"v_mfma_f32_32x32x16_bf16 a[16:31], v[192:195], v[68:71], a[16:31]\n"
"ds_write_b32 v10, v112 offset:576\n"
"ds_write_b16 v11, v114 offset:576\n"
"v_cvt_pk_bf16_f32 v72, v56, v57\n"
"v_cvt_pk_bf16_f32 v73, v58, v59\n"
"v_exp_f32_e32 v64, v52\n"
"v_exp_f32_e32 v65, v53\n"
"v_exp_f32_e32 v66, v54\n"
"v_exp_f32_e32 v67, v55\n"
"v_mfma_f32_32x32x16_bf16 v[40:55], v[212:215], v[84:87], 0\n"
"buffer_load_dwordx4 a[216:219], v0, s[48:51], s88 offen\n"
"buffer_load_dwordx4 a[220:223], v0, s[48:51], s88 offen offset:1024\n"
"v_cvt_pk_bf16_f32 v74, v64, v65\n"
"v_cvt_pk_bf16_f32 v75, v66, v67\n"
"v_exp_f32_e32 v56, v24\n"
"v_exp_f32_e32 v57, v25\n"
"v_exp_f32_e32 v58, v26\n"
"v_exp_f32_e32 v59, v27\n"
"v_mfma_f32_32x32x16_bf16 a[16:31], v[196:199], v[72:75], a[16:31]\n"
"s_waitcnt lgkmcnt(0)\n"
"s_barrier\n"
"v_cvt_pk_bf16_f32 v68, v56, v57\n"
"v_cvt_pk_bf16_f32 v69, v58, v59\n"
"v_exp_f32_e32 v60, v28\n"
"v_exp_f32_e32 v61, v29\n"
"v_exp_f32_e32 v62, v30\n"
"v_exp_f32_e32 v63, v31\n"
"v_mfma_f32_32x32x8bf16_1k v[40:55], a[36:37], v[90:91], v[40:55]\n"
"v_cvt_pk_bf16_f32 v70, v60, v61\n"
"v_cvt_pk_bf16_f32 v71, v62, v63\n"
"v_exp_f32_e32 v56, v32\n"
"v_exp_f32_e32 v57, v33\n"
"v_exp_f32_e32 v58, v34\n"
"v_exp_f32_e32 v59, v35\n"
"v_mfma_f32_32x32x16_bf16 a[16:31], v[204:207], v[68:71], a[16:31]\n"
"ds_read_b128 v[144:147], v12 offset:576\n"
"ds_read_b128 v[148:151], v13 offset:576\n"
"v_cvt_pk_bf16_f32 v72, v56, v57\n"
"v_cvt_pk_bf16_f32 v73, v58, v59\n"
"v_exp_f32_e32 v64, v36\n"
"v_exp_f32_e32 v65, v37\n"
"v_exp_f32_e32 v66, v38\n"
"v_exp_f32_e32 v67, v39\n"
"v_mfma_f32_32x32x16_bf16 v[24:39], v[224:227], v[84:87], 0\n"
"buffer_load_dwordx4 v[76:79], v3, s[48:51], s55 offen offset:864\n"
"buffer_load_dwordx2 v[80:81], v7, s[48:51], s55 offen offset:864\n"
"v_cvt_pk_bf16_f32 v74, v64, v65\n"
"v_cvt_pk_bf16_f32 v75, v66, v67\n"
"v_exp_f32_e32 v56, v40\n"
"v_exp_f32_e32 v57, v41\n"
"v_exp_f32_e32 v58, v42\n"
"v_exp_f32_e32 v59, v43\n"
"v_mfma_f32_32x32x16_bf16 a[16:31], v[208:211], v[72:75], a[16:31]\n"
"s_waitcnt vmcnt(24) lgkmcnt(0)\n"
"v_cvt_pk_bf16_f32 v68, v56, v57\n"
"v_cvt_pk_bf16_f32 v69, v58, v59\n"
"v_exp_f32_e32 v60, v44\n"
"v_exp_f32_e32 v61, v45\n"
"v_mfma_f32_16x16x32_bf16 v[92:95], a[228:231], v[144:147], v[92:95]\n"
"v_exp_f32_e32 v62, v46\n"
"v_exp_f32_e32 v63, v47\n"
"v_mfma_f32_32x32x8bf16_1k v[24:39], a[38:39], v[88:89], v[24:39]\n"
"v_cvt_pk_bf16_f32 v70, v60, v61\n"
"v_cvt_pk_bf16_f32 v71, v62, v63\n"
"v_exp_f32_e32 v56, v48\n"
"v_exp_f32_e32 v57, v49\n"
"v_exp_f32_e32 v58, v50\n"
"v_exp_f32_e32 v59, v51\n"
"v_mfma_f32_32x32x16_bf16 a[16:31], v[216:219], v[68:71], a[16:31]\n"
"buffer_load_dword v6, v22, s[76:79], s69 offen\n"
"s_add_u32 s69, s69, 0x2400\n"
"v_cvt_pk_bf16_f32 v72, v56, v57\n"
"v_cvt_pk_bf16_f32 v73, v58, v59\n"
"v_exp_f32_e32 v64, v52\n"
"v_exp_f32_e32 v65, v53\n"
"v_mfma_f32_16x16x32_bf16 v[96:99], a[232:235], v[144:147], v[96:99]\n"
"v_exp_f32_e32 v66, v54\n"
"v_exp_f32_e32 v67, v55\n"
"v_mfma_f32_32x32x16_bf16 v[40:55], v[236:239], v[84:87], 0\n"
"buffer_load_dwordx4 a[224:227], v0, s[48:51], s88 offen offset:2048\n"
"buffer_load_dwordx4 a[228:231], v0, s[48:51], s88 offen offset:3072\n"
"v_cvt_pk_bf16_f32 v74, v64, v65\n"
"v_cvt_pk_bf16_f32 v75, v66, v67\n"
"v_exp_f32_e32 v56, v24\n"
"v_exp_f32_e32 v57, v25\n"
"v_mfma_f32_16x16x32_bf16 v[100:103], a[236:239], v[144:147], v[100:103]\n"
"v_exp_f32_e32 v58, v26\n"
"v_exp_f32_e32 v59, v27\n"
"v_mfma_f32_32x32x16_bf16 a[16:31], v[220:223], v[72:75], a[16:31]\n"
"s_add_u32 s88, s59, 0x1c000\n"
"v_cvt_pk_bf16_f32 v68, v56, v57\n"
"v_cvt_pk_bf16_f32 v69, v58, v59\n"
"v_exp_f32_e32 v60, v28\n"
"v_exp_f32_e32 v61, v29\n"
"v_exp_f32_e32 v62, v30\n"
"v_exp_f32_e32 v63, v31\n"
"v_mfma_f32_32x32x8bf16_1k v[40:55], a[38:39], v[90:91], v[40:55]\n"
"v_cvt_pk_bf16_f32 v70, v60, v61\n"
"v_cvt_pk_bf16_f32 v71, v62, v63\n"
"v_exp_f32_e32 v56, v32\n"
"v_exp_f32_e32 v57, v33\n"
"v_exp_f32_e32 v58, v34\n"
"v_exp_f32_e32 v59, v35\n"
"v_mfma_f32_32x32x16_bf16 a[16:31], v[228:231], v[68:71], a[16:31]\n"
"v_cvt_pk_bf16_f32 v72, v56, v57\n"
"v_cvt_pk_bf16_f32 v73, v58, v59\n"
"v_exp_f32_e32 v64, v36\n"
"v_exp_f32_e32 v65, v37\n"
"v_mfma_f32_16x16x32_bf16 v[104:107], a[240:243], v[144:147], v[104:107]\n"
"v_exp_f32_e32 v66, v38\n"
"v_exp_f32_e32 v67, v39\n"
"v_mfma_f32_32x32x16_bf16 v[24:39], a[48:51], v[84:87], 0\n"
"buffer_load_dwordx4 a[232:235], v0, s[48:51], s88 offen\n"
"buffer_load_dwordx4 a[236:239], v0, s[48:51], s88 offen offset:1024\n"
"v_cvt_pk_bf16_f32 v74, v64, v65\n"
"v_cvt_pk_bf16_f32 v75, v66, v67\n"
"v_exp_f32_e32 v56, v40\n"
"v_exp_f32_e32 v57, v41\n"
"v_mfma_f32_16x16x32_bf16 v[108:111], a[244:247], v[144:147], v[108:111]\n"
"v_exp_f32_e32 v58, v42\n"
"v_exp_f32_e32 v59, v43\n"
"v_mfma_f32_32x32x16_bf16 a[16:31], v[232:235], v[72:75], a[16:31]\n"
"v_cvt_pk_bf16_f32 v68, v56, v57\n"
"v_cvt_pk_bf16_f32 v69, v58, v59\n"
"v_exp_f32_e32 v60, v44\n"
"v_exp_f32_e32 v61, v45\n"
"v_exp_f32_e32 v62, v46\n"
"v_exp_f32_e32 v63, v47\n"
"v_mfma_f32_32x32x8bf16_1k v[24:39], a[40:41], v[88:89], v[24:39]\n"
"v_cvt_pk_bf16_f32 v70, v60, v61\n"
"v_cvt_pk_bf16_f32 v71, v62, v63\n"
"v_exp_f32_e32 v56, v48\n"
"v_exp_f32_e32 v57, v49\n"
"v_exp_f32_e32 v58, v50\n"
"v_exp_f32_e32 v59, v51\n"
"v_mfma_f32_32x32x16_bf16 a[16:31], v[240:243], v[68:71], a[16:31]\n"
"v_cvt_pk_bf16_f32 v72, v56, v57\n"
"v_cvt_pk_bf16_f32 v73, v58, v59\n"
"v_exp_f32_e32 v64, v52\n"
"v_exp_f32_e32 v65, v53\n"
"v_mfma_f32_16x16x32_bf16 v[92:95], a[248:251], v[148:151], v[92:95]\n"
"v_exp_f32_e32 v66, v54\n"
"v_exp_f32_e32 v67, v55\n"
"v_mfma_f32_32x32x16_bf16 v[40:55], a[60:63], v[84:87], 0\n"
"buffer_load_dwordx4 a[240:243], v0, s[48:51], s88 offen offset:2048\n"
"buffer_load_dwordx4 a[244:247], v0, s[48:51], s88 offen offset:3072\n"
"v_cvt_pk_bf16_f32 v74, v64, v65\n"
"v_cvt_pk_bf16_f32 v75, v66, v67\n"
"v_exp_f32_e32 v56, v24\n"
"v_exp_f32_e32 v57, v25\n"
"v_mfma_f32_16x16x32_bf16 v[96:99], a[252:255], v[148:151], v[96:99]\n"
"v_exp_f32_e32 v58, v26\n"
"v_exp_f32_e32 v59, v27\n"
"v_mfma_f32_32x32x16_bf16 a[16:31], v[244:247], v[72:75], a[16:31]\n"
"s_add_u32 s88, s59, 0x1d000\n"
"v_cvt_pk_bf16_f32 v68, v56, v57\n"
"v_cvt_pk_bf16_f32 v69, v58, v59\n"
"v_exp_f32_e32 v60, v28\n"
"v_exp_f32_e32 v61, v29\n"
"v_exp_f32_e32 v62, v30\n"
"v_exp_f32_e32 v63, v31\n"
"v_mfma_f32_32x32x8bf16_1k v[40:55], a[40:41], v[90:91], v[40:55]\n"
"v_cvt_pk_bf16_f32 v70, v60, v61\n"
"v_cvt_pk_bf16_f32 v71, v62, v63\n"
"v_exp_f32_e32 v56, v32\n"
"v_exp_f32_e32 v57, v33\n"
"v_exp_f32_e32 v58, v34\n"
"v_exp_f32_e32 v59, v35\n"
"v_mfma_f32_32x32x16_bf16 a[16:31], a[52:55], v[68:71], a[16:31]\n"
"v_cvt_pk_bf16_f32 v72, v56, v57\n"
"v_cvt_pk_bf16_f32 v73, v58, v59\n"
"v_exp_f32_e32 v64, v36\n"
"v_exp_f32_e32 v65, v37\n"
"v_mfma_f32_16x16x32_bf16 v[100:103], v[248:251], v[148:151], v[100:103]\n"
"v_exp_f32_e32 v66, v38\n"
"v_exp_f32_e32 v67, v39\n"
"v_mfma_f32_32x32x16_bf16 v[24:39], a[72:75], v[84:87], 0\n"
"ds_read_b128 v[136:139], v12 offset:640\n"
"ds_read_b128 v[140:143], v13 offset:640\n"
"v_cvt_pk_bf16_f32 v74, v64, v65\n"
"v_cvt_pk_bf16_f32 v75, v66, v67\n"
"v_exp_f32_e32 v56, v40\n"
"v_exp_f32_e32 v57, v41\n"
"v_mfma_f32_16x16x32_bf16 v[104:107], v[252:255], v[148:151], v[104:107]\n"
"v_exp_f32_e32 v58, v42\n"
"v_exp_f32_e32 v59, v43\n"
"v_mfma_f32_32x32x16_bf16 a[16:31], a[56:59], v[72:75], a[16:31]\n"
"buffer_load_dwordx4 a[248:251], v0, s[48:51], s88 offen\n"
"buffer_load_dwordx4 a[252:255], v0, s[48:51], s88 offen offset:1024\n"
"v_cvt_pk_bf16_f32 v68, v56, v57\n"
"v_cvt_pk_bf16_f32 v69, v58, v59\n"
"v_exp_f32_e32 v60, v44\n"
"v_exp_f32_e32 v61, v45\n"
"v_exp_f32_e32 v62, v46\n"
"v_exp_f32_e32 v63, v47\n"
"v_mfma_f32_32x32x8bf16_1k v[24:39], a[42:43], v[88:89], v[24:39]\n"
"v_cvt_pk_bf16_f32 v70, v60, v61\n"
"v_cvt_pk_bf16_f32 v71, v62, v63\n"
"v_exp_f32_e32 v56, v48\n"
"v_exp_f32_e32 v57, v49\n"
"v_exp_f32_e32 v58, v50\n"
"v_exp_f32_e32 v59, v51\n"
"v_mfma_f32_32x32x16_bf16 a[16:31], a[64:67], v[68:71], a[16:31]\n"
"s_waitcnt vmcnt(23) lgkmcnt(0)\n"
"v_cvt_pk_bf16_f32 v72, v56, v57\n"
"v_cvt_pk_bf16_f32 v73, v58, v59\n"
"v_exp_f32_e32 v64, v52\n"
"v_exp_f32_e32 v65, v53\n"
"v_mfma_f32_16x16x32_bf16 v[92:95], a[144:147], v[136:139], v[92:95]\n"
"v_exp_f32_e32 v66, v54\n"
"v_exp_f32_e32 v67, v55\n"
"v_mfma_f32_32x32x16_bf16 v[40:55], a[84:87], v[84:87], 0\n"
"buffer_load_dwordx4 v[248:251], v0, s[48:51], s88 offen offset:2048\n"
"buffer_load_dwordx4 v[252:255], v0, s[48:51], s88 offen offset:3072\n"
"v_cvt_pk_bf16_f32 v74, v64, v65\n"
"v_cvt_pk_bf16_f32 v75, v66, v67\n"
"v_exp_f32_e32 v56, v24\n"
"v_exp_f32_e32 v57, v25\n"
"v_mfma_f32_16x16x32_bf16 v[96:99], a[148:151], v[136:139], v[96:99]\n"
"v_exp_f32_e32 v58, v26\n"
"v_exp_f32_e32 v59, v27\n"
"v_mfma_f32_32x32x16_bf16 a[16:31], a[68:71], v[72:75], a[16:31]\n"
"s_mov_b32 m0, s64\n"
"v_cvt_pk_bf16_f32 v68, v56, v57\n"
"v_cvt_pk_bf16_f32 v69, v58, v59\n"
"v_exp_f32_e32 v60, v28\n"
"v_exp_f32_e32 v61, v29\n"
"v_exp_f32_e32 v62, v30\n"
"v_exp_f32_e32 v63, v31\n"
"v_mfma_f32_32x32x8bf16_1k v[40:55], a[42:43], v[90:91], v[40:55]\n"
"v_cvt_pk_bf16_f32 v70, v60, v61\n"
"v_cvt_pk_bf16_f32 v71, v62, v63\n"
"v_exp_f32_e32 v56, v32\n"
"v_exp_f32_e32 v57, v33\n"
"v_exp_f32_e32 v58, v34\n"
"v_exp_f32_e32 v59, v35\n"
"v_mfma_f32_32x32x16_bf16 a[16:31], a[76:79], v[68:71], a[16:31]\n"
"buffer_load_dwordx4 v0, s[44:47], s61 offen lds\n"
"buffer_load_dwordx4 v0, s[44:47], s61 offen offset:1024 lds\n"
"v_cvt_pk_bf16_f32 v72, v56, v57\n"
"v_cvt_pk_bf16_f32 v73, v58, v59\n"
"v_exp_f32_e32 v64, v36\n"
"v_exp_f32_e32 v65, v37\n"
"v_mfma_f32_16x16x32_bf16 v[100:103], a[152:155], v[136:139], v[100:103]\n"
"v_exp_f32_e32 v66, v38\n"
"v_exp_f32_e32 v67, v39\n"
"v_mfma_f32_32x32x16_bf16 v[24:39], a[96:99], v[84:87], 0\n"
"s_add_u32 s88, s59, 0x1e000\n"
"v_cvt_pk_bf16_f32 v74, v64, v65\n"
"v_cvt_pk_bf16_f32 v75, v66, v67\n"
"v_exp_f32_e32 v56, v40\n"
"v_exp_f32_e32 v57, v41\n"
"v_mfma_f32_16x16x32_bf16 v[104:107], a[156:159], v[136:139], v[104:107]\n"
"v_exp_f32_e32 v58, v42\n"
"v_exp_f32_e32 v59, v43\n"
"v_mfma_f32_32x32x16_bf16 a[16:31], a[80:83], v[72:75], a[16:31]\n"
"s_add_u32 m0, s64, 0x2400\n"
"v_cvt_pk_bf16_f32 v68, v56, v57\n"
"v_cvt_pk_bf16_f32 v69, v58, v59\n"
"v_exp_f32_e32 v60, v44\n"
"v_exp_f32_e32 v61, v45\n"
"v_exp_f32_e32 v62, v46\n"
"v_exp_f32_e32 v63, v47\n"
"v_mfma_f32_32x32x8bf16_1k v[24:39], a[44:45], v[88:89], v[24:39]\n"
"v_cvt_pk_bf16_f32 v70, v60, v61\n"
"v_cvt_pk_bf16_f32 v71, v62, v63\n"
"v_exp_f32_e32 v56, v48\n"
"v_exp_f32_e32 v57, v49\n"
"v_exp_f32_e32 v58, v50\n"
"v_exp_f32_e32 v59, v51\n"
"v_mfma_f32_32x32x16_bf16 a[16:31], a[88:91], v[68:71], a[16:31]\n"
"buffer_load_dwordx4 v0, s[48:51], s63 offen lds\n"
"buffer_load_dwordx4 v0, s[48:51], s63 offen offset:1024 lds\n"
"v_cvt_pk_bf16_f32 v72, v56, v57\n"
"v_cvt_pk_bf16_f32 v73, v58, v59\n"
"v_exp_f32_e32 v64, v52\n"
"v_exp_f32_e32 v65, v53\n"
"v_mfma_f32_16x16x32_bf16 v[108:111], a[160:163], v[136:139], v[108:111]\n"
"v_exp_f32_e32 v66, v54\n"
"v_exp_f32_e32 v67, v55\n"
"v_mfma_f32_32x32x16_bf16 v[40:55], a[108:111], v[84:87], 0\n"
"buffer_load_dwordx4 a[144:147], v0, s[48:51], s88 offen\n"
"buffer_load_dwordx4 a[148:151], v0, s[48:51], s88 offen offset:1024\n"
"v_cvt_pk_bf16_f32 v74, v64, v65\n"
"v_cvt_pk_bf16_f32 v75, v66, v67\n"
"v_exp_f32_e32 v56, v24\n"
"v_exp_f32_e32 v57, v25\n"
"v_mfma_f32_16x16x32_bf16 v[92:95], a[164:167], v[140:143], v[92:95]\n"
"v_exp_f32_e32 v58, v26\n"
"v_exp_f32_e32 v59, v27\n"
"v_mfma_f32_32x32x16_bf16 a[16:31], a[92:95], v[72:75], a[16:31]\n"
"s_mov_b32 m0, 0xb000\n"
"s_add_u32 s62, s62, 0x2000\n"
"v_cvt_pk_bf16_f32 v68, v56, v57\n"
"v_cvt_pk_bf16_f32 v69, v58, v59\n"
"v_exp_f32_e32 v60, v28\n"
"v_exp_f32_e32 v61, v29\n"
"v_exp_f32_e32 v62, v30\n"
"v_exp_f32_e32 v63, v31\n"
"v_mfma_f32_32x32x8bf16_1k v[40:55], a[44:45], v[90:91], v[40:55]\n"
"v_cvt_pk_bf16_f32 v70, v60, v61\n"
"v_cvt_pk_bf16_f32 v71, v62, v63\n"
"v_exp_f32_e32 v56, v32\n"
"v_exp_f32_e32 v57, v33\n"
"v_exp_f32_e32 v58, v34\n"
"v_exp_f32_e32 v59, v35\n"
"v_mfma_f32_32x32x16_bf16 a[16:31], a[100:103], v[68:71], a[16:31]\n"
"buffer_load_dwordx4 v0, s[44:47], s62 offen lds\n"
"v_cvt_pk_bf16_f32 v72, v56, v57\n"
"v_cvt_pk_bf16_f32 v73, v58, v59\n"
"v_exp_f32_e32 v64, v36\n"
"v_exp_f32_e32 v65, v37\n"
"v_mfma_f32_16x16x32_bf16 v[96:99], a[168:171], v[140:143], v[96:99]\n"
"v_exp_f32_e32 v66, v38\n"
"v_exp_f32_e32 v67, v39\n"
"v_mfma_f32_32x32x16_bf16 v[24:39], a[120:123], v[84:87], 0\n"
"buffer_load_dwordx4 a[152:155], v0, s[48:51], s88 offen offset:2048\n"
"buffer_load_dwordx4 a[156:159], v0, s[48:51], s88 offen offset:3072\n"
"v_cvt_pk_bf16_f32 v74, v64, v65\n"
"v_cvt_pk_bf16_f32 v75, v66, v67\n"
"v_exp_f32_e32 v56, v40\n"
"v_exp_f32_e32 v57, v41\n"
"v_mfma_f32_16x16x32_bf16 v[100:103], a[172:175], v[140:143], v[100:103]\n"
"v_exp_f32_e32 v58, v42\n"
"v_exp_f32_e32 v59, v43\n"
"v_mfma_f32_32x32x16_bf16 a[16:31], a[104:107], v[72:75], a[16:31]\n"
"s_add_u32 s88, s59, 0x1f000\n"
"v_cvt_pk_bf16_f32 v68, v56, v57\n"
"v_cvt_pk_bf16_f32 v69, v58, v59\n"
"v_exp_f32_e32 v60, v44\n"
"v_exp_f32_e32 v61, v45\n"
"v_exp_f32_e32 v62, v46\n"
"v_exp_f32_e32 v63, v47\n"
"v_mfma_f32_32x32x8bf16_1k v[24:39], a[46:47], v[88:89], v[24:39]\n"
"v_cvt_pk_bf16_f32 v70, v60, v61\n"
"v_cvt_pk_bf16_f32 v71, v62, v63\n"
".LK3_pad5:\n"
"s_cbranch_execz .LK1_ret\n"
"v_exp_f32_e32 v56, v48\n"
"v_exp_f32_e32 v57, v49\n"
"v_exp_f32_e32 v58, v50\n"
"v_exp_f32_e32 v59, v51\n"
"v_mfma_f32_32x32x16_bf16 a[16:31], a[112:115], v[68:71], a[16:31]\n"
"v_cvt_pk_bf16_f32 v72, v56, v57\n"
"v_cvt_pk_bf16_f32 v73, v58, v59\n"
"v_exp_f32_e32 v64, v52\n"
"v_exp_f32_e32 v65, v53\n"
"v_mfma_f32_16x16x32_bf16 v[104:107], a[176:179], v[140:143], v[104:107]\n"
"v_exp_f32_e32 v66, v54\n"
"v_exp_f32_e32 v67, v55\n"
"v_mfma_f32_32x32x16_bf16 v[40:55], a[132:135], v[84:87], 0\n"
"buffer_load_dwordx4 a[160:163], v0, s[48:51], s88 offen\n"
"buffer_load_dwordx4 a[164:167], v0, s[48:51], s88 offen offset:1024\n"
"v_cvt_pk_bf16_f32 v74, v64, v65\n"
"v_cvt_pk_bf16_f32 v75, v66, v67\n"
"v_exp_f32_e32 v56, v24\n"
"v_exp_f32_e32 v57, v25\n"
"v_exp_f32_e32 v58, v26\n"
"v_exp_f32_e32 v59, v27\n"
"v_mfma_f32_32x32x16_bf16 a[16:31], a[116:119], v[72:75], a[16:31]\n"
"s_waitcnt vmcnt(22)\n"
"v_cvt_pk_bf16_f32 v68, v56, v57\n"
"v_cvt_pk_bf16_f32 v69, v58, v59\n"
"v_exp_f32_e32 v60, v28\n"
"v_exp_f32_e32 v61, v29\n"
"v_cndmask_b32_e64 v82, 0, v81, s[36:37]\n"
"v_mov_b32_e32 v83, v80\n"
"v_cndmask_b32_e64 v81, 0, v81, s[36:37]\n"
"v_exp_f32_e32 v62, v30\n"
"v_exp_f32_e32 v63, v31\n"
"v_mfma_f32_32x32x8bf16_1k v[40:55], a[46:47], v[90:91], v[40:55]\n"
"v_cvt_pk_bf16_f32 v70, v60, v61\n"
"v_cvt_pk_bf16_f32 v71, v62, v63\n"
"v_exp_f32_e32 v56, v32\n"
"v_exp_f32_e32 v57, v33\n"
"v_exp_f32_e32 v58, v34\n"
"v_exp_f32_e32 v59, v35\n"
"v_mfma_f32_32x32x16_bf16 a[16:31], a[124:127], v[68:71], a[16:31]\n"
"buffer_load_dword v6, v22, s[76:79], s69 offen\n"
"s_add_u32 s69, s69, 0x2400\n"
"v_cvt_pk_bf16_f32 v72, v56, v57\n"
"v_cvt_pk_bf16_f32 v73, v58, v59\n"
"v_exp_f32_e32 v64, v36\n"
"v_exp_f32_e32 v65, v37\n"
"v_exp_f32_e32 v66, v38\n"
"v_exp_f32_e32 v67, v39\n"
"v_mfma_f32_32x32x16_bf16 v[24:39], v[152:155], v[76:79], 0\n"
"buffer_load_dwordx4 a[168:171], v0, s[48:51], s88 offen offset:2048\n"
"buffer_load_dwordx4 a[172:175], v0, s[48:51], s88 offen offset:3072\n"
"v_cvt_pk_bf16_f32 v74, v64, v65\n"
"v_cvt_pk_bf16_f32 v75, v66, v67\n"
"v_exp_f32_e32 v56, v40\n"
"v_exp_f32_e32 v57, v41\n"
"v_exp_f32_e32 v58, v42\n"
"v_exp_f32_e32 v59, v43\n"
"v_mfma_f32_32x32x16_bf16 a[16:31], a[128:131], v[72:75], a[16:31]\n"
"s_add_u32 s88, s59, 0x20000\n"
"v_cvt_pk_bf16_f32 v68, v56, v57\n"
"v_cvt_pk_bf16_f32 v69, v58, v59\n"
"v_exp_f32_e32 v60, v44\n"
"v_exp_f32_e32 v61, v45\n"
"v_exp_f32_e32 v62, v46\n"
"v_exp_f32_e32 v63, v47\n"
"v_mfma_f32_32x32x8bf16_1k v[24:39], a[32:33], v[80:81], v[24:39]\n"
"v_cvt_pk_bf16_f32 v70, v60, v61\n"
"v_cvt_pk_bf16_f32 v71, v62, v63\n"
"v_exp_f32_e32 v56, v48\n"
"v_exp_f32_e32 v57, v49\n"
"v_exp_f32_e32 v58, v50\n"
"v_exp_f32_e32 v59, v51\n"
"v_mfma_f32_32x32x16_bf16 a[16:31], a[136:139], v[68:71], a[16:31]\n"
"v_cvt_pk_bf16_f32 v72, v56, v57\n"
"v_cvt_pk_bf16_f32 v73, v58, v59\n"
"v_exp_f32_e32 v64, v52\n"
"v_exp_f32_e32 v65, v53\n"
"v_exp_f32_e32 v66, v54\n"
"v_exp_f32_e32 v67, v55\n"
"v_mfma_f32_32x32x16_bf16 v[40:55], v[164:167], v[76:79], 0\n"
"v_cvt_pk_bf16_f32 v74, v64, v65\n"
"v_cvt_pk_bf16_f32 v75, v66, v67\n"
"v_exp_f32_e32 v56, v24\n"
"v_exp_f32_e32 v57, v25\n"
"v_exp_f32_e32 v58, v26\n"
"v_exp_f32_e32 v59, v27\n"
"v_mfma_f32_32x32x16_bf16 a[16:31], a[140:143], v[72:75], a[16:31]\n"
"v_cvt_pk_bf16_f32 v68, v56, v57\n"
"v_cvt_pk_bf16_f32 v69, v58, v59\n"
"v_exp_f32_e32 v60, v28\n"
"v_exp_f32_e32 v61, v29\n"
"v_exp_f32_e32 v62, v30\n"
"v_exp_f32_e32 v63, v31\n"
"v_mfma_f32_32x32x8bf16_1k v[40:55], a[32:33], v[82:83], v[40:55]\n"
"v_cvt_pk_bf16_f32 v70, v60, v61\n"
"v_cvt_pk_bf16_f32 v71, v62, v63\n"
"v_exp_f32_e32 v56, v32\n"
"v_exp_f32_e32 v57, v33\n"
"v_exp_f32_e32 v58, v34\n"
"v_exp_f32_e32 v59, v35\n"
"v_mfma_f32_32x32x16_bf16 a[0:15], v[156:159], v[68:71], 0\n"
"ds_write_b128 v8, a[16:19] offset:12288\n"
"v_cvt_pk_bf16_f32 v72, v56, v57\n"
"v_cvt_pk_bf16_f32 v73, v58, v59\n"
"v_exp_f32_e32 v64, v36\n"
"v_exp_f32_e32 v65, v37\n"
"v_exp_f32_e32 v66, v38\n"
"v_exp_f32_e32 v67, v39\n"
"v_mfma_f32_32x32x16_bf16 v[24:39], v[176:179], v[76:79], 0\n"
"ds_write_b128 v8, a[20:23] offset:13312\n"
"v_cvt_pk_bf16_f32 v74, v64, v65\n"
"v_cvt_pk_bf16_f32 v75, v66, v67\n"
"v_exp_f32_e32 v56, v40\n"
"v_exp_f32_e32 v57, v41\n"
"v_exp_f32_e32 v58, v42\n"
"v_exp_f32_e32 v59, v43\n"
"v_mfma_f32_32x32x16_bf16 a[0:15], v[160:163], v[72:75], a[0:15]\n"
"ds_write_b128 v8, a[24:27] offset:14336\n"
"v_cvt_pk_bf16_f32 v68, v56, v57\n"
"v_cvt_pk_bf16_f32 v69, v58, v59\n"
"v_exp_f32_e32 v60, v44\n"
"v_exp_f32_e32 v61, v45\n"
"v_exp_f32_e32 v62, v46\n"
"v_exp_f32_e32 v63, v47\n"
"v_mfma_f32_32x32x8bf16_1k v[24:39], a[34:35], v[80:81], v[24:39]\n"
"v_cvt_pk_bf16_f32 v70, v60, v61\n"
"v_cvt_pk_bf16_f32 v71, v62, v63\n"
"v_exp_f32_e32 v56, v48\n"
"v_exp_f32_e32 v57, v49\n"
"v_exp_f32_e32 v58, v50\n"
"v_exp_f32_e32 v59, v51\n"
"v_mfma_f32_32x32x16_bf16 a[0:15], v[168:171], v[68:71], a[0:15]\n"
"s_waitcnt lgkmcnt(0)\n"
"s_barrier\n"
"buffer_store_dwordx4 v[132:135], v0, s[48:51], s71 offen offset:0 sc1\n"
"v_cvt_pk_bf16_f32 v72, v56, v57\n"
"v_cvt_pk_bf16_f32 v73, v58, v59\n"
"v_exp_f32_e32 v64, v52\n"
"v_exp_f32_e32 v65, v53\n"
"v_exp_f32_e32 v66, v54\n"
"v_exp_f32_e32 v67, v55\n"
"v_mfma_f32_32x32x16_bf16 v[40:55], v[188:191], v[76:79], 0\n"
"ds_read_b128 v[112:115], v9 offset:12288\n"
"ds_read_b128 v[116:119], v9 offset:15360\n"
"v_cvt_pk_bf16_f32 v74, v64, v65\n"
"v_cvt_pk_bf16_f32 v75, v66, v67\n"
"v_exp_f32_e32 v56, v24\n"
"v_exp_f32_e32 v57, v25\n"
"v_exp_f32_e32 v58, v26\n"
"v_exp_f32_e32 v59, v27\n"
"v_mfma_f32_32x32x16_bf16 a[0:15], v[172:175], v[72:75], a[0:15]\n"
"ds_read_b128 v[120:123], v9 offset:18432\n"
"ds_read_b128 v[124:127], v9 offset:21504\n"
"v_cvt_pk_bf16_f32 v68, v56, v57\n"
"v_cvt_pk_bf16_f32 v69, v58, v59\n"
"v_exp_f32_e32 v60, v28\n"
"v_exp_f32_e32 v61, v29\n"
"v_exp_f32_e32 v62, v30\n"
"v_exp_f32_e32 v63, v31\n"
"v_mfma_f32_32x32x8bf16_1k v[40:55], a[34:35], v[82:83], v[40:55]\n"
"v_cvt_pk_bf16_f32 v70, v60, v61\n"
"v_cvt_pk_bf16_f32 v71, v62, v63\n"
"v_exp_f32_e32 v56, v32\n"
"v_exp_f32_e32 v57, v33\n"
"v_exp_f32_e32 v58, v34\n"
"v_exp_f32_e32 v59, v35\n"
"v_mfma_f32_32x32x16_bf16 a[0:15], v[180:183], v[68:71], a[0:15]\n"
"s_waitcnt lgkmcnt(0)\n"
"v_cvt_pk_bf16_f32 v72, v56, v57\n"
"v_cvt_pk_bf16_f32 v73, v58, v59\n"
"v_exp_f32_e32 v64, v36\n"
"v_exp_f32_e32 v65, v37\n"
"v_add_f32_e32 v115, v115, v119\n"
"v_add_f32_e32 v112, v112, v116\n"
"v_add_f32_e32 v113, v113, v117\n"
"v_add_f32_e32 v114, v114, v118\n"
"v_add_f32_e32 v123, v123, v127\n"
"v_add_f32_e32 v120, v120, v124\n"
"v_exp_f32_e32 v66, v38\n"
"v_exp_f32_e32 v67, v39\n"
"v_mfma_f32_32x32x16_bf16 v[24:39], v[200:203], v[76:79], 0\n"
"v_cvt_pk_bf16_f32 v74, v64, v65\n"
"v_cvt_pk_bf16_f32 v75, v66, v67\n"
"v_exp_f32_e32 v56, v40\n"
"v_exp_f32_e32 v57, v41\n"
"v_add_f32_e32 v121, v121, v125\n"
"v_add_f32_e32 v122, v122, v126\n"
"v_add_f32_e32 v115, v115, v123\n"
"v_rcp_f32_e32 v115, v115\n"
"v_add_f32_e32 v112, v112, v120\n"
"v_add_f32_e32 v113, v113, v121\n"
"v_exp_f32_e32 v58, v42\n"
"v_exp_f32_e32 v59, v43\n"
"v_mfma_f32_32x32x16_bf16 a[0:15], v[184:187], v[72:75], a[0:15]\n"
"v_cvt_pk_bf16_f32 v68, v56, v57\n"
"v_cvt_pk_bf16_f32 v69, v58, v59\n"
"v_exp_f32_e32 v60, v44\n"
"v_exp_f32_e32 v61, v45\n"
"v_add_f32_e32 v114, v114, v122\n"
"v_mul_f32_e32 v112, v112, v115\n"
"v_mul_f32_e32 v113, v113, v115\n"
"v_mul_f32_e32 v114, v114, v115\n"
"v_cvt_pk_bf16_f32 v112, v112, v113\n"
"v_cvt_pk_bf16_f32 v114, v114, v114\n"
"v_exp_f32_e32 v62, v46\n"
"v_exp_f32_e32 v63, v47\n"
"v_mfma_f32_32x32x8bf16_1k v[24:39], a[36:37], v[80:81], v[24:39]\n"
"v_cvt_pk_bf16_f32 v70, v60, v61\n"
"v_cvt_pk_bf16_f32 v71, v62, v63\n"
"v_exp_f32_e32 v56, v48\n"
"v_exp_f32_e32 v57, v49\n"
"v_exp_f32_e32 v58, v50\n"
"v_exp_f32_e32 v59, v51\n"
"v_mfma_f32_32x32x16_bf16 a[0:15], v[192:195], v[68:71], a[0:15]\n"
"ds_write_b32 v10, v112 offset:720\n"
"ds_write_b16 v11, v114 offset:720\n"
"s_getpc_b64 s[92:93]\n"
".LK3_pc_%=:\n"
"s_mul_i32 s90, s81, 0x180\n"
"s_min_u32 s90, s90, 0x9c88\n"
"s_add_u32 s90, s90, .LKF_pad0-.LK3_pc_%=\n"
"s_add_u32 s92, s92, s90\n"
"s_addc_u32 s93, s93, 0\n"
"s_mov_b32 exec_lo, 0x7\n"
"s_mov_b32 exec_hi, 0x0\n"
"global_load_dword v6, v4, s[92:93]\n"
"s_mov_b64 exec, -1\n"
"buffer_store_dwordx4 v[132:135], v0, s[48:51], s71 offen offset:2048 sc1\n"
"v_cvt_pk_bf16_f32 v72, v56, v57\n"
"v_cvt_pk_bf16_f32 v73, v58, v59\n"
"v_exp_f32_e32 v64, v52\n"
"v_exp_f32_e32 v65, v53\n"
"v_exp_f32_e32 v66, v54\n"
"v_exp_f32_e32 v67, v55\n"
"v_mfma_f32_32x32x16_bf16 v[40:55], v[212:215], v[76:79], 0\n"
"v_cvt_pk_bf16_f32 v74, v64, v65\n"
"v_cvt_pk_bf16_f32 v75, v66, v67\n"
"v_exp_f32_e32 v56, v24\n"
"v_exp_f32_e32 v57, v25\n"
"v_exp_f32_e32 v58, v26\n"
"v_exp_f32_e32 v59, v27\n"
"v_mfma_f32_32x32x16_bf16 a[0:15], v[196:199], v[72:75], a[0:15]\n"
"s_waitcnt lgkmcnt(0)\n"
"s_barrier\n"
"v_cvt_pk_bf16_f32 v68, v56, v57\n"
"v_cvt_pk_bf16_f32 v69, v58, v59\n"
"v_exp_f32_e32 v60, v28\n"
"v_exp_f32_e32 v61, v29\n"
"v_exp_f32_e32 v62, v30\n"
"v_exp_f32_e32 v63, v31\n"
"v_mfma_f32_32x32x8bf16_1k v[40:55], a[36:37], v[82:83], v[40:55]\n"
"v_cvt_pk_bf16_f32 v70, v60, v61\n"
"v_cvt_pk_bf16_f32 v71, v62, v63\n"
"v_exp_f32_e32 v56, v32\n"
"v_exp_f32_e32 v57, v33\n"
"v_exp_f32_e32 v58, v34\n"
"v_exp_f32_e32 v59, v35\n"
"v_mfma_f32_32x32x16_bf16 a[0:15], v[204:207], v[68:71], a[0:15]\n"
"ds_read_b128 v[144:147], v12 offset:704\n"
"ds_read_b128 v[148:151], v13 offset:704\n"
"v_cvt_pk_bf16_f32 v72, v56, v57\n"
"v_cvt_pk_bf16_f32 v73, v58, v59\n"
"v_exp_f32_e32 v64, v36\n"
"v_exp_f32_e32 v65, v37\n"
"v_exp_f32_e32 v66, v38\n"
"v_exp_f32_e32 v67, v39\n"
"v_mfma_f32_32x32x16_bf16 v[24:39], v[224:227], v[76:79], 0\n"
"buffer_load_dwordx4 v[84:87], v3, s[48:51], s55 offen offset:1008\n"
"buffer_load_dwordx2 v[88:89], v7, s[48:51], s55 offen offset:1008\n"
"v_cvt_pk_bf16_f32 v74, v64, v65\n"
"v_cvt_pk_bf16_f32 v75, v66, v67\n"
"v_exp_f32_e32 v56, v40\n"
"v_exp_f32_e32 v57, v41\n"
"v_exp_f32_e32 v58, v42\n"
"v_exp_f32_e32 v59, v43\n"
"v_mfma_f32_32x32x16_bf16 a[0:15], v[208:211], v[72:75], a[0:15]\n"
"s_waitcnt lgkmcnt(0)\n"
"v_cvt_pk_bf16_f32 v68, v56, v57\n"
"v_cvt_pk_bf16_f32 v69, v58, v59\n"
"v_exp_f32_e32 v60, v44\n"
"v_exp_f32_e32 v61, v45\n"
"v_mfma_f32_16x16x32_bf16 v[92:95], a[180:183], v[144:147], v[92:95]\n"
"v_exp_f32_e32 v62, v46\n"
"v_exp_f32_e32 v63, v47\n"
"v_mfma_f32_32x32x8bf16_1k v[24:39], a[38:39], v[80:81], v[24:39]\n"
"v_cvt_pk_bf16_f32 v70, v60, v61\n"
"v_cvt_pk_bf16_f32 v71, v62, v63\n"
"v_exp_f32_e32 v56, v48\n"
"v_exp_f32_e32 v57, v49\n"
"v_exp_f32_e32 v58, v50\n"
"v_exp_f32_e32 v59, v51\n"
"v_mfma_f32_32x32x16_bf16 a[0:15], v[216:219], v[68:71], a[0:15]\n"
"buffer_store_dwordx4 v[132:135], v0, s[48:51], s71 offen offset:3072 sc1\n"
"v_cvt_pk_bf16_f32 v72, v56, v57\n"
"v_cvt_pk_bf16_f32 v73, v58, v59\n"
"v_exp_f32_e32 v64, v52\n"
"v_exp_f32_e32 v65, v53\n"
"v_mfma_f32_16x16x32_bf16 v[96:99], a[184:187], v[144:147], v[96:99]\n"
"v_exp_f32_e32 v66, v54\n"
"v_exp_f32_e32 v67, v55\n"
"v_mfma_f32_32x32x16_bf16 v[40:55], v[236:239], v[76:79], 0\n"
"buffer_load_dwordx4 a[176:179], v0, s[48:51], s88 offen\n"
"buffer_load_dwordx4 a[180:183], v0, s[48:51], s88 offen offset:1024\n"
"v_cvt_pk_bf16_f32 v74, v64, v65\n"
"v_cvt_pk_bf16_f32 v75, v66, v67\n"
"v_exp_f32_e32 v56, v24\n"
"v_exp_f32_e32 v57, v25\n"
"v_mfma_f32_16x16x32_bf16 v[100:103], a[188:191], v[144:147], v[100:103]\n"
"v_exp_f32_e32 v58, v26\n"
"v_exp_f32_e32 v59, v27\n"
"v_mfma_f32_32x32x16_bf16 a[0:15], v[220:223], v[72:75], a[0:15]\n"
"v_cvt_pk_bf16_f32 v68, v56, v57\n"
"v_cvt_pk_bf16_f32 v69, v58, v59\n"
"v_exp_f32_e32 v60, v28\n"
"v_exp_f32_e32 v61, v29\n"
"v_exp_f32_e32 v62, v30\n"
"v_exp_f32_e32 v63, v31\n"
"v_mfma_f32_32x32x8bf16_1k v[40:55], a[38:39], v[82:83], v[40:55]\n"
"v_cvt_pk_bf16_f32 v70, v60, v61\n"
"v_cvt_pk_bf16_f32 v71, v62, v63\n"
"v_exp_f32_e32 v56, v32\n"
"v_exp_f32_e32 v57, v33\n"
"v_exp_f32_e32 v58, v34\n"
"v_exp_f32_e32 v59, v35\n"
"v_mfma_f32_32x32x16_bf16 a[0:15], v[228:231], v[68:71], a[0:15]\n"
"v_cvt_pk_bf16_f32 v72, v56, v57\n"
"v_cvt_pk_bf16_f32 v73, v58, v59\n"
"v_exp_f32_e32 v64, v36\n"
"v_exp_f32_e32 v65, v37\n"
"v_mfma_f32_16x16x32_bf16 v[104:107], a[192:195], v[144:147], v[104:107]\n"
"v_exp_f32_e32 v66, v38\n"
"v_exp_f32_e32 v67, v39\n"
"v_mfma_f32_32x32x16_bf16 v[24:39], a[48:51], v[76:79], 0\n"
"buffer_load_dwordx4 a[184:187], v0, s[48:51], s88 offen offset:2048\n"
"buffer_load_dwordx4 a[188:191], v0, s[48:51], s88 offen offset:3072\n"
"v_cvt_pk_bf16_f32 v74, v64, v65\n"
"v_cvt_pk_bf16_f32 v75, v66, v67\n"
"v_exp_f32_e32 v56, v40\n"
"v_exp_f32_e32 v57, v41\n"
"v_mfma_f32_16x16x32_bf16 v[108:111], a[196:199], v[144:147], v[108:111]\n"
"v_exp_f32_e32 v58, v42\n"
"v_exp_f32_e32 v59, v43\n"
"v_mfma_f32_32x32x16_bf16 a[0:15], v[232:235], v[72:75], a[0:15]\n"
"s_add_u32 s88, s59, 0x21000\n"
"v_cvt_pk_bf16_f32 v68, v56, v57\n"
"v_cvt_pk_bf16_f32 v69, v58, v59\n"
"v_exp_f32_e32 v60, v44\n"
"v_exp_f32_e32 v61, v45\n"
"v_exp_f32_e32 v62, v46\n"
"v_exp_f32_e32 v63, v47\n"
"v_mfma_f32_32x32x8bf16_1k v[24:39], a[40:41], v[80:81], v[24:39]\n"
"v_cvt_pk_bf16_f32 v70, v60, v61\n"
"v_cvt_pk_bf16_f32 v71, v62, v63\n"
"v_exp_f32_e32 v56, v48\n"
"v_exp_f32_e32 v57, v49\n"
"v_exp_f32_e32 v58, v50\n"
"v_exp_f32_e32 v59, v51\n"
"v_mfma_f32_32x32x16_bf16 a[0:15], v[240:243], v[68:71], a[0:15]\n"
"buffer_load_dword v6, v22, s[48:51], s70 offen\n"
"v_cvt_pk_bf16_f32 v72, v56, v57\n"
"v_cvt_pk_bf16_f32 v73, v58, v59\n"
"v_exp_f32_e32 v64, v52\n"
"v_exp_f32_e32 v65, v53\n"
"v_mfma_f32_16x16x32_bf16 v[92:95], a[200:203], v[148:151], v[92:95]\n"
"v_exp_f32_e32 v66, v54\n"
"v_exp_f32_e32 v67, v55\n"
"v_mfma_f32_32x32x16_bf16 v[40:55], a[60:63], v[76:79], 0\n"
"buffer_load_dwordx4 a[192:195], v0, s[48:51], s88 offen\n"
"buffer_load_dwordx4 a[196:199], v0, s[48:51], s88 offen offset:1024\n"
"v_cvt_pk_bf16_f32 v74, v64, v65\n"
"v_cvt_pk_bf16_f32 v75, v66, v67\n"
"v_exp_f32_e32 v56, v24\n"
"v_exp_f32_e32 v57, v25\n"
"v_mfma_f32_16x16x32_bf16 v[96:99], a[204:207], v[148:151], v[96:99]\n"
"v_exp_f32_e32 v58, v26\n"
"v_exp_f32_e32 v59, v27\n"
"v_mfma_f32_32x32x16_bf16 a[0:15], v[244:247], v[72:75], a[0:15]\n"
"v_cvt_pk_bf16_f32 v68, v56, v57\n"
"v_cvt_pk_bf16_f32 v69, v58, v59\n"
"v_exp_f32_e32 v60, v28\n"
"v_exp_f32_e32 v61, v29\n"
"v_exp_f32_e32 v62, v30\n"
"v_exp_f32_e32 v63, v31\n"
"v_mfma_f32_32x32x8bf16_1k v[40:55], a[40:41], v[82:83], v[40:55]\n"
"v_cvt_pk_bf16_f32 v70, v60, v61\n"
"v_cvt_pk_bf16_f32 v71, v62, v63\n"
"v_exp_f32_e32 v56, v32\n"
"v_exp_f32_e32 v57, v33\n"
"v_exp_f32_e32 v58, v34\n"
"v_exp_f32_e32 v59, v35\n"
"v_mfma_f32_32x32x16_bf16 a[0:15], a[52:55], v[68:71], a[0:15]\n"
"v_cvt_pk_bf16_f32 v72, v56, v57\n"
"v_cvt_pk_bf16_f32 v73, v58, v59\n"
"v_exp_f32_e32 v64, v36\n"
"v_exp_f32_e32 v65, v37\n"
"v_mfma_f32_16x16x32_bf16 v[100:103], a[208:211], v[148:151], v[100:103]\n"
"v_exp_f32_e32 v66, v38\n"
"v_exp_f32_e32 v67, v39\n"
"v_mfma_f32_32x32x16_bf16 v[24:39], a[72:75], v[76:79], 0\n"
"ds_read_b128 v[136:139], v12 offset:768\n"
"ds_read_b128 v[140:143], v13 offset:768\n"
"v_cvt_pk_bf16_f32 v74, v64, v65\n"
"v_cvt_pk_bf16_f32 v75, v66, v67\n"
"v_exp_f32_e32 v56, v40\n"
"v_exp_f32_e32 v57, v41\n"
"v_mfma_f32_16x16x32_bf16 v[104:107], a[212:215], v[148:151], v[104:107]\n"
"v_exp_f32_e32 v58, v42\n"
"v_exp_f32_e32 v59, v43\n"
"v_mfma_f32_32x32x16_bf16 a[0:15], a[56:59], v[72:75], a[0:15]\n"
"buffer_load_dwordx4 a[200:203], v0, s[48:51], s88 offen offset:2048\n"
"buffer_load_dwordx4 a[204:207], v0, s[48:51], s88 offen offset:3072\n"
"v_cvt_pk_bf16_f32 v68, v56, v57\n"
"v_cvt_pk_bf16_f32 v69, v58, v59\n"
"v_exp_f32_e32 v60, v44\n"
"v_exp_f32_e32 v61, v45\n"
"v_exp_f32_e32 v62, v46\n"
"v_exp_f32_e32 v63, v47\n"
"v_mfma_f32_32x32x8bf16_1k v[24:39], a[42:43], v[80:81], v[24:39]\n"
"v_cvt_pk_bf16_f32 v70, v60, v61\n"
"v_cvt_pk_bf16_f32 v71, v62, v63\n"
"v_exp_f32_e32 v56, v48\n"
"v_exp_f32_e32 v57, v49\n"
"v_exp_f32_e32 v58, v50\n"
"v_exp_f32_e32 v59, v51\n"
"v_mfma_f32_32x32x16_bf16 a[0:15], a[64:67], v[68:71], a[0:15]\n"
"s_waitcnt vmcnt(32) lgkmcnt(0)\n"
"s_add_u32 s71, s71, 0x1000\n"
"buffer_store_dwordx4 v[132:135], v0, s[48:51], s71 offen offset:0 sc1\n"
"v_cvt_pk_bf16_f32 v72, v56, v57\n"
"v_cvt_pk_bf16_f32 v73, v58, v59\n"
"v_exp_f32_e32 v64, v52\n"
"v_exp_f32_e32 v65, v53\n"
"v_mfma_f32_16x16x32_bf16 v[92:95], a[216:219], v[136:139], v[92:95]\n"
"v_exp_f32_e32 v66, v54\n"
"v_exp_f32_e32 v67, v55\n"
"v_mfma_f32_32x32x16_bf16 v[40:55], a[84:87], v[76:79], 0\n"
"s_add_u32 s88, s59, 0x22000\n"
"v_cvt_pk_bf16_f32 v74, v64, v65\n"
"v_cvt_pk_bf16_f32 v75, v66, v67\n"
"v_exp_f32_e32 v56, v24\n"
"v_exp_f32_e32 v57, v25\n"
"v_mfma_f32_16x16x32_bf16 v[96:99], a[220:223], v[136:139], v[96:99]\n"
"v_exp_f32_e32 v58, v26\n"
"v_exp_f32_e32 v59, v27\n"
"v_mfma_f32_32x32x16_bf16 a[0:15], a[68:71], v[72:75], a[0:15]\n"
"buffer_load_dwordx4 a[208:211], v0, s[48:51], s88 offen\n"
"buffer_load_dwordx4 a[212:215], v0, s[48:51], s88 offen offset:1024\n"
"v_cvt_pk_bf16_f32 v68, v56, v57\n"
"v_cvt_pk_bf16_f32 v69, v58, v59\n"
"v_exp_f32_e32 v60, v28\n"
"v_exp_f32_e32 v61, v29\n"
"v_exp_f32_e32 v62, v30\n"
"v_exp_f32_e32 v63, v31\n"
"v_mfma_f32_32x32x8bf16_1k v[40:55], a[42:43], v[82:83], v[40:55]\n"
"v_cvt_pk_bf16_f32 v70, v60, v61\n"
"v_cvt_pk_bf16_f32 v71, v62, v63\n"
"v_exp_f32_e32 v56, v32\n"
"v_exp_f32_e32 v57, v33\n"
"v_exp_f32_e32 v58, v34\n"
"v_exp_f32_e32 v59, v35\n"
"v_mfma_f32_32x32x16_bf16 a[0:15], a[76:79], v[68:71], a[0:15]\n"
"v_cvt_pk_bf16_f32 v72, v56, v57\n"
"v_cvt_pk_bf16_f32 v73, v58, v59\n"
"v_exp_f32_e32 v64, v36\n"
"v_exp_f32_e32 v65, v37\n"
"v_mfma_f32_16x16x32_bf16 v[100:103], a[224:227], v[136:139], v[100:103]\n"
"v_exp_f32_e32 v66, v38\n"
"v_exp_f32_e32 v67, v39\n"
"v_mfma_f32_32x32x16_bf16 v[24:39], a[96:99], v[76:79], 0\n"
"buffer_load_dwordx4 a[216:219], v0, s[48:51], s88 offen offset:2048\n"
"buffer_load_dwordx4 a[220:223], v0, s[48:51], s88 offen offset:3072\n"
"v_cvt_pk_bf16_f32 v74, v64, v65\n"
"v_cvt_pk_bf16_f32 v75, v66, v67\n"
"v_exp_f32_e32 v56, v40\n"
"v_exp_f32_e32 v57, v41\n"
"v_mfma_f32_16x16x32_bf16 v[104:107], a[228:231], v[136:139], v[104:107]\n"
"v_exp_f32_e32 v58, v42\n"
"v_exp_f32_e32 v59, v43\n"
"v_mfma_f32_32x32x16_bf16 a[0:15], a[80:83], v[72:75], a[0:15]\n"
"s_add_u32 s88, s59, 0x23000\n"
"v_cvt_pk_bf16_f32 v68, v56, v57\n"
"v_cvt_pk_bf16_f32 v69, v58, v59\n"
"v_exp_f32_e32 v60, v44\n"
"v_exp_f32_e32 v61, v45\n"
"v_exp_f32_e32 v62, v46\n"
"v_exp_f32_e32 v63, v47\n"
"v_mfma_f32_32x32x8bf16_1k v[24:39], a[44:45], v[80:81], v[24:39]\n"
"v_cvt_pk_bf16_f32 v70, v60, v61\n"
"v_cvt_pk_bf16_f32 v71, v62, v63\n"
"v_exp_f32_e32 v56, v48\n"
"v_exp_f32_e32 v57, v49\n"
"v_exp_f32_e32 v58, v50\n"
"v_exp_f32_e32 v59, v51\n"
"v_mfma_f32_32x32x16_bf16 a[0:15], a[88:91], v[68:71], a[0:15]\n"
"buffer_store_dwordx4 v[132:135], v0, s[48:51], s71 offen offset:1024 sc1\n"
"v_cvt_pk_bf16_f32 v72, v56, v57\n"
"v_cvt_pk_bf16_f32 v73, v58, v59\n"
"v_exp_f32_e32 v64, v52\n"
"v_exp_f32_e32 v65, v53\n"
"v_mfma_f32_16x16x32_bf16 v[108:111], a[232:235], v[136:139], v[108:111]\n"
"v_exp_f32_e32 v66, v54\n"
"v_exp_f32_e32 v67, v55\n"
"v_mfma_f32_32x32x16_bf16 v[40:55], a[108:111], v[76:79], 0\n"
"buffer_load_dwordx4 a[224:227], v0, s[48:51], s88 offen\n"
"buffer_load_dwordx4 a[228:231], v0, s[48:51], s88 offen offset:1024\n"
"v_cvt_pk_bf16_f32 v74, v64, v65\n"
"v_cvt_pk_bf16_f32 v75, v66, v67\n"
"v_exp_f32_e32 v56, v24\n"
"v_exp_f32_e32 v57, v25\n"
"v_mfma_f32_16x16x32_bf16 v[92:95], a[236:239], v[140:143], v[92:95]\n"
"v_exp_f32_e32 v58, v26\n"
"v_exp_f32_e32 v59, v27\n"
"v_mfma_f32_32x32x16_bf16 a[0:15], a[92:95], v[72:75], a[0:15]\n"
"v_cvt_pk_bf16_f32 v68, v56, v57\n"
"v_cvt_pk_bf16_f32 v69, v58, v59\n"
"v_exp_f32_e32 v60, v28\n"
"v_exp_f32_e32 v61, v29\n"
"v_exp_f32_e32 v62, v30\n"
"v_exp_f32_e32 v63, v31\n"
"v_mfma_f32_32x32x8bf16_1k v[40:55], a[44:45], v[82:83], v[40:55]\n"
"v_cvt_pk_bf16_f32 v70, v60, v61\n"
"v_cvt_pk_bf16_f32 v71, v62, v63\n"
"v_exp_f32_e32 v56, v32\n"
"v_exp_f32_e32 v57, v33\n"
"v_exp_f32_e32 v58, v34\n"
"v_exp_f32_e32 v59, v35\n"
"v_mfma_f32_32x32x16_bf16 a[0:15], a[100:103], v[68:71], a[0:15]\n"
"v_cvt_pk_bf16_f32 v72, v56, v57\n"
"v_cvt_pk_bf16_f32 v73, v58, v59\n"
"v_exp_f32_e32 v64, v36\n"
"v_exp_f32_e32 v65, v37\n"
"v_mfma_f32_16x16x32_bf16 v[96:99], a[240:243], v[140:143], v[96:99]\n"
"v_exp_f32_e32 v66, v38\n"
"v_exp_f32_e32 v67, v39\n"
"v_mfma_f32_32x32x16_bf16 v[24:39], a[120:123], v[76:79], 0\n"
"buffer_load_dwordx4 a[232:235], v0, s[48:51], s88 offen offset:2048\n"
"buffer_load_dwordx4 a[236:239], v0, s[48:51], s88 offen offset:3072\n"
"v_cvt_pk_bf16_f32 v74, v64, v65\n"
"v_cvt_pk_bf16_f32 v75, v66, v67\n"
"v_exp_f32_e32 v56, v40\n"
"v_exp_f32_e32 v57, v41\n"
"v_mfma_f32_16x16x32_bf16 v[100:103], a[244:247], v[140:143], v[100:103]\n"
"v_exp_f32_e32 v58, v42\n"
"v_exp_f32_e32 v59, v43\n"
"v_mfma_f32_32x32x16_bf16 a[0:15], a[104:107], v[72:75], a[0:15]\n"
"s_add_u32 s88, s59, 0x24000\n"
"v_cvt_pk_bf16_f32 v68, v56, v57\n"
"v_cvt_pk_bf16_f32 v69, v58, v59\n"
"v_exp_f32_e32 v60, v44\n"
"v_exp_f32_e32 v61, v45\n"
"v_exp_f32_e32 v62, v46\n"
"v_exp_f32_e32 v63, v47\n"
"v_mfma_f32_32x32x8bf16_1k v[24:39], a[46:47], v[80:81], v[24:39]\n"
"v_cvt_pk_bf16_f32 v70, v60, v61\n"
"v_cvt_pk_bf16_f32 v71, v62, v63\n"
"v_exp_f32_e32 v56, v48\n"
"v_exp_f32_e32 v57, v49\n"
"v_exp_f32_e32 v58, v50\n"
"v_exp_f32_e32 v59, v51\n"
"v_mfma_f32_32x32x16_bf16 a[0:15], a[112:115], v[68:71], a[0:15]\n"
"buffer_store_dwordx4 v[132:135], v0, s[48:51], s71 offen offset:2048 sc1\n"
"v_cvt_pk_bf16_f32 v72, v56, v57\n"
"v_cvt_pk_bf16_f32 v73, v58, v59\n"
"v_exp_f32_e32 v64, v52\n"
"v_exp_f32_e32 v65, v53\n"
"v_mfma_f32_16x16x32_bf16 v[104:107], a[248:251], v[140:143], v[104:107]\n"
"v_exp_f32_e32 v66, v54\n"
"v_exp_f32_e32 v67, v55\n"
"v_mfma_f32_32x32x16_bf16 v[40:55], a[132:135], v[76:79], 0\n"
"buffer_load_dwordx4 a[240:243], v0, s[48:51], s88 offen\n"
"buffer_load_dwordx4 a[244:247], v0, s[48:51], s88 offen offset:1024\n"
"v_cvt_pk_bf16_f32 v74, v64, v65\n"
"v_cvt_pk_bf16_f32 v75, v66, v67\n"
"v_exp_f32_e32 v56, v24\n"
"v_exp_f32_e32 v57, v25\n"
"v_exp_f32_e32 v58, v26\n"
"v_exp_f32_e32 v59, v27\n"
"v_mfma_f32_32x32x16_bf16 a[0:15], a[116:119], v[72:75], a[0:15]\n"
"s_waitcnt vmcnt(23)\n"
"v_cvt_pk_bf16_f32 v68, v56, v57\n"
"v_cvt_pk_bf16_f32 v69, v58, v59\n"
"v_exp_f32_e32 v60, v28\n"
"v_exp_f32_e32 v61, v29\n"
"v_cndmask_b32_e64 v90, 0, v89, s[36:37]\n"
"v_mov_b32_e32 v91, v88\n"
"v_cndmask_b32_e64 v89, 0, v89, s[36:37]\n"
"v_exp_f32_e32 v62, v30\n"
"v_exp_f32_e32 v63, v31\n"
"v_mfma_f32_32x32x8bf16_1k v[40:55], a[46:47], v[82:83], v[40:55]\n"
"v_cvt_pk_bf16_f32 v70, v60, v61\n"
"v_cvt_pk_bf16_f32 v71, v62, v63\n"
"v_exp_f32_e32 v56, v32\n"
"v_exp_f32_e32 v57, v33\n"
"v_exp_f32_e32 v58, v34\n"
"v_exp_f32_e32 v59, v35\n"
"v_mfma_f32_32x32x16_bf16 a[0:15], a[124:127], v[68:71], a[0:15]\n"
"v_cvt_pk_bf16_f32 v72, v56, v57\n"
"v_cvt_pk_bf16_f32 v73, v58, v59\n"
"v_exp_f32_e32 v64, v36\n"
"v_exp_f32_e32 v65, v37\n"
"v_exp_f32_e32 v66, v38\n"
"v_exp_f32_e32 v67, v39\n"
"v_mfma_f32_32x32x16_bf16 v[24:39], v[152:155], v[84:87], 0\n"
"v_cvt_pk_bf16_f32 v74, v64, v65\n"
"v_cvt_pk_bf16_f32 v75, v66, v67\n"
"v_exp_f32_e32 v56, v40\n"
"v_exp_f32_e32 v57, v41\n"
"v_exp_f32_e32 v58, v42\n"
"v_exp_f32_e32 v59, v43\n"
"v_mfma_f32_32x32x16_bf16 a[0:15], a[128:131], v[72:75], a[0:15]\n"
"v_cvt_pk_bf16_f32 v68, v56, v57\n"
"v_cvt_pk_bf16_f32 v69, v58, v59\n"
"v_exp_f32_e32 v60, v44\n"
"v_exp_f32_e32 v61, v45\n"
"v_exp_f32_e32 v62, v46\n"
"v_exp_f32_e32 v63, v47\n"
"v_mfma_f32_32x32x8bf16_1k v[24:39], a[32:33], v[88:89], v[24:39]\n"
"v_cvt_pk_bf16_f32 v70, v60, v61\n"
"v_cvt_pk_bf16_f32 v71, v62, v63\n"
"v_exp_f32_e32 v56, v48\n"
"v_exp_f32_e32 v57, v49\n"
"v_exp_f32_e32 v58, v50\n"
"v_exp_f32_e32 v59, v51\n"
"v_mfma_f32_32x32x16_bf16 a[0:15], a[136:139], v[68:71], a[0:15]\n"
"buffer_store_dwordx4 v[132:135], v0, s[48:51], s71 offen offset:3072 sc1\n"
"v_cvt_pk_bf16_f32 v72, v56, v57\n"
"v_cvt_pk_bf16_f32 v73, v58, v59\n"
"v_exp_f32_e32 v64, v52\n"
"v_exp_f32_e32 v65, v53\n"
"v_exp_f32_e32 v66, v54\n"
"v_exp_f32_e32 v67, v55\n"
"v_mfma_f32_32x32x16_bf16 v[40:55], v[164:167], v[84:87], 0\n"
"v_cvt_pk_bf16_f32 v74, v64, v65\n"
"v_cvt_pk_bf16_f32 v75, v66, v67\n"
"v_exp_f32_e32 v56, v24\n"
"v_exp_f32_e32 v57, v25\n"
"v_exp_f32_e32 v58, v26\n"
"v_exp_f32_e32 v59, v27\n"
"v_mfma_f32_32x32x16_bf16 a[0:15], a[140:143], v[72:75], a[0:15]\n"
"v_cvt_pk_bf16_f32 v68, v56, v57\n"
"v_cvt_pk_bf16_f32 v69, v58, v59\n"
"v_exp_f32_e32 v60, v28\n"
"v_exp_f32_e32 v61, v29\n"
"v_exp_f32_e32 v62, v30\n"
"v_exp_f32_e32 v63, v31\n"
"v_mfma_f32_32x32x8bf16_1k v[40:55], a[32:33], v[90:91], v[40:55]\n"
"v_cvt_pk_bf16_f32 v70, v60, v61\n"
"v_cvt_pk_bf16_f32 v71, v62, v63\n"
"v_exp_f32_e32 v56, v32\n"
"v_exp_f32_e32 v57, v33\n"
"v_exp_f32_e32 v58, v34\n"
"v_exp_f32_e32 v59, v35\n"
"v_mfma_f32_32x32x16_bf16 a[16:31], v[156:159], v[68:71], 0\n"
"ds_write_b128 v8, a[0:3] offset:0\n"
"buffer_load_dword v6, v22, s[48:51], s70 offen offset:256\n"
"v_cvt_pk_bf16_f32 v72, v56, v57\n"
"v_cvt_pk_bf16_f32 v73, v58, v59\n"
"v_exp_f32_e32 v64, v36\n"
"v_exp_f32_e32 v65, v37\n"
"v_exp_f32_e32 v66, v38\n"
"v_exp_f32_e32 v67, v39\n"
"v_mfma_f32_32x32x16_bf16 v[24:39], v[176:179], v[84:87], 0\n"
"ds_write_b128 v8, a[4:7] offset:1024\n"
"v_cvt_pk_bf16_f32 v74, v64, v65\n"
"v_cvt_pk_bf16_f32 v75, v66, v67\n"
"v_exp_f32_e32 v56, v40\n"
"v_exp_f32_e32 v57, v41\n"
"v_exp_f32_e32 v58, v42\n"
"v_exp_f32_e32 v59, v43\n"
"v_mfma_f32_32x32x16_bf16 a[16:31], v[160:163], v[72:75], a[16:31]\n"
"ds_write_b128 v8, a[8:11] offset:2048\n"
"v_cvt_pk_bf16_f32 v68, v56, v57\n"
"v_cvt_pk_bf16_f32 v69, v58, v59\n"
"v_exp_f32_e32 v60, v44\n"
"v_exp_f32_e32 v61, v45\n"
"v_exp_f32_e32 v62, v46\n"
"v_exp_f32_e32 v63, v47\n"
"v_mfma_f32_32x32x8bf16_1k v[24:39], a[34:35], v[88:89], v[24:39]\n"
"v_cvt_pk_bf16_f32 v70, v60, v61\n"
"v_cvt_pk_bf16_f32 v71, v62, v63\n"
"v_exp_f32_e32 v56, v48\n"
"v_exp_f32_e32 v57, v49\n"
"v_exp_f32_e32 v58, v50\n"
"v_exp_f32_e32 v59, v51\n"
"v_mfma_f32_32x32x16_bf16 a[16:31], v[168:171], v[68:71], a[16:31]\n"
"s_waitcnt lgkmcnt(0)\n"
"s_barrier\n"
"v_cvt_pk_bf16_f32 v72, v56, v57\n"
"v_cvt_pk_bf16_f32 v73, v58, v59\n"
"v_exp_f32_e32 v64, v52\n"
"v_exp_f32_e32 v65, v53\n"
"v_exp_f32_e32 v66, v54\n"
"v_exp_f32_e32 v67, v55\n"
"v_mfma_f32_32x32x16_bf16 v[40:55], v[188:191], v[84:87], 0\n"
".LK3_pad6:\n"
"s_cbranch_execz .LK1_ret\n"
"ds_read_b128 v[112:115], v9 offset:0\n"
"ds_read_b128 v[116:119], v9 offset:3072\n"
"v_cvt_pk_bf16_f32 v74, v64, v65\n"
"v_cvt_pk_bf16_f32 v75, v66, v67\n"
"v_exp_f32_e32 v56, v24\n"
"v_exp_f32_e32 v57, v25\n"
"v_exp_f32_e32 v58, v26\n"
"v_exp_f32_e32 v59, v27\n"
"v_mfma_f32_32x32x16_bf16 a[16:31], v[172:175], v[72:75], a[16:31]\n"
"ds_read_b128 v[120:123], v9 offset:6144\n"
"ds_read_b128 v[124:127], v9 offset:9216\n"
"v_cvt_pk_bf16_f32 v68, v56, v57\n"
"v_cvt_pk_bf16_f32 v69, v58, v59\n"
"v_exp_f32_e32 v60, v28\n"
"v_exp_f32_e32 v61, v29\n"
"v_exp_f32_e32 v62, v30\n"
"v_exp_f32_e32 v63, v31\n"
"v_mfma_f32_32x32x8bf16_1k v[40:55], a[34:35], v[90:91], v[40:55]\n"
"v_cvt_pk_bf16_f32 v70, v60, v61\n"
"v_cvt_pk_bf16_f32 v71, v62, v63\n"
"v_exp_f32_e32 v56, v32\n"
"v_exp_f32_e32 v57, v33\n"
"v_exp_f32_e32 v58, v34\n"
"v_exp_f32_e32 v59, v35\n"
"v_mfma_f32_32x32x16_bf16 a[16:31], v[180:183], v[68:71], a[16:31]\n"
"s_waitcnt lgkmcnt(0)\n"
"v_cvt_pk_bf16_f32 v72, v56, v57\n"
"v_cvt_pk_bf16_f32 v73, v58, v59\n"
"v_exp_f32_e32 v64, v36\n"
"v_exp_f32_e32 v65, v37\n"
"v_add_f32_e32 v115, v115, v119\n"
"v_add_f32_e32 v112, v112, v116\n"
"v_add_f32_e32 v113, v113, v117\n"
"v_add_f32_e32 v114, v114, v118\n"
"v_add_f32_e32 v123, v123, v127\n"
"v_add_f32_e32 v120, v120, v124\n"
"v_exp_f32_e32 v66, v38\n"
"v_exp_f32_e32 v67, v39\n"
"v_mfma_f32_32x32x16_bf16 v[24:39], v[200:203], v[84:87], 0\n"
"v_cvt_pk_bf16_f32 v74, v64, v65\n"
"v_cvt_pk_bf16_f32 v75, v66, v67\n"
"v_exp_f32_e32 v56, v40\n"
"v_exp_f32_e32 v57, v41\n"
"v_add_f32_e32 v121, v121, v125\n"
"v_add_f32_e32 v122, v122, v126\n"
"v_add_f32_e32 v115, v115, v123\n"
"v_rcp_f32_e32 v115, v115\n"
"v_add_f32_e32 v112, v112, v120\n"
"v_add_f32_e32 v113, v113, v121\n"
"v_exp_f32_e32 v58, v42\n"
"v_exp_f32_e32 v59, v43\n"
"v_mfma_f32_32x32x16_bf16 a[16:31], v[184:187], v[72:75], a[16:31]\n"
"v_cvt_pk_bf16_f32 v68, v56, v57\n"
"v_cvt_pk_bf16_f32 v69, v58, v59\n"
"v_exp_f32_e32 v60, v44\n"
"v_exp_f32_e32 v61, v45\n"
"v_add_f32_e32 v114, v114, v122\n"
"v_mul_f32_e32 v112, v112, v115\n"
"v_mul_f32_e32 v113, v113, v115\n"
"v_mul_f32_e32 v114, v114, v115\n"
"v_cvt_pk_bf16_f32 v112, v112, v113\n"
"v_cvt_pk_bf16_f32 v114, v114, v114\n"
"v_exp_f32_e32 v62, v46\n"
"v_exp_f32_e32 v63, v47\n"
"v_mfma_f32_32x32x8bf16_1k v[24:39], a[36:37], v[88:89], v[24:39]\n"
"v_cvt_pk_bf16_f32 v70, v60, v61\n"
"v_cvt_pk_bf16_f32 v71, v62, v63\n"
"v_exp_f32_e32 v56, v48\n"
"v_exp_f32_e32 v57, v49\n"
"v_exp_f32_e32 v58, v50\n"
"v_exp_f32_e32 v59, v51\n"
"v_mfma_f32_32x32x16_bf16 a[16:31], v[192:195], v[68:71], a[16:31]\n"
"ds_write_b32 v10, v112 offset:864\n"
"ds_write_b16 v11, v114 offset:864\n"
"v_cvt_pk_bf16_f32 v72, v56, v57\n"
"v_cvt_pk_bf16_f32 v73, v58, v59\n"
"v_exp_f32_e32 v64, v52\n"
"v_exp_f32_e32 v65, v53\n"
"v_exp_f32_e32 v66, v54\n"
"v_exp_f32_e32 v67, v55\n"
"v_mfma_f32_32x32x16_bf16 v[40:55], v[212:215], v[84:87], 0\n"
"v_cvt_pk_bf16_f32 v74, v64, v65\n"
"v_cvt_pk_bf16_f32 v75, v66, v67\n"
"v_exp_f32_e32 v56, v24\n"
"v_exp_f32_e32 v57, v25\n"
"v_exp_f32_e32 v58, v26\n"
"v_exp_f32_e32 v59, v27\n"
"v_mfma_f32_32x32x16_bf16 a[16:31], v[196:199], v[72:75], a[16:31]\n"
"s_waitcnt lgkmcnt(0)\n"
"s_barrier\n"
"v_cvt_pk_bf16_f32 v68, v56, v57\n"
"v_cvt_pk_bf16_f32 v69, v58, v59\n"
"v_exp_f32_e32 v60, v28\n"
"v_exp_f32_e32 v61, v29\n"
"v_exp_f32_e32 v62, v30\n"
"v_exp_f32_e32 v63, v31\n"
"v_mfma_f32_32x32x8bf16_1k v[40:55], a[36:37], v[90:91], v[40:55]\n"
"v_cvt_pk_bf16_f32 v70, v60, v61\n"
"v_cvt_pk_bf16_f32 v71, v62, v63\n"
"v_exp_f32_e32 v56, v32\n"
"v_exp_f32_e32 v57, v33\n"
"v_exp_f32_e32 v58, v34\n"
"v_exp_f32_e32 v59, v35\n"
"v_mfma_f32_32x32x16_bf16 a[16:31], v[204:207], v[68:71], a[16:31]\n"
"ds_read_b128 v[144:147], v12 offset:832\n"
"ds_read_b128 v[148:151], v13 offset:832\n"
"v_cvt_pk_bf16_f32 v72, v56, v57\n"
"v_cvt_pk_bf16_f32 v73, v58, v59\n"
"v_exp_f32_e32 v64, v36\n"
"v_exp_f32_e32 v65, v37\n"
"v_exp_f32_e32 v66, v38\n"
"v_exp_f32_e32 v67, v39\n"
"v_mfma_f32_32x32x16_bf16 v[24:39], v[224:227], v[84:87], 0\n"
"v_cvt_pk_bf16_f32 v74, v64, v65\n"
"v_cvt_pk_bf16_f32 v75, v66, v67\n"
"v_exp_f32_e32 v56, v40\n"
"v_exp_f32_e32 v57, v41\n"
"v_exp_f32_e32 v58, v42\n"
"v_exp_f32_e32 v59, v43\n"
"v_mfma_f32_32x32x16_bf16 a[16:31], v[208:211], v[72:75], a[16:31]\n"
"s_waitcnt lgkmcnt(0)\n"
"v_cvt_pk_bf16_f32 v68, v56, v57\n"
"v_cvt_pk_bf16_f32 v69, v58, v59\n"
"v_exp_f32_e32 v60, v44\n"
"v_exp_f32_e32 v61, v45\n"
"v_mfma_f32_16x16x32_bf16 v[92:95], a[252:255], v[144:147], v[92:95]\n"
"v_exp_f32_e32 v62, v46\n"
"v_exp_f32_e32 v63, v47\n"
"v_mfma_f32_32x32x8bf16_1k v[24:39], a[38:39], v[88:89], v[24:39]\n"
"v_cvt_pk_bf16_f32 v70, v60, v61\n"
"v_cvt_pk_bf16_f32 v71, v62, v63\n"
"v_exp_f32_e32 v56, v48\n"
"v_exp_f32_e32 v57, v49\n"
"v_exp_f32_e32 v58, v50\n"
"v_exp_f32_e32 v59, v51\n"
"v_mfma_f32_32x32x16_bf16 a[16:31], v[216:219], v[68:71], a[16:31]\n"
"buffer_load_dwordx4 a[248:251], v0, s[48:51], s88 offen offset:2048\n"
"buffer_load_dwordx4 a[252:255], v0, s[48:51], s88 offen offset:3072\n"
"v_cvt_pk_bf16_f32 v72, v56, v57\n"
"v_cvt_pk_bf16_f32 v73, v58, v59\n"
"v_exp_f32_e32 v64, v52\n"
"v_exp_f32_e32 v65, v53\n"
"v_mfma_f32_16x16x32_bf16 v[96:99], v[248:251], v[144:147], v[96:99]\n"
"v_exp_f32_e32 v66, v54\n"
"v_exp_f32_e32 v67, v55\n"
"v_mfma_f32_32x32x16_bf16 v[40:55], v[236:239], v[84:87], 0\n"
"s_add_u32 s88, s59, 0x25000\n"
"v_cvt_pk_bf16_f32 v74, v64, v65\n"
"v_cvt_pk_bf16_f32 v75, v66, v67\n"
"v_exp_f32_e32 v56, v24\n"
"v_exp_f32_e32 v57, v25\n"
"v_mfma_f32_16x16x32_bf16 v[100:103], v[252:255], v[144:147], v[100:103]\n"
"v_exp_f32_e32 v58, v26\n"
"v_exp_f32_e32 v59, v27\n"
"v_mfma_f32_32x32x16_bf16 a[16:31], v[220:223], v[72:75], a[16:31]\n"
"buffer_load_dwordx4 v[248:251], v0, s[48:51], s88 offen\n"
"buffer_load_dwordx4 v[252:255], v0, s[48:51], s88 offen offset:1024\n"
"v_cvt_pk_bf16_f32 v68, v56, v57\n"
"v_cvt_pk_bf16_f32 v69, v58, v59\n"
"v_exp_f32_e32 v60, v28\n"
"v_exp_f32_e32 v61, v29\n"
"v_mfma_f32_16x16x32_bf16 v[104:107], a[144:147], v[144:147], v[104:107]\n"
"v_exp_f32_e32 v62, v30\n"
"v_exp_f32_e32 v63, v31\n"
"v_mfma_f32_32x32x8bf16_1k v[40:55], a[38:39], v[90:91], v[40:55]\n"
"v_cvt_pk_bf16_f32 v70, v60, v61\n"
"v_cvt_pk_bf16_f32 v71, v62, v63\n"
"v_exp_f32_e32 v56, v32\n"
"v_exp_f32_e32 v57, v33\n"
"v_exp_f32_e32 v58, v34\n"
"v_exp_f32_e32 v59, v35\n"
"v_mfma_f32_32x32x16_bf16 a[16:31], v[228:231], v[68:71], a[16:31]\n"
"v_cvt_pk_bf16_f32 v72, v56, v57\n"
"v_cvt_pk_bf16_f32 v73, v58, v59\n"
"v_exp_f32_e32 v64, v36\n"
"v_exp_f32_e32 v65, v37\n"
"v_mfma_f32_16x16x32_bf16 v[108:111], a[148:151], v[144:147], v[108:111]\n"
"v_exp_f32_e32 v66, v38\n"
"v_exp_f32_e32 v67, v39\n"
"v_mfma_f32_32x32x16_bf16 v[24:39], a[48:51], v[84:87], 0\n"
"buffer_load_dwordx4 a[144:147], v0, s[48:51], s88 offen offset:2048\n"
"buffer_load_dwordx4 a[148:151], v0, s[48:51], s88 offen offset:3072\n"
"v_cvt_pk_bf16_f32 v74, v64, v65\n"
"v_cvt_pk_bf16_f32 v75, v66, v67\n"
"v_exp_f32_e32 v56, v40\n"
"v_exp_f32_e32 v57, v41\n"
"v_mfma_f32_16x16x32_bf16 v[92:95], a[152:155], v[148:151], v[92:95]\n"
"v_exp_f32_e32 v58, v42\n"
"v_exp_f32_e32 v59, v43\n"
"v_mfma_f32_32x32x16_bf16 a[16:31], v[232:235], v[72:75], a[16:31]\n"
"s_add_u32 s88, s59, 0x26000\n"
"v_cvt_pk_bf16_f32 v68, v56, v57\n"
"v_cvt_pk_bf16_f32 v69, v58, v59\n"
"v_exp_f32_e32 v60, v44\n"
"v_exp_f32_e32 v61, v45\n"
"v_mfma_f32_16x16x32_bf16 v[96:99], a[156:159], v[148:151], v[96:99]\n"
"v_exp_f32_e32 v62, v46\n"
"v_exp_f32_e32 v63, v47\n"
"v_mfma_f32_32x32x8bf16_1k v[24:39], a[40:41], v[88:89], v[24:39]\n"
"v_cvt_pk_bf16_f32 v70, v60, v61\n"
"v_cvt_pk_bf16_f32 v71, v62, v63\n"
"v_exp_f32_e32 v56, v48\n"
"v_exp_f32_e32 v57, v49\n"
"v_exp_f32_e32 v58, v50\n"
"v_exp_f32_e32 v59, v51\n"
"v_mfma_f32_32x32x16_bf16 a[16:31], v[240:243], v[68:71], a[16:31]\n"
"ds_read_b128 v[136:139], v12 offset:896\n"
"ds_read_b128 v[140:143], v13 offset:896\n"
"v_cvt_pk_bf16_f32 v72, v56, v57\n"
"v_cvt_pk_bf16_f32 v73, v58, v59\n"
"v_exp_f32_e32 v64, v52\n"
"v_exp_f32_e32 v65, v53\n"
"v_mfma_f32_16x16x32_bf16 v[100:103], a[160:163], v[148:151], v[100:103]\n"
"v_exp_f32_e32 v66, v54\n"
"v_exp_f32_e32 v67, v55\n"
"v_mfma_f32_32x32x16_bf16 v[40:55], a[60:63], v[84:87], 0\n"
"buffer_load_dwordx4 a[152:155], v0, s[48:51], s88 offen\n"
"buffer_load_dwordx4 a[156:159], v0, s[48:51], s88 offen offset:1024\n"
"v_cvt_pk_bf16_f32 v74, v64, v65\n"
"v_cvt_pk_bf16_f32 v75, v66, v67\n"
"v_exp_f32_e32 v56, v24\n"
"v_exp_f32_e32 v57, v25\n"
"v_mfma_f32_16x16x32_bf16 v[104:107], a[164:167], v[148:151], v[104:107]\n"
"v_exp_f32_e32 v58, v26\n"
"v_exp_f32_e32 v59, v27\n"
"v_mfma_f32_32x32x16_bf16 a[16:31], v[244:247], v[72:75], a[16:31]\n"
"s_waitcnt vmcnt(24) lgkmcnt(0)\n"
"v_cvt_pk_bf16_f32 v68, v56, v57\n"
"v_cvt_pk_bf16_f32 v69, v58, v59\n"
"v_exp_f32_e32 v60, v28\n"
"v_exp_f32_e32 v61, v29\n"
"v_mfma_f32_16x16x32_bf16 v[92:95], a[168:171], v[136:139], v[92:95]\n"
"v_exp_f32_e32 v62, v30\n"
"v_exp_f32_e32 v63, v31\n"
"v_mfma_f32_32x32x8bf16_1k v[40:55], a[40:41], v[90:91], v[40:55]\n"
"v_cvt_pk_bf16_f32 v70, v60, v61\n"
"v_cvt_pk_bf16_f32 v71, v62, v63\n"
"v_exp_f32_e32 v56, v32\n"
"v_exp_f32_e32 v57, v33\n"
"v_exp_f32_e32 v58, v34\n"
"v_exp_f32_e32 v59, v35\n"
"v_mfma_f32_32x32x16_bf16 a[16:31], a[52:55], v[68:71], a[16:31]\n"
"buffer_load_dwordx4 a[160:163], v0, s[48:51], s88 offen offset:2048\n"
"buffer_load_dwordx4 a[164:167], v0, s[48:51], s88 offen offset:3072\n"
"v_cvt_pk_bf16_f32 v72, v56, v57\n"
"v_cvt_pk_bf16_f32 v73, v58, v59\n"
"v_exp_f32_e32 v64, v36\n"
"v_exp_f32_e32 v65, v37\n"
"v_mfma_f32_16x16x32_bf16 v[96:99], a[172:175], v[136:139], v[96:99]\n"
"v_exp_f32_e32 v66, v38\n"
"v_exp_f32_e32 v67, v39\n"
"v_mfma_f32_32x32x16_bf16 v[24:39], a[72:75], v[84:87], 0\n"
"s_add_u32 s88, s59, 0x27000\n"
"v_cvt_pk_bf16_f32 v74, v64, v65\n"
"v_cvt_pk_bf16_f32 v75, v66, v67\n"
"v_exp_f32_e32 v56, v40\n"
"v_exp_f32_e32 v57, v41\n"
"v_mfma_f32_16x16x32_bf16 v[100:103], a[176:179], v[136:139], v[100:103]\n"
"v_exp_f32_e32 v58, v42\n"
"v_exp_f32_e32 v59, v43\n"
"v_mfma_f32_32x32x16_bf16 a[16:31], a[56:59], v[72:75], a[16:31]\n"
"buffer_load_dwordx4 a[168:171], v0, s[48:51], s88 offen\n"
"buffer_load_dwordx4 a[172:175], v0, s[48:51], s88 offen offset:1024\n"
"v_cvt_pk_bf16_f32 v68, v56, v57\n"
"v_cvt_pk_bf16_f32 v69, v58, v59\n"
"v_exp_f32_e32 v60, v44\n"
"v_exp_f32_e32 v61, v45\n"
"v_mfma_f32_16x16x32_bf16 v[104:107], a[180:183], v[136:139], v[104:107]\n"
"v_exp_f32_e32 v62, v46\n"
"v_exp_f32_e32 v63, v47\n"
"v_mfma_f32_32x32x8bf16_1k v[24:39], a[42:43], v[88:89], v[24:39]\n"
"v_cvt_pk_bf16_f32 v70, v60, v61\n"
"v_cvt_pk_bf16_f32 v71, v62, v63\n"
"v_exp_f32_e32 v56, v48\n"
"v_exp_f32_e32 v57, v49\n"
"v_exp_f32_e32 v58, v50\n"
"v_exp_f32_e32 v59, v51\n"
"v_mfma_f32_32x32x16_bf16 a[16:31], a[64:67], v[68:71], a[16:31]\n"
"buffer_load_dwordx4 a[176:179], v0, s[48:51], s88 offen offset:2048\n"
"buffer_load_dwordx4 a[180:183], v0, s[48:51], s88 offen offset:3072\n"
"v_cvt_pk_bf16_f32 v72, v56, v57\n"
"v_cvt_pk_bf16_f32 v73, v58, v59\n"
"v_exp_f32_e32 v64, v52\n"
"v_exp_f32_e32 v65, v53\n"
"v_mfma_f32_16x16x32_bf16 v[108:111], a[184:187], v[136:139], v[108:111]\n"
"v_exp_f32_e32 v66, v54\n"
"v_exp_f32_e32 v67, v55\n"
"v_mfma_f32_32x32x16_bf16 v[40:55], a[84:87], v[84:87], 0\n"
"s_add_u32 s88, s59, 0x28000\n"
"v_cvt_pk_bf16_f32 v74, v64, v65\n"
"v_cvt_pk_bf16_f32 v75, v66, v67\n"
"v_exp_f32_e32 v56, v24\n"
"v_exp_f32_e32 v57, v25\n"
"v_mfma_f32_16x16x32_bf16 v[92:95], a[188:191], v[140:143], v[92:95]\n"
"v_exp_f32_e32 v58, v26\n"
"v_exp_f32_e32 v59, v27\n"
"v_mfma_f32_32x32x16_bf16 a[16:31], a[68:71], v[72:75], a[16:31]\n"
"buffer_load_dwordx4 a[48:51], v0, s[48:51], s88 offen\n"
"buffer_load_dwordx4 a[52:55], v0, s[48:51], s88 offen offset:1024\n"
"v_cvt_pk_bf16_f32 v68, v56, v57\n"
"v_cvt_pk_bf16_f32 v69, v58, v59\n"
"v_exp_f32_e32 v60, v28\n"
"v_exp_f32_e32 v61, v29\n"
"v_mfma_f32_16x16x32_bf16 v[96:99], a[192:195], v[140:143], v[96:99]\n"
"v_exp_f32_e32 v62, v30\n"
"v_exp_f32_e32 v63, v31\n"
"v_mfma_f32_32x32x8bf16_1k v[40:55], a[42:43], v[90:91], v[40:55]\n"
"v_cvt_pk_bf16_f32 v70, v60, v61\n"
"v_cvt_pk_bf16_f32 v71, v62, v63\n"
"v_exp_f32_e32 v56, v32\n"
"v_exp_f32_e32 v57, v33\n"
"v_exp_f32_e32 v58, v34\n"
"v_exp_f32_e32 v59, v35\n"
"v_mfma_f32_32x32x16_bf16 a[16:31], a[76:79], v[68:71], a[16:31]\n"
"v_cvt_pk_bf16_f32 v72, v56, v57\n"
"v_cvt_pk_bf16_f32 v73, v58, v59\n"
"v_exp_f32_e32 v64, v36\n"
"v_exp_f32_e32 v65, v37\n"
"v_mfma_f32_16x16x32_bf16 v[100:103], a[196:199], v[140:143], v[100:103]\n"
"v_exp_f32_e32 v66, v38\n"
"v_exp_f32_e32 v67, v39\n"
"v_mfma_f32_32x32x16_bf16 v[24:39], a[96:99], v[84:87], 0\n"
"s_add_u32 m0, s17, 0xe700\n"
"s_add_u32 s14, s16, 0x0\n"
"buffer_load_dwordx4 v1, s[76:79], s14 offen lds\n"
"v_cvt_pk_bf16_f32 v74, v64, v65\n"
"v_cvt_pk_bf16_f32 v75, v66, v67\n"
"v_exp_f32_e32 v56, v40\n"
"v_exp_f32_e32 v57, v41\n"
"v_mfma_f32_16x16x32_bf16 v[104:107], a[200:203], v[140:143], v[104:107]\n"
"v_exp_f32_e32 v58, v42\n"
"v_exp_f32_e32 v59, v43\n"
"v_mfma_f32_32x32x16_bf16 a[16:31], a[80:83], v[72:75], a[16:31]\n"
"buffer_load_dwordx4 v2, s[76:79], s14 offen offset:1056 lds\n"
"v_cvt_pk_bf16_f32 v68, v56, v57\n"
"v_cvt_pk_bf16_f32 v69, v58, v59\n"
"v_exp_f32_e32 v60, v44\n"
"v_exp_f32_e32 v61, v45\n"
"v_exp_f32_e32 v62, v46\n"
"v_exp_f32_e32 v63, v47\n"
"v_mfma_f32_32x32x8bf16_1k v[24:39], a[44:45], v[88:89], v[24:39]\n"
"v_cvt_pk_bf16_f32 v70, v60, v61\n"
"v_cvt_pk_bf16_f32 v71, v62, v63\n"
"v_exp_f32_e32 v56, v48\n"
"v_exp_f32_e32 v57, v49\n"
"v_exp_f32_e32 v58, v50\n"
"v_exp_f32_e32 v59, v51\n"
"v_mfma_f32_32x32x16_bf16 a[16:31], a[88:91], v[68:71], a[16:31]\n"
"s_add_u32 m0, s17, 0x10800\n"
"s_add_u32 s14, s16, 0x48000\n"
"buffer_load_dwordx4 v1, s[76:79], s14 offen lds\n"
"v_cvt_pk_bf16_f32 v72, v56, v57\n"
"v_cvt_pk_bf16_f32 v73, v58, v59\n"
"v_exp_f32_e32 v64, v52\n"
"v_exp_f32_e32 v65, v53\n"
"v_exp_f32_e32 v66, v54\n"
"v_exp_f32_e32 v67, v55\n"
"v_mfma_f32_32x32x16_bf16 v[40:55], a[108:111], v[84:87], 0\n"
"buffer_load_dwordx4 v2, s[76:79], s14 offen offset:1056 lds\n"
"v_cvt_pk_bf16_f32 v74, v64, v65\n"
"v_cvt_pk_bf16_f32 v75, v66, v67\n"
"v_exp_f32_e32 v56, v24\n"
"v_exp_f32_e32 v57, v25\n"
"v_exp_f32_e32 v58, v26\n"
"v_exp_f32_e32 v59, v27\n"
"v_mfma_f32_32x32x16_bf16 a[16:31], a[92:95], v[72:75], a[16:31]\n"
"s_add_u32 m0, s17, 0x12900\n"
"s_add_u32 s14, s16, 0x90000\n"
"buffer_load_dwordx4 v1, s[76:79], s14 offen lds\n"
"v_cvt_pk_bf16_f32 v68, v56, v57\n"
"v_cvt_pk_bf16_f32 v69, v58, v59\n"
"v_exp_f32_e32 v60, v28\n"
"v_exp_f32_e32 v61, v29\n"
"v_exp_f32_e32 v62, v30\n"
"v_exp_f32_e32 v63, v31\n"
"v_mfma_f32_32x32x8bf16_1k v[40:55], a[44:45], v[90:91], v[40:55]\n"
"v_cvt_pk_bf16_f32 v70, v60, v61\n"
"v_cvt_pk_bf16_f32 v71, v62, v63\n"
"v_exp_f32_e32 v56, v32\n"
"v_exp_f32_e32 v57, v33\n"
"v_exp_f32_e32 v58, v34\n"
"v_exp_f32_e32 v59, v35\n"
"v_mfma_f32_32x32x16_bf16 a[16:31], a[100:103], v[68:71], a[16:31]\n"
"buffer_load_dwordx4 v2, s[76:79], s14 offen offset:1056 lds\n"
"v_cvt_pk_bf16_f32 v72, v56, v57\n"
"v_cvt_pk_bf16_f32 v73, v58, v59\n"
"v_exp_f32_e32 v64, v36\n"
"v_exp_f32_e32 v65, v37\n"
"v_exp_f32_e32 v66, v38\n"
"v_exp_f32_e32 v67, v39\n"
"v_mfma_f32_32x32x16_bf16 v[24:39], a[120:123], v[84:87], 0\n"
"s_add_u32 m0, s17, 0x14a00\n"
"s_add_u32 s14, s16, 0xd8000\n"
"buffer_load_dwordx4 v1, s[76:79], s14 offen lds\n"
"v_cvt_pk_bf16_f32 v74, v64, v65\n"
"v_cvt_pk_bf16_f32 v75, v66, v67\n"
"v_exp_f32_e32 v56, v40\n"
"v_exp_f32_e32 v57, v41\n"
"v_exp_f32_e32 v58, v42\n"
"v_exp_f32_e32 v59, v43\n"
"v_mfma_f32_32x32x16_bf16 a[16:31], a[104:107], v[72:75], a[16:31]\n"
"buffer_load_dwordx4 v2, s[76:79], s14 offen offset:1056 lds\n"
"v_cvt_pk_bf16_f32 v68, v56, v57\n"
"v_cvt_pk_bf16_f32 v69, v58, v59\n"
"v_exp_f32_e32 v60, v44\n"
"v_exp_f32_e32 v61, v45\n"
"v_exp_f32_e32 v62, v46\n"
"v_exp_f32_e32 v63, v47\n"
"v_mfma_f32_32x32x8bf16_1k v[24:39], a[46:47], v[88:89], v[24:39]\n"
"v_cvt_pk_bf16_f32 v70, v60, v61\n"
"v_cvt_pk_bf16_f32 v71, v62, v63\n"
"v_exp_f32_e32 v56, v48\n"
"v_exp_f32_e32 v57, v49\n"
"v_exp_f32_e32 v58, v50\n"
"v_exp_f32_e32 v59, v51\n"
"v_mfma_f32_32x32x16_bf16 a[16:31], a[112:115], v[68:71], a[16:31]\n"
"s_add_u32 m0, s17, 0x16b00\n"
"s_add_u32 s14, s16, 0x120000\n"
"buffer_load_dwordx4 v1, s[76:79], s14 offen lds\n"
"v_cvt_pk_bf16_f32 v72, v56, v57\n"
"v_cvt_pk_bf16_f32 v73, v58, v59\n"
"v_exp_f32_e32 v64, v52\n"
"v_exp_f32_e32 v65, v53\n"
"v_exp_f32_e32 v66, v54\n"
"v_exp_f32_e32 v67, v55\n"
"v_mfma_f32_32x32x16_bf16 v[40:55], a[132:135], v[84:87], 0\n"
"buffer_load_dwordx4 v2, s[76:79], s14 offen offset:1056 lds\n"
"v_cvt_pk_bf16_f32 v74, v64, v65\n"
"v_cvt_pk_bf16_f32 v75, v66, v67\n"
"v_exp_f32_e32 v56, v24\n"
"v_exp_f32_e32 v57, v25\n"
"v_exp_f32_e32 v58, v26\n"
"v_exp_f32_e32 v59, v27\n"
"v_mfma_f32_32x32x16_bf16 a[16:31], a[116:119], v[72:75], a[16:31]\n"
"v_cvt_pk_bf16_f32 v68, v56, v57\n"
"v_cvt_pk_bf16_f32 v69, v58, v59\n"
"v_exp_f32_e32 v60, v28\n"
"v_exp_f32_e32 v61, v29\n"
"v_exp_f32_e32 v62, v30\n"
"v_exp_f32_e32 v63, v31\n"
"v_mfma_f32_32x32x8bf16_1k v[40:55], a[46:47], v[90:91], v[40:55]\n"
"v_cvt_pk_bf16_f32 v70, v60, v61\n"
"v_cvt_pk_bf16_f32 v71, v62, v63\n"
"v_exp_f32_e32 v56, v32\n"
"v_exp_f32_e32 v57, v33\n"
"v_exp_f32_e32 v58, v34\n"
"v_exp_f32_e32 v59, v35\n"
"v_mfma_f32_32x32x16_bf16 a[16:31], a[124:127], v[68:71], a[16:31]\n"
"v_cvt_pk_bf16_f32 v72, v56, v57\n"
"v_cvt_pk_bf16_f32 v73, v58, v59\n"
"v_exp_f32_e32 v64, v36\n"
"v_exp_f32_e32 v65, v37\n"
"v_exp_f32_e32 v66, v38\n"
"v_exp_f32_e32 v67, v39\n"
"s_nop 0\n"
"v_cvt_pk_bf16_f32 v74, v64, v65\n"
"v_cvt_pk_bf16_f32 v75, v66, v67\n"
"v_exp_f32_e32 v56, v40\n"
"v_exp_f32_e32 v57, v41\n"
"v_exp_f32_e32 v58, v42\n"
"v_exp_f32_e32 v59, v43\n"
"v_mfma_f32_32x32x16_bf16 a[16:31], a[128:131], v[72:75], a[16:31]\n"
"v_cvt_pk_bf16_f32 v68, v56, v57\n"
"v_cvt_pk_bf16_f32 v69, v58, v59\n"
"v_exp_f32_e32 v60, v44\n"
"v_exp_f32_e32 v61, v45\n"
"v_exp_f32_e32 v62, v46\n"
"v_exp_f32_e32 v63, v47\n"
"s_nop 0\n"
"v_cvt_pk_bf16_f32 v70, v60, v61\n"
"v_cvt_pk_bf16_f32 v71, v62, v63\n"
"v_exp_f32_e32 v56, v48\n"
"v_exp_f32_e32 v57, v49\n"
"v_exp_f32_e32 v58, v50\n"
"v_exp_f32_e32 v59, v51\n"
"v_mfma_f32_32x32x16_bf16 a[16:31], a[136:139], v[68:71], a[16:31]\n"
"v_cvt_pk_bf16_f32 v72, v56, v57\n"
"v_cvt_pk_bf16_f32 v73, v58, v59\n"
"v_exp_f32_e32 v64, v52\n"
"v_exp_f32_e32 v65, v53\n"
"v_exp_f32_e32 v66, v54\n"
"v_exp_f32_e32 v67, v55\n"
"s_nop 0\n"
"v_cvt_pk_bf16_f32 v74, v64, v65\n"
"v_cvt_pk_bf16_f32 v75, v66, v67\n"
"s_nop 1\n"
"v_mfma_f32_32x32x16_bf16 a[16:31], a[140:143], v[72:75], a[16:31]\n"
"s_add_u32 m0, s17, 0x18c00\n"
"s_add_u32 s14, s16, 0x168000\n"
"buffer_load_dwordx4 v1, s[76:79], s14 offen lds\n"
"buffer_load_dwordx4 v2, s[76:79], s14 offen offset:1056 lds\n"
"s_nop 7\n"
"ds_write_b128 v8, a[16:19] offset:12288\n"
"ds_write_b128 v8, a[20:23] offset:13312\n"
"ds_write_b128 v8, a[24:27] offset:14336\n"
"s_waitcnt lgkmcnt(0)\n"
"s_barrier\n"
"ds_read_b128 v[112:115], v9 offset:12288\n"
"ds_read_b128 v[116:119], v9 offset:15360\n"
"ds_read_b128 v[120:123], v9 offset:18432\n"
"ds_read_b128 v[124:127], v9 offset:21504\n"
"s_waitcnt lgkmcnt(2)\n"
"v_pk_add_f32 v[114:115], v[114:115], v[118:119]\n"
"v_pk_add_f32 v[112:113], v[112:113], v[116:117]\n"
"s_waitcnt lgkmcnt(0)\n"
"v_pk_add_f32 v[122:123], v[122:123], v[126:127]\n"
"v_pk_add_f32 v[120:121], v[120:121], v[124:125]\n"
"v_pk_add_f32 v[114:115], v[114:115], v[122:123]\n"
"v_rcp_f32_e32 v115, v115\n"
"v_pk_add_f32 v[112:113], v[112:113], v[120:121]\n"
"v_mul_f32_e32 v112, v112, v115\n"
"v_mul_f32_e32 v113, v113, v115\n"
"v_mul_f32_e32 v114, v114, v115\n"
"v_cvt_pk_bf16_f32 v112, v112, v113\n"
"v_cvt_pk_bf16_f32 v114, v114, v114\n"
"ds_write_b32 v10, v112 offset:1008\n"
"ds_write_b16 v11, v114 offset:1008\n"
"s_waitcnt lgkmcnt(0)\n"
"s_barrier\n"
"ds_read_b128 v[144:147], v12 offset:960\n"
"ds_read_b128 v[148:151], v13 offset:960\n"
"ds_read_b128 v[136:139], v12 offset:1024\n"
"ds_read_b128 v[140:143], v13 offset:1024\n"
"ds_read_b128 v[120:123], v12 offset:1088\n"
"ds_read_b128 v[124:127], v13 offset:1088\n"
"s_waitcnt vmcnt(12)\n"
"s_waitcnt lgkmcnt(5)\n"
"v_mfma_f32_16x16x32_bf16 v[92:95], a[204:207], v[144:147], v[92:95]\n"
"ds_read_b64 v[152:153], v14 offset:0\n"
"v_mfma_f32_16x16x32_bf16 v[96:99], a[208:211], v[144:147], v[96:99]\n"
"ds_read_b64 v[154:155], v14 offset:32\n"
"v_mfma_f32_16x16x32_bf16 v[100:103], a[212:215], v[144:147], v[100:103]\n"
"ds_read_b64 v[156:157], v14 offset:128\n"
"v_mfma_f32_16x16x32_bf16 v[104:107], a[216:219], v[144:147], v[104:107]\n"
"ds_read_b64 v[158:159], v14 offset:160\n"
"v_mfma_f32_16x16x32_bf16 v[108:111], a[220:223], v[144:147], v[108:111]\n"
"ds_read_b64 v[160:161], v15 offset:0\n"
"s_waitcnt lgkmcnt(9)\n"
"v_mfma_f32_16x16x32_bf16 v[92:95], a[224:227], v[148:151], v[92:95]\n"
"ds_read_b128 v[184:187], v16 offset:0\n"
"v_mfma_f32_16x16x32_bf16 v[96:99], a[228:231], v[148:151], v[96:99]\n"
"ds_read_b128 v[188:191], v16 offset:64\n"
"v_mfma_f32_16x16x32_bf16 v[100:103], a[232:235], v[148:151], v[100:103]\n"
"ds_read_b128 v[192:195], v16 offset:256\n"
"v_mfma_f32_16x16x32_bf16 v[104:107], a[236:239], v[148:151], v[104:107]\n"
"ds_read_b128 v[196:199], v16 offset:320\n"
"s_waitcnt lgkmcnt(12)\n"
"v_mfma_f32_16x16x32_bf16 v[92:95], a[240:243], v[136:139], v[92:95]\n"
"ds_read_b128 v[200:203], v17 offset:0\n"
"v_mfma_f32_16x16x32_bf16 v[96:99], a[244:247], v[136:139], v[96:99]\n"
"ds_read_b128 v[204:207], v16 offset:2304\n"
"v_mfma_f32_16x16x32_bf16 v[100:103], a[248:251], v[136:139], v[100:103]\n"
"ds_read_b128 v[208:211], v16 offset:2368\n"
"s_waitcnt lgkmcnt(7)\n"
"v_mfma_f32_16x16x32_bf16 v[104:107], a[252:255], v[136:139], v[104:107]\n"
"ds_read_b128 v[212:215], v16 offset:2560\n"
"v_lshlrev_b32_e32 v164, 16, v152\n"
"v_mfma_f32_16x16x32_bf16 v[108:111], v[248:251], v[136:139], v[108:111]\n"
"ds_read_b128 v[216:219], v16 offset:2624\n"
"v_and_b32_e32 v165, 0xffff0000, v152\n"
"v_mfma_f32_16x16x32_bf16 v[92:95], v[252:255], v[140:143], v[92:95]\n"
"ds_read_b128 v[220:223], v17 offset:2304\n"
"v_lshlrev_b32_e32 v166, 16, v153\n"
"v_mfma_f32_16x16x32_bf16 v[96:99], a[144:147], v[140:143], v[96:99]\n"
"ds_read_b128 v[224:227], v16 offset:4608\n"
"v_and_b32_e32 v167, 0xffff0000, v153\n"
"v_mfma_f32_16x16x32_bf16 v[100:103], a[148:151], v[140:143], v[100:103]\n"
"ds_read_b128 v[228:231], v16 offset:4672\n"
"v_lshlrev_b32_e32 v168, 16, v154\n"
"v_mfma_f32_16x16x32_bf16 v[104:107], a[152:155], v[140:143], v[104:107]\n"
"ds_read_b128 v[232:235], v16 offset:4864\n"
"v_and_b32_e32 v169, 0xffff0000, v154\n"
"v_mfma_f32_16x16x32_bf16 v[92:95], a[156:159], v[120:123], v[92:95]\n"
"ds_read_b128 v[236:239], v16 offset:4928\n"
"v_lshlrev_b32_e32 v170, 16, v155\n"
"v_mfma_f32_16x16x32_bf16 v[96:99], a[160:163], v[120:123], v[96:99]\n"
"ds_read_b128 v[240:243], v17 offset:4608\n"
"v_and_b32_e32 v171, 0xffff0000, v155\n"
"v_mfma_f32_16x16x32_bf16 v[100:103], a[164:167], v[120:123], v[100:103]\n"
"v_lshlrev_b32_e32 v172, 16, v156\n"
"v_and_b32_e32 v173, 0xffff0000, v156\n"
"v_mfma_f32_16x16x32_bf16 v[104:107], a[168:171], v[120:123], v[104:107]\n"
"v_lshlrev_b32_e32 v174, 16, v157\n"
"v_and_b32_e32 v175, 0xffff0000, v157\n"
"v_mfma_f32_16x16x32_bf16 v[108:111], a[172:175], v[120:123], v[108:111]\n"
"v_lshlrev_b32_e32 v176, 16, v158\n"
"v_and_b32_e32 v177, 0xffff0000, v158\n"
"v_mfma_f32_16x16x32_bf16 v[92:95], a[176:179], v[124:127], v[92:95]\n"
"v_lshlrev_b32_e32 v178, 16, v159\n"
"v_and_b32_e32 v179, 0xffff0000, v159\n"
"v_mfma_f32_16x16x32_bf16 v[96:99], a[180:183], v[124:127], v[96:99]\n"
"v_lshlrev_b32_e32 v180, 16, v160\n"
"v_and_b32_e32 v181, 0xffff0000, v160\n"
"v_mfma_f32_16x16x32_bf16 v[100:103], a[48:51], v[124:127], v[100:103]\n"
"v_lshlrev_b32_e32 v182, 16, v161\n"
"v_and_b32_e32 v183, 0xffff0000, v161\n"
"v_mfma_f32_16x16x32_bf16 v[104:107], a[52:55], v[124:127], v[104:107]\n"
"s_waitcnt lgkmcnt(0)\n"
"s_nop 3\n"
"v_pk_fma_f32 v[108:109], v[108:109], v[200:201], v[180:181]\n"
"v_pk_fma_f32 v[110:111], v[110:111], v[202:203], v[182:183]\n"
"v_pk_fma_f32 v[92:93], v[92:93], v[184:185], v[164:165]\n"
"v_pk_fma_f32 v[94:95], v[94:95], v[186:187], v[166:167]\n"
"v_pk_fma_f32 v[96:97], v[96:97], v[188:189], v[168:169]\n"
"v_pk_fma_f32 v[98:99], v[98:99], v[190:191], v[170:171]\n"
"v_pk_fma_f32 v[100:101], v[100:101], v[192:193], v[172:173]\n"
"v_pk_fma_f32 v[102:103], v[102:103], v[194:195], v[174:175]\n"
"v_pk_fma_f32 v[104:105], v[104:105], v[196:197], v[176:177]\n"
"v_pk_fma_f32 v[106:107], v[106:107], v[198:199], v[178:179]\n"
"v_pk_mul_f32 v[130:131], v[108:109], v[108:109]\n"
"v_pk_fma_f32 v[130:131], v[110:111], v[110:111], v[130:131]\n"
"v_pk_add_f32 v[128:129], v[108:109], v[110:111]\n"
"v_pk_fma_f32 v[130:131], v[92:93], v[92:93], v[130:131]\n"
"v_pk_add_f32 v[128:129], v[128:129], v[92:93]\n"
"v_pk_fma_f32 v[130:131], v[94:95], v[94:95], v[130:131]\n"
"v_pk_add_f32 v[128:129], v[128:129], v[94:95]\n"
"v_pk_fma_f32 v[130:131], v[96:97], v[96:97], v[130:131]\n"
"v_pk_add_f32 v[128:129], v[128:129], v[96:97]\n"
"v_pk_fma_f32 v[130:131], v[98:99], v[98:99], v[130:131]\n"
"v_pk_add_f32 v[128:129], v[128:129], v[98:99]\n"
"v_pk_fma_f32 v[130:131], v[100:101], v[100:101], v[130:131]\n"
"v_pk_add_f32 v[128:129], v[128:129], v[100:101]\n"
"v_pk_fma_f32 v[130:131], v[102:103], v[102:103], v[130:131]\n"
"v_pk_add_f32 v[128:129], v[128:129], v[102:103]\n"
"v_pk_fma_f32 v[130:131], v[104:105], v[104:105], v[130:131]\n"
"v_pk_add_f32 v[128:129], v[128:129], v[104:105]\n"
"v_pk_fma_f32 v[130:131], v[106:107], v[106:107], v[130:131]\n"
"v_pk_add_f32 v[128:129], v[128:129], v[106:107]\n"
"v_add_f32_e32 v130, v130, v131\n"
"v_add_f32_e32 v128, v128, v129\n"
"v_cvt_pk_bf16_f32 v152, v92, v93\n"
"v_cvt_pk_bf16_f32 v153, v94, v95\n"
"v_permlane32_swap_b32_e32 v128, v130\n"
"v_cvt_pk_bf16_f32 v154, v96, v97\n"
"v_add_f32_e32 v128, v128, v130\n"
"v_cvt_pk_bf16_f32 v155, v98, v99\n"
"v_cvt_pk_bf16_f32 v156, v100, v101\n"
"v_add_f32_dpp v128, v128, v128 row_ror:8 row_mask:0xf bank_mask:0xf\n"
"v_mov_b32_e32 v130, v128\n"
"v_cvt_pk_bf16_f32 v157, v102, v103\n"
"v_cvt_pk_bf16_f32 v158, v104, v105\n"
"v_permlane16_swap_b32_e32 v128, v130\n"
"v_cvt_pk_bf16_f32 v159, v106, v107\n"
"v_add_f32_e32 v128, v128, v130\n"
"ds_write_b32 v20, v128\n"
"v_cvt_pk_bf16_f32 v160, v108, v109\n"
"v_cvt_pk_bf16_f32 v161, v110, v111\n"
"s_add_u32 s88, s65, 0x0\n"
"s_add_u32 s89, s65, 0x2000\n"
"s_add_u32 s90, s66, 0x10000\n"
"s_waitcnt lgkmcnt(0)\n"
"s_barrier\n"
"ds_read_b128 v[112:115], v21 offset:0\n"
"ds_read_b128 v[116:119], v21 offset:16\n"
"s_add_u32 s88, s88, 0x240000\n"
".LK3_pad7:\n"
"s_cbranch_execz .LK1_ret\n"
"s_add_u32 s89, s89, 0x240000\n"
"s_add_u32 s90, s90, 0x240000\n"
"s_waitcnt lgkmcnt(0)\n"
"v_pk_add_f32 v[112:113], v[112:113], v[114:115]\n"
"v_pk_add_f32 v[116:117], v[116:117], v[118:119]\n"
"v_add_f32_e32 v112, v112, v113\n"
"v_add_f32_e32 v116, v116, v117\n"
"v_mul_f32_e32 v112, 0x3ae38e39, v112\n"
"v_mul_f32_e32 v116, 0x3ae38e39, v116\n"
"v_fma_f32 v116, -v112, v112, v116\n"
"v_max_f32_e32 v116, 0, v116\n"
"v_add_f32_e32 v116, 0x3727c5ac, v116\n"
"v_rsq_f32_e32 v116, v116\n"
"s_nop 0\n"
"v_mul_f32_e64 v117, -v112, v116\n"
"v_pk_fma_f32 v[92:93], v[92:93], v[116:117], v[116:117] op_sel:[0,0,1] op_sel_hi:[1,0,1]\n"
"v_pk_fma_f32 v[94:95], v[94:95], v[116:117], v[116:117] op_sel:[0,0,1] op_sel_hi:[1,0,1]\n"
"v_pk_fma_f32 v[96:97], v[96:97], v[116:117], v[116:117] op_sel:[0,0,1] op_sel_hi:[1,0,1]\n"
"v_pk_fma_f32 v[98:99], v[98:99], v[116:117], v[116:117] op_sel:[0,0,1] op_sel_hi:[1,0,1]\n"
"v_pk_fma_f32 v[92:93], v[92:93], v[224:225], v[204:205]\n"
"v_pk_fma_f32 v[94:95], v[94:95], v[226:227], v[206:207]\n"
"v_pk_fma_f32 v[96:97], v[96:97], v[228:229], v[208:209]\n"
"v_pk_fma_f32 v[98:99], v[98:99], v[230:231], v[210:211]\n"
"v_cvt_pk_bf16_f32 v164, v92, v93\n"
"v_cvt_pk_bf16_f32 v165, v94, v95\n"
"v_cvt_pk_bf16_f32 v166, v96, v97\n"
"v_cvt_pk_bf16_f32 v167, v98, v99\n"
"buffer_store_dwordx4 v[164:167], v18, s[48:51], s88 offen sc1\n"
"v_pk_fma_f32 v[100:101], v[100:101], v[116:117], v[116:117] op_sel:[0,0,1] op_sel_hi:[1,0,1]\n"
"v_pk_fma_f32 v[102:103], v[102:103], v[116:117], v[116:117] op_sel:[0,0,1] op_sel_hi:[1,0,1]\n"
"v_pk_fma_f32 v[104:105], v[104:105], v[116:117], v[116:117] op_sel:[0,0,1] op_sel_hi:[1,0,1]\n"
"v_pk_fma_f32 v[106:107], v[106:107], v[116:117], v[116:117] op_sel:[0,0,1] op_sel_hi:[1,0,1]\n"
"v_pk_fma_f32 v[100:101], v[100:101], v[232:233], v[212:213]\n"
"v_pk_fma_f32 v[102:103], v[102:103], v[234:235], v[214:215]\n"
"v_pk_fma_f32 v[104:105], v[104:105], v[236:237], v[216:217]\n"
"v_pk_fma_f32 v[106:107], v[106:107], v[238:239], v[218:219]\n"
"v_cvt_pk_bf16_f32 v168, v100, v101\n"
"v_cvt_pk_bf16_f32 v169, v102, v103\n"
"v_cvt_pk_bf16_f32 v170, v104, v105\n"
"v_cvt_pk_bf16_f32 v171, v106, v107\n"
"buffer_store_dwordx4 v[168:171], v18, s[48:51], s89 offen sc1\n"
"v_pk_fma_f32 v[108:109], v[108:109], v[116:117], v[116:117] op_sel:[0,0,1] op_sel_hi:[1,0,1]\n"
"v_pk_fma_f32 v[110:111], v[110:111], v[116:117], v[116:117] op_sel:[0,0,1] op_sel_hi:[1,0,1]\n"
"v_pk_fma_f32 v[108:109], v[108:109], v[240:241], v[220:221]\n"
"v_pk_fma_f32 v[110:111], v[110:111], v[242:243], v[222:223]\n"
"v_cvt_pk_bf16_f32 v172, v108, v109\n"
"v_cvt_pk_bf16_f32 v173, v110, v111\n"
"buffer_store_dwordx2 v[172:173], v19, s[48:51], s90 offen sc1\n"
"s_add_u32 s88, s65, 0x0\n"
"s_add_u32 s89, s65, 0x2000\n"
"s_add_u32 s90, s66, 0x10000\n"
"buffer_store_dwordx4 v[152:155], v18, s[48:51], s88 offen sc1\n"
"buffer_store_dwordx4 v[156:159], v18, s[48:51], s89 offen sc1\n"
"buffer_store_dwordx2 v[160:161], v19, s[48:51], s90 offen sc1\n"
".LKF_pad0:\n"
"s_cbranch_execz .LKF_pad4\n"
"s_load_dwordx2 s[44:45], s[0:1], 0x20\n"
"s_load_dwordx2 s[52:53], s[0:1], 0x28\n"
"s_add_u32 s89, s15, 0x0\n"
"s_add_u32 s18, s15, 0x1000\n"
"s_mov_b32 s20, s80\n"
"v_lshrrev_b32_e32 v24, 4, v0\n"
"s_bfe_u32 s21, s2, 0x20000\n"
"s_bfe_u32 s22, s2, 0x10002\n"
"s_bfe_u32 s23, s2, 0x20003\n"
"s_bfe_u32 s24, s2, 0x30005\n"
"s_bfe_u32 s25, s20, 0x10000\n"
"v_lshrrev_b32_e32 v25, 1, v24\n"
"v_and_b32_e32 v25, 24, v25\n"
"v_bfe_u32 v26, v24, 3, 1\n"
"v_or_b32_e32 v27, v25, v26\n"
"v_and_b32_e32 v25, 0x70, v0\n"
"s_lshl4_add_u32 s35, s22, s20\n"
"s_lshl2_add_u32 s35, s23, s35\n"
"s_lshl_b32 s81, s20, 11\n"
"s_mov_b64 s[40:41], s[76:77]\n"
"s_mov_b64 s[42:43], s[78:79]\n"
"s_lshl_b32 s90, s2, 6\n"
"s_and_b32 s90, s90, 64\n"
"v_xor_b32_e32 v25, s90, v25\n"
"v_mul_u32_u24_e32 v3, 0x480, v27\n"
"v_add_u32_e32 v3, v3, v25\n"
"v_add_u32_e32 v4, 0x4e0, v3\n"
"v_and_b32_e32 v18, 16, v24\n"
"v_or_b32_e32 v18, v18, v26\n"
"v_lshrrev_b32_e32 v26, 5, v24\n"
"v_lshl_add_u32 v18, v26, 2, v18\n"
"v_mul_u32_u24_e32 v18, 0x480, v18\n"
"v_add_u32_e32 v18, v18, v25\n"
"v_bfe_u32 v25, v24, 3, 2\n"
"v_mul_u32_u24_e32 v5, 0x420, v25\n"
"v_lshl_add_u32 v5, v26, 8, v5\n"
"v_bfe_u32 v25, v24, 2, 1\n"
"v_lshl_add_u32 v5, v25, 7, v5\n"
"v_and_b32_e32 v25, 3, v24\n"
"v_lshl_add_u32 v5, v25, 3, v5\n"
"v_lshlrev_b32_e32 v11, 8, v25\n"
"v_mov_b32_e32 v8, v0\n"
"v_add_u32_e32 v6, 0xe700, v5\n"
"v_add_u32_e32 v9, 0xe700, v0\n"
"v_add_u32_e32 v7, 0x1ce00, v5\n"
"v_add_u32_e32 v10, 0x1ce00, v0\n"
"v_lshlrev_b32_e32 v12, 2, v24\n"
"v_and_b32_e32 v14, 48, v24\n"
"v_and_b32_e32 v25, 15, v24\n"
"v_mul_u32_u24_e32 v13, 0x900, v25\n"
"v_add_u32_e32 v13, v13, v14\n"
"v_lshrrev_b32_e32 v13, 1, v13\n"
"v_add_u32_e32 v16, s81, v0\n"
"v_xor_b32_e32 v17, 0x1000, v16\n"
"v_lshlrev_b32_e32 v19, 4, v25\n"
"v_lshl_add_u32 v19, v26, 8, v19\n"
"v_and_b32_e32 v26, 16, v24\n"
"v_lshl_add_u32 v19, v26, 6, v19\n"
"v_lshrrev_b32_e32 v26, 3, v24\n"
"v_lshlrev_b32_e32 v15, 7, v24\n"
"v_lshl_add_u32 v15, v26, 7, v15\n"
"v_mov_b32_e32 v60, 0\n"
"v_mov_b32_e32 v61, 0\n"
"v_mov_b32_e32 v62, 0\n"
"v_mov_b32_e32 v63, 0\n"
"s_mov_b32 exec_lo, 0x100001\n"
"s_mov_b32 exec_hi, 0x10000100\n"
"v_mov_b32_e32 v60, 0x3f80\n"
"s_mov_b32 exec_lo, 0x200002\n"
"s_mov_b32 exec_hi, 0x20000200\n"
"v_mov_b32_e32 v60, 0x3f800000\n"
"s_mov_b32 exec_lo, 0x400004\n"
"s_mov_b32 exec_hi, 0x40000400\n"
"v_mov_b32_e32 v61, 0x3f80\n"
"s_mov_b32 exec_lo, 0x800008\n"
"s_mov_b32 exec_hi, 0x80000800\n"
"v_mov_b32_e32 v61, 0x3f800000\n"
"s_mov_b64 exec, -1\n"
"s_waitcnt lgkmcnt(0)\n"
"s_mov_b64 s[46:47], s[42:43]\n"
"s_mov_b32 s54, 0x240000\n"
"s_mov_b32 s55, 0x20000\n"
"s_bfe_u32 s26, s20, 0x10001\n"
"s_bfe_u32 s27, s2, 0x10001\n"
"s_bfe_u32 s28, s2, 0x30003\n"
"s_bfe_u32 s29, s2, 0x10007\n"
"s_bfe_u32 s30, s2, 0x20005\n"
"s_bfe_u32 s31, s2, 0x10006\n"
"s_bfe_u32 s32, s2, 0x10005\n"
"s_bfe_u32 s33, s2, 0x20006\n"
"s_bfe_u32 s34, s2, 0x50003\n"
"s_mul_i32 s90, s21, 0x48000\n"
"s_add_u32 s59, s81, s90\n"
"s_mul_i32 s90, s24, 0x9000\n"
"s_add_u32 s59, s59, s90\n"
"s_add_u32 s59, s59, 0x9ec400\n"
"s_lshl_b32 s69, s35, 13\n"
"s_lshl_b32 s90, s21, 11\n"
"s_add_u32 s69, s69, s90\n"
"s_lshl_b32 s90, s24, 8\n"
"s_add_u32 s69, s69, s90\n"
"s_add_u32 s69, s69, 0x25a0000\n"
"s_mul_i32 s35, s35, 0x90000\n"
"s_mul_i32 s90, s21, 0x24000\n"
"s_add_u32 s35, s35, s90\n"
"s_mul_i32 s90, s29, 0x12800\n"
"s_add_u32 s35, s35, s90\n"
"s_add_u32 s35, s35, 0xb9c400\n"
"s_lshl_b32 s90, s30, 12\n"
"s_add_u32 s65, s35, s90\n"
"s_lshl_b32 s90, s31, 12\n"
"s_add_u32 s67, s35, s90\n"
"s_lshl_b32 s90, s32, 9\n"
"s_add_u32 s67, s67, s90\n"
"s_add_u32 s67, s67, 0x8000\n"
"s_mul_i32 s90, s29, 0xfffff000\n"
"s_add_u32 s66, s65, s90\n"
"s_add_u32 s66, s66, 0x800\n"
"s_mul_i32 s90, s29, 0xfffff000\n"
"s_add_u32 s68, s67, s90\n"
"s_add_u32 s68, s68, 0x800\n"
"s_lshl_b32 s36, s33, 8\n"
"s_lshl_b32 s90, s27, 7\n"
"s_add_u32 s36, s36, s90\n"
"s_mul_i32 s90, s21, 0x120000\n"
"s_add_u32 s60, s36, s90\n"
"s_mul_i32 s90, s26, 0x90000\n"
"s_add_u32 s60, s60, s90\n"
"s_mul_i32 s90, s25, 0x1200\n"
"s_add_u32 s60, s60, s90\n"
"s_mul_i32 s90, s21, 0x24000\n"
"s_add_u32 s62, s36, s90\n"
"s_mul_i32 s90, s26, 0x12000\n"
"s_add_u32 s62, s62, s90\n"
"s_mul_i32 s90, s25, 0x2400\n"
"s_add_u32 s62, s62, s90\n"
"s_add_u32 s62, s62, 0x480000\n"
"s_mul_i32 s90, s27, 0xffffff00\n"
"s_add_u32 s61, s60, s90\n"
"s_add_u32 s61, s61, 0x80\n"
"s_mul_i32 s90, s27, 0xffffff00\n"
"s_add_u32 s63, s62, s90\n"
"s_add_u32 s63, s63, 0x80\n"
"s_lshl2_add_u32 s36, s33, s21\n"
"s_mul_i32 s86, s36, 0x9000\n"
"s_add_u32 s86, s86, 0xb0c400\n"
"s_add_u32 s64, s81, s86\n"
"s_mul_i32 s90, s28, 0x1200\n"
"s_add_u32 s86, s86, s90\n"
"s_mul_i32 s90, s20, 0x480\n"
"s_add_u32 s86, s86, s90\n"
"s_lshl4_add_u32 s39, s22, s25\n"
"s_lshl1_add_u32 s39, s28, s39\n"
"s_lshl_b32 s70, s39, 13\n"
"s_lshl_b32 s90, s21, 11\n"
"s_add_u32 s70, s70, s90\n"
"s_lshl_b32 s90, s26, 10\n"
"s_add_u32 s70, s70, s90\n"
"s_add_u32 s70, s70, 0x25a0000\n"
"s_mul_i32 s71, s39, 0x90000\n"
"s_mul_i32 s90, s21, 0x24000\n"
"s_add_u32 s71, s71, s90\n"
"s_mul_i32 s90, s26, 0x12000\n"
"s_add_u32 s71, s71, s90\n"
"s_add_u32 s71, s71, 0xb9c400\n"
"s_lshl_b32 s72, s22, 22\n"
"s_lshl_b32 s90, s28, 19\n"
"s_add_u32 s72, s72, s90\n"
"s_lshl_b32 s90, s33, 17\n"
"s_add_u32 s72, s72, s90\n"
"s_lshl_b32 s90, s20, 15\n"
"s_add_u32 s72, s72, s90\n"
"s_lshl_b32 s90, s21, 13\n"
"s_add_u32 s72, s72, s90\n"
"s_add_u32 s72, s72, 0x1da0000\n"
"s_mul_i32 s37, s39, 0x12000\n"
"s_lshl_b32 s90, s26, 11\n"
"s_add_u32 s37, s37, s90\n"
"s_add_u32 s37, s37, 0x56c400\n"
"s_lshl_b32 s90, s36, 12\n"
"s_add_u32 s73, s37, s90\n"
"s_lshl_b32 s90, s29, 12\n"
"s_add_u32 s74, s37, s90\n"
"s_lshl3_add_u32 s74, s31, s74\n"
"s_add_u32 s74, s74, 0x10000\n"
"s_lshl_b32 s75, s36, 7\n"
"s_add_u32 s75, s75, 0x2d00\n"
"s_lshl_b32 s76, s33, 6\n"
"s_add_u32 s76, s76, 0x3500\n"
"s_mul_i32 s38, s39, 0x12000\n"
"s_mul_i32 s90, s26, 0x9000\n"
"s_add_u32 s38, s38, s90\n"
"s_lshl_b32 s90, s36, 6\n"
"s_add_u32 s77, s38, s90\n"
"s_lshl_b32 s90, s33, 5\n"
"s_add_u32 s78, s38, s90\n"
"s_add_u32 s78, s78, 0x400\n"
"s_mul_i32 s79, s26, 0x1080\n"
"s_lshl_b32 s80, s26, 10\n"
"s_lshl_b32 s90, s21, 2\n"
"s_lshl_b32 s82, 0xf000f, s90\n"
"s_lshl2_add_u32 s34, s34, s20\n"
"s_mul_i32 s84, s21, 0x120000\n"
"s_mul_i32 s90, s34, 0x2400\n"
"s_add_u32 s84, s84, s90\n"
"s_mul_i32 s85, s21, 0x24000\n"
"s_mul_i32 s90, s34, 0x480\n"
"s_add_u32 s85, s85, s90\n"
"s_add_u32 s85, s85, 0x480000\n"
"s_mov_b32 s94, 0x40053526\n"
"buffer_load_dwordx4 v[224:227], v0, s[48:51], s89 offen offset:0 sc1\n"
"buffer_load_dwordx4 v[228:231], v0, s[48:51], s89 offen offset:1024 sc1\n"
"buffer_load_dwordx4 v[232:235], v0, s[48:51], s89 offen offset:2048 sc1\n"
"buffer_load_dwordx4 v[236:239], v0, s[48:51], s89 offen offset:3072 sc1\n"
"buffer_load_dwordx4 v[240:243], v0, s[48:51], s18 offen offset:0 sc1\n"
"buffer_load_dwordx4 v[244:247], v0, s[48:51], s18 offen offset:1024 sc1\n"
"buffer_load_dwordx4 v[248:251], v0, s[48:51], s18 offen offset:2048 sc1\n"
"buffer_load_dwordx4 v[252:255], v0, s[48:51], s18 offen offset:3072 sc1\n"
"s_waitcnt vmcnt(14)\n"
"s_barrier\n"
"ds_read_b64_tr_b16 v[24:25], v6 offset:0\n"
"ds_read_b64_tr_b16 v[26:27], v6 offset:512\n"
"ds_read_b64_tr_b16 v[28:29], v6 offset:4224\n"
"ds_read_b64_tr_b16 v[30:31], v6 offset:4736\n"
"ds_read_b64_tr_b16 v[32:33], v6 offset:32\n"
"ds_read_b64_tr_b16 v[34:35], v6 offset:544\n"
"ds_read_b64_tr_b16 v[36:37], v6 offset:4256\n"
"ds_read_b64_tr_b16 v[38:39], v6 offset:4768\n"
"s_mov_b32 s91, 0x100000\n"
"s_add_u32 s89, s15, 0x2000\n"
"buffer_load_dwordx4 a[0:3], v0, s[48:51], s89 offen offset:0 sc1\n"
"buffer_load_dwordx4 a[4:7], v0, s[48:51], s89 offen offset:1024 sc1\n"
"buffer_load_dwordx4 a[8:11], v0, s[48:51], s89 offen offset:2048 sc1\n"
"buffer_load_dwordx4 a[12:15], v0, s[48:51], s89 offen offset:3072 sc1\n"
"s_add_u32 s89, s15, 0x0\n"
"s_waitcnt vmcnt(4)\n"
".LKF_gate_%=:\n"
"v_max3_u32 v44, v224, v225, v226\n"
"v_max3_u32 v45, v228, v229, v230\n"
"v_max3_u32 v46, v232, v233, v234\n"
"v_max3_u32 v47, v236, v237, v238\n"
"v_max3_u32 v44, v44, v227, v240\n"
"v_max3_u32 v45, v45, v231, v244\n"
"v_max3_u32 v46, v46, v235, v248\n"
"v_max3_u32 v47, v47, v239, v252\n"
"v_max3_u32 v44, v44, v241, v242\n"
"v_max3_u32 v45, v45, v245, v246\n"
"v_max3_u32 v46, v46, v249, v250\n"
"v_max3_u32 v47, v47, v253, v254\n"
"v_max_u32_e32 v44, v44, v243\n"
"v_max_u32_e32 v45, v45, v247\n"
"v_max_u32_e32 v46, v46, v251\n"
"v_max_u32_e32 v47, v47, v255\n"
"v_max3_u32 v22, v44, v45, v46\n"
"v_max_u32_e32 v22, v22, v47\n"
"v_cmp_eq_u32_e32 vcc, -1, v22\n"
"s_cbranch_vccz .LKF_open_%=\n"
"v_cmp_eq_u32_e32 vcc, -1, v44\n"
"s_cbranch_vccz .LKF_g0_%=\n"
"buffer_load_dwordx4 v[224:227], v0, s[48:51], s89 offen offset:0 sc1\n"
"buffer_load_dwordx4 v[240:243], v0, s[48:51], s18 offen offset:0 sc1\n"
"s_add_u32 s90, s15, 0x2000\n"
"buffer_load_dwordx4 a[0:3], v0, s[48:51], s90 offen offset:0 sc1\n"
".LKF_g0_%=:\n"
"v_cmp_eq_u32_e32 vcc, -1, v45\n"
"s_cbranch_vccz .LKF_g1_%=\n"
"buffer_load_dwordx4 v[228:231], v0, s[48:51], s89 offen offset:1024 sc1\n"
"buffer_load_dwordx4 v[244:247], v0, s[48:51], s18 offen offset:1024 sc1\n"
"s_add_u32 s90, s15, 0x2000\n"
"buffer_load_dwordx4 a[4:7], v0, s[48:51], s90 offen offset:1024 sc1\n"
".LKF_g1_%=:\n"
"v_cmp_eq_u32_e32 vcc, -1, v46\n"
"s_cbranch_vccz .LKF_g2_%=\n"
"buffer_load_dwordx4 v[232:235], v0, s[48:51], s89 offen offset:2048 sc1\n"
"buffer_load_dwordx4 v[248:251], v0, s[48:51], s18 offen offset:2048 sc1\n"
"s_add_u32 s90, s15, 0x2000\n"
"buffer_load_dwordx4 a[8:11], v0, s[48:51], s90 offen offset:2048 sc1\n"
".LKF_g2_%=:\n"
"v_cmp_eq_u32_e32 vcc, -1, v47\n"
"s_cbranch_vccz .LKF_g3_%=\n"
"buffer_load_dwordx4 v[236:239], v0, s[48:51], s89 offen offset:3072 sc1\n"
"buffer_load_dwordx4 v[252:255], v0, s[48:51], s18 offen offset:3072 sc1\n"
"s_add_u32 s90, s15, 0x2000\n"
"buffer_load_dwordx4 a[12:15], v0, s[48:51], s90 offen offset:3072 sc1\n"
".LKF_g3_%=:\n"
"s_waitcnt vmcnt(0)\n"
"s_sleep 4\n"
"s_sub_u32 s91, s91, 1\n"
"s_cbranch_scc0 .LKF_gate_%=\n"
".LKF_open_%=:\n"
"s_add_u32 s89, s15, 0x3000\n"
"buffer_load_dwordx4 a[16:19], v0, s[48:51], s89 offen offset:0 sc1\n"
"buffer_load_dwordx4 a[20:23], v0, s[48:51], s89 offen offset:1024 sc1\n"
"buffer_load_dwordx4 a[24:27], v0, s[48:51], s89 offen offset:2048 sc1\n"
"buffer_load_dwordx4 a[28:31], v0, s[48:51], s89 offen offset:3072 sc1\n"
"s_add_u32 s89, s15, 0x4000\n"
"buffer_load_dwordx4 a[32:35], v0, s[48:51], s89 offen offset:0 sc1\n"
"buffer_load_dwordx4 a[36:39], v0, s[48:51], s89 offen offset:1024 sc1\n"
"buffer_load_dwordx4 a[40:43], v0, s[48:51], s89 offen offset:2048 sc1\n"
"buffer_load_dwordx4 a[44:47], v0, s[48:51], s89 offen offset:3072 sc1\n"
"s_waitcnt lgkmcnt(4)\n"
"v_mfma_f32_16x16x32_bf16 v[64:67], v[24:27], v[224:227], 0\n"
"ds_read_b64_tr_b16 v[40:41], v6 offset:64\n"
"ds_read_b64_tr_b16 v[42:43], v6 offset:576\n"
"v_mfma_f32_16x16x32_bf16 v[68:71], v[24:27], v[228:231], 0\n"
"s_add_u32 s89, s15, 0x5000\n"
"buffer_load_dwordx4 a[48:51], v0, s[48:51], s89 offen offset:0 sc1\n"
"v_mfma_f32_16x16x32_bf16 v[72:75], v[24:27], v[232:235], 0\n"
"v_mfma_f32_16x16x32_bf16 v[76:79], v[24:27], v[236:239], 0\n"
"v_mfma_f32_16x16x32_bf16 v[80:83], v[28:31], v[224:227], 0\n"
"ds_read_b64_tr_b16 v[24:25], v6 offset:4288\n"
"ds_read_b64_tr_b16 v[26:27], v6 offset:4800\n"
"v_mfma_f32_16x16x32_bf16 v[84:87], v[28:31], v[228:231], 0\n"
"buffer_load_dwordx4 a[52:55], v0, s[48:51], s89 offen offset:1024 sc1\n"
"v_mfma_f32_16x16x32_bf16 v[88:91], v[28:31], v[232:235], 0\n"
"v_mfma_f32_16x16x32_bf16 v[92:95], v[28:31], v[236:239], 0\n"
"s_waitcnt lgkmcnt(4)\n"
"v_mfma_f32_16x16x32_bf16 v[96:99], v[32:35], v[224:227], 0\n"
"ds_read_b64_tr_b16 v[28:29], v6 offset:96\n"
"ds_read_b64_tr_b16 v[30:31], v6 offset:608\n"
"v_mfma_f32_16x16x32_bf16 v[100:103], v[32:35], v[228:231], 0\n"
"buffer_load_dwordx4 a[56:59], v0, s[48:51], s89 offen offset:2048 sc1\n"
"v_mfma_f32_16x16x32_bf16 v[104:107], v[32:35], v[232:235], 0\n"
"v_mfma_f32_16x16x32_bf16 v[108:111], v[32:35], v[236:239], 0\n"
"v_mfma_f32_16x16x32_bf16 v[112:115], v[36:39], v[224:227], 0\n"
"ds_read_b64_tr_b16 v[32:33], v6 offset:4320\n"
"ds_read_b64_tr_b16 v[34:35], v6 offset:4832\n"
"v_mfma_f32_16x16x32_bf16 v[116:119], v[36:39], v[228:231], 0\n"
"buffer_load_dwordx4 a[60:63], v0, s[48:51], s89 offen offset:3072 sc1\n"
"v_mfma_f32_16x16x32_bf16 v[120:123], v[36:39], v[232:235], 0\n"
"v_mfma_f32_16x16x32_bf16 v[124:127], v[36:39], v[236:239], 0\n"
"s_waitcnt lgkmcnt(4)\n"
"v_mfma_f32_16x16x32_bf16 v[128:131], v[40:43], v[224:227], 0\n"
"ds_read_b64_tr_b16 v[36:37], v6 offset:8448\n"
"ds_read_b64_tr_b16 v[38:39], v6 offset:8960\n"
"v_mfma_f32_16x16x32_bf16 v[132:135], v[40:43], v[228:231], 0\n"
"s_add_u32 m0, s17, 0x1ad00\n"
"s_add_u32 s88, s16, 0x1b0000\n"
"v_mfma_f32_16x16x32_bf16 v[136:139], v[40:43], v[232:235], 0\n"
"buffer_load_dwordx4 v1, s[40:43], s88 offen lds\n"
"v_mfma_f32_16x16x32_bf16 v[140:143], v[40:43], v[236:239], 0\n"
"v_mfma_f32_16x16x32_bf16 v[144:147], v[24:27], v[224:227], 0\n"
"ds_read_b64_tr_b16 v[40:41], v6 offset:12672\n"
"ds_read_b64_tr_b16 v[42:43], v6 offset:13184\n"
"v_mfma_f32_16x16x32_bf16 v[148:151], v[24:27], v[228:231], 0\n"
"buffer_load_dwordx4 v2, s[40:43], s88 offen offset:1056 lds\n"
"v_mfma_f32_16x16x32_bf16 v[152:155], v[24:27], v[232:235], 0\n"
"v_mfma_f32_16x16x32_bf16 v[156:159], v[24:27], v[236:239], 0\n"
"s_waitcnt lgkmcnt(4)\n"
"v_mfma_f32_16x16x32_bf16 v[160:163], v[28:31], v[224:227], 0\n"
"ds_read_b64_tr_b16 v[24:25], v6 offset:8480\n"
"ds_read_b64_tr_b16 v[26:27], v6 offset:8992\n"
"v_mfma_f32_16x16x32_bf16 v[164:167], v[28:31], v[228:231], 0\n"
"s_add_u32 m0, s17, 0x1ce00\n"
"s_add_u32 s88, s16, 0x1f8000\n"
"v_mfma_f32_16x16x32_bf16 v[168:171], v[28:31], v[232:235], 0\n"
"buffer_load_dwordx4 v1, s[40:43], s88 offen lds\n"
"v_mfma_f32_16x16x32_bf16 v[172:175], v[28:31], v[236:239], 0\n"
"v_mfma_f32_16x16x32_bf16 v[176:179], v[32:35], v[224:227], 0\n"
"ds_read_b64_tr_b16 v[28:29], v6 offset:12704\n"
"ds_read_b64_tr_b16 v[30:31], v6 offset:13216\n"
"v_mfma_f32_16x16x32_bf16 v[180:183], v[32:35], v[228:231], 0\n"
"buffer_load_dwordx4 v2, s[40:43], s88 offen offset:1056 lds\n"
"v_mfma_f32_16x16x32_bf16 v[184:187], v[32:35], v[232:235], 0\n"
"v_mfma_f32_16x16x32_bf16 v[188:191], v[32:35], v[236:239], 0\n"
"s_waitcnt lgkmcnt(4)\n"
"v_mfma_f32_16x16x32_bf16 v[64:67], v[36:39], v[240:243], v[64:67]\n"
"ds_read_b64_tr_b16 v[32:33], v6 offset:8512\n"
"ds_read_b64_tr_b16 v[34:35], v6 offset:9024\n"
"v_mfma_f32_16x16x32_bf16 v[68:71], v[36:39], v[244:247], v[68:71]\n"
"s_add_u32 s89, s15, 0x6000\n"
"buffer_load_dwordx4 a[64:67], v0, s[48:51], s89 offen offset:0 sc1\n"
"v_mfma_f32_16x16x32_bf16 v[72:75], v[36:39], v[248:251], v[72:75]\n"
"v_mfma_f32_16x16x32_bf16 v[76:79], v[36:39], v[252:255], v[76:79]\n"
"v_mfma_f32_16x16x32_bf16 v[80:83], v[40:43], v[240:243], v[80:83]\n"
"ds_read_b64_tr_b16 v[36:37], v6 offset:12736\n"
"ds_read_b64_tr_b16 v[38:39], v6 offset:13248\n"
"v_mfma_f32_16x16x32_bf16 v[84:87], v[40:43], v[244:247], v[84:87]\n"
"buffer_load_dwordx4 a[68:71], v0, s[48:51], s89 offen offset:1024 sc1\n"
"v_mfma_f32_16x16x32_bf16 v[88:91], v[40:43], v[248:251], v[88:91]\n"
"v_mfma_f32_16x16x32_bf16 v[92:95], v[40:43], v[252:255], v[92:95]\n"
"s_waitcnt lgkmcnt(4)\n"
"v_mfma_f32_16x16x32_bf16 v[96:99], v[24:27], v[240:243], v[96:99]\n"
"ds_read_b64_tr_b16 v[40:41], v6 offset:8544\n"
"ds_read_b64_tr_b16 v[42:43], v6 offset:9056\n"
"v_mfma_f32_16x16x32_bf16 v[100:103], v[24:27], v[244:247], v[100:103]\n"
"buffer_load_dwordx4 a[72:75], v0, s[48:51], s89 offen offset:2048 sc1\n"
"v_mfma_f32_16x16x32_bf16 v[104:107], v[24:27], v[248:251], v[104:107]\n"
"v_mfma_f32_16x16x32_bf16 v[108:111], v[24:27], v[252:255], v[108:111]\n"
"v_mfma_f32_16x16x32_bf16 v[112:115], v[28:31], v[240:243], v[112:115]\n"
"ds_read_b64_tr_b16 v[24:25], v6 offset:12768\n"
"ds_read_b64_tr_b16 v[26:27], v6 offset:13280\n"
"v_mfma_f32_16x16x32_bf16 v[116:119], v[28:31], v[244:247], v[116:119]\n"
"buffer_load_dwordx4 a[76:79], v0, s[48:51], s89 offen offset:3072 sc1\n"
"v_mfma_f32_16x16x32_bf16 v[120:123], v[28:31], v[248:251], v[120:123]\n"
"v_mfma_f32_16x16x32_bf16 v[124:127], v[28:31], v[252:255], v[124:127]\n"
"s_waitcnt lgkmcnt(4)\n"
"v_mfma_f32_16x16x32_bf16 v[128:131], v[32:35], v[240:243], v[128:131]\n"
"ds_read_b64_tr_b16 v[28:29], v6 offset:16896\n"
"ds_read_b64_tr_b16 v[30:31], v6 offset:17408\n"
"v_mfma_f32_16x16x32_bf16 v[132:135], v[32:35], v[244:247], v[132:135]\n"
"s_add_u32 s89, s15, 0x7000\n"
"buffer_load_dwordx4 a[80:83], v0, s[48:51], s89 offen offset:0 sc1\n"
"v_mfma_f32_16x16x32_bf16 v[136:139], v[32:35], v[248:251], v[136:139]\n"
"v_mfma_f32_16x16x32_bf16 v[140:143], v[32:35], v[252:255], v[140:143]\n"
"v_mfma_f32_16x16x32_bf16 v[144:147], v[36:39], v[240:243], v[144:147]\n"
"ds_read_b64_tr_b16 v[32:33], v6 offset:21120\n"
"ds_read_b64_tr_b16 v[34:35], v6 offset:21632\n"
"v_mfma_f32_16x16x32_bf16 v[148:151], v[36:39], v[244:247], v[148:151]\n"
"buffer_load_dwordx4 a[84:87], v0, s[48:51], s89 offen offset:1024 sc1\n"
"v_mfma_f32_16x16x32_bf16 v[152:155], v[36:39], v[248:251], v[152:155]\n"
"v_mfma_f32_16x16x32_bf16 v[156:159], v[36:39], v[252:255], v[156:159]\n"
"s_waitcnt lgkmcnt(4)\n"
"v_mfma_f32_16x16x32_bf16 v[160:163], v[40:43], v[240:243], v[160:163]\n"
"ds_read_b64_tr_b16 v[36:37], v6 offset:16928\n"
"ds_read_b64_tr_b16 v[38:39], v6 offset:17440\n"
"v_mfma_f32_16x16x32_bf16 v[164:167], v[40:43], v[244:247], v[164:167]\n"
"s_waitcnt vmcnt(22)\n"
"v_accvgpr_read_b32 v44, a0\n"
"v_mfma_f32_16x16x32_bf16 v[168:171], v[40:43], v[248:251], v[168:171]\n"
"v_accvgpr_read_b32 v45, a4\n"
"v_accvgpr_read_b32 v46, a8\n"
"v_mfma_f32_16x16x32_bf16 v[172:175], v[40:43], v[252:255], v[172:175]\n"
"v_accvgpr_read_b32 v47, a12\n"
"v_max3_u32 v44, v44, v45, v46\n"
"v_mfma_f32_16x16x32_bf16 v[176:179], v[24:27], v[240:243], v[176:179]\n"
"ds_read_b64_tr_b16 v[40:41], v6 offset:21152\n"
"ds_read_b64_tr_b16 v[42:43], v6 offset:21664\n"
"v_mfma_f32_16x16x32_bf16 v[180:183], v[24:27], v[244:247], v[180:183]\n"
"v_max_u32_e32 v44, v44, v47\n"
"v_cmp_eq_u32_e32 vcc, -1, v44\n"
"v_mfma_f32_16x16x32_bf16 v[184:187], v[24:27], v[248:251], v[184:187]\n"
"buffer_load_dwordx4 a[88:91], v0, s[48:51], s89 offen offset:2048 sc1\n"
"v_mfma_f32_16x16x32_bf16 v[188:191], v[24:27], v[252:255], v[188:191]\n"
"s_waitcnt lgkmcnt(4)\n"
"s_cbranch_vccnz .LKF_sH2_%=\n"
".LKF_rH2_%=:\n"
"v_mfma_f32_16x16x32_bf16 v[64:67], v[28:31], a[0:3], v[64:67]\n"
"ds_read_b64_tr_b16 v[24:25], v6 offset:16960\n"
"ds_read_b64_tr_b16 v[26:27], v6 offset:17472\n"
"v_mfma_f32_16x16x32_bf16 v[68:71], v[28:31], a[4:7], v[68:71]\n"
"buffer_load_dwordx4 a[92:95], v0, s[48:51], s89 offen offset:3072 sc1\n"
"v_mfma_f32_16x16x32_bf16 v[72:75], v[28:31], a[8:11], v[72:75]\n"
"v_mfma_f32_16x16x32_bf16 v[76:79], v[28:31], a[12:15], v[76:79]\n"
"v_mfma_f32_16x16x32_bf16 v[80:83], v[32:35], a[0:3], v[80:83]\n"
"ds_read_b64_tr_b16 v[28:29], v6 offset:21184\n"
"ds_read_b64_tr_b16 v[30:31], v6 offset:21696\n"
"v_mfma_f32_16x16x32_bf16 v[84:87], v[32:35], a[4:7], v[84:87]\n"
"s_add_u32 m0, s17, 0x1ef00\n"
"s_add_u32 s88, s16, 0x240000\n"
"v_mfma_f32_16x16x32_bf16 v[88:91], v[32:35], a[8:11], v[88:91]\n"
"buffer_load_dwordx4 v1, s[40:43], s88 offen lds\n"
"v_mfma_f32_16x16x32_bf16 v[92:95], v[32:35], a[12:15], v[92:95]\n"
"s_waitcnt lgkmcnt(4)\n"
"v_mfma_f32_16x16x32_bf16 v[96:99], v[36:39], a[0:3], v[96:99]\n"
"ds_read_b64_tr_b16 v[32:33], v6 offset:16992\n"
"ds_read_b64_tr_b16 v[34:35], v6 offset:17504\n"
"v_mfma_f32_16x16x32_bf16 v[100:103], v[36:39], a[4:7], v[100:103]\n"
"buffer_load_dwordx4 v2, s[40:43], s88 offen offset:1056 lds\n"
"v_mfma_f32_16x16x32_bf16 v[104:107], v[36:39], a[8:11], v[104:107]\n"
"v_mfma_f32_16x16x32_bf16 v[108:111], v[36:39], a[12:15], v[108:111]\n"
"v_mfma_f32_16x16x32_bf16 v[112:115], v[40:43], a[0:3], v[112:115]\n"
"ds_read_b64_tr_b16 v[36:37], v6 offset:21216\n"
"ds_read_b64_tr_b16 v[38:39], v6 offset:21728\n"
"v_mfma_f32_16x16x32_bf16 v[116:119], v[40:43], a[4:7], v[116:119]\n"
"s_add_u32 m0, s17, 0x21000\n"
"s_add_u32 s88, s16, 0x288000\n"
"v_mfma_f32_16x16x32_bf16 v[120:123], v[40:43], a[8:11], v[120:123]\n"
"buffer_load_dwordx4 v1, s[40:43], s88 offen lds\n"
"v_mfma_f32_16x16x32_bf16 v[124:127], v[40:43], a[12:15], v[124:127]\n"
"s_waitcnt lgkmcnt(4)\n"
"v_mfma_f32_16x16x32_bf16 v[128:131], v[24:27], a[0:3], v[128:131]\n"
"ds_read_b64_tr_b16 v[40:41], v6 offset:25344\n"
"ds_read_b64_tr_b16 v[42:43], v6 offset:25856\n"
"v_mfma_f32_16x16x32_bf16 v[132:135], v[24:27], a[4:7], v[132:135]\n"
"buffer_load_dwordx4 v2, s[40:43], s88 offen offset:1056 lds\n"
"v_mfma_f32_16x16x32_bf16 v[136:139], v[24:27], a[8:11], v[136:139]\n"
"v_mfma_f32_16x16x32_bf16 v[140:143], v[24:27], a[12:15], v[140:143]\n"
"v_mfma_f32_16x16x32_bf16 v[144:147], v[28:31], a[0:3], v[144:147]\n"
"ds_read_b64_tr_b16 v[24:25], v6 offset:29568\n"
"ds_read_b64_tr_b16 v[26:27], v6 offset:30080\n"
"v_mfma_f32_16x16x32_bf16 v[148:151], v[28:31], a[4:7], v[148:151]\n"
"s_add_u32 s89, s15, 0x8000\n"
"buffer_load_dwordx4 a[96:99], v0, s[48:51], s89 offen offset:0 sc1\n"
"v_mfma_f32_16x16x32_bf16 v[152:155], v[28:31], a[8:11], v[152:155]\n"
"v_mfma_f32_16x16x32_bf16 v[156:159], v[28:31], a[12:15], v[156:159]\n"
"s_waitcnt lgkmcnt(4)\n"
"v_mfma_f32_16x16x32_bf16 v[160:163], v[32:35], a[0:3], v[160:163]\n"
"ds_read_b64_tr_b16 v[28:29], v6 offset:25376\n"
"ds_read_b64_tr_b16 v[30:31], v6 offset:25888\n"
"v_mfma_f32_16x16x32_bf16 v[164:167], v[32:35], a[4:7], v[164:167]\n"
"s_waitcnt vmcnt(25)\n"
"v_accvgpr_read_b32 v44, a16\n"
"v_mfma_f32_16x16x32_bf16 v[168:171], v[32:35], a[8:11], v[168:171]\n"
"v_accvgpr_read_b32 v45, a20\n"
"v_accvgpr_read_b32 v46, a24\n"
"v_mfma_f32_16x16x32_bf16 v[172:175], v[32:35], a[12:15], v[172:175]\n"
"v_accvgpr_read_b32 v47, a28\n"
"v_max3_u32 v44, v44, v45, v46\n"
"v_mfma_f32_16x16x32_bf16 v[176:179], v[36:39], a[0:3], v[176:179]\n"
"ds_read_b64_tr_b16 v[32:33], v6 offset:29600\n"
"ds_read_b64_tr_b16 v[34:35], v6 offset:30112\n"
"v_mfma_f32_16x16x32_bf16 v[180:183], v[36:39], a[4:7], v[180:183]\n"
"v_max_u32_e32 v44, v44, v47\n"
"v_cmp_eq_u32_e32 vcc, -1, v44\n"
"v_mfma_f32_16x16x32_bf16 v[184:187], v[36:39], a[8:11], v[184:187]\n"
"buffer_load_dwordx4 a[100:103], v0, s[48:51], s89 offen offset:1024 sc1\n"
"v_mfma_f32_16x16x32_bf16 v[188:191], v[36:39], a[12:15], v[188:191]\n"
"s_waitcnt lgkmcnt(4)\n"
"s_cbranch_vccnz .LKF_sH3_%=\n"
".LKF_rH3_%=:\n"
"v_mfma_f32_16x16x32_bf16 v[64:67], v[40:43], a[16:19], v[64:67]\n"
"ds_read_b64_tr_b16 v[36:37], v6 offset:25408\n"
"ds_read_b64_tr_b16 v[38:39], v6 offset:25920\n"
"v_mfma_f32_16x16x32_bf16 v[68:71], v[40:43], a[20:23], v[68:71]\n"
"buffer_load_dwordx4 a[104:107], v0, s[48:51], s89 offen offset:2048 sc1\n"
"v_mfma_f32_16x16x32_bf16 v[72:75], v[40:43], a[24:27], v[72:75]\n"
"v_mfma_f32_16x16x32_bf16 v[76:79], v[40:43], a[28:31], v[76:79]\n"
"v_mfma_f32_16x16x32_bf16 v[80:83], v[24:27], a[16:19], v[80:83]\n"
"ds_read_b64_tr_b16 v[40:41], v6 offset:29632\n"
"ds_read_b64_tr_b16 v[42:43], v6 offset:30144\n"
"v_mfma_f32_16x16x32_bf16 v[84:87], v[24:27], a[20:23], v[84:87]\n"
"buffer_load_dwordx4 a[108:111], v0, s[48:51], s89 offen offset:3072 sc1\n"
"v_mfma_f32_16x16x32_bf16 v[88:91], v[24:27], a[24:27], v[88:91]\n"
"v_mfma_f32_16x16x32_bf16 v[92:95], v[24:27], a[28:31], v[92:95]\n"
"s_waitcnt lgkmcnt(4)\n"
"v_mfma_f32_16x16x32_bf16 v[96:99], v[28:31], a[16:19], v[96:99]\n"
"ds_read_b64_tr_b16 v[24:25], v6 offset:25440\n"
"ds_read_b64_tr_b16 v[26:27], v6 offset:25952\n"
"v_mfma_f32_16x16x32_bf16 v[100:103], v[28:31], a[20:23], v[100:103]\n"
"s_add_u32 s89, s15, 0x9000\n"
"buffer_load_dwordx4 a[112:115], v0, s[48:51], s89 offen offset:0 sc1\n"
"v_mfma_f32_16x16x32_bf16 v[104:107], v[28:31], a[24:27], v[104:107]\n"
"v_mfma_f32_16x16x32_bf16 v[108:111], v[28:31], a[28:31], v[108:111]\n"
"v_mfma_f32_16x16x32_bf16 v[112:115], v[32:35], a[16:19], v[112:115]\n"
"ds_read_b64_tr_b16 v[28:29], v6 offset:29664\n"
"ds_read_b64_tr_b16 v[30:31], v6 offset:30176\n"
"v_mfma_f32_16x16x32_bf16 v[116:119], v[32:35], a[20:23], v[116:119]\n"
"buffer_load_dwordx4 a[116:119], v0, s[48:51], s89 offen offset:1024 sc1\n"
"v_mfma_f32_16x16x32_bf16 v[120:123], v[32:35], a[24:27], v[120:123]\n"
"v_mfma_f32_16x16x32_bf16 v[124:127], v[32:35], a[28:31], v[124:127]\n"
"s_waitcnt lgkmcnt(4)\n"
"v_mfma_f32_16x16x32_bf16 v[128:131], v[36:39], a[16:19], v[128:131]\n"
"ds_read_b64_tr_b16 v[32:33], v6 offset:33792\n"
"ds_read_b64_tr_b16 v[34:35], v6 offset:34304\n"
"v_mfma_f32_16x16x32_bf16 v[132:135], v[36:39], a[20:23], v[132:135]\n"
"buffer_load_dwordx4 a[120:123], v0, s[48:51], s89 offen offset:2048 sc1\n"
".LKF_pad1:\n"
"s_cbranch_execz .LKF_pad5\n"
"v_mfma_f32_16x16x32_bf16 v[136:139], v[36:39], a[24:27], v[136:139]\n"
"v_mfma_f32_16x16x32_bf16 v[140:143], v[36:39], a[28:31], v[140:143]\n"
"v_mfma_f32_16x16x32_bf16 v[144:147], v[40:43], a[16:19], v[144:147]\n"
"ds_read_b64_tr_b16 v[36:37], v6 offset:38016\n"
"ds_read_b64_tr_b16 v[38:39], v6 offset:38528\n"
"v_mfma_f32_16x16x32_bf16 v[148:151], v[40:43], a[20:23], v[148:151]\n"
"buffer_load_dwordx4 a[124:127], v0, s[48:51], s89 offen offset:3072 sc1\n"
"v_mfma_f32_16x16x32_bf16 v[152:155], v[40:43], a[24:27], v[152:155]\n"
"v_mfma_f32_16x16x32_bf16 v[156:159], v[40:43], a[28:31], v[156:159]\n"
"s_waitcnt lgkmcnt(4)\n"
"v_mfma_f32_16x16x32_bf16 v[160:163], v[24:27], a[16:19], v[160:163]\n"
"ds_read_b64_tr_b16 v[40:41], v6 offset:33824\n"
"ds_read_b64_tr_b16 v[42:43], v6 offset:34336\n"
"v_mfma_f32_16x16x32_bf16 v[164:167], v[24:27], a[20:23], v[164:167]\n"
"s_waitcnt vmcnt(28)\n"
"v_accvgpr_read_b32 v44, a32\n"
"v_mfma_f32_16x16x32_bf16 v[168:171], v[24:27], a[24:27], v[168:171]\n"
"v_accvgpr_read_b32 v45, a36\n"
"v_accvgpr_read_b32 v46, a40\n"
"v_mfma_f32_16x16x32_bf16 v[172:175], v[24:27], a[28:31], v[172:175]\n"
"v_accvgpr_read_b32 v47, a44\n"
"v_max3_u32 v44, v44, v45, v46\n"
"v_mfma_f32_16x16x32_bf16 v[176:179], v[28:31], a[16:19], v[176:179]\n"
"ds_read_b64_tr_b16 v[24:25], v6 offset:38048\n"
"ds_read_b64_tr_b16 v[26:27], v6 offset:38560\n"
"v_mfma_f32_16x16x32_bf16 v[180:183], v[28:31], a[20:23], v[180:183]\n"
"v_max_u32_e32 v44, v44, v47\n"
"v_cmp_eq_u32_e32 vcc, -1, v44\n"
"v_mfma_f32_16x16x32_bf16 v[184:187], v[28:31], a[24:27], v[184:187]\n"
"s_add_u32 m0, s17, 0x23100\n"
"s_add_u32 s88, s16, 0x2d0000\n"
"v_mfma_f32_16x16x32_bf16 v[188:191], v[28:31], a[28:31], v[188:191]\n"
"buffer_load_dwordx4 v1, s[40:43], s88 offen lds\n"
"s_waitcnt lgkmcnt(4)\n"
"s_cbranch_vccnz .LKF_sH4_%=\n"
".LKF_rH4_%=:\n"
"v_mfma_f32_16x16x32_bf16 v[64:67], v[32:35], a[32:35], v[64:67]\n"
"ds_read_b64_tr_b16 v[28:29], v6 offset:33856\n"
"ds_read_b64_tr_b16 v[30:31], v6 offset:34368\n"
"v_mfma_f32_16x16x32_bf16 v[68:71], v[32:35], a[36:39], v[68:71]\n"
"buffer_load_dwordx4 v2, s[40:43], s88 offen offset:1056 lds\n"
"v_mfma_f32_16x16x32_bf16 v[72:75], v[32:35], a[40:43], v[72:75]\n"
"v_mfma_f32_16x16x32_bf16 v[76:79], v[32:35], a[44:47], v[76:79]\n"
"v_mfma_f32_16x16x32_bf16 v[80:83], v[36:39], a[32:35], v[80:83]\n"
"ds_read_b64_tr_b16 v[32:33], v6 offset:38080\n"
"ds_read_b64_tr_b16 v[34:35], v6 offset:38592\n"
"v_mfma_f32_16x16x32_bf16 v[84:87], v[36:39], a[36:39], v[84:87]\n"
"s_add_u32 m0, s17, 0x25200\n"
"s_add_u32 s88, s16, 0x318000\n"
"v_mfma_f32_16x16x32_bf16 v[88:91], v[36:39], a[40:43], v[88:91]\n"
"buffer_load_dwordx4 v1, s[40:43], s88 offen lds\n"
"v_mfma_f32_16x16x32_bf16 v[92:95], v[36:39], a[44:47], v[92:95]\n"
"s_waitcnt lgkmcnt(4)\n"
"v_mfma_f32_16x16x32_bf16 v[96:99], v[40:43], a[32:35], v[96:99]\n"
"ds_read_b64_tr_b16 v[36:37], v6 offset:33888\n"
"ds_read_b64_tr_b16 v[38:39], v6 offset:34400\n"
"v_mfma_f32_16x16x32_bf16 v[100:103], v[40:43], a[36:39], v[100:103]\n"
"buffer_load_dwordx4 v2, s[40:43], s88 offen offset:1056 lds\n"
"v_mfma_f32_16x16x32_bf16 v[104:107], v[40:43], a[40:43], v[104:107]\n"
"v_mfma_f32_16x16x32_bf16 v[108:111], v[40:43], a[44:47], v[108:111]\n"
"v_mfma_f32_16x16x32_bf16 v[112:115], v[24:27], a[32:35], v[112:115]\n"
"ds_read_b64_tr_b16 v[40:41], v6 offset:38112\n"
"ds_read_b64_tr_b16 v[42:43], v6 offset:38624\n"
"v_mfma_f32_16x16x32_bf16 v[116:119], v[24:27], a[36:39], v[116:119]\n"
"s_add_u32 s89, s15, 0xa000\n"
"buffer_load_dwordx4 a[128:131], v0, s[48:51], s89 offen offset:0 sc1\n"
"v_mfma_f32_16x16x32_bf16 v[120:123], v[24:27], a[40:43], v[120:123]\n"
"v_mfma_f32_16x16x32_bf16 v[124:127], v[24:27], a[44:47], v[124:127]\n"
"s_waitcnt lgkmcnt(4)\n"
"v_mfma_f32_16x16x32_bf16 v[128:131], v[28:31], a[32:35], v[128:131]\n"
"ds_read_b64_tr_b16 v[24:25], v6 offset:42240\n"
"ds_read_b64_tr_b16 v[26:27], v6 offset:42752\n"
"v_mfma_f32_16x16x32_bf16 v[132:135], v[28:31], a[36:39], v[132:135]\n"
"buffer_load_dwordx4 a[132:135], v0, s[48:51], s89 offen offset:1024 sc1\n"
"v_mfma_f32_16x16x32_bf16 v[136:139], v[28:31], a[40:43], v[136:139]\n"
"v_mfma_f32_16x16x32_bf16 v[140:143], v[28:31], a[44:47], v[140:143]\n"
"v_mfma_f32_16x16x32_bf16 v[144:147], v[32:35], a[32:35], v[144:147]\n"
"ds_read_b64_tr_b16 v[28:29], v6 offset:46464\n"
"ds_read_b64_tr_b16 v[30:31], v6 offset:46976\n"
"v_mfma_f32_16x16x32_bf16 v[148:151], v[32:35], a[36:39], v[148:151]\n"
"buffer_load_dwordx4 a[136:139], v0, s[48:51], s89 offen offset:2048 sc1\n"
"v_mfma_f32_16x16x32_bf16 v[152:155], v[32:35], a[40:43], v[152:155]\n"
"v_mfma_f32_16x16x32_bf16 v[156:159], v[32:35], a[44:47], v[156:159]\n"
"s_waitcnt lgkmcnt(4)\n"
"v_mfma_f32_16x16x32_bf16 v[160:163], v[36:39], a[32:35], v[160:163]\n"
"ds_read_b64_tr_b16 v[32:33], v6 offset:42272\n"
"ds_read_b64_tr_b16 v[34:35], v6 offset:42784\n"
"v_mfma_f32_16x16x32_bf16 v[164:167], v[36:39], a[36:39], v[164:167]\n"
"s_waitcnt vmcnt(31)\n"
"v_accvgpr_read_b32 v44, a48\n"
"v_mfma_f32_16x16x32_bf16 v[168:171], v[36:39], a[40:43], v[168:171]\n"
"v_accvgpr_read_b32 v45, a52\n"
"v_accvgpr_read_b32 v46, a56\n"
"v_mfma_f32_16x16x32_bf16 v[172:175], v[36:39], a[44:47], v[172:175]\n"
"v_accvgpr_read_b32 v47, a60\n"
"v_max3_u32 v44, v44, v45, v46\n"
"v_mfma_f32_16x16x32_bf16 v[176:179], v[40:43], a[32:35], v[176:179]\n"
"ds_read_b64_tr_b16 v[36:37], v6 offset:46496\n"
"ds_read_b64_tr_b16 v[38:39], v6 offset:47008\n"
"v_mfma_f32_16x16x32_bf16 v[180:183], v[40:43], a[36:39], v[180:183]\n"
"v_max_u32_e32 v44, v44, v47\n"
"v_cmp_eq_u32_e32 vcc, -1, v44\n"
"v_mfma_f32_16x16x32_bf16 v[184:187], v[40:43], a[40:43], v[184:187]\n"
"buffer_load_dwordx4 a[140:143], v0, s[48:51], s89 offen offset:3072 sc1\n"
"v_mfma_f32_16x16x32_bf16 v[188:191], v[40:43], a[44:47], v[188:191]\n"
"s_waitcnt lgkmcnt(4)\n"
"s_cbranch_vccnz .LKF_sH5_%=\n"
".LKF_rH5_%=:\n"
"v_mfma_f32_16x16x32_bf16 v[64:67], v[24:27], a[48:51], v[64:67]\n"
"ds_read_b64_tr_b16 v[40:41], v6 offset:42304\n"
"ds_read_b64_tr_b16 v[42:43], v6 offset:42816\n"
"v_mfma_f32_16x16x32_bf16 v[68:71], v[24:27], a[52:55], v[68:71]\n"
"s_add_u32 s89, s15, 0xb000\n"
"buffer_load_dwordx4 a[144:147], v0, s[48:51], s89 offen offset:0 sc1\n"
"v_mfma_f32_16x16x32_bf16 v[72:75], v[24:27], a[56:59], v[72:75]\n"
"v_mfma_f32_16x16x32_bf16 v[76:79], v[24:27], a[60:63], v[76:79]\n"
"v_mfma_f32_16x16x32_bf16 v[80:83], v[28:31], a[48:51], v[80:83]\n"
"ds_read_b64_tr_b16 v[24:25], v6 offset:46528\n"
"ds_read_b64_tr_b16 v[26:27], v6 offset:47040\n"
"v_mfma_f32_16x16x32_bf16 v[84:87], v[28:31], a[52:55], v[84:87]\n"
"buffer_load_dwordx4 a[148:151], v0, s[48:51], s89 offen offset:1024 sc1\n"
"v_mfma_f32_16x16x32_bf16 v[88:91], v[28:31], a[56:59], v[88:91]\n"
"v_mfma_f32_16x16x32_bf16 v[92:95], v[28:31], a[60:63], v[92:95]\n"
"s_waitcnt lgkmcnt(4)\n"
"v_mfma_f32_16x16x32_bf16 v[96:99], v[32:35], a[48:51], v[96:99]\n"
"ds_read_b64_tr_b16 v[28:29], v6 offset:42336\n"
"ds_read_b64_tr_b16 v[30:31], v6 offset:42848\n"
"v_mfma_f32_16x16x32_bf16 v[100:103], v[32:35], a[52:55], v[100:103]\n"
"buffer_load_dwordx4 a[152:155], v0, s[48:51], s89 offen offset:2048 sc1\n"
"v_mfma_f32_16x16x32_bf16 v[104:107], v[32:35], a[56:59], v[104:107]\n"
"v_mfma_f32_16x16x32_bf16 v[108:111], v[32:35], a[60:63], v[108:111]\n"
"v_mfma_f32_16x16x32_bf16 v[112:115], v[36:39], a[48:51], v[112:115]\n"
"ds_read_b64_tr_b16 v[32:33], v6 offset:46560\n"
"ds_read_b64_tr_b16 v[34:35], v6 offset:47072\n"
"v_mfma_f32_16x16x32_bf16 v[116:119], v[36:39], a[52:55], v[116:119]\n"
"buffer_load_dwordx4 a[156:159], v0, s[48:51], s89 offen offset:3072 sc1\n"
"v_mfma_f32_16x16x32_bf16 v[120:123], v[36:39], a[56:59], v[120:123]\n"
"v_mfma_f32_16x16x32_bf16 v[124:127], v[36:39], a[60:63], v[124:127]\n"
"s_waitcnt vmcnt(32) lgkmcnt(4)\n"
"s_barrier\n"
"v_mfma_f32_16x16x32_bf16 v[128:131], v[40:43], a[48:51], v[128:131]\n"
"ds_read_b64_tr_b16 v[36:37], v6 offset:50688\n"
"ds_read_b64_tr_b16 v[38:39], v6 offset:51200\n"
"v_mfma_f32_16x16x32_bf16 v[132:135], v[40:43], a[52:55], v[132:135]\n"
"s_add_u32 m0, s17, 0x0\n"
"s_add_u32 s88, s16, 0x360000\n"
"v_mfma_f32_16x16x32_bf16 v[136:139], v[40:43], a[56:59], v[136:139]\n"
"buffer_load_dwordx4 v1, s[40:43], s88 offen lds\n"
"v_mfma_f32_16x16x32_bf16 v[140:143], v[40:43], a[60:63], v[140:143]\n"
"v_mfma_f32_16x16x32_bf16 v[144:147], v[24:27], a[48:51], v[144:147]\n"
"ds_read_b64_tr_b16 v[40:41], v6 offset:54912\n"
"ds_read_b64_tr_b16 v[42:43], v6 offset:55424\n"
"v_mfma_f32_16x16x32_bf16 v[148:151], v[24:27], a[52:55], v[148:151]\n"
"buffer_load_dwordx4 v2, s[40:43], s88 offen offset:1056 lds\n"
"v_mfma_f32_16x16x32_bf16 v[152:155], v[24:27], a[56:59], v[152:155]\n"
"v_mfma_f32_16x16x32_bf16 v[156:159], v[24:27], a[60:63], v[156:159]\n"
"s_waitcnt lgkmcnt(4)\n"
"v_mfma_f32_16x16x32_bf16 v[160:163], v[28:31], a[48:51], v[160:163]\n"
"ds_read_b64_tr_b16 v[24:25], v6 offset:50720\n"
"ds_read_b64_tr_b16 v[26:27], v6 offset:51232\n"
"v_mfma_f32_16x16x32_bf16 v[164:167], v[28:31], a[52:55], v[164:167]\n"
"s_waitcnt vmcnt(30)\n"
"v_accvgpr_read_b32 v44, a64\n"
"v_mfma_f32_16x16x32_bf16 v[168:171], v[28:31], a[56:59], v[168:171]\n"
"v_accvgpr_read_b32 v45, a68\n"
"v_accvgpr_read_b32 v46, a72\n"
"v_mfma_f32_16x16x32_bf16 v[172:175], v[28:31], a[60:63], v[172:175]\n"
"v_accvgpr_read_b32 v47, a76\n"
"v_max3_u32 v44, v44, v45, v46\n"
"v_mfma_f32_16x16x32_bf16 v[176:179], v[32:35], a[48:51], v[176:179]\n"
"ds_read_b64_tr_b16 v[28:29], v6 offset:54944\n"
"ds_read_b64_tr_b16 v[30:31], v6 offset:55456\n"
"v_mfma_f32_16x16x32_bf16 v[180:183], v[32:35], a[52:55], v[180:183]\n"
"v_max_u32_e32 v44, v44, v47\n"
"v_cmp_eq_u32_e32 vcc, -1, v44\n"
"v_mfma_f32_16x16x32_bf16 v[184:187], v[32:35], a[56:59], v[184:187]\n"
"s_add_u32 m0, s17, 0x2100\n"
"s_add_u32 s88, s16, 0x3a8000\n"
"v_mfma_f32_16x16x32_bf16 v[188:191], v[32:35], a[60:63], v[188:191]\n"
"buffer_load_dwordx4 v1, s[40:43], s88 offen lds\n"
"s_waitcnt lgkmcnt(4)\n"
"s_cbranch_vccnz .LKF_sH6_%=\n"
".LKF_rH6_%=:\n"
"v_mfma_f32_16x16x32_bf16 v[64:67], v[36:39], a[64:67], v[64:67]\n"
"ds_read_b64_tr_b16 v[32:33], v6 offset:50752\n"
"ds_read_b64_tr_b16 v[34:35], v6 offset:51264\n"
"v_mfma_f32_16x16x32_bf16 v[68:71], v[36:39], a[68:71], v[68:71]\n"
"buffer_load_dwordx4 v2, s[40:43], s88 offen offset:1056 lds\n"
"v_mfma_f32_16x16x32_bf16 v[72:75], v[36:39], a[72:75], v[72:75]\n"
"v_mfma_f32_16x16x32_bf16 v[76:79], v[36:39], a[76:79], v[76:79]\n"
"v_mfma_f32_16x16x32_bf16 v[80:83], v[40:43], a[64:67], v[80:83]\n"
"ds_read_b64_tr_b16 v[36:37], v6 offset:54976\n"
"ds_read_b64_tr_b16 v[38:39], v6 offset:55488\n"
"v_mfma_f32_16x16x32_bf16 v[84:87], v[40:43], a[68:71], v[84:87]\n"
"s_add_u32 s89, s15, 0xc000\n"
"buffer_load_dwordx4 a[160:163], v0, s[48:51], s89 offen offset:0 sc1\n"
"v_mfma_f32_16x16x32_bf16 v[88:91], v[40:43], a[72:75], v[88:91]\n"
"v_mfma_f32_16x16x32_bf16 v[92:95], v[40:43], a[76:79], v[92:95]\n"
"s_waitcnt lgkmcnt(4)\n"
"v_mfma_f32_16x16x32_bf16 v[96:99], v[24:27], a[64:67], v[96:99]\n"
"ds_read_b64_tr_b16 v[40:41], v6 offset:50784\n"
"ds_read_b64_tr_b16 v[42:43], v6 offset:51296\n"
"v_mfma_f32_16x16x32_bf16 v[100:103], v[24:27], a[68:71], v[100:103]\n"
"buffer_load_dwordx4 a[164:167], v0, s[48:51], s89 offen offset:1024 sc1\n"
"v_mfma_f32_16x16x32_bf16 v[104:107], v[24:27], a[72:75], v[104:107]\n"
"v_mfma_f32_16x16x32_bf16 v[108:111], v[24:27], a[76:79], v[108:111]\n"
"v_mfma_f32_16x16x32_bf16 v[112:115], v[28:31], a[64:67], v[112:115]\n"
"ds_read_b64_tr_b16 v[24:25], v6 offset:55008\n"
"ds_read_b64_tr_b16 v[26:27], v6 offset:55520\n"
"v_mfma_f32_16x16x32_bf16 v[116:119], v[28:31], a[68:71], v[116:119]\n"
"buffer_load_dwordx4 a[168:171], v0, s[48:51], s89 offen offset:2048 sc1\n"
"v_mfma_f32_16x16x32_bf16 v[120:123], v[28:31], a[72:75], v[120:123]\n"
"v_mfma_f32_16x16x32_bf16 v[124:127], v[28:31], a[76:79], v[124:127]\n"
"s_waitcnt lgkmcnt(4)\n"
"v_mfma_f32_16x16x32_bf16 v[128:131], v[32:35], a[64:67], v[128:131]\n"
"ds_read_b64_tr_b16 v[28:29], v7 offset:0\n"
"ds_read_b64_tr_b16 v[30:31], v7 offset:512\n"
"v_mfma_f32_16x16x32_bf16 v[132:135], v[32:35], a[68:71], v[132:135]\n"
"buffer_load_dwordx4 a[172:175], v0, s[48:51], s89 offen offset:3072 sc1\n"
"v_mfma_f32_16x16x32_bf16 v[136:139], v[32:35], a[72:75], v[136:139]\n"
"v_mfma_f32_16x16x32_bf16 v[140:143], v[32:35], a[76:79], v[140:143]\n"
"v_mfma_f32_16x16x32_bf16 v[144:147], v[36:39], a[64:67], v[144:147]\n"
"ds_read_b64_tr_b16 v[32:33], v7 offset:4224\n"
"ds_read_b64_tr_b16 v[34:35], v7 offset:4736\n"
"v_mfma_f32_16x16x32_bf16 v[148:151], v[36:39], a[68:71], v[148:151]\n"
"s_add_u32 s89, s15, 0xd000\n"
"buffer_load_dwordx4 a[176:179], v0, s[48:51], s89 offen offset:0 sc1\n"
"v_mfma_f32_16x16x32_bf16 v[152:155], v[36:39], a[72:75], v[152:155]\n"
"v_mfma_f32_16x16x32_bf16 v[156:159], v[36:39], a[76:79], v[156:159]\n"
"s_waitcnt lgkmcnt(4)\n"
"v_mfma_f32_16x16x32_bf16 v[160:163], v[40:43], a[64:67], v[160:163]\n"
"ds_read_b64_tr_b16 v[36:37], v7 offset:32\n"
"ds_read_b64_tr_b16 v[38:39], v7 offset:544\n"
"v_mfma_f32_16x16x32_bf16 v[164:167], v[40:43], a[68:71], v[164:167]\n"
"s_waitcnt vmcnt(33)\n"
"v_accvgpr_read_b32 v44, a80\n"
"v_mfma_f32_16x16x32_bf16 v[168:171], v[40:43], a[72:75], v[168:171]\n"
"v_accvgpr_read_b32 v45, a84\n"
"v_accvgpr_read_b32 v46, a88\n"
"v_mfma_f32_16x16x32_bf16 v[172:175], v[40:43], a[76:79], v[172:175]\n"
"v_accvgpr_read_b32 v47, a92\n"
"v_max3_u32 v44, v44, v45, v46\n"
"v_mfma_f32_16x16x32_bf16 v[176:179], v[24:27], a[64:67], v[176:179]\n"
"ds_read_b64_tr_b16 v[40:41], v7 offset:4256\n"
"ds_read_b64_tr_b16 v[42:43], v7 offset:4768\n"
"v_mfma_f32_16x16x32_bf16 v[180:183], v[24:27], a[68:71], v[180:183]\n"
"v_max_u32_e32 v44, v44, v47\n"
"v_cmp_eq_u32_e32 vcc, -1, v44\n"
"v_mfma_f32_16x16x32_bf16 v[184:187], v[24:27], a[72:75], v[184:187]\n"
"buffer_load_dwordx4 a[180:183], v0, s[48:51], s89 offen offset:1024 sc1\n"
"v_mfma_f32_16x16x32_bf16 v[188:191], v[24:27], a[76:79], v[188:191]\n"
"s_waitcnt lgkmcnt(4)\n"
"s_cbranch_vccnz .LKF_sH7_%=\n"
".LKF_rH7_%=:\n"
"v_mfma_f32_16x16x32_bf16 v[64:67], v[28:31], a[80:83], v[64:67]\n"
"ds_read_b64_tr_b16 v[24:25], v7 offset:64\n"
"ds_read_b64_tr_b16 v[26:27], v7 offset:576\n"
"v_mfma_f32_16x16x32_bf16 v[68:71], v[28:31], a[84:87], v[68:71]\n"
"buffer_load_dwordx4 a[184:187], v0, s[48:51], s89 offen offset:2048 sc1\n"
"v_mfma_f32_16x16x32_bf16 v[72:75], v[28:31], a[88:91], v[72:75]\n"
"v_mfma_f32_16x16x32_bf16 v[76:79], v[28:31], a[92:95], v[76:79]\n"
"v_mfma_f32_16x16x32_bf16 v[80:83], v[32:35], a[80:83], v[80:83]\n"
"ds_read_b64_tr_b16 v[28:29], v7 offset:4288\n"
"ds_read_b64_tr_b16 v[30:31], v7 offset:4800\n"
"v_mfma_f32_16x16x32_bf16 v[84:87], v[32:35], a[84:87], v[84:87]\n"
"buffer_load_dwordx4 a[188:191], v0, s[48:51], s89 offen offset:3072 sc1\n"
"v_mfma_f32_16x16x32_bf16 v[88:91], v[32:35], a[88:91], v[88:91]\n"
"v_mfma_f32_16x16x32_bf16 v[92:95], v[32:35], a[92:95], v[92:95]\n"
"s_waitcnt lgkmcnt(4)\n"
"v_mfma_f32_16x16x32_bf16 v[96:99], v[36:39], a[80:83], v[96:99]\n"
"ds_read_b64_tr_b16 v[32:33], v7 offset:96\n"
"ds_read_b64_tr_b16 v[34:35], v7 offset:608\n"
"v_mfma_f32_16x16x32_bf16 v[100:103], v[36:39], a[84:87], v[100:103]\n"
"s_add_u32 m0, s17, 0x4200\n"
"s_add_u32 s88, s16, 0x3f0000\n"
"v_mfma_f32_16x16x32_bf16 v[104:107], v[36:39], a[88:91], v[104:107]\n"
"buffer_load_dwordx4 v1, s[40:43], s88 offen lds\n"
"v_mfma_f32_16x16x32_bf16 v[108:111], v[36:39], a[92:95], v[108:111]\n"
"v_mfma_f32_16x16x32_bf16 v[112:115], v[40:43], a[80:83], v[112:115]\n"
"ds_read_b64_tr_b16 v[36:37], v7 offset:4320\n"
"ds_read_b64_tr_b16 v[38:39], v7 offset:4832\n"
"v_mfma_f32_16x16x32_bf16 v[116:119], v[40:43], a[84:87], v[116:119]\n"
"buffer_load_dwordx4 v2, s[40:43], s88 offen offset:1056 lds\n"
"v_mfma_f32_16x16x32_bf16 v[120:123], v[40:43], a[88:91], v[120:123]\n"
"v_mfma_f32_16x16x32_bf16 v[124:127], v[40:43], a[92:95], v[124:127]\n"
"s_waitcnt vmcnt(34) lgkmcnt(4)\n"
"s_barrier\n"
"v_mfma_f32_16x16x32_bf16 v[128:131], v[24:27], a[80:83], v[128:131]\n"
"ds_read_b64_tr_b16 v[40:41], v7 offset:8448\n"
"ds_read_b64_tr_b16 v[42:43], v7 offset:8960\n"
"v_mfma_f32_16x16x32_bf16 v[132:135], v[24:27], a[84:87], v[132:135]\n"
"s_add_u32 m0, s17, 0x6300\n"
"s_add_u32 s88, s16, 0x438000\n"
"v_mfma_f32_16x16x32_bf16 v[136:139], v[24:27], a[88:91], v[136:139]\n"
"buffer_load_dwordx4 v1, s[40:43], s88 offen lds\n"
"v_mfma_f32_16x16x32_bf16 v[140:143], v[24:27], a[92:95], v[140:143]\n"
"v_mfma_f32_16x16x32_bf16 v[144:147], v[28:31], a[80:83], v[144:147]\n"
"ds_read_b64_tr_b16 v[24:25], v7 offset:12672\n"
"ds_read_b64_tr_b16 v[26:27], v7 offset:13184\n"
"v_mfma_f32_16x16x32_bf16 v[148:151], v[28:31], a[84:87], v[148:151]\n"
"buffer_load_dwordx4 v2, s[40:43], s88 offen offset:1056 lds\n"
"v_mfma_f32_16x16x32_bf16 v[152:155], v[28:31], a[88:91], v[152:155]\n"
"v_mfma_f32_16x16x32_bf16 v[156:159], v[28:31], a[92:95], v[156:159]\n"
"s_waitcnt lgkmcnt(4)\n"
"v_mfma_f32_16x16x32_bf16 v[160:163], v[32:35], a[80:83], v[160:163]\n"
"ds_read_b64_tr_b16 v[28:29], v7 offset:8480\n"
"ds_read_b64_tr_b16 v[30:31], v7 offset:8992\n"
"v_mfma_f32_16x16x32_bf16 v[164:167], v[32:35], a[84:87], v[164:167]\n"
"s_waitcnt vmcnt(32)\n"
"v_accvgpr_read_b32 v44, a96\n"
"v_mfma_f32_16x16x32_bf16 v[168:171], v[32:35], a[88:91], v[168:171]\n"
"v_accvgpr_read_b32 v45, a100\n"
"v_accvgpr_read_b32 v46, a104\n"
"v_mfma_f32_16x16x32_bf16 v[172:175], v[32:35], a[92:95], v[172:175]\n"
"v_accvgpr_read_b32 v47, a108\n"
"v_max3_u32 v44, v44, v45, v46\n"
"v_mfma_f32_16x16x32_bf16 v[176:179], v[36:39], a[80:83], v[176:179]\n"
"ds_read_b64_tr_b16 v[32:33], v7 offset:12704\n"
"ds_read_b64_tr_b16 v[34:35], v7 offset:13216\n"
"v_mfma_f32_16x16x32_bf16 v[180:183], v[36:39], a[84:87], v[180:183]\n"
"v_max_u32_e32 v44, v44, v47\n"
"v_cmp_eq_u32_e32 vcc, -1, v44\n"
"v_mfma_f32_16x16x32_bf16 v[184:187], v[36:39], a[88:91], v[184:187]\n"
"s_add_u32 m0, s17, 0x8400\n"
"s_add_u32 s88, s16, 0x480000\n"
"v_mfma_f32_16x16x32_bf16 v[188:191], v[36:39], a[92:95], v[188:191]\n"
"buffer_load_dwordx4 v1, s[40:43], s88 offen lds\n"
"s_waitcnt lgkmcnt(4)\n"
"s_cbranch_vccnz .LKF_sH8_%=\n"
".LKF_rH8_%=:\n"
"v_mfma_f32_16x16x32_bf16 v[64:67], v[40:43], a[96:99], v[64:67]\n"
"ds_read_b64_tr_b16 v[36:37], v7 offset:8512\n"
"ds_read_b64_tr_b16 v[38:39], v7 offset:9024\n"
"v_mfma_f32_16x16x32_bf16 v[68:71], v[40:43], a[100:103], v[68:71]\n"
"buffer_load_dwordx4 v2, s[40:43], s88 offen offset:1056 lds\n"
"v_mfma_f32_16x16x32_bf16 v[72:75], v[40:43], a[104:107], v[72:75]\n"
"v_mfma_f32_16x16x32_bf16 v[76:79], v[40:43], a[108:111], v[76:79]\n"
"v_mfma_f32_16x16x32_bf16 v[80:83], v[24:27], a[96:99], v[80:83]\n"
"ds_read_b64_tr_b16 v[40:41], v7 offset:12736\n"
"ds_read_b64_tr_b16 v[42:43], v7 offset:13248\n"
"v_mfma_f32_16x16x32_bf16 v[84:87], v[24:27], a[100:103], v[84:87]\n"
"s_add_u32 m0, s17, 0xa500\n"
"s_add_u32 s88, s16, 0x4c8000\n"
"v_mfma_f32_16x16x32_bf16 v[88:91], v[24:27], a[104:107], v[88:91]\n"
"buffer_load_dwordx4 v1, s[40:43], s88 offen lds\n"
"v_mfma_f32_16x16x32_bf16 v[92:95], v[24:27], a[108:111], v[92:95]\n"
"s_waitcnt lgkmcnt(4)\n"
"v_mfma_f32_16x16x32_bf16 v[96:99], v[28:31], a[96:99], v[96:99]\n"
"ds_read_b64_tr_b16 v[24:25], v7 offset:8544\n"
"ds_read_b64_tr_b16 v[26:27], v7 offset:9056\n"
"v_mfma_f32_16x16x32_bf16 v[100:103], v[28:31], a[100:103], v[100:103]\n"
"buffer_load_dwordx4 v2, s[40:43], s88 offen offset:1056 lds\n"
"v_mfma_f32_16x16x32_bf16 v[104:107], v[28:31], a[104:107], v[104:107]\n"
"v_mfma_f32_16x16x32_bf16 v[108:111], v[28:31], a[108:111], v[108:111]\n"
"v_mfma_f32_16x16x32_bf16 v[112:115], v[32:35], a[96:99], v[112:115]\n"
"ds_read_b64_tr_b16 v[28:29], v7 offset:12768\n"
"ds_read_b64_tr_b16 v[30:31], v7 offset:13280\n"
"v_mfma_f32_16x16x32_bf16 v[116:119], v[32:35], a[100:103], v[116:119]\n"
"s_add_u32 s89, s15, 0xe000\n"
"buffer_load_dwordx4 a[192:195], v0, s[48:51], s89 offen offset:0 sc1\n"
"v_mfma_f32_16x16x32_bf16 v[120:123], v[32:35], a[104:107], v[120:123]\n"
"v_mfma_f32_16x16x32_bf16 v[124:127], v[32:35], a[108:111], v[124:127]\n"
"s_waitcnt lgkmcnt(4)\n"
"v_mfma_f32_16x16x32_bf16 v[128:131], v[36:39], a[96:99], v[128:131]\n"
"ds_read_b64_tr_b16 v[32:33], v7 offset:16896\n"
"ds_read_b64_tr_b16 v[34:35], v7 offset:17408\n"
"v_mfma_f32_16x16x32_bf16 v[132:135], v[36:39], a[100:103], v[132:135]\n"
"s_mov_b64 exec, 0xff\n"
"buffer_load_dword v22, v15, s[44:47], s84 offen offset:0\n"
"s_mov_b64 exec, -1\n"
"v_mfma_f32_16x16x32_bf16 v[136:139], v[36:39], a[104:107], v[136:139]\n"
"buffer_load_dwordx4 a[196:199], v0, s[48:51], s89 offen offset:1024 sc1\n"
"v_mfma_f32_16x16x32_bf16 v[140:143], v[36:39], a[108:111], v[140:143]\n"
"v_mfma_f32_16x16x32_bf16 v[144:147], v[40:43], a[96:99], v[144:147]\n"
"ds_read_b64_tr_b16 v[36:37], v7 offset:21120\n"
"ds_read_b64_tr_b16 v[38:39], v7 offset:21632\n"
"v_mfma_f32_16x16x32_bf16 v[148:151], v[40:43], a[100:103], v[148:151]\n"
"s_add_u32 s84, s84, 0x480\n"
"v_mfma_f32_16x16x32_bf16 v[152:155], v[40:43], a[104:107], v[152:155]\n"
"v_mfma_f32_16x16x32_bf16 v[156:159], v[40:43], a[108:111], v[156:159]\n"
"s_waitcnt lgkmcnt(4)\n"
"v_mfma_f32_16x16x32_bf16 v[160:163], v[24:27], a[96:99], v[160:163]\n"
"ds_read_b64_tr_b16 v[40:41], v7 offset:16928\n"
"ds_read_b64_tr_b16 v[42:43], v7 offset:17440\n"
"v_mfma_f32_16x16x32_bf16 v[164:167], v[24:27], a[100:103], v[164:167]\n"
"s_waitcnt vmcnt(35)\n"
"v_accvgpr_read_b32 v44, a112\n"
"v_mfma_f32_16x16x32_bf16 v[168:171], v[24:27], a[104:107], v[168:171]\n"
"v_accvgpr_read_b32 v45, a116\n"
"v_accvgpr_read_b32 v46, a120\n"
"v_mfma_f32_16x16x32_bf16 v[172:175], v[24:27], a[108:111], v[172:175]\n"
"v_accvgpr_read_b32 v47, a124\n"
"v_max3_u32 v44, v44, v45, v46\n"
"v_mfma_f32_16x16x32_bf16 v[176:179], v[28:31], a[96:99], v[176:179]\n"
"ds_read_b64_tr_b16 v[24:25], v7 offset:21152\n"
"ds_read_b64_tr_b16 v[26:27], v7 offset:21664\n"
"v_mfma_f32_16x16x32_bf16 v[180:183], v[28:31], a[100:103], v[180:183]\n"
"v_max_u32_e32 v44, v44, v47\n"
"v_cmp_eq_u32_e32 vcc, -1, v44\n"
"v_mfma_f32_16x16x32_bf16 v[184:187], v[28:31], a[104:107], v[184:187]\n"
"buffer_load_dwordx4 a[200:203], v0, s[48:51], s89 offen offset:2048 sc1\n"
"v_mfma_f32_16x16x32_bf16 v[188:191], v[28:31], a[108:111], v[188:191]\n"
"s_waitcnt lgkmcnt(4)\n"
"s_cbranch_vccnz .LKF_sH9_%=\n"
".LKF_rH9_%=:\n"
"v_mfma_f32_16x16x32_bf16 v[64:67], v[32:35], a[112:115], v[64:67]\n"
"ds_read_b64_tr_b16 v[28:29], v7 offset:16960\n"
"ds_read_b64_tr_b16 v[30:31], v7 offset:17472\n"
"v_mfma_f32_16x16x32_bf16 v[68:71], v[32:35], a[116:119], v[68:71]\n"
"buffer_load_dwordx4 a[204:207], v0, s[48:51], s89 offen offset:3072 sc1\n"
"v_mfma_f32_16x16x32_bf16 v[72:75], v[32:35], a[120:123], v[72:75]\n"
"v_mfma_f32_16x16x32_bf16 v[76:79], v[32:35], a[124:127], v[76:79]\n"
"v_mfma_f32_16x16x32_bf16 v[80:83], v[36:39], a[112:115], v[80:83]\n"
"ds_read_b64_tr_b16 v[32:33], v7 offset:21184\n"
"ds_read_b64_tr_b16 v[34:35], v7 offset:21696\n"
"v_mfma_f32_16x16x32_bf16 v[84:87], v[36:39], a[116:119], v[84:87]\n"
"s_add_u32 s89, s15, 0xf000\n"
"buffer_load_dwordx4 a[208:211], v0, s[48:51], s89 offen offset:0 sc1\n"
"v_mfma_f32_16x16x32_bf16 v[88:91], v[36:39], a[120:123], v[88:91]\n"
"v_mfma_f32_16x16x32_bf16 v[92:95], v[36:39], a[124:127], v[92:95]\n"
"s_waitcnt lgkmcnt(4)\n"
"v_mfma_f32_16x16x32_bf16 v[96:99], v[40:43], a[112:115], v[96:99]\n"
"ds_read_b64_tr_b16 v[36:37], v7 offset:16992\n"
"ds_read_b64_tr_b16 v[38:39], v7 offset:17504\n"
"v_mfma_f32_16x16x32_bf16 v[100:103], v[40:43], a[116:119], v[100:103]\n"
"buffer_load_dwordx4 a[212:215], v0, s[48:51], s89 offen offset:1024 sc1\n"
"v_mfma_f32_16x16x32_bf16 v[104:107], v[40:43], a[120:123], v[104:107]\n"
"v_mfma_f32_16x16x32_bf16 v[108:111], v[40:43], a[124:127], v[108:111]\n"
"v_mfma_f32_16x16x32_bf16 v[112:115], v[24:27], a[112:115], v[112:115]\n"
"ds_read_b64_tr_b16 v[40:41], v7 offset:21216\n"
"ds_read_b64_tr_b16 v[42:43], v7 offset:21728\n"
"v_mfma_f32_16x16x32_bf16 v[116:119], v[24:27], a[116:119], v[116:119]\n"
"buffer_load_dwordx4 a[216:219], v0, s[48:51], s89 offen offset:2048 sc1\n"
"v_mfma_f32_16x16x32_bf16 v[120:123], v[24:27], a[120:123], v[120:123]\n"
"v_mfma_f32_16x16x32_bf16 v[124:127], v[24:27], a[124:127], v[124:127]\n"
"s_waitcnt vmcnt(36) lgkmcnt(4)\n"
"s_barrier\n"
"v_mfma_f32_16x16x32_bf16 v[128:131], v[28:31], a[112:115], v[128:131]\n"
"ds_read_b64_tr_b16 v[24:25], v7 offset:25344\n"
"ds_read_b64_tr_b16 v[26:27], v7 offset:25856\n"
"v_mfma_f32_16x16x32_bf16 v[132:135], v[28:31], a[116:119], v[132:135]\n"
"buffer_load_dwordx4 a[220:223], v0, s[48:51], s89 offen offset:3072 sc1\n"
"v_mfma_f32_16x16x32_bf16 v[136:139], v[28:31], a[120:123], v[136:139]\n"
"v_mfma_f32_16x16x32_bf16 v[140:143], v[28:31], a[124:127], v[140:143]\n"
"v_mfma_f32_16x16x32_bf16 v[144:147], v[32:35], a[112:115], v[144:147]\n"
"ds_read_b64_tr_b16 v[28:29], v7 offset:29568\n"
"ds_read_b64_tr_b16 v[30:31], v7 offset:30080\n"
"v_mfma_f32_16x16x32_bf16 v[148:151], v[32:35], a[116:119], v[148:151]\n"
"v_mfma_f32_16x16x32_bf16 v[152:155], v[32:35], a[120:123], v[152:155]\n"
"v_mfma_f32_16x16x32_bf16 v[156:159], v[32:35], a[124:127], v[156:159]\n"
"s_waitcnt lgkmcnt(4)\n"
"v_mfma_f32_16x16x32_bf16 v[160:163], v[36:39], a[112:115], v[160:163]\n"
"ds_read_b64_tr_b16 v[32:33], v7 offset:25376\n"
"ds_read_b64_tr_b16 v[34:35], v7 offset:25888\n"
"v_mfma_f32_16x16x32_bf16 v[164:167], v[36:39], a[116:119], v[164:167]\n"
"s_waitcnt vmcnt(33)\n"
"v_accvgpr_read_b32 v44, a128\n"
"v_mfma_f32_16x16x32_bf16 v[168:171], v[36:39], a[120:123], v[168:171]\n"
"v_accvgpr_read_b32 v45, a132\n"
"v_accvgpr_read_b32 v46, a136\n"
"v_mfma_f32_16x16x32_bf16 v[172:175], v[36:39], a[124:127], v[172:175]\n"
"v_accvgpr_read_b32 v47, a140\n"
"v_max3_u32 v44, v44, v45, v46\n"
"v_mfma_f32_16x16x32_bf16 v[176:179], v[40:43], a[112:115], v[176:179]\n"
"ds_read_b64_tr_b16 v[36:37], v7 offset:29600\n"
"ds_read_b64_tr_b16 v[38:39], v7 offset:30112\n"
"v_mfma_f32_16x16x32_bf16 v[180:183], v[40:43], a[116:119], v[180:183]\n"
"v_max_u32_e32 v44, v44, v47\n"
"v_cmp_eq_u32_e32 vcc, -1, v44\n"
"v_mfma_f32_16x16x32_bf16 v[184:187], v[40:43], a[120:123], v[184:187]\n"
"s_add_u32 s89, s15, 0x10000\n"
"buffer_load_dwordx4 a[224:227], v0, s[48:51], s89 offen offset:0 sc1\n"
"v_mfma_f32_16x16x32_bf16 v[188:191], v[40:43], a[124:127], v[188:191]\n"
"s_waitcnt lgkmcnt(4)\n"
"s_cbranch_vccnz .LKF_sH10_%=\n"
".LKF_rH10_%=:\n"
"v_mfma_f32_16x16x32_bf16 v[64:67], v[24:27], a[128:131], v[64:67]\n"
"ds_read_b64_tr_b16 v[40:41], v7 offset:25408\n"
"ds_read_b64_tr_b16 v[42:43], v7 offset:25920\n"
"v_mfma_f32_16x16x32_bf16 v[68:71], v[24:27], a[132:135], v[68:71]\n"
"buffer_load_dwordx4 a[228:231], v0, s[48:51], s89 offen offset:1024 sc1\n"
"v_mfma_f32_16x16x32_bf16 v[72:75], v[24:27], a[136:139], v[72:75]\n"
"v_mfma_f32_16x16x32_bf16 v[76:79], v[24:27], a[140:143], v[76:79]\n"
"v_mfma_f32_16x16x32_bf16 v[80:83], v[28:31], a[128:131], v[80:83]\n"
"ds_read_b64_tr_b16 v[24:25], v7 offset:29632\n"
"ds_read_b64_tr_b16 v[26:27], v7 offset:30144\n"
"v_mfma_f32_16x16x32_bf16 v[84:87], v[28:31], a[132:135], v[84:87]\n"
"buffer_load_dwordx4 a[232:235], v0, s[48:51], s89 offen offset:2048 sc1\n"
"v_mfma_f32_16x16x32_bf16 v[88:91], v[28:31], a[136:139], v[88:91]\n"
"v_mfma_f32_16x16x32_bf16 v[92:95], v[28:31], a[140:143], v[92:95]\n"
"s_waitcnt lgkmcnt(4)\n"
"v_mfma_f32_16x16x32_bf16 v[96:99], v[32:35], a[128:131], v[96:99]\n"
"ds_read_b64_tr_b16 v[28:29], v7 offset:25440\n"
"ds_read_b64_tr_b16 v[30:31], v7 offset:25952\n"
"v_mfma_f32_16x16x32_bf16 v[100:103], v[32:35], a[132:135], v[100:103]\n"
"buffer_load_dwordx4 a[236:239], v0, s[48:51], s89 offen offset:3072 sc1\n"
"v_mfma_f32_16x16x32_bf16 v[104:107], v[32:35], a[136:139], v[104:107]\n"
"v_mfma_f32_16x16x32_bf16 v[108:111], v[32:35], a[140:143], v[108:111]\n"
"v_mfma_f32_16x16x32_bf16 v[112:115], v[36:39], a[128:131], v[112:115]\n"
"ds_read_b64_tr_b16 v[32:33], v7 offset:29664\n"
"ds_read_b64_tr_b16 v[34:35], v7 offset:30176\n"
"v_mfma_f32_16x16x32_bf16 v[116:119], v[36:39], a[132:135], v[116:119]\n"
"s_add_u32 s89, s15, 0x11000\n"
"buffer_load_dwordx4 a[240:243], v0, s[48:51], s89 offen offset:0 sc1\n"
"v_mfma_f32_16x16x32_bf16 v[120:123], v[36:39], a[136:139], v[120:123]\n"
"v_mfma_f32_16x16x32_bf16 v[124:127], v[36:39], a[140:143], v[124:127]\n"
"s_waitcnt lgkmcnt(4)\n"
"v_mfma_f32_16x16x32_bf16 v[128:131], v[40:43], a[128:131], v[128:131]\n"
"ds_read_b64_tr_b16 v[36:37], v7 offset:33792\n"
"ds_read_b64_tr_b16 v[38:39], v7 offset:34304\n"
"v_mfma_f32_16x16x32_bf16 v[132:135], v[40:43], a[132:135], v[132:135]\n"
"buffer_load_dwordx4 a[244:247], v0, s[48:51], s89 offen offset:1024 sc1\n"
"v_mfma_f32_16x16x32_bf16 v[136:139], v[40:43], a[136:139], v[136:139]\n"
"v_mfma_f32_16x16x32_bf16 v[140:143], v[40:43], a[140:143], v[140:143]\n"
"v_mfma_f32_16x16x32_bf16 v[144:147], v[24:27], a[128:131], v[144:147]\n"
"ds_read_b64_tr_b16 v[40:41], v7 offset:38016\n"
"ds_read_b64_tr_b16 v[42:43], v7 offset:38528\n"
"v_mfma_f32_16x16x32_bf16 v[148:151], v[24:27], a[132:135], v[148:151]\n"
"v_mfma_f32_16x16x32_bf16 v[152:155], v[24:27], a[136:139], v[152:155]\n"
"v_mfma_f32_16x16x32_bf16 v[156:159], v[24:27], a[140:143], v[156:159]\n"
"s_waitcnt lgkmcnt(4)\n"
"v_mfma_f32_16x16x32_bf16 v[160:163], v[28:31], a[128:131], v[160:163]\n"
"ds_read_b64_tr_b16 v[24:25], v7 offset:33824\n"
"ds_read_b64_tr_b16 v[26:27], v7 offset:34336\n"
"v_mfma_f32_16x16x32_bf16 v[164:167], v[28:31], a[132:135], v[164:167]\n"
"s_waitcnt vmcnt(35)\n"
"v_accvgpr_read_b32 v44, a144\n"
"v_mfma_f32_16x16x32_bf16 v[168:171], v[28:31], a[136:139], v[168:171]\n"
"v_accvgpr_read_b32 v45, a148\n"
"v_accvgpr_read_b32 v46, a152\n"
"v_mfma_f32_16x16x32_bf16 v[172:175], v[28:31], a[140:143], v[172:175]\n"
"v_accvgpr_read_b32 v47, a156\n"
"v_max3_u32 v44, v44, v45, v46\n"
"v_mfma_f32_16x16x32_bf16 v[176:179], v[32:35], a[128:131], v[176:179]\n"
"ds_read_b64_tr_b16 v[28:29], v7 offset:38048\n"
"ds_read_b64_tr_b16 v[30:31], v7 offset:38560\n"
"v_mfma_f32_16x16x32_bf16 v[180:183], v[32:35], a[132:135], v[180:183]\n"
"v_max_u32_e32 v44, v44, v47\n"
"v_cmp_eq_u32_e32 vcc, -1, v44\n"
"v_mfma_f32_16x16x32_bf16 v[184:187], v[32:35], a[136:139], v[184:187]\n"
"buffer_load_dwordx4 a[248:251], v0, s[48:51], s89 offen offset:2048 sc1\n"
"v_mfma_f32_16x16x32_bf16 v[188:191], v[32:35], a[140:143], v[188:191]\n"
"s_waitcnt lgkmcnt(4)\n"
"s_cbranch_vccnz .LKF_sH11_%=\n"
".LKF_rH11_%=:\n"
"v_mfma_f32_16x16x32_bf16 v[64:67], v[36:39], a[144:147], v[64:67]\n"
"ds_read_b64_tr_b16 v[32:33], v7 offset:33856\n"
"ds_read_b64_tr_b16 v[34:35], v7 offset:34368\n"
"v_mfma_f32_16x16x32_bf16 v[68:71], v[36:39], a[148:151], v[68:71]\n"
"s_mov_b64 exec, 0xff\n"
"buffer_load_dword v22, v15, s[44:47], s84 offen offset:0\n"
"s_mov_b64 exec, -1\n"
"v_mfma_f32_16x16x32_bf16 v[72:75], v[36:39], a[152:155], v[72:75]\n"
".LKF_pad2:\n"
"s_cbranch_execz .LKF_pad6\n"
"buffer_load_dwordx4 a[252:255], v0, s[48:51], s89 offen offset:3072 sc1\n"
"v_mfma_f32_16x16x32_bf16 v[76:79], v[36:39], a[156:159], v[76:79]\n"
"v_mfma_f32_16x16x32_bf16 v[80:83], v[40:43], a[144:147], v[80:83]\n"
"ds_read_b64_tr_b16 v[36:37], v7 offset:38080\n"
"ds_read_b64_tr_b16 v[38:39], v7 offset:38592\n"
"v_mfma_f32_16x16x32_bf16 v[84:87], v[40:43], a[148:151], v[84:87]\n"
"s_add_u32 s84, s84, 0x480\n"
"s_add_u32 m0, s17, 0xc600\n"
"v_mfma_f32_16x16x32_bf16 v[88:91], v[40:43], a[152:155], v[88:91]\n"
"s_add_u32 s88, s59, 0x0\n"
"buffer_load_dwordx4 v0, s[48:51], s88 offen lds\n"
"v_mfma_f32_16x16x32_bf16 v[92:95], v[40:43], a[156:159], v[92:95]\n"
"s_waitcnt lgkmcnt(4)\n"
"v_mfma_f32_16x16x32_bf16 v[96:99], v[24:27], a[144:147], v[96:99]\n"
"ds_read_b64_tr_b16 v[40:41], v7 offset:33888\n"
"ds_read_b64_tr_b16 v[42:43], v7 offset:34400\n"
"v_mfma_f32_16x16x32_bf16 v[100:103], v[24:27], a[148:151], v[100:103]\n"
"buffer_load_dwordx4 v0, s[48:51], s88 offen offset:1024 lds\n"
"v_mfma_f32_16x16x32_bf16 v[104:107], v[24:27], a[152:155], v[104:107]\n"
"v_mfma_f32_16x16x32_bf16 v[108:111], v[24:27], a[156:159], v[108:111]\n"
"v_mfma_f32_16x16x32_bf16 v[112:115], v[28:31], a[144:147], v[112:115]\n"
"ds_read_b64_tr_b16 v[24:25], v7 offset:38112\n"
"ds_read_b64_tr_b16 v[26:27], v7 offset:38624\n"
"v_mfma_f32_16x16x32_bf16 v[116:119], v[28:31], a[148:151], v[116:119]\n"
"s_add_u32 m0, s17, 0xe700\n"
"s_add_u32 s88, s59, 0x2000\n"
"v_mfma_f32_16x16x32_bf16 v[120:123], v[28:31], a[152:155], v[120:123]\n"
"buffer_load_dwordx4 v0, s[48:51], s88 offen lds\n"
"v_mfma_f32_16x16x32_bf16 v[124:127], v[28:31], a[156:159], v[124:127]\n"
"s_waitcnt vmcnt(37) lgkmcnt(4)\n"
"s_barrier\n"
"v_mfma_f32_16x16x32_bf16 v[128:131], v[32:35], a[144:147], v[128:131]\n"
"ds_read_b64_tr_b16 v[28:29], v5 offset:0\n"
"ds_read_b64_tr_b16 v[30:31], v5 offset:512\n"
"v_mfma_f32_16x16x32_bf16 v[132:135], v[32:35], a[148:151], v[132:135]\n"
"buffer_load_dwordx4 v0, s[48:51], s88 offen offset:1024 lds\n"
"v_mfma_f32_16x16x32_bf16 v[136:139], v[32:35], a[152:155], v[136:139]\n"
"v_mfma_f32_16x16x32_bf16 v[140:143], v[32:35], a[156:159], v[140:143]\n"
"v_mfma_f32_16x16x32_bf16 v[144:147], v[36:39], a[144:147], v[144:147]\n"
"ds_read_b64_tr_b16 v[32:33], v5 offset:4224\n"
"ds_read_b64_tr_b16 v[34:35], v5 offset:4736\n"
"v_mfma_f32_16x16x32_bf16 v[148:151], v[36:39], a[148:151], v[148:151]\n"
"v_mfma_f32_16x16x32_bf16 v[152:155], v[36:39], a[152:155], v[152:155]\n"
"v_mfma_f32_16x16x32_bf16 v[156:159], v[36:39], a[156:159], v[156:159]\n"
"s_waitcnt lgkmcnt(4)\n"
"v_mfma_f32_16x16x32_bf16 v[160:163], v[40:43], a[144:147], v[160:163]\n"
"ds_read_b64_tr_b16 v[36:37], v5 offset:32\n"
"ds_read_b64_tr_b16 v[38:39], v5 offset:544\n"
"v_mfma_f32_16x16x32_bf16 v[164:167], v[40:43], a[148:151], v[164:167]\n"
"s_waitcnt vmcnt(34)\n"
"v_accvgpr_read_b32 v44, a160\n"
"v_mfma_f32_16x16x32_bf16 v[168:171], v[40:43], a[152:155], v[168:171]\n"
"v_accvgpr_read_b32 v45, a164\n"
"v_accvgpr_read_b32 v46, a168\n"
"v_mfma_f32_16x16x32_bf16 v[172:175], v[40:43], a[156:159], v[172:175]\n"
"v_accvgpr_read_b32 v47, a172\n"
"v_max3_u32 v44, v44, v45, v46\n"
"v_mfma_f32_16x16x32_bf16 v[176:179], v[24:27], a[144:147], v[176:179]\n"
"ds_read_b64_tr_b16 v[40:41], v5 offset:4256\n"
"ds_read_b64_tr_b16 v[42:43], v5 offset:4768\n"
"v_mfma_f32_16x16x32_bf16 v[180:183], v[24:27], a[148:151], v[180:183]\n"
"v_max_u32_e32 v44, v44, v47\n"
"v_cmp_eq_u32_e32 vcc, -1, v44\n"
"v_mfma_f32_16x16x32_bf16 v[184:187], v[24:27], a[152:155], v[184:187]\n"
"s_add_u32 m0, s17, 0x10800\n"
"s_add_u32 s88, s59, 0x4000\n"
"v_mfma_f32_16x16x32_bf16 v[188:191], v[24:27], a[156:159], v[188:191]\n"
"buffer_load_dwordx4 v0, s[48:51], s88 offen lds\n"
"s_waitcnt lgkmcnt(4)\n"
"s_cbranch_vccnz .LKF_sH12_%=\n"
".LKF_rH12_%=:\n"
"v_mfma_f32_16x16x32_bf16 v[64:67], v[28:31], a[160:163], v[64:67]\n"
"ds_read_b64_tr_b16 v[24:25], v5 offset:64\n"
"ds_read_b64_tr_b16 v[26:27], v5 offset:576\n"
"v_mfma_f32_16x16x32_bf16 v[68:71], v[28:31], a[164:167], v[68:71]\n"
"buffer_load_dwordx4 v0, s[48:51], s88 offen offset:1024 lds\n"
"v_mfma_f32_16x16x32_bf16 v[72:75], v[28:31], a[168:171], v[72:75]\n"
"v_mfma_f32_16x16x32_bf16 v[76:79], v[28:31], a[172:175], v[76:79]\n"
"v_mfma_f32_16x16x32_bf16 v[80:83], v[32:35], a[160:163], v[80:83]\n"
"ds_read_b64_tr_b16 v[28:29], v5 offset:4288\n"
"ds_read_b64_tr_b16 v[30:31], v5 offset:4800\n"
"v_mfma_f32_16x16x32_bf16 v[84:87], v[32:35], a[164:167], v[84:87]\n"
"s_add_u32 m0, s17, 0x12900\n"
"s_add_u32 s88, s59, 0x6000\n"
"v_mfma_f32_16x16x32_bf16 v[88:91], v[32:35], a[168:171], v[88:91]\n"
"buffer_load_dwordx4 v0, s[48:51], s88 offen lds\n"
"v_mfma_f32_16x16x32_bf16 v[92:95], v[32:35], a[172:175], v[92:95]\n"
"s_waitcnt lgkmcnt(4)\n"
"v_mfma_f32_16x16x32_bf16 v[96:99], v[36:39], a[160:163], v[96:99]\n"
"ds_read_b64_tr_b16 v[32:33], v5 offset:96\n"
"ds_read_b64_tr_b16 v[34:35], v5 offset:608\n"
"v_mfma_f32_16x16x32_bf16 v[100:103], v[36:39], a[164:167], v[100:103]\n"
"buffer_load_dwordx4 v0, s[48:51], s88 offen offset:1024 lds\n"
"v_mfma_f32_16x16x32_bf16 v[104:107], v[36:39], a[168:171], v[104:107]\n"
"v_mfma_f32_16x16x32_bf16 v[108:111], v[36:39], a[172:175], v[108:111]\n"
"v_mfma_f32_16x16x32_bf16 v[112:115], v[40:43], a[160:163], v[112:115]\n"
"ds_read_b64_tr_b16 v[36:37], v5 offset:4320\n"
"ds_read_b64_tr_b16 v[38:39], v5 offset:4832\n"
"v_mfma_f32_16x16x32_bf16 v[116:119], v[40:43], a[164:167], v[116:119]\n"
"s_add_u32 m0, s17, 0x14a00\n"
"s_add_u32 s88, s59, 0x8000\n"
"v_mfma_f32_16x16x32_bf16 v[120:123], v[40:43], a[168:171], v[120:123]\n"
"buffer_load_dwordx4 v0, s[48:51], s88 offen lds\n"
"v_mfma_f32_16x16x32_bf16 v[124:127], v[40:43], a[172:175], v[124:127]\n"
"s_waitcnt lgkmcnt(4)\n"
"v_mfma_f32_16x16x32_bf16 v[128:131], v[24:27], a[160:163], v[128:131]\n"
"ds_read_b64_tr_b16 v[40:41], v5 offset:8448\n"
"ds_read_b64_tr_b16 v[42:43], v5 offset:8960\n"
"v_mfma_f32_16x16x32_bf16 v[132:135], v[24:27], a[164:167], v[132:135]\n"
"buffer_load_dwordx4 v0, s[48:51], s88 offen offset:1024 lds\n"
"v_mfma_f32_16x16x32_bf16 v[136:139], v[24:27], a[168:171], v[136:139]\n"
"v_mfma_f32_16x16x32_bf16 v[140:143], v[24:27], a[172:175], v[140:143]\n"
"v_mfma_f32_16x16x32_bf16 v[144:147], v[28:31], a[160:163], v[144:147]\n"
"ds_read_b64_tr_b16 v[24:25], v5 offset:12672\n"
"ds_read_b64_tr_b16 v[26:27], v5 offset:13184\n"
"v_mfma_f32_16x16x32_bf16 v[148:151], v[28:31], a[164:167], v[148:151]\n"
"v_mfma_f32_16x16x32_bf16 v[152:155], v[28:31], a[168:171], v[152:155]\n"
"v_mfma_f32_16x16x32_bf16 v[156:159], v[28:31], a[172:175], v[156:159]\n"
"s_waitcnt lgkmcnt(4)\n"
"v_mfma_f32_16x16x32_bf16 v[160:163], v[32:35], a[160:163], v[160:163]\n"
"ds_read_b64_tr_b16 v[28:29], v5 offset:8480\n"
"ds_read_b64_tr_b16 v[30:31], v5 offset:8992\n"
"v_mfma_f32_16x16x32_bf16 v[164:167], v[32:35], a[164:167], v[164:167]\n"
"s_waitcnt vmcnt(36)\n"
"v_accvgpr_read_b32 v44, a176\n"
"v_mfma_f32_16x16x32_bf16 v[168:171], v[32:35], a[168:171], v[168:171]\n"
"v_accvgpr_read_b32 v45, a180\n"
"v_accvgpr_read_b32 v46, a184\n"
"v_mfma_f32_16x16x32_bf16 v[172:175], v[32:35], a[172:175], v[172:175]\n"
"v_accvgpr_read_b32 v47, a188\n"
"v_max3_u32 v44, v44, v45, v46\n"
"v_mfma_f32_16x16x32_bf16 v[176:179], v[36:39], a[160:163], v[176:179]\n"
"ds_read_b64_tr_b16 v[32:33], v5 offset:12704\n"
"ds_read_b64_tr_b16 v[34:35], v5 offset:13216\n"
"v_mfma_f32_16x16x32_bf16 v[180:183], v[36:39], a[164:167], v[180:183]\n"
"v_max_u32_e32 v44, v44, v47\n"
"v_cmp_eq_u32_e32 vcc, -1, v44\n"
"v_mfma_f32_16x16x32_bf16 v[184:187], v[36:39], a[168:171], v[184:187]\n"
"s_add_u32 m0, s17, 0x16b00\n"
"s_add_u32 s88, s16, 0x80\n"
"v_mfma_f32_16x16x32_bf16 v[188:191], v[36:39], a[172:175], v[188:191]\n"
"buffer_load_dwordx4 v1, s[40:43], s88 offen lds\n"
"s_waitcnt lgkmcnt(4)\n"
"s_cbranch_vccnz .LKF_sH13_%=\n"
".LKF_rH13_%=:\n"
"v_mfma_f32_16x16x32_bf16 v[64:67], v[40:43], a[176:179], v[64:67]\n"
"ds_read_b64_tr_b16 v[36:37], v5 offset:8512\n"
"ds_read_b64_tr_b16 v[38:39], v5 offset:9024\n"
"v_mfma_f32_16x16x32_bf16 v[68:71], v[40:43], a[180:183], v[68:71]\n"
"buffer_load_dwordx4 v2, s[40:43], s88 offen offset:1056 lds\n"
"v_mfma_f32_16x16x32_bf16 v[72:75], v[40:43], a[184:187], v[72:75]\n"
"v_mfma_f32_16x16x32_bf16 v[76:79], v[40:43], a[188:191], v[76:79]\n"
"v_mfma_f32_16x16x32_bf16 v[80:83], v[24:27], a[176:179], v[80:83]\n"
"ds_read_b64_tr_b16 v[40:41], v5 offset:12736\n"
"ds_read_b64_tr_b16 v[42:43], v5 offset:13248\n"
"v_mfma_f32_16x16x32_bf16 v[84:87], v[24:27], a[180:183], v[84:87]\n"
"s_add_u32 m0, s17, 0x18c00\n"
"s_add_u32 s88, s16, 0x48080\n"
"v_mfma_f32_16x16x32_bf16 v[88:91], v[24:27], a[184:187], v[88:91]\n"
"buffer_load_dwordx4 v1, s[40:43], s88 offen lds\n"
"v_mfma_f32_16x16x32_bf16 v[92:95], v[24:27], a[188:191], v[92:95]\n"
"s_waitcnt lgkmcnt(4)\n"
"v_mfma_f32_16x16x32_bf16 v[96:99], v[28:31], a[176:179], v[96:99]\n"
"ds_read_b64_tr_b16 v[24:25], v5 offset:8544\n"
"ds_read_b64_tr_b16 v[26:27], v5 offset:9056\n"
"v_mfma_f32_16x16x32_bf16 v[100:103], v[28:31], a[180:183], v[100:103]\n"
"buffer_load_dwordx4 v2, s[40:43], s88 offen offset:1056 lds\n"
"v_mfma_f32_16x16x32_bf16 v[104:107], v[28:31], a[184:187], v[104:107]\n"
"v_mfma_f32_16x16x32_bf16 v[108:111], v[28:31], a[188:191], v[108:111]\n"
"v_mfma_f32_16x16x32_bf16 v[112:115], v[32:35], a[176:179], v[112:115]\n"
"ds_read_b64_tr_b16 v[28:29], v5 offset:12768\n"
"ds_read_b64_tr_b16 v[30:31], v5 offset:13280\n"
"v_mfma_f32_16x16x32_bf16 v[116:119], v[32:35], a[180:183], v[116:119]\n"
"s_add_u32 m0, s17, 0x1ad00\n"
"s_add_u32 s88, s16, 0x90080\n"
"v_mfma_f32_16x16x32_bf16 v[120:123], v[32:35], a[184:187], v[120:123]\n"
"buffer_load_dwordx4 v1, s[40:43], s88 offen lds\n"
"v_mfma_f32_16x16x32_bf16 v[124:127], v[32:35], a[188:191], v[124:127]\n"
"s_waitcnt vmcnt(33) lgkmcnt(4)\n"
"s_barrier\n"
"v_mfma_f32_16x16x32_bf16 v[128:131], v[36:39], a[176:179], v[128:131]\n"
"ds_read_b64_tr_b16 v[32:33], v5 offset:16896\n"
"ds_read_b64_tr_b16 v[34:35], v5 offset:17408\n"
"v_mfma_f32_16x16x32_bf16 v[132:135], v[36:39], a[180:183], v[132:135]\n"
"s_mov_b64 exec, 0xff\n"
"buffer_load_dword v22, v15, s[44:47], s84 offen offset:0\n"
"s_mov_b64 exec, -1\n"
"v_mfma_f32_16x16x32_bf16 v[136:139], v[36:39], a[184:187], v[136:139]\n"
"buffer_load_dwordx4 v2, s[40:43], s88 offen offset:1056 lds\n"
"v_mfma_f32_16x16x32_bf16 v[140:143], v[36:39], a[188:191], v[140:143]\n"
"v_mfma_f32_16x16x32_bf16 v[144:147], v[40:43], a[176:179], v[144:147]\n"
"ds_read_b64_tr_b16 v[36:37], v5 offset:21120\n"
"ds_read_b64_tr_b16 v[38:39], v5 offset:21632\n"
"v_mfma_f32_16x16x32_bf16 v[148:151], v[40:43], a[180:183], v[148:151]\n"
"s_add_u32 s84, s84, 0x480\n"
"v_mfma_f32_16x16x32_bf16 v[152:155], v[40:43], a[184:187], v[152:155]\n"
"v_mfma_f32_16x16x32_bf16 v[156:159], v[40:43], a[188:191], v[156:159]\n"
"s_waitcnt lgkmcnt(4)\n"
"v_mfma_f32_16x16x32_bf16 v[160:163], v[24:27], a[176:179], v[160:163]\n"
"ds_read_b64_tr_b16 v[40:41], v5 offset:16928\n"
"ds_read_b64_tr_b16 v[42:43], v5 offset:17440\n"
"v_mfma_f32_16x16x32_bf16 v[164:167], v[24:27], a[180:183], v[164:167]\n"
"s_waitcnt vmcnt(30)\n"
"v_accvgpr_read_b32 v44, a192\n"
"v_mfma_f32_16x16x32_bf16 v[168:171], v[24:27], a[184:187], v[168:171]\n"
"v_accvgpr_read_b32 v45, a196\n"
"v_accvgpr_read_b32 v46, a200\n"
"v_mfma_f32_16x16x32_bf16 v[172:175], v[24:27], a[188:191], v[172:175]\n"
"v_accvgpr_read_b32 v47, a204\n"
"v_max3_u32 v44, v44, v45, v46\n"
"v_mfma_f32_16x16x32_bf16 v[176:179], v[28:31], a[176:179], v[176:179]\n"
"ds_read_b64_tr_b16 v[24:25], v5 offset:21152\n"
"ds_read_b64_tr_b16 v[26:27], v5 offset:21664\n"
"v_mfma_f32_16x16x32_bf16 v[180:183], v[28:31], a[180:183], v[180:183]\n"
"v_max_u32_e32 v44, v44, v47\n"
"v_cmp_eq_u32_e32 vcc, -1, v44\n"
"v_mfma_f32_16x16x32_bf16 v[184:187], v[28:31], a[184:187], v[184:187]\n"
"s_add_u32 m0, s17, 0x1ce00\n"
"s_add_u32 s88, s16, 0xd8080\n"
"v_mfma_f32_16x16x32_bf16 v[188:191], v[28:31], a[188:191], v[188:191]\n"
"buffer_load_dwordx4 v1, s[40:43], s88 offen lds\n"
"s_waitcnt lgkmcnt(4)\n"
"s_cbranch_vccnz .LKF_sH14_%=\n"
".LKF_rH14_%=:\n"
"v_mfma_f32_16x16x32_bf16 v[64:67], v[32:35], a[192:195], v[64:67]\n"
"ds_read_b64_tr_b16 v[28:29], v5 offset:25344\n"
"ds_read_b64_tr_b16 v[30:31], v5 offset:25856\n"
"v_mfma_f32_16x16x32_bf16 v[68:71], v[32:35], a[196:199], v[68:71]\n"
"buffer_load_dwordx4 v2, s[40:43], s88 offen offset:1056 lds\n"
"v_mfma_f32_16x16x32_bf16 v[72:75], v[32:35], a[200:203], v[72:75]\n"
"v_mfma_f32_16x16x32_bf16 v[76:79], v[32:35], a[204:207], v[76:79]\n"
"v_mfma_f32_16x16x32_bf16 v[80:83], v[36:39], a[192:195], v[80:83]\n"
"ds_read_b64_tr_b16 v[32:33], v5 offset:29568\n"
"ds_read_b64_tr_b16 v[34:35], v5 offset:30080\n"
"v_mfma_f32_16x16x32_bf16 v[84:87], v[36:39], a[196:199], v[84:87]\n"
"s_add_u32 m0, s17, 0x1ef00\n"
"s_add_u32 s88, s16, 0x120080\n"
"v_mfma_f32_16x16x32_bf16 v[88:91], v[36:39], a[200:203], v[88:91]\n"
"buffer_load_dwordx4 v1, s[40:43], s88 offen lds\n"
"v_mfma_f32_16x16x32_bf16 v[92:95], v[36:39], a[204:207], v[92:95]\n"
"s_waitcnt lgkmcnt(4)\n"
"v_mfma_f32_16x16x32_bf16 v[96:99], v[40:43], a[192:195], v[96:99]\n"
"ds_read_b64_tr_b16 v[36:37], v5 offset:25376\n"
"ds_read_b64_tr_b16 v[38:39], v5 offset:25888\n"
"v_mfma_f32_16x16x32_bf16 v[100:103], v[40:43], a[196:199], v[100:103]\n"
"s_waitcnt vmcnt(29)\n"
"v_accvgpr_read_b32 v44, a208\n"
"v_mfma_f32_16x16x32_bf16 v[104:107], v[40:43], a[200:203], v[104:107]\n"
"v_accvgpr_read_b32 v45, a212\n"
"v_accvgpr_read_b32 v46, a216\n"
"v_mfma_f32_16x16x32_bf16 v[108:111], v[40:43], a[204:207], v[108:111]\n"
"v_accvgpr_read_b32 v47, a220\n"
"v_max3_u32 v44, v44, v45, v46\n"
"v_mfma_f32_16x16x32_bf16 v[112:115], v[24:27], a[192:195], v[112:115]\n"
"ds_read_b64_tr_b16 v[40:41], v5 offset:29600\n"
"ds_read_b64_tr_b16 v[42:43], v5 offset:30112\n"
"v_mfma_f32_16x16x32_bf16 v[116:119], v[24:27], a[196:199], v[116:119]\n"
"v_max_u32_e32 v44, v44, v47\n"
"v_cmp_eq_u32_e32 vcc, -1, v44\n"
"v_mfma_f32_16x16x32_bf16 v[120:123], v[24:27], a[200:203], v[120:123]\n"
"buffer_load_dwordx4 v2, s[40:43], s88 offen offset:1056 lds\n"
"v_mfma_f32_16x16x32_bf16 v[124:127], v[24:27], a[204:207], v[124:127]\n"
"s_waitcnt lgkmcnt(4)\n"
"s_cbranch_vccnz .LKF_sH15_%=\n"
".LKF_rH15_%=:\n"
"v_mfma_f32_16x16x32_bf16 v[64:67], v[28:31], a[208:211], v[64:67]\n"
"ds_read_b64_tr_b16 v[24:25], v5 offset:33792\n"
"ds_read_b64_tr_b16 v[26:27], v5 offset:34304\n"
"v_mfma_f32_16x16x32_bf16 v[68:71], v[28:31], a[212:215], v[68:71]\n"
"s_add_u32 m0, s17, 0x21000\n"
"s_add_u32 s88, s16, 0x168080\n"
"v_mfma_f32_16x16x32_bf16 v[72:75], v[28:31], a[216:219], v[72:75]\n"
"buffer_load_dwordx4 v1, s[40:43], s88 offen lds\n"
"v_mfma_f32_16x16x32_bf16 v[76:79], v[28:31], a[220:223], v[76:79]\n"
"v_mfma_f32_16x16x32_bf16 v[80:83], v[32:35], a[208:211], v[80:83]\n"
"ds_read_b64_tr_b16 v[28:29], v5 offset:38016\n"
"ds_read_b64_tr_b16 v[30:31], v5 offset:38528\n"
"v_mfma_f32_16x16x32_bf16 v[84:87], v[32:35], a[212:215], v[84:87]\n"
"s_waitcnt vmcnt(27)\n"
"v_accvgpr_read_b32 v44, a224\n"
"v_mfma_f32_16x16x32_bf16 v[88:91], v[32:35], a[216:219], v[88:91]\n"
"v_accvgpr_read_b32 v45, a228\n"
"v_accvgpr_read_b32 v46, a232\n"
"v_mfma_f32_16x16x32_bf16 v[92:95], v[32:35], a[220:223], v[92:95]\n"
"v_accvgpr_read_b32 v47, a236\n"
"s_waitcnt lgkmcnt(4)\n"
"v_mfma_f32_16x16x32_bf16 v[96:99], v[36:39], a[208:211], v[96:99]\n"
"ds_read_b64_tr_b16 v[32:33], v5 offset:33824\n"
"ds_read_b64_tr_b16 v[34:35], v5 offset:34336\n"
"v_mfma_f32_16x16x32_bf16 v[100:103], v[36:39], a[212:215], v[100:103]\n"
"v_accvgpr_read_b32 v48, a226\n"
"v_accvgpr_read_b32 v49, a230\n"
"v_mfma_f32_16x16x32_bf16 v[104:107], v[36:39], a[216:219], v[104:107]\n"
"v_accvgpr_read_b32 v50, a234\n"
"v_accvgpr_read_b32 v51, a238\n"
"v_mfma_f32_16x16x32_bf16 v[108:111], v[36:39], a[220:223], v[108:111]\n"
"v_max3_u32 v44, v44, v48, v49\n"
"v_max3_u32 v45, v45, v50, v51\n"
"v_mfma_f32_16x16x32_bf16 v[112:115], v[40:43], a[208:211], v[112:115]\n"
"ds_read_b64_tr_b16 v[36:37], v5 offset:38048\n"
"ds_read_b64_tr_b16 v[38:39], v5 offset:38560\n"
"v_mfma_f32_16x16x32_bf16 v[116:119], v[40:43], a[212:215], v[116:119]\n"
"v_max3_u32 v44, v44, v45, v46\n"
"v_max_u32_e32 v44, v44, v47\n"
"v_mfma_f32_16x16x32_bf16 v[120:123], v[40:43], a[216:219], v[120:123]\n"
"v_cmp_eq_u32_e32 vcc, -1, v44\n"
"buffer_load_dwordx4 v2, s[40:43], s88 offen offset:1056 lds\n"
"v_mfma_f32_16x16x32_bf16 v[124:127], v[40:43], a[220:223], v[124:127]\n"
"s_waitcnt lgkmcnt(4)\n"
"s_cbranch_vccnz .LKF_sH16_%=\n"
".LKF_rH16_%=:\n"
"v_mfma_f32_16x16x32_bf16 v[64:67], v[24:27], a[224:227], v[64:67]\n"
"ds_read_b64_tr_b16 v[40:41], v5 offset:42240\n"
"ds_read_b64_tr_b16 v[42:43], v5 offset:42752\n"
"v_mfma_f32_16x16x32_bf16 v[68:71], v[24:27], a[228:231], v[68:71]\n"
"s_add_u32 m0, s17, 0x23100\n"
"s_add_u32 s88, s16, 0x1b0080\n"
"v_mfma_f32_16x16x32_bf16 v[72:75], v[24:27], a[232:235], v[72:75]\n"
"buffer_load_dwordx4 v1, s[40:43], s88 offen lds\n"
"v_mfma_f32_16x16x32_bf16 v[76:79], v[24:27], a[236:239], v[76:79]\n"
"v_mfma_f32_16x16x32_bf16 v[80:83], v[28:31], a[224:227], v[80:83]\n"
"ds_read_b64_tr_b16 v[24:25], v5 offset:46464\n"
"ds_read_b64_tr_b16 v[26:27], v5 offset:46976\n"
"v_mfma_f32_16x16x32_bf16 v[84:87], v[28:31], a[228:231], v[84:87]\n"
"s_waitcnt vmcnt(24)\n"
"v_accvgpr_read_b32 v44, a240\n"
"v_mfma_f32_16x16x32_bf16 v[88:91], v[28:31], a[232:235], v[88:91]\n"
"v_accvgpr_read_b32 v45, a244\n"
"v_accvgpr_read_b32 v46, a248\n"
"v_mfma_f32_16x16x32_bf16 v[92:95], v[28:31], a[236:239], v[92:95]\n"
"v_accvgpr_read_b32 v47, a252\n"
"s_waitcnt lgkmcnt(4)\n"
"v_mfma_f32_16x16x32_bf16 v[96:99], v[32:35], a[224:227], v[96:99]\n"
"ds_read_b64_tr_b16 v[28:29], v5 offset:42272\n"
"ds_read_b64_tr_b16 v[30:31], v5 offset:42784\n"
"v_mfma_f32_16x16x32_bf16 v[100:103], v[32:35], a[228:231], v[100:103]\n"
"v_accvgpr_read_b32 v48, a242\n"
"v_accvgpr_read_b32 v49, a246\n"
"v_mfma_f32_16x16x32_bf16 v[104:107], v[32:35], a[232:235], v[104:107]\n"
"v_accvgpr_read_b32 v50, a250\n"
"v_accvgpr_read_b32 v51, a254\n"
"v_mfma_f32_16x16x32_bf16 v[108:111], v[32:35], a[236:239], v[108:111]\n"
"v_max3_u32 v44, v44, v48, v49\n"
"v_max3_u32 v45, v45, v50, v51\n"
"v_mfma_f32_16x16x32_bf16 v[112:115], v[36:39], a[224:227], v[112:115]\n"
"ds_read_b64_tr_b16 v[32:33], v5 offset:46496\n"
"ds_read_b64_tr_b16 v[34:35], v5 offset:47008\n"
"v_mfma_f32_16x16x32_bf16 v[116:119], v[36:39], a[228:231], v[116:119]\n"
"v_max3_u32 v44, v44, v45, v46\n"
"v_max_u32_e32 v44, v44, v47\n"
"v_mfma_f32_16x16x32_bf16 v[120:123], v[36:39], a[232:235], v[120:123]\n"
"v_cmp_eq_u32_e32 vcc, -1, v44\n"
"buffer_load_dwordx4 v2, s[40:43], s88 offen offset:1056 lds\n"
"v_mfma_f32_16x16x32_bf16 v[124:127], v[36:39], a[236:239], v[124:127]\n"
"s_waitcnt lgkmcnt(4)\n"
"s_barrier\n"
"s_cbranch_vccnz .LKF_sH17_%=\n"
".LKF_rH17_%=:\n"
"v_mfma_f32_16x16x32_bf16 v[64:67], v[40:43], a[240:243], v[64:67]\n"
"ds_read_b64_tr_b16 v[36:37], v5 offset:16960\n"
"ds_read_b64_tr_b16 v[38:39], v5 offset:17472\n"
"v_mfma_f32_16x16x32_bf16 v[68:71], v[40:43], a[244:247], v[68:71]\n"
"s_add_u32 m0, s17, 0x0\n"
"s_add_u32 s88, s16, 0x1f8080\n"
"v_mfma_f32_16x16x32_bf16 v[72:75], v[40:43], a[248:251], v[72:75]\n"
"buffer_load_dwordx4 v1, s[40:43], s88 offen lds\n"
"v_mfma_f32_16x16x32_bf16 v[76:79], v[40:43], a[252:255], v[76:79]\n"
"v_mfma_f32_16x16x32_bf16 v[80:83], v[24:27], a[240:243], v[80:83]\n"
"ds_read_b64_tr_b16 v[40:41], v5 offset:21184\n"
"ds_read_b64_tr_b16 v[42:43], v5 offset:21696\n"
"v_mfma_f32_16x16x32_bf16 v[84:87], v[24:27], a[244:247], v[84:87]\n"
"buffer_load_dwordx4 v2, s[40:43], s88 offen offset:1056 lds\n"
"v_mfma_f32_16x16x32_bf16 v[88:91], v[24:27], a[248:251], v[88:91]\n"
"v_mfma_f32_16x16x32_bf16 v[92:95], v[24:27], a[252:255], v[92:95]\n"
"s_waitcnt lgkmcnt(4)\n"
"v_mfma_f32_16x16x32_bf16 v[96:99], v[28:31], a[240:243], v[96:99]\n"
"ds_read_b64_tr_b16 v[24:25], v5 offset:16992\n"
"ds_read_b64_tr_b16 v[26:27], v5 offset:17504\n"
"v_mfma_f32_16x16x32_bf16 v[100:103], v[28:31], a[244:247], v[100:103]\n"
"s_add_u32 m0, s17, 0x25200\n"
"s_add_u32 s88, s16, 0x240080\n"
"v_mfma_f32_16x16x32_bf16 v[104:107], v[28:31], a[248:251], v[104:107]\n"
"buffer_load_dwordx4 v1, s[40:43], s88 offen lds\n"
"v_mfma_f32_16x16x32_bf16 v[108:111], v[28:31], a[252:255], v[108:111]\n"
"v_mfma_f32_16x16x32_bf16 v[112:115], v[32:35], a[240:243], v[112:115]\n"
"ds_read_b64_tr_b16 v[28:29], v5 offset:21216\n"
"ds_read_b64_tr_b16 v[30:31], v5 offset:21728\n"
"v_mfma_f32_16x16x32_bf16 v[116:119], v[32:35], a[244:247], v[116:119]\n"
"buffer_load_dwordx4 v2, s[40:43], s88 offen offset:1056 lds\n"
"v_mul_f32_e32 v80, v64, v80\n"
"v_mfma_f32_16x16x32_bf16 v[120:123], v[32:35], a[248:251], v[120:123]\n"
"v_exp_f32_e64 v64, -v64\n"
"v_mfma_f32_16x16x32_bf16 v[124:127], v[32:35], a[252:255], v[124:127]\n"
"v_fma_f32 v64, v64, s94, s94\n"
"s_waitcnt lgkmcnt(4)\n"
"v_mfma_f32_16x16x32_bf16 v[128:131], v[36:39], a[192:195], v[128:131]\n"
"ds_read_b64_tr_b16 v[32:33], v5 offset:25408\n"
"ds_read_b64_tr_b16 v[34:35], v5 offset:25920\n"
"v_mfma_f32_16x16x32_bf16 v[132:135], v[36:39], a[196:199], v[132:135]\n"
"s_mov_b64 exec, 0xff\n"
"buffer_load_dword v22, v15, s[44:47], s84 offen offset:0\n"
"s_mov_b64 exec, -1\n"
"v_mfma_f32_16x16x32_bf16 v[136:139], v[36:39], a[200:203], v[136:139]\n"
"s_add_u32 m0, s17, 0x2100\n"
"s_add_u32 s88, s16, 0x288080\n"
"v_mfma_f32_16x16x32_bf16 v[140:143], v[36:39], a[204:207], v[140:143]\n"
"buffer_load_dwordx4 v1, s[40:43], s88 offen lds\n"
"v_mul_f32_e32 v81, v65, v81\n"
"v_mfma_f32_16x16x32_bf16 v[144:147], v[40:43], a[192:195], v[144:147]\n"
"ds_read_b64_tr_b16 v[36:37], v5 offset:29632\n"
"ds_read_b64_tr_b16 v[38:39], v5 offset:30144\n"
"v_mfma_f32_16x16x32_bf16 v[148:151], v[40:43], a[196:199], v[148:151]\n"
"v_rcp_f32_e32 v64, v64\n"
"v_mfma_f32_16x16x32_bf16 v[152:155], v[40:43], a[200:203], v[152:155]\n"
"v_exp_f32_e64 v65, -v65\n"
"v_mfma_f32_16x16x32_bf16 v[156:159], v[40:43], a[204:207], v[156:159]\n"
"s_add_u32 s84, s84, 0x480\n"
"s_waitcnt lgkmcnt(4)\n"
"v_mfma_f32_16x16x32_bf16 v[160:163], v[24:27], a[192:195], v[160:163]\n"
"ds_read_b64_tr_b16 v[40:41], v5 offset:25440\n"
"ds_read_b64_tr_b16 v[42:43], v5 offset:25952\n"
"v_mfma_f32_16x16x32_bf16 v[164:167], v[24:27], a[196:199], v[164:167]\n"
"buffer_load_dwordx4 v2, s[40:43], s88 offen offset:1056 lds\n"
"v_mul_f32_e32 v80, v80, v64\n"
"v_mfma_f32_16x16x32_bf16 v[168:171], v[24:27], a[200:203], v[168:171]\n"
"v_fma_f32 v65, v65, s94, s94\n"
"v_mul_f32_e32 v82, v66, v82\n"
"v_mfma_f32_16x16x32_bf16 v[172:175], v[24:27], a[204:207], v[172:175]\n"
"v_rcp_f32_e32 v65, v65\n"
"v_mfma_f32_16x16x32_bf16 v[176:179], v[28:31], a[192:195], v[176:179]\n"
"ds_read_b64_tr_b16 v[24:25], v5 offset:29664\n"
"ds_read_b64_tr_b16 v[26:27], v5 offset:30176\n"
"v_mfma_f32_16x16x32_bf16 v[180:183], v[28:31], a[196:199], v[180:183]\n"
"v_exp_f32_e64 v66, -v66\n"
"v_mfma_f32_16x16x32_bf16 v[184:187], v[28:31], a[200:203], v[184:187]\n"
"v_mul_f32_e32 v81, v81, v65\n"
"v_fma_f32 v66, v66, s94, s94\n"
"v_mfma_f32_16x16x32_bf16 v[188:191], v[28:31], a[204:207], v[188:191]\n"
"v_cvt_pk_bf16_f32 v44, v80, v81\n"
"s_waitcnt lgkmcnt(4)\n"
"v_mfma_f32_16x16x32_bf16 v[128:131], v[32:35], a[208:211], v[128:131]\n"
"ds_read_b64_tr_b16 v[28:29], v5 offset:33856\n"
"ds_read_b64_tr_b16 v[30:31], v5 offset:34368\n"
"v_mfma_f32_16x16x32_bf16 v[132:135], v[32:35], a[212:215], v[132:135]\n"
"v_rcp_f32_e32 v66, v66\n"
"v_mfma_f32_16x16x32_bf16 v[136:139], v[32:35], a[216:219], v[136:139]\n"
"v_mul_f32_e32 v82, v82, v66\n"
"v_mul_f32_e32 v83, v67, v83\n"
"v_mfma_f32_16x16x32_bf16 v[140:143], v[32:35], a[220:223], v[140:143]\n"
"v_exp_f32_e64 v67, -v67\n"
"v_mfma_f32_16x16x32_bf16 v[144:147], v[36:39], a[208:211], v[144:147]\n"
"ds_read_b64_tr_b16 v[32:33], v5 offset:38080\n"
"ds_read_b64_tr_b16 v[34:35], v5 offset:38592\n"
"v_mfma_f32_16x16x32_bf16 v[148:151], v[36:39], a[212:215], v[148:151]\n"
"v_fma_f32 v67, v67, s94, s94\n"
"v_mul_f32_e32 v84, v68, v84\n"
"v_mfma_f32_16x16x32_bf16 v[152:155], v[36:39], a[216:219], v[152:155]\n"
"v_rcp_f32_e32 v67, v67\n"
"v_mfma_f32_16x16x32_bf16 v[156:159], v[36:39], a[220:223], v[156:159]\n"
"v_mul_f32_e32 v83, v83, v67\n"
"s_waitcnt lgkmcnt(4)\n"
"v_mfma_f32_16x16x32_bf16 v[160:163], v[40:43], a[208:211], v[160:163]\n"
"ds_read_b64_tr_b16 v[36:37], v5 offset:33888\n"
"ds_read_b64_tr_b16 v[38:39], v5 offset:34400\n"
"v_mfma_f32_16x16x32_bf16 v[164:167], v[40:43], a[212:215], v[164:167]\n"
"v_exp_f32_e64 v68, -v68\n"
"v_mfma_f32_16x16x32_bf16 v[168:171], v[40:43], a[216:219], v[168:171]\n"
"v_cvt_pk_bf16_f32 v45, v82, v83\n"
"v_fma_f32 v68, v68, s94, s94\n"
"v_mfma_f32_16x16x32_bf16 v[172:175], v[40:43], a[220:223], v[172:175]\n"
"v_rcp_f32_e32 v68, v68\n"
"v_mfma_f32_16x16x32_bf16 v[176:179], v[24:27], a[208:211], v[176:179]\n"
"ds_read_b64_tr_b16 v[40:41], v5 offset:38112\n"
"ds_read_b64_tr_b16 v[42:43], v5 offset:38624\n"
"v_mfma_f32_16x16x32_bf16 v[180:183], v[24:27], a[212:215], v[180:183]\n"
"v_mul_f32_e32 v84, v84, v68\n"
"v_mul_f32_e32 v85, v69, v85\n"
"v_mfma_f32_16x16x32_bf16 v[184:187], v[24:27], a[216:219], v[184:187]\n"
"v_exp_f32_e64 v69, -v69\n"
"v_mfma_f32_16x16x32_bf16 v[188:191], v[24:27], a[220:223], v[188:191]\n"
"v_fma_f32 v69, v69, s94, s94\n"
"s_waitcnt lgkmcnt(4)\n"
"v_mfma_f32_16x16x32_bf16 v[128:131], v[28:31], a[224:227], v[128:131]\n"
"ds_read_b64_tr_b16 v[24:25], v5 offset:42304\n"
"ds_read_b64_tr_b16 v[26:27], v5 offset:42816\n"
"v_mfma_f32_16x16x32_bf16 v[132:135], v[28:31], a[228:231], v[132:135]\n"
"v_rcp_f32_e32 v69, v69\n"
"v_mfma_f32_16x16x32_bf16 v[136:139], v[28:31], a[232:235], v[136:139]\n"
"v_mul_f32_e32 v85, v85, v69\n"
"v_mul_f32_e32 v86, v70, v86\n"
"v_mfma_f32_16x16x32_bf16 v[140:143], v[28:31], a[236:239], v[140:143]\n"
"v_exp_f32_e64 v70, -v70\n"
"v_mfma_f32_16x16x32_bf16 v[144:147], v[32:35], a[224:227], v[144:147]\n"
"ds_read_b64_tr_b16 v[28:29], v5 offset:46528\n"
"ds_read_b64_tr_b16 v[30:31], v5 offset:47040\n"
"v_mfma_f32_16x16x32_bf16 v[148:151], v[32:35], a[228:231], v[148:151]\n"
"v_cvt_pk_bf16_f32 v48, v84, v85\n"
"v_fma_f32 v70, v70, s94, s94\n"
"v_mfma_f32_16x16x32_bf16 v[152:155], v[32:35], a[232:235], v[152:155]\n"
"v_rcp_f32_e32 v70, v70\n"
"v_mfma_f32_16x16x32_bf16 v[156:159], v[32:35], a[236:239], v[156:159]\n"
"v_mul_f32_e32 v86, v86, v70\n"
"s_waitcnt lgkmcnt(4)\n"
"v_mfma_f32_16x16x32_bf16 v[160:163], v[36:39], a[224:227], v[160:163]\n"
"ds_read_b64_tr_b16 v[32:33], v5 offset:42336\n"
"ds_read_b64_tr_b16 v[34:35], v5 offset:42848\n"
"v_mfma_f32_16x16x32_bf16 v[164:167], v[36:39], a[228:231], v[164:167]\n"
"v_mul_f32_e32 v87, v71, v87\n"
"v_mul_f32_e32 v88, v72, v88\n"
"v_mfma_f32_16x16x32_bf16 v[168:171], v[36:39], a[232:235], v[168:171]\n"
"v_exp_f32_e64 v71, -v71\n"
"v_mfma_f32_16x16x32_bf16 v[172:175], v[36:39], a[236:239], v[172:175]\n"
"v_exp_f32_e64 v72, -v72\n"
"v_mfma_f32_16x16x32_bf16 v[176:179], v[40:43], a[224:227], v[176:179]\n"
"ds_read_b64_tr_b16 v[36:37], v5 offset:46560\n"
"ds_read_b64_tr_b16 v[38:39], v5 offset:47072\n"
"v_mfma_f32_16x16x32_bf16 v[180:183], v[40:43], a[228:231], v[180:183]\n"
"v_fma_f32 v71, v71, s94, s94\n"
"v_fma_f32 v72, v72, s94, s94\n"
"v_mfma_f32_16x16x32_bf16 v[184:187], v[40:43], a[232:235], v[184:187]\n"
"v_rcp_f32_e32 v71, v71\n"
"v_mfma_f32_16x16x32_bf16 v[188:191], v[40:43], a[236:239], v[188:191]\n"
"v_mul_f32_e32 v87, v87, v71\n"
"s_waitcnt vmcnt(28) lgkmcnt(4)\n"
"s_barrier\n"
"v_mfma_f32_16x16x32_bf16 v[128:131], v[24:27], a[240:243], v[128:131]\n"
"ds_read_b128 v[40:43], v8 offset:50688\n"
"s_add_u32 m0, s17, 0x4200\n"
"v_mfma_f32_16x16x32_bf16 v[132:135], v[24:27], a[244:247], v[132:135]\n"
"s_add_u32 s88, s16, 0x2d0080\n"
"buffer_load_dwordx4 v1, s[40:43], s88 offen lds\n"
"v_mfma_f32_16x16x32_bf16 v[136:139], v[24:27], a[248:251], v[136:139]\n"
"v_rcp_f32_e32 v72, v72\n"
"v_mfma_f32_16x16x32_bf16 v[140:143], v[24:27], a[252:255], v[140:143]\n"
"v_cvt_pk_bf16_f32 v49, v86, v87\n"
"v_mul_f32_e32 v88, v88, v72\n"
"v_mfma_f32_16x16x32_bf16 v[144:147], v[28:31], a[240:243], v[144:147]\n"
"ds_read_b128 v[24:27], v8 offset:51712\n"
"buffer_load_dwordx4 v2, s[40:43], s88 offen offset:1056 lds\n"
"v_mfma_f32_16x16x32_bf16 v[148:151], v[28:31], a[244:247], v[148:151]\n"
"v_mul_f32_e32 v89, v73, v89\n"
"v_mul_f32_e32 v90, v74, v90\n"
"v_mfma_f32_16x16x32_bf16 v[152:155], v[28:31], a[248:251], v[152:155]\n"
"v_exp_f32_e64 v73, -v73\n"
"v_mfma_f32_16x16x32_bf16 v[156:159], v[28:31], a[252:255], v[156:159]\n"
"v_fma_f32 v73, v73, s94, s94\n"
"s_waitcnt lgkmcnt(2)\n"
"v_mfma_f32_16x16x32_bf16 v[160:163], v[32:35], a[240:243], v[160:163]\n"
"ds_read_b128 v[28:31], v8 offset:52800\n"
"s_add_u32 m0, s17, 0x6300\n"
"v_mfma_f32_16x16x32_bf16 v[164:167], v[32:35], a[244:247], v[164:167]\n"
"s_add_u32 s88, s16, 0x318080\n"
"buffer_load_dwordx4 v1, s[40:43], s88 offen lds\n"
"v_mfma_f32_16x16x32_bf16 v[168:171], v[32:35], a[248:251], v[168:171]\n"
"v_rcp_f32_e32 v73, v73\n"
"v_mfma_f32_16x16x32_bf16 v[172:175], v[32:35], a[252:255], v[172:175]\n"
"v_exp_f32_e64 v74, -v74\n"
"v_mfma_f32_16x16x32_bf16 v[176:179], v[36:39], a[240:243], v[176:179]\n"
"ds_read_b128 v[32:35], v8 offset:53824\n"
"buffer_load_dwordx4 v2, s[40:43], s88 offen offset:1056 lds\n"
"v_mfma_f32_16x16x32_bf16 v[180:183], v[36:39], a[244:247], v[180:183]\n"
"v_mul_f32_e32 v89, v89, v73\n"
"v_fma_f32 v74, v74, s94, s94\n"
".LKF_pad3:\n"
"s_cbranch_execz .LKF_pad7\n"
"v_mfma_f32_16x16x32_bf16 v[184:187], v[36:39], a[248:251], v[184:187]\n"
"v_rcp_f32_e32 v74, v74\n"
"v_mfma_f32_16x16x32_bf16 v[188:191], v[36:39], a[252:255], v[188:191]\n"
"v_cvt_pk_bf16_f32 v52, v88, v89\n"
"s_waitcnt lgkmcnt(2)\n"
"v_mfma_f32_16x16x32_bf16 v[192:195], v[40:43], v[224:227], 0\n"
"ds_read_b128 v[36:39], v8 offset:54912\n"
"s_add_u32 m0, s17, 0x8400\n"
"v_mfma_f32_16x16x32_bf16 v[196:199], v[40:43], v[228:231], 0\n"
"s_add_u32 s88, s16, 0x360080\n"
"buffer_load_dwordx4 v1, s[40:43], s88 offen lds\n"
"v_mfma_f32_16x16x32_bf16 v[200:203], v[40:43], v[232:235], 0\n"
"v_mul_f32_e32 v90, v90, v74\n"
"v_mul_f32_e32 v91, v75, v91\n"
"v_mfma_f32_16x16x32_bf16 v[204:207], v[40:43], v[236:239], 0\n"
"v_exp_f32_e64 v75, -v75\n"
"v_mfma_f32_16x16x32_bf16 v[208:211], v[24:27], v[224:227], 0\n"
"ds_read_b128 v[40:43], v8 offset:55936\n"
"v_fma_f32 v75, v75, s94, s94\n"
"v_mfma_f32_16x16x32_bf16 v[212:215], v[24:27], v[228:231], 0\n"
"v_rcp_f32_e32 v75, v75\n"
"v_mfma_f32_16x16x32_bf16 v[216:219], v[24:27], v[232:235], 0\n"
"v_mul_f32_e32 v91, v91, v75\n"
"v_mul_f32_e32 v92, v76, v92\n"
"v_mfma_f32_16x16x32_bf16 v[220:223], v[24:27], v[236:239], 0\n"
"v_cvt_pk_bf16_f32 v53, v90, v91\n"
"s_waitcnt lgkmcnt(2)\n"
"v_mfma_f32_16x16x32_bf16 v[192:195], v[28:31], v[240:243], v[192:195]\n"
"ds_read_b128 v[24:27], v8 offset:57024\n"
"buffer_load_dwordx4 v2, s[40:43], s88 offen offset:1056 lds\n"
"v_mfma_f32_16x16x32_bf16 v[196:199], v[28:31], v[244:247], v[196:199]\n"
"v_exp_f32_e64 v76, -v76\n"
"v_mfma_f32_16x16x32_bf16 v[200:203], v[28:31], v[248:251], v[200:203]\n"
"v_fma_f32 v76, v76, s94, s94\n"
"v_mul_f32_e32 v93, v77, v93\n"
"v_mfma_f32_16x16x32_bf16 v[204:207], v[28:31], v[252:255], v[204:207]\n"
"v_rcp_f32_e32 v76, v76\n"
"v_mfma_f32_16x16x32_bf16 v[208:211], v[32:35], v[240:243], v[208:211]\n"
"ds_read_b128 v[28:31], v8 offset:58048\n"
"v_mul_f32_e32 v92, v92, v76\n"
"v_mfma_f32_16x16x32_bf16 v[212:215], v[32:35], v[244:247], v[212:215]\n"
"v_exp_f32_e64 v77, -v77\n"
"v_mfma_f32_16x16x32_bf16 v[216:219], v[32:35], v[248:251], v[216:219]\n"
"v_fma_f32 v77, v77, s94, s94\n"
"v_mul_f32_e32 v94, v78, v94\n"
"v_mfma_f32_16x16x32_bf16 v[220:223], v[32:35], v[252:255], v[220:223]\n"
"v_mul_f32_e32 v95, v79, v95\n"
"s_waitcnt lgkmcnt(2)\n"
"v_mfma_f32_16x16x32_bf16 v[192:195], v[36:39], a[0:3], v[192:195]\n"
"ds_read_b128 v[32:35], v9 offset:0\n"
"v_mul_f32_e32 v112, v96, v112\n"
"v_mfma_f32_16x16x32_bf16 v[196:199], v[36:39], a[4:7], v[196:199]\n"
"s_mov_b64 exec, 0xff\n"
"buffer_load_dword v22, v15, s[44:47], s84 offen offset:0\n"
"s_mov_b64 exec, -1\n"
"v_mfma_f32_16x16x32_bf16 v[200:203], v[36:39], a[8:11], v[200:203]\n"
"v_rcp_f32_e32 v77, v77\n"
"v_mfma_f32_16x16x32_bf16 v[204:207], v[36:39], a[12:15], v[204:207]\n"
"v_exp_f32_e64 v78, -v78\n"
"v_mfma_f32_16x16x32_bf16 v[208:211], v[40:43], a[0:3], v[208:211]\n"
"ds_read_b128 v[36:39], v9 offset:1024\n"
"s_add_u32 s84, s84, 0x480\n"
"v_mfma_f32_16x16x32_bf16 v[212:215], v[40:43], a[4:7], v[212:215]\n"
"v_mul_f32_e32 v93, v93, v77\n"
"v_fma_f32 v78, v78, s94, s94\n"
"v_mfma_f32_16x16x32_bf16 v[216:219], v[40:43], a[8:11], v[216:219]\n"
"v_rcp_f32_e32 v78, v78\n"
"v_mfma_f32_16x16x32_bf16 v[220:223], v[40:43], a[12:15], v[220:223]\n"
"v_cvt_pk_bf16_f32 v56, v92, v93\n"
"s_waitcnt lgkmcnt(2)\n"
"v_mfma_f32_16x16x32_bf16 v[192:195], v[24:27], a[16:19], v[192:195]\n"
"ds_read_b128 v[40:43], v9 offset:2112\n"
"v_mul_f32_e32 v94, v94, v78\n"
"v_mfma_f32_16x16x32_bf16 v[196:199], v[24:27], a[20:23], v[196:199]\n"
"v_exp_f32_e64 v79, -v79\n"
"v_mfma_f32_16x16x32_bf16 v[200:203], v[24:27], a[24:27], v[200:203]\n"
"v_exp_f32_e64 v96, -v96\n"
"v_mfma_f32_16x16x32_bf16 v[204:207], v[24:27], a[28:31], v[204:207]\n"
"v_fma_f32 v79, v79, s94, s94\n"
"v_fma_f32 v96, v96, s94, s94\n"
"v_mfma_f32_16x16x32_bf16 v[208:211], v[28:31], a[16:19], v[208:211]\n"
"ds_read_b128 v[24:27], v9 offset:3136\n"
"v_mul_f32_e32 v113, v97, v113\n"
"v_mfma_f32_16x16x32_bf16 v[212:215], v[28:31], a[20:23], v[212:215]\n"
"v_rcp_f32_e32 v79, v79\n"
"v_mfma_f32_16x16x32_bf16 v[216:219], v[28:31], a[24:27], v[216:219]\n"
"v_rcp_f32_e32 v96, v96\n"
"v_mfma_f32_16x16x32_bf16 v[220:223], v[28:31], a[28:31], v[220:223]\n"
"v_mul_f32_e32 v95, v95, v79\n"
"s_waitcnt lgkmcnt(2)\n"
"v_mfma_f32_16x16x32_bf16 v[192:195], v[32:35], a[32:35], v[192:195]\n"
"ds_read_b128 v[28:31], v9 offset:4224\n"
"v_cvt_pk_bf16_f32 v57, v94, v95\n"
"v_mfma_f32_16x16x32_bf16 v[196:199], v[32:35], a[36:39], v[196:199]\n"
"v_exp_f32_e64 v97, -v97\n"
"v_mfma_f32_16x16x32_bf16 v[200:203], v[32:35], a[40:43], v[200:203]\n"
"v_mul_f32_e32 v112, v112, v96\n"
"v_fma_f32 v97, v97, s94, s94\n"
"v_mfma_f32_16x16x32_bf16 v[204:207], v[32:35], a[44:47], v[204:207]\n"
"v_rcp_f32_e32 v97, v97\n"
"v_mfma_f32_16x16x32_bf16 v[208:211], v[36:39], a[32:35], v[208:211]\n"
"ds_read_b128 v[32:35], v9 offset:5248\n"
"v_mul_f32_e32 v113, v113, v97\n"
"v_mfma_f32_16x16x32_bf16 v[212:215], v[36:39], a[36:39], v[212:215]\n"
"v_cvt_pk_bf16_f32 v46, v112, v113\n"
"v_mul_f32_e32 v114, v98, v114\n"
"v_mfma_f32_16x16x32_bf16 v[216:219], v[36:39], a[40:43], v[216:219]\n"
"v_exp_f32_e64 v98, -v98\n"
"v_mfma_f32_16x16x32_bf16 v[220:223], v[36:39], a[44:47], v[220:223]\n"
"v_fma_f32 v98, v98, s94, s94\n"
"s_waitcnt lgkmcnt(2)\n"
"v_mfma_f32_16x16x32_bf16 v[192:195], v[40:43], a[48:51], v[192:195]\n"
"ds_read_b128 v[36:39], v9 offset:6336\n"
"v_mul_f32_e32 v115, v99, v115\n"
"v_mfma_f32_16x16x32_bf16 v[196:199], v[40:43], a[52:55], v[196:199]\n"
"v_rcp_f32_e32 v98, v98\n"
"v_mfma_f32_16x16x32_bf16 v[200:203], v[40:43], a[56:59], v[200:203]\n"
"v_exp_f32_e64 v99, -v99\n"
"v_mfma_f32_16x16x32_bf16 v[204:207], v[40:43], a[60:63], v[204:207]\n"
"v_mul_f32_e32 v114, v114, v98\n"
"v_fma_f32 v99, v99, s94, s94\n"
"v_mfma_f32_16x16x32_bf16 v[208:211], v[24:27], a[48:51], v[208:211]\n"
"ds_read_b128 v[40:43], v9 offset:7360\n"
"v_mul_f32_e32 v116, v100, v116\n"
"v_mfma_f32_16x16x32_bf16 v[212:215], v[24:27], a[52:55], v[212:215]\n"
"v_rcp_f32_e32 v99, v99\n"
"v_mfma_f32_16x16x32_bf16 v[216:219], v[24:27], a[56:59], v[216:219]\n"
"v_exp_f32_e64 v100, -v100\n"
"v_mfma_f32_16x16x32_bf16 v[220:223], v[24:27], a[60:63], v[220:223]\n"
"v_mul_f32_e32 v115, v115, v99\n"
"s_waitcnt vmcnt(29) lgkmcnt(2)\n"
"s_barrier\n"
"v_mfma_f32_16x16x32_bf16 v[192:195], v[28:31], a[64:67], v[192:195]\n"
"ds_read_b128 v[24:27], v9 offset:8448\n"
"s_add_u32 m0, s17, 0xa500\n"
"v_mfma_f32_16x16x32_bf16 v[196:199], v[28:31], a[68:71], v[196:199]\n"
"s_add_u32 s88, s16, 0x3a8080\n"
"buffer_load_dwordx4 v1, s[40:43], s88 offen lds\n"
"v_mfma_f32_16x16x32_bf16 v[200:203], v[28:31], a[72:75], v[200:203]\n"
"v_cvt_pk_bf16_f32 v47, v114, v115\n"
"v_fma_f32 v100, v100, s94, s94\n"
"v_mfma_f32_16x16x32_bf16 v[204:207], v[28:31], a[76:79], v[204:207]\n"
"s_add_u32 s90, s65, 0x0\n"
"buffer_store_dwordx4 v[44:47], v0, s[48:51], s90 offen offset:0 sc1\n"
"v_mfma_f32_16x16x32_bf16 v[208:211], v[32:35], a[64:67], v[208:211]\n"
"ds_read_b128 v[28:31], v9 offset:9472\n"
"buffer_load_dwordx4 v2, s[40:43], s88 offen offset:1056 lds\n"
"v_mfma_f32_16x16x32_bf16 v[212:215], v[32:35], a[68:71], v[212:215]\n"
"v_rcp_f32_e32 v100, v100\n"
"v_mfma_f32_16x16x32_bf16 v[216:219], v[32:35], a[72:75], v[216:219]\n"
"v_mul_f32_e32 v116, v116, v100\n"
"v_mul_f32_e32 v117, v101, v117\n"
"v_mfma_f32_16x16x32_bf16 v[220:223], v[32:35], a[76:79], v[220:223]\n"
"v_mul_f32_e32 v118, v102, v118\n"
"s_waitcnt lgkmcnt(2)\n"
"v_mfma_f32_16x16x32_bf16 v[192:195], v[36:39], a[80:83], v[192:195]\n"
"ds_read_b128 v[32:35], v9 offset:10560\n"
"s_add_u32 m0, s17, 0xc600\n"
"v_mfma_f32_16x16x32_bf16 v[196:199], v[36:39], a[84:87], v[196:199]\n"
"s_add_u32 s88, s16, 0x3f0080\n"
"buffer_load_dwordx4 v1, s[40:43], s88 offen lds\n"
"v_mfma_f32_16x16x32_bf16 v[200:203], v[36:39], a[88:91], v[200:203]\n"
"v_exp_f32_e64 v101, -v101\n"
"v_mfma_f32_16x16x32_bf16 v[204:207], v[36:39], a[92:95], v[204:207]\n"
"v_exp_f32_e64 v102, -v102\n"
"v_mfma_f32_16x16x32_bf16 v[208:211], v[40:43], a[80:83], v[208:211]\n"
"ds_read_b128 v[36:39], v9 offset:11584\n"
"buffer_load_dwordx4 v2, s[40:43], s88 offen offset:1056 lds\n"
"v_mfma_f32_16x16x32_bf16 v[212:215], v[40:43], a[84:87], v[212:215]\n"
"v_fma_f32 v101, v101, s94, s94\n"
"v_fma_f32 v102, v102, s94, s94\n"
"v_mfma_f32_16x16x32_bf16 v[216:219], v[40:43], a[88:91], v[216:219]\n"
"v_rcp_f32_e32 v101, v101\n"
"v_mfma_f32_16x16x32_bf16 v[220:223], v[40:43], a[92:95], v[220:223]\n"
"v_mul_f32_e32 v117, v117, v101\n"
"s_waitcnt lgkmcnt(2)\n"
"v_mfma_f32_16x16x32_bf16 v[192:195], v[24:27], a[96:99], v[192:195]\n"
"ds_read_b128 v[40:43], v9 offset:12672\n"
"v_cvt_pk_bf16_f32 v50, v116, v117\n"
"v_mfma_f32_16x16x32_bf16 v[196:199], v[24:27], a[100:103], v[196:199]\n"
"v_rcp_f32_e32 v102, v102\n"
"v_mfma_f32_16x16x32_bf16 v[200:203], v[24:27], a[104:107], v[200:203]\n"
"v_mul_f32_e32 v118, v118, v102\n"
"v_mul_f32_e32 v119, v103, v119\n"
"v_mfma_f32_16x16x32_bf16 v[204:207], v[24:27], a[108:111], v[204:207]\n"
"v_exp_f32_e64 v103, -v103\n"
"v_mfma_f32_16x16x32_bf16 v[208:211], v[28:31], a[96:99], v[208:211]\n"
"ds_read_b128 v[24:27], v9 offset:13696\n"
"v_fma_f32 v103, v103, s94, s94\n"
"v_mfma_f32_16x16x32_bf16 v[212:215], v[28:31], a[100:103], v[212:215]\n"
"v_rcp_f32_e32 v103, v103\n"
"v_mfma_f32_16x16x32_bf16 v[216:219], v[28:31], a[104:107], v[216:219]\n"
"v_mul_f32_e32 v119, v119, v103\n"
"v_mul_f32_e32 v120, v104, v120\n"
"v_mfma_f32_16x16x32_bf16 v[220:223], v[28:31], a[108:111], v[220:223]\n"
"v_cvt_pk_bf16_f32 v51, v118, v119\n"
"s_waitcnt lgkmcnt(2)\n"
"v_mfma_f32_16x16x32_bf16 v[192:195], v[32:35], a[112:115], v[192:195]\n"
"ds_read_b128 v[28:31], v9 offset:14784\n"
"v_mul_f32_e32 v121, v105, v121\n"
"v_mfma_f32_16x16x32_bf16 v[196:199], v[32:35], a[116:119], v[196:199]\n"
"s_add_u32 s90, s65, 0x0\n"
"buffer_store_dwordx4 v[48:51], v0, s[48:51], s90 offen offset:1024 sc1\n"
"v_mfma_f32_16x16x32_bf16 v[200:203], v[32:35], a[120:123], v[200:203]\n"
"v_exp_f32_e64 v104, -v104\n"
"v_mfma_f32_16x16x32_bf16 v[204:207], v[32:35], a[124:127], v[204:207]\n"
"v_exp_f32_e64 v105, -v105\n"
"v_mfma_f32_16x16x32_bf16 v[208:211], v[36:39], a[112:115], v[208:211]\n"
"ds_read_b128 v[32:35], v9 offset:15808\n"
"v_fma_f32 v104, v104, s94, s94\n"
"v_mfma_f32_16x16x32_bf16 v[212:215], v[36:39], a[116:119], v[212:215]\n"
"v_rcp_f32_e32 v104, v104\n"
"v_mfma_f32_16x16x32_bf16 v[216:219], v[36:39], a[120:123], v[216:219]\n"
"v_mul_f32_e32 v120, v120, v104\n"
"v_fma_f32 v105, v105, s94, s94\n"
"v_mfma_f32_16x16x32_bf16 v[220:223], v[36:39], a[124:127], v[220:223]\n"
"v_mul_f32_e32 v122, v106, v122\n"
"s_waitcnt lgkmcnt(2)\n"
"v_mfma_f32_16x16x32_bf16 v[192:195], v[40:43], a[128:131], v[192:195]\n"
"ds_read_b128 v[36:39], v9 offset:16896\n"
"v_mul_f32_e32 v123, v107, v123\n"
"v_mfma_f32_16x16x32_bf16 v[196:199], v[40:43], a[132:135], v[196:199]\n"
"v_rcp_f32_e32 v105, v105\n"
"v_mfma_f32_16x16x32_bf16 v[200:203], v[40:43], a[136:139], v[200:203]\n"
"v_exp_f32_e64 v106, -v106\n"
"v_mfma_f32_16x16x32_bf16 v[204:207], v[40:43], a[140:143], v[204:207]\n"
"v_mul_f32_e32 v121, v121, v105\n"
"v_fma_f32 v106, v106, s94, s94\n"
"v_mfma_f32_16x16x32_bf16 v[208:211], v[24:27], a[128:131], v[208:211]\n"
"ds_read_b128 v[40:43], v9 offset:17920\n"
"v_cvt_pk_bf16_f32 v54, v120, v121\n"
"v_mfma_f32_16x16x32_bf16 v[212:215], v[24:27], a[132:135], v[212:215]\n"
"v_rcp_f32_e32 v106, v106\n"
"v_mfma_f32_16x16x32_bf16 v[216:219], v[24:27], a[136:139], v[216:219]\n"
"v_exp_f32_e64 v107, -v107\n"
"v_mfma_f32_16x16x32_bf16 v[220:223], v[24:27], a[140:143], v[220:223]\n"
"v_mul_f32_e32 v122, v122, v106\n"
"s_waitcnt lgkmcnt(2)\n"
"v_mfma_f32_16x16x32_bf16 v[192:195], v[28:31], a[144:147], v[192:195]\n"
"ds_read_b128 v[24:27], v9 offset:19008\n"
"v_fma_f32 v107, v107, s94, s94\n"
"v_mfma_f32_16x16x32_bf16 v[196:199], v[28:31], a[148:151], v[196:199]\n"
"v_rcp_f32_e32 v107, v107\n"
"v_mfma_f32_16x16x32_bf16 v[200:203], v[28:31], a[152:155], v[200:203]\n"
"v_mul_f32_e32 v123, v123, v107\n"
"v_mul_f32_e32 v124, v108, v124\n"
"v_mfma_f32_16x16x32_bf16 v[204:207], v[28:31], a[156:159], v[204:207]\n"
"v_exp_f32_e64 v108, -v108\n"
"v_mfma_f32_16x16x32_bf16 v[208:211], v[32:35], a[144:147], v[208:211]\n"
"ds_read_b128 v[28:31], v9 offset:20032\n"
"v_cvt_pk_bf16_f32 v55, v122, v123\n"
"v_mfma_f32_16x16x32_bf16 v[212:215], v[32:35], a[148:151], v[212:215]\n"
"s_add_u32 s90, s66, 0x0\n"
"buffer_store_dwordx4 v[52:55], v0, s[48:51], s90 offen offset:0 sc1\n"
"v_mfma_f32_16x16x32_bf16 v[216:219], v[32:35], a[152:155], v[216:219]\n"
"v_fma_f32 v108, v108, s94, s94\n"
"v_mul_f32_e32 v125, v109, v125\n"
"v_mfma_f32_16x16x32_bf16 v[220:223], v[32:35], a[156:159], v[220:223]\n"
"v_mul_f32_e32 v126, v110, v126\n"
"s_waitcnt lgkmcnt(2)\n"
"v_mfma_f32_16x16x32_bf16 v[192:195], v[36:39], a[160:163], v[192:195]\n"
"ds_read_b128 v[32:35], v9 offset:21120\n"
"v_mul_f32_e32 v127, v111, v127\n"
"v_mfma_f32_16x16x32_bf16 v[196:199], v[36:39], a[164:167], v[196:199]\n"
"s_mov_b64 exec, 0xff\n"
"buffer_load_dword v22, v15, s[44:47], s84 offen offset:0\n"
"s_mov_b64 exec, -1\n"
"v_mfma_f32_16x16x32_bf16 v[200:203], v[36:39], a[168:171], v[200:203]\n"
"v_rcp_f32_e32 v108, v108\n"
"v_mfma_f32_16x16x32_bf16 v[204:207], v[36:39], a[172:175], v[204:207]\n"
"v_exp_f32_e64 v109, -v109\n"
"v_mfma_f32_16x16x32_bf16 v[208:211], v[40:43], a[160:163], v[208:211]\n"
"ds_read_b128 v[36:39], v9 offset:22144\n"
"s_add_u32 s84, s84, 0x480\n"
"v_mfma_f32_16x16x32_bf16 v[212:215], v[40:43], a[164:167], v[212:215]\n"
"v_mul_f32_e32 v124, v124, v108\n"
"v_fma_f32 v109, v109, s94, s94\n"
"v_mfma_f32_16x16x32_bf16 v[216:219], v[40:43], a[168:171], v[216:219]\n"
"v_rcp_f32_e32 v109, v109\n"
"v_mfma_f32_16x16x32_bf16 v[220:223], v[40:43], a[172:175], v[220:223]\n"
"v_mul_f32_e32 v125, v125, v109\n"
"s_waitcnt lgkmcnt(2)\n"
"v_mfma_f32_16x16x32_bf16 v[192:195], v[24:27], a[176:179], v[192:195]\n"
"ds_read_b128 v[40:43], v9 offset:23232\n"
"v_cvt_pk_bf16_f32 v58, v124, v125\n"
"v_mfma_f32_16x16x32_bf16 v[196:199], v[24:27], a[180:183], v[196:199]\n"
"v_exp_f32_e64 v110, -v110\n"
"v_mfma_f32_16x16x32_bf16 v[200:203], v[24:27], a[184:187], v[200:203]\n"
"v_exp_f32_e64 v111, -v111\n"
"v_mfma_f32_16x16x32_bf16 v[204:207], v[24:27], a[188:191], v[204:207]\n"
"v_fma_f32 v110, v110, s94, s94\n"
"v_fma_f32 v111, v111, s94, s94\n"
"v_mfma_f32_16x16x32_bf16 v[208:211], v[28:31], a[176:179], v[208:211]\n"
"ds_read_b128 v[24:27], v9 offset:24256\n"
"v_mul_f32_e32 v144, v128, v144\n"
"v_mfma_f32_16x16x32_bf16 v[212:215], v[28:31], a[180:183], v[212:215]\n"
"v_rcp_f32_e32 v110, v110\n"
"v_mfma_f32_16x16x32_bf16 v[216:219], v[28:31], a[184:187], v[216:219]\n"
"v_rcp_f32_e32 v111, v111\n"
"v_mfma_f32_16x16x32_bf16 v[220:223], v[28:31], a[188:191], v[220:223]\n"
"v_mul_f32_e32 v126, v126, v110\n"
"s_waitcnt lgkmcnt(2)\n"
"v_mfma_f32_16x16x32_bf16 v[192:195], v[32:35], a[192:195], v[192:195]\n"
"ds_read_b128 v[28:31], v9 offset:25344\n"
"v_mul_f32_e32 v127, v127, v111\n"
"v_mfma_f32_16x16x32_bf16 v[196:199], v[32:35], a[196:199], v[196:199]\n"
"v_exp_f32_e64 v128, -v128\n"
"v_mfma_f32_16x16x32_bf16 v[200:203], v[32:35], a[200:203], v[200:203]\n"
"v_cvt_pk_bf16_f32 v59, v126, v127\n"
"v_fma_f32 v128, v128, s94, s94\n"
"v_mfma_f32_16x16x32_bf16 v[204:207], v[32:35], a[204:207], v[204:207]\n"
"s_add_u32 s90, s66, 0x0\n"
"buffer_store_dwordx4 v[56:59], v0, s[48:51], s90 offen offset:1024 sc1\n"
"v_mfma_f32_16x16x32_bf16 v[208:211], v[36:39], a[192:195], v[208:211]\n"
"ds_read_b128 v[32:35], v9 offset:26368\n"
"v_mul_f32_e32 v145, v129, v145\n"
"v_mfma_f32_16x16x32_bf16 v[212:215], v[36:39], a[196:199], v[212:215]\n"
"v_rcp_f32_e32 v128, v128\n"
"v_mfma_f32_16x16x32_bf16 v[216:219], v[36:39], a[200:203], v[216:219]\n"
"v_exp_f32_e64 v129, -v129\n"
"v_mfma_f32_16x16x32_bf16 v[220:223], v[36:39], a[204:207], v[220:223]\n"
"v_mul_f32_e32 v144, v144, v128\n"
"s_waitcnt lgkmcnt(2)\n"
"v_mfma_f32_16x16x32_bf16 v[192:195], v[40:43], a[208:211], v[192:195]\n"
"ds_read_b128 v[36:39], v9 offset:27456\n"
"v_fma_f32 v129, v129, s94, s94\n"
"v_mfma_f32_16x16x32_bf16 v[196:199], v[40:43], a[212:215], v[196:199]\n"
"v_rcp_f32_e32 v129, v129\n"
"v_mfma_f32_16x16x32_bf16 v[200:203], v[40:43], a[216:219], v[200:203]\n"
"v_mul_f32_e32 v145, v145, v129\n"
"v_mul_f32_e32 v146, v130, v146\n"
"v_mfma_f32_16x16x32_bf16 v[204:207], v[40:43], a[220:223], v[204:207]\n"
"v_exp_f32_e64 v130, -v130\n"
"v_mfma_f32_16x16x32_bf16 v[208:211], v[24:27], a[208:211], v[208:211]\n"
"ds_read_b128 v[40:43], v9 offset:28480\n"
"v_cvt_pk_bf16_f32 v44, v144, v145\n"
"v_mfma_f32_16x16x32_bf16 v[212:215], v[24:27], a[212:215], v[212:215]\n"
"v_fma_f32 v130, v130, s94, s94\n"
"v_mul_f32_e32 v147, v131, v147\n"
"v_mfma_f32_16x16x32_bf16 v[216:219], v[24:27], a[216:219], v[216:219]\n"
"v_rcp_f32_e32 v130, v130\n"
"v_mfma_f32_16x16x32_bf16 v[220:223], v[24:27], a[220:223], v[220:223]\n"
"v_mul_f32_e32 v146, v146, v130\n"
"s_waitcnt vmcnt(25) lgkmcnt(2)\n"
"s_barrier\n"
"v_mfma_f32_16x16x32_bf16 v[192:195], v[28:31], a[224:227], v[192:195]\n"
"ds_read_b64_tr_b16 v[24:25], v6 offset:33792\n"
"ds_read_b64_tr_b16 v[26:27], v6 offset:34304\n"
"v_mfma_f32_16x16x32_bf16 v[196:199], v[28:31], a[228:231], v[196:199]\n"
"s_add_u32 m0, s17, 0xe700\n"
"s_add_u32 s88, s16, 0x438080\n"
"v_mfma_f32_16x16x32_bf16 v[200:203], v[28:31], a[232:235], v[200:203]\n"
"v_exp_f32_e64 v131, -v131\n"
"v_mfma_f32_16x16x32_bf16 v[204:207], v[28:31], a[236:239], v[204:207]\n"
"buffer_load_dwordx4 v1, s[40:43], s88 offen lds\n"
"v_fma_f32 v131, v131, s94, s94\n"
"v_mfma_f32_16x16x32_bf16 v[208:211], v[32:35], a[224:227], v[208:211]\n"
"ds_read_b64_tr_b16 v[28:29], v6 offset:38016\n"
"ds_read_b64_tr_b16 v[30:31], v6 offset:38528\n"
"v_mfma_f32_16x16x32_bf16 v[212:215], v[32:35], a[228:231], v[212:215]\n"
"v_rcp_f32_e32 v131, v131\n"
"v_mfma_f32_16x16x32_bf16 v[216:219], v[32:35], a[232:235], v[216:219]\n"
"buffer_load_dwordx4 v2, s[40:43], s88 offen offset:1056 lds\n"
"v_mul_f32_e32 v147, v147, v131\n"
"v_mfma_f32_16x16x32_bf16 v[220:223], v[32:35], a[236:239], v[220:223]\n"
"v_cvt_pk_bf16_f32 v45, v146, v147\n"
"s_waitcnt lgkmcnt(4)\n"
"v_mfma_f32_16x16x32_bf16 v[192:195], v[36:39], a[240:243], v[192:195]\n"
"ds_read_b64_tr_b16 v[32:33], v6 offset:42240\n"
"ds_read_b64_tr_b16 v[34:35], v6 offset:42752\n"
"v_mfma_f32_16x16x32_bf16 v[196:199], v[36:39], a[244:247], v[196:199]\n"
"s_add_u32 m0, s17, 0x10800\n"
"s_add_u32 s88, s16, 0x480080\n"
"v_mfma_f32_16x16x32_bf16 v[200:203], v[36:39], a[248:251], v[200:203]\n"
"buffer_load_dwordx4 v1, s[40:43], s88 offen lds\n"
"v_mul_f32_e32 v148, v132, v148\n"
"v_mfma_f32_16x16x32_bf16 v[204:207], v[36:39], a[252:255], v[204:207]\n"
"v_exp_f32_e64 v132, -v132\n"
"v_mfma_f32_16x16x32_bf16 v[208:211], v[40:43], a[240:243], v[208:211]\n"
"ds_read_b64_tr_b16 v[36:37], v6 offset:46464\n"
"ds_read_b64_tr_b16 v[38:39], v6 offset:46976\n"
"v_mfma_f32_16x16x32_bf16 v[212:215], v[40:43], a[244:247], v[212:215]\n"
"buffer_load_dwordx4 v2, s[40:43], s88 offen offset:1056 lds\n"
"v_fma_f32 v132, v132, s94, s94\n"
"v_mfma_f32_16x16x32_bf16 v[216:219], v[40:43], a[248:251], v[216:219]\n"
"v_rcp_f32_e32 v132, v132\n"
"v_mfma_f32_16x16x32_bf16 v[220:223], v[40:43], a[252:255], v[220:223]\n"
"v_mul_f32_e32 v148, v148, v132\n"
"s_waitcnt lgkmcnt(4)\n"
"v_mfma_f32_16x16x32_bf16 v[64:67], v[24:27], v[224:227], 0\n"
"ds_read_b64_tr_b16 v[40:41], v6 offset:50688\n"
"ds_read_b64_tr_b16 v[42:43], v6 offset:51200\n"
"v_mfma_f32_16x16x32_bf16 v[68:71], v[24:27], v[228:231], 0\n"
"s_add_u32 m0, s17, 0x12900\n"
"s_add_u32 s88, s16, 0x4c8080\n"
"v_mfma_f32_16x16x32_bf16 v[72:75], v[24:27], v[232:235], 0\n"
"buffer_load_dwordx4 v1, s[40:43], s88 offen lds\n"
"v_mul_f32_e32 v149, v133, v149\n"
"v_mfma_f32_16x16x32_bf16 v[76:79], v[24:27], v[236:239], 0\n"
"v_exp_f32_e64 v133, -v133\n"
"v_mfma_f32_16x16x32_bf16 v[80:83], v[28:31], v[224:227], 0\n"
"ds_read_b64_tr_b16 v[24:25], v6 offset:54912\n"
"ds_read_b64_tr_b16 v[26:27], v6 offset:55424\n"
"v_mfma_f32_16x16x32_bf16 v[84:87], v[28:31], v[228:231], 0\n"
"v_fma_f32 v133, v133, s94, s94\n"
"v_mul_f32_e32 v150, v134, v150\n"
"v_mfma_f32_16x16x32_bf16 v[88:91], v[28:31], v[232:235], 0\n"
"v_rcp_f32_e32 v133, v133\n"
"v_mfma_f32_16x16x32_bf16 v[92:95], v[28:31], v[236:239], 0\n"
"v_mul_f32_e32 v149, v149, v133\n"
"s_waitcnt lgkmcnt(4)\n"
"v_mfma_f32_16x16x32_bf16 v[64:67], v[32:35], v[240:243], v[64:67]\n"
"ds_read_b64_tr_b16 v[28:29], v7 offset:0\n"
"ds_read_b64_tr_b16 v[30:31], v7 offset:512\n"
"v_mfma_f32_16x16x32_bf16 v[68:71], v[32:35], v[244:247], v[68:71]\n"
"v_exp_f32_e64 v134, -v134\n"
"v_mfma_f32_16x16x32_bf16 v[72:75], v[32:35], v[248:251], v[72:75]\n"
"buffer_load_dwordx4 v2, s[40:43], s88 offen offset:1056 lds\n"
"v_cvt_pk_bf16_f32 v48, v148, v149\n"
"v_mfma_f32_16x16x32_bf16 v[76:79], v[32:35], v[252:255], v[76:79]\n"
"v_fma_f32 v134, v134, s94, s94\n"
"v_mul_f32_e32 v151, v135, v151\n"
"v_mfma_f32_16x16x32_bf16 v[80:83], v[36:39], v[240:243], v[80:83]\n"
"ds_read_b64_tr_b16 v[32:33], v7 offset:4224\n"
"ds_read_b64_tr_b16 v[34:35], v7 offset:4736\n"
"v_mfma_f32_16x16x32_bf16 v[84:87], v[36:39], v[244:247], v[84:87]\n"
"v_rcp_f32_e32 v134, v134\n"
"v_mfma_f32_16x16x32_bf16 v[88:91], v[36:39], v[248:251], v[88:91]\n"
"v_exp_f32_e64 v135, -v135\n"
"v_mfma_f32_16x16x32_bf16 v[92:95], v[36:39], v[252:255], v[92:95]\n"
"v_mul_f32_e32 v150, v150, v134\n"
"s_waitcnt lgkmcnt(4)\n"
"v_mfma_f32_16x16x32_bf16 v[64:67], v[40:43], a[0:3], v[64:67]\n"
"ds_read_b64_tr_b16 v[36:37], v6 offset:33824\n"
"ds_read_b64_tr_b16 v[38:39], v6 offset:34336\n"
"v_mfma_f32_16x16x32_bf16 v[68:71], v[40:43], a[4:7], v[68:71]\n"
"v_fma_f32 v135, v135, s94, s94\n"
"v_mul_f32_e32 v152, v136, v152\n"
"v_mfma_f32_16x16x32_bf16 v[72:75], v[40:43], a[8:11], v[72:75]\n"
"v_rcp_f32_e32 v135, v135\n"
"v_mfma_f32_16x16x32_bf16 v[76:79], v[40:43], a[12:15], v[76:79]\n"
"v_exp_f32_e64 v136, -v136\n"
"v_mfma_f32_16x16x32_bf16 v[80:83], v[24:27], a[0:3], v[80:83]\n"
"ds_read_b64_tr_b16 v[40:41], v6 offset:38048\n"
"ds_read_b64_tr_b16 v[42:43], v6 offset:38560\n"
"v_mfma_f32_16x16x32_bf16 v[84:87], v[24:27], a[4:7], v[84:87]\n"
"v_mul_f32_e32 v151, v151, v135\n"
"v_fma_f32 v136, v136, s94, s94\n"
"v_mfma_f32_16x16x32_bf16 v[88:91], v[24:27], a[8:11], v[88:91]\n"
"v_rcp_f32_e32 v136, v136\n"
"v_mfma_f32_16x16x32_bf16 v[92:95], v[24:27], a[12:15], v[92:95]\n"
"v_cvt_pk_bf16_f32 v49, v150, v151\n"
"s_waitcnt lgkmcnt(4)\n"
"v_mfma_f32_16x16x32_bf16 v[64:67], v[28:31], a[16:19], v[64:67]\n"
"ds_read_b64_tr_b16 v[24:25], v7 offset:8448\n"
"ds_read_b64_tr_b16 v[26:27], v7 offset:8960\n"
"v_mfma_f32_16x16x32_bf16 v[68:71], v[28:31], a[20:23], v[68:71]\n"
"v_mov_b32_e32 v21, 1\n"
"v_mul_f32_e32 v152, v152, v136\n"
"v_mfma_f32_16x16x32_bf16 v[72:75], v[28:31], a[24:27], v[72:75]\n"
"s_waitcnt vmcnt(6)\n"
"buffer_store_dword v21, v12, s[48:51], s69 offen sc1\n"
"v_mfma_f32_16x16x32_bf16 v[76:79], v[28:31], a[28:31], v[76:79]\n"
"v_mul_f32_e32 v153, v137, v153\n"
"v_mul_f32_e32 v154, v138, v154\n"
"v_mfma_f32_16x16x32_bf16 v[80:83], v[32:35], a[16:19], v[80:83]\n"
"ds_read_b64_tr_b16 v[28:29], v7 offset:12672\n"
"ds_read_b64_tr_b16 v[30:31], v7 offset:13184\n"
"v_mfma_f32_16x16x32_bf16 v[84:87], v[32:35], a[20:23], v[84:87]\n"
"v_exp_f32_e64 v137, -v137\n"
"v_mfma_f32_16x16x32_bf16 v[88:91], v[32:35], a[24:27], v[88:91]\n"
"v_exp_f32_e64 v138, -v138\n"
"v_mfma_f32_16x16x32_bf16 v[92:95], v[32:35], a[28:31], v[92:95]\n"
"v_fma_f32 v137, v137, s94, s94\n"
"s_waitcnt lgkmcnt(4)\n"
"v_mfma_f32_16x16x32_bf16 v[96:99], v[36:39], v[224:227], 0\n"
"ds_read_b64_tr_b16 v[32:33], v6 offset:42272\n"
"ds_read_b64_tr_b16 v[34:35], v6 offset:42784\n"
"v_mfma_f32_16x16x32_bf16 v[100:103], v[36:39], v[228:231], 0\n"
"s_mov_b64 exec, 0xff\n"
"buffer_load_dword v22, v15, s[44:47], s84 offen offset:0\n"
"s_mov_b64 exec, -1\n"
"v_mfma_f32_16x16x32_bf16 v[104:107], v[36:39], v[232:235], 0\n"
"v_rcp_f32_e32 v137, v137\n"
"v_mfma_f32_16x16x32_bf16 v[108:111], v[36:39], v[236:239], 0\n"
"v_mul_f32_e32 v153, v153, v137\n"
"v_fma_f32 v138, v138, s94, s94\n"
"v_mfma_f32_16x16x32_bf16 v[112:115], v[40:43], v[224:227], 0\n"
"ds_read_b64_tr_b16 v[36:37], v6 offset:46496\n"
"ds_read_b64_tr_b16 v[38:39], v6 offset:47008\n"
"v_mfma_f32_16x16x32_bf16 v[116:119], v[40:43], v[228:231], 0\n"
"v_rcp_f32_e32 v138, v138\n"
"v_mfma_f32_16x16x32_bf16 v[120:123], v[40:43], v[232:235], 0\n"
"s_add_u32 s84, s84, 0x480\n"
"v_cvt_pk_bf16_f32 v52, v152, v153\n"
"v_mfma_f32_16x16x32_bf16 v[124:127], v[40:43], v[236:239], 0\n"
"v_mul_f32_e32 v154, v154, v138\n"
"s_waitcnt lgkmcnt(4)\n"
"v_mfma_f32_16x16x32_bf16 v[64:67], v[24:27], a[32:35], v[64:67]\n"
"ds_read_b64_tr_b16 v[40:41], v7 offset:16896\n"
"ds_read_b64_tr_b16 v[42:43], v7 offset:17408\n"
"v_mfma_f32_16x16x32_bf16 v[68:71], v[24:27], a[36:39], v[68:71]\n"
"v_mul_f32_e32 v155, v139, v155\n"
"v_mul_f32_e32 v156, v140, v156\n"
"v_mfma_f32_16x16x32_bf16 v[72:75], v[24:27], a[40:43], v[72:75]\n"
"v_exp_f32_e64 v139, -v139\n"
"v_mfma_f32_16x16x32_bf16 v[76:79], v[24:27], a[44:47], v[76:79]\n"
"v_exp_f32_e64 v140, -v140\n"
"v_mfma_f32_16x16x32_bf16 v[80:83], v[28:31], a[32:35], v[80:83]\n"
"ds_read_b64_tr_b16 v[24:25], v7 offset:21120\n"
"ds_read_b64_tr_b16 v[26:27], v7 offset:21632\n"
"v_mfma_f32_16x16x32_bf16 v[84:87], v[28:31], a[36:39], v[84:87]\n"
"v_fma_f32 v139, v139, s94, s94\n"
"v_fma_f32 v140, v140, s94, s94\n"
"v_mfma_f32_16x16x32_bf16 v[88:91], v[28:31], a[40:43], v[88:91]\n"
"v_rcp_f32_e32 v139, v139\n"
"v_mfma_f32_16x16x32_bf16 v[92:95], v[28:31], a[44:47], v[92:95]\n"
"v_mul_f32_e32 v155, v155, v139\n"
"s_waitcnt lgkmcnt(4)\n"
"s_barrier\n"
"v_mfma_f32_16x16x32_bf16 v[96:99], v[32:35], v[240:243], v[96:99]\n"
"ds_read_b64_tr_b16 v[28:29], v6 offset:50720\n"
"ds_read_b64_tr_b16 v[30:31], v6 offset:51232\n"
"v_mfma_f32_16x16x32_bf16 v[100:103], v[32:35], v[244:247], v[100:103]\n"
"s_add_u32 m0, s17, 0x14a00\n"
"s_add_u32 s88, s61, 0x0\n"
"v_mfma_f32_16x16x32_bf16 v[104:107], v[32:35], v[248:251], v[104:107]\n"
"v_rcp_f32_e32 v140, v140\n"
"v_mfma_f32_16x16x32_bf16 v[108:111], v[32:35], v[252:255], v[108:111]\n"
"buffer_load_dwordx4 v3, s[44:47], s88 offen lds\n"
"v_cvt_pk_bf16_f32 v53, v154, v155\n"
"v_mfma_f32_16x16x32_bf16 v[112:115], v[36:39], v[240:243], v[112:115]\n"
"ds_read_b64_tr_b16 v[32:33], v6 offset:54944\n"
"ds_read_b64_tr_b16 v[34:35], v6 offset:55456\n"
"v_mfma_f32_16x16x32_bf16 v[116:119], v[36:39], v[244:247], v[116:119]\n"
"buffer_load_dwordx4 v4, s[44:47], s88 offen offset:1056 lds\n"
"v_mul_f32_e32 v156, v156, v140\n"
"v_mfma_f32_16x16x32_bf16 v[120:123], v[36:39], v[248:251], v[120:123]\n"
"v_mul_f32_e32 v157, v141, v157\n"
"v_mul_f32_e32 v158, v142, v158\n"
"v_mfma_f32_16x16x32_bf16 v[124:127], v[36:39], v[252:255], v[124:127]\n"
"v_mul_f32_e32 v159, v143, v159\n"
"s_waitcnt lgkmcnt(4)\n"
"v_mfma_f32_16x16x32_bf16 v[64:67], v[40:43], a[48:51], v[64:67]\n"
"ds_read_b64_tr_b16 v[36:37], v7 offset:25344\n"
"ds_read_b64_tr_b16 v[38:39], v7 offset:25856\n"
"v_mfma_f32_16x16x32_bf16 v[68:71], v[40:43], a[52:55], v[68:71]\n"
"v_exp_f32_e64 v141, -v141\n"
"v_mfma_f32_16x16x32_bf16 v[72:75], v[40:43], a[56:59], v[72:75]\n"
"v_exp_f32_e64 v142, -v142\n"
"v_mfma_f32_16x16x32_bf16 v[76:79], v[40:43], a[60:63], v[76:79]\n"
"v_fma_f32 v141, v141, s94, s94\n"
"v_fma_f32 v142, v142, s94, s94\n"
"v_mfma_f32_16x16x32_bf16 v[80:83], v[24:27], a[48:51], v[80:83]\n"
"ds_read_b64_tr_b16 v[40:41], v7 offset:29568\n"
"ds_read_b64_tr_b16 v[42:43], v7 offset:30080\n"
"v_mfma_f32_16x16x32_bf16 v[84:87], v[24:27], a[52:55], v[84:87]\n"
"v_rcp_f32_e32 v141, v141\n"
"v_mfma_f32_16x16x32_bf16 v[88:91], v[24:27], a[56:59], v[88:91]\n"
"v_rcp_f32_e32 v142, v142\n"
"v_mfma_f32_16x16x32_bf16 v[92:95], v[24:27], a[60:63], v[92:95]\n"
"v_mul_f32_e32 v157, v157, v141\n"
"s_waitcnt lgkmcnt(4)\n"
"v_mfma_f32_16x16x32_bf16 v[96:99], v[28:31], a[0:3], v[96:99]\n"
"ds_read_b64_tr_b16 v[24:25], v7 offset:32\n"
"ds_read_b64_tr_b16 v[26:27], v7 offset:544\n"
"v_mfma_f32_16x16x32_bf16 v[100:103], v[28:31], a[4:7], v[100:103]\n"
"v_cvt_pk_bf16_f32 v56, v156, v157\n"
"v_mul_f32_e32 v158, v158, v142\n"
"v_mfma_f32_16x16x32_bf16 v[104:107], v[28:31], a[8:11], v[104:107]\n"
"v_exp_f32_e64 v143, -v143\n"
"v_mfma_f32_16x16x32_bf16 v[108:111], v[28:31], a[12:15], v[108:111]\n"
"v_fma_f32 v143, v143, s94, s94\n"
"v_mul_f32_e32 v176, v160, v176\n"
"v_mfma_f32_16x16x32_bf16 v[112:115], v[32:35], a[0:3], v[112:115]\n"
"ds_read_b64_tr_b16 v[28:29], v7 offset:4256\n"
"ds_read_b64_tr_b16 v[30:31], v7 offset:4768\n"
"v_mfma_f32_16x16x32_bf16 v[116:119], v[32:35], a[4:7], v[116:119]\n"
"v_rcp_f32_e32 v143, v143\n"
"v_mfma_f32_16x16x32_bf16 v[120:123], v[32:35], a[8:11], v[120:123]\n"
"v_exp_f32_e64 v160, -v160\n"
"v_mfma_f32_16x16x32_bf16 v[124:127], v[32:35], a[12:15], v[124:127]\n"
"v_mul_f32_e32 v159, v159, v143\n"
"s_waitcnt lgkmcnt(4)\n"
"v_mfma_f32_16x16x32_bf16 v[64:67], v[36:39], a[64:67], v[64:67]\n"
"ds_read_b64_tr_b16 v[32:33], v5 offset:0\n"
"ds_read_b64_tr_b16 v[34:35], v5 offset:512\n"
"v_mfma_f32_16x16x32_bf16 v[68:71], v[36:39], a[68:71], v[68:71]\n"
"v_cvt_pk_bf16_f32 v57, v158, v159\n"
".LKF_pad4:\n"
"s_cbranch_execz .LKF_pad8\n"
"v_fma_f32 v160, v160, s94, s94\n"
"v_mfma_f32_16x16x32_bf16 v[72:75], v[36:39], a[72:75], v[72:75]\n"
"v_rcp_f32_e32 v160, v160\n"
"v_mfma_f32_16x16x32_bf16 v[76:79], v[36:39], a[76:79], v[76:79]\n"
"v_mul_f32_e32 v176, v176, v160\n"
"v_mul_f32_e32 v177, v161, v177\n"
"v_mfma_f32_16x16x32_bf16 v[80:83], v[40:43], a[64:67], v[80:83]\n"
"ds_read_b64_tr_b16 v[36:37], v5 offset:4224\n"
"ds_read_b64_tr_b16 v[38:39], v5 offset:4736\n"
"v_mfma_f32_16x16x32_bf16 v[84:87], v[40:43], a[68:71], v[84:87]\n"
"v_exp_f32_e64 v161, -v161\n"
"v_mfma_f32_16x16x32_bf16 v[88:91], v[40:43], a[72:75], v[88:91]\n"
"v_fma_f32 v161, v161, s94, s94\n"
"v_mul_f32_e32 v178, v162, v178\n"
"v_mfma_f32_16x16x32_bf16 v[92:95], v[40:43], a[76:79], v[92:95]\n"
"v_mul_f32_e32 v179, v163, v179\n"
"s_waitcnt lgkmcnt(4)\n"
"v_mfma_f32_16x16x32_bf16 v[96:99], v[24:27], a[16:19], v[96:99]\n"
"ds_read_b64_tr_b16 v[40:41], v7 offset:8480\n"
"ds_read_b64_tr_b16 v[42:43], v7 offset:8992\n"
"v_mfma_f32_16x16x32_bf16 v[100:103], v[24:27], a[20:23], v[100:103]\n"
"v_rcp_f32_e32 v161, v161\n"
"v_mfma_f32_16x16x32_bf16 v[104:107], v[24:27], a[24:27], v[104:107]\n"
"v_exp_f32_e64 v162, -v162\n"
"v_mfma_f32_16x16x32_bf16 v[108:111], v[24:27], a[28:31], v[108:111]\n"
"v_mul_f32_e32 v177, v177, v161\n"
"v_fma_f32 v162, v162, s94, s94\n"
"v_mfma_f32_16x16x32_bf16 v[112:115], v[28:31], a[16:19], v[112:115]\n"
"ds_read_b64_tr_b16 v[24:25], v7 offset:12704\n"
"ds_read_b64_tr_b16 v[26:27], v7 offset:13216\n"
"v_mfma_f32_16x16x32_bf16 v[116:119], v[28:31], a[20:23], v[116:119]\n"
"v_rcp_f32_e32 v162, v162\n"
"v_mfma_f32_16x16x32_bf16 v[120:123], v[28:31], a[24:27], v[120:123]\n"
"v_cvt_pk_bf16_f32 v46, v176, v177\n"
"v_mul_f32_e32 v178, v178, v162\n"
"v_mfma_f32_16x16x32_bf16 v[124:127], v[28:31], a[28:31], v[124:127]\n"
"v_mul_f32_e32 v180, v164, v180\n"
"s_waitcnt lgkmcnt(4)\n"
"v_mfma_f32_16x16x32_bf16 v[64:67], v[32:35], a[80:83], v[64:67]\n"
"ds_read_b64_tr_b16 v[28:29], v7 offset:33792\n"
"ds_read_b64_tr_b16 v[30:31], v7 offset:34304\n"
"v_mfma_f32_16x16x32_bf16 v[68:71], v[32:35], a[84:87], v[68:71]\n"
"v_exp_f32_e64 v163, -v163\n"
"v_mfma_f32_16x16x32_bf16 v[72:75], v[32:35], a[88:91], v[72:75]\n"
"v_exp_f32_e64 v164, -v164\n"
"v_mfma_f32_16x16x32_bf16 v[76:79], v[32:35], a[92:95], v[76:79]\n"
"v_fma_f32 v163, v163, s94, s94\n"
"v_fma_f32 v164, v164, s94, s94\n"
"v_mfma_f32_16x16x32_bf16 v[80:83], v[36:39], a[80:83], v[80:83]\n"
"ds_read_b64_tr_b16 v[32:33], v7 offset:38016\n"
"ds_read_b64_tr_b16 v[34:35], v7 offset:38528\n"
"v_mfma_f32_16x16x32_bf16 v[84:87], v[36:39], a[84:87], v[84:87]\n"
"v_rcp_f32_e32 v163, v163\n"
"v_mfma_f32_16x16x32_bf16 v[88:91], v[36:39], a[88:91], v[88:91]\n"
"v_rcp_f32_e32 v164, v164\n"
"v_mfma_f32_16x16x32_bf16 v[92:95], v[36:39], a[92:95], v[92:95]\n"
"v_mul_f32_e32 v179, v179, v163\n"
"s_waitcnt lgkmcnt(4)\n"
"v_mfma_f32_16x16x32_bf16 v[96:99], v[40:43], a[32:35], v[96:99]\n"
"ds_read_b64_tr_b16 v[36:37], v7 offset:16928\n"
"ds_read_b64_tr_b16 v[38:39], v7 offset:17440\n"
"v_mfma_f32_16x16x32_bf16 v[100:103], v[40:43], a[36:39], v[100:103]\n"
"v_cvt_pk_bf16_f32 v47, v178, v179\n"
"v_mul_f32_e32 v180, v180, v164\n"
"v_mfma_f32_16x16x32_bf16 v[104:107], v[40:43], a[40:43], v[104:107]\n"
"s_add_u32 s90, s65, 0x4000\n"
"buffer_store_dwordx4 v[44:47], v0, s[48:51], s90 offen offset:0 sc1\n"
"v_mfma_f32_16x16x32_bf16 v[108:111], v[40:43], a[44:47], v[108:111]\n"
"v_mul_f32_e32 v181, v165, v181\n"
"v_mul_f32_e32 v182, v166, v182\n"
"v_mfma_f32_16x16x32_bf16 v[112:115], v[24:27], a[32:35], v[112:115]\n"
"ds_read_b64_tr_b16 v[40:41], v7 offset:21152\n"
"ds_read_b64_tr_b16 v[42:43], v7 offset:21664\n"
"v_mfma_f32_16x16x32_bf16 v[116:119], v[24:27], a[36:39], v[116:119]\n"
"v_exp_f32_e64 v165, -v165\n"
"v_mfma_f32_16x16x32_bf16 v[120:123], v[24:27], a[40:43], v[120:123]\n"
"v_exp_f32_e64 v166, -v166\n"
"v_mfma_f32_16x16x32_bf16 v[124:127], v[24:27], a[44:47], v[124:127]\n"
"v_fma_f32 v165, v165, s94, s94\n"
"s_waitcnt lgkmcnt(4)\n"
"v_mfma_f32_16x16x32_bf16 v[64:67], v[28:31], a[96:99], v[64:67]\n"
"ds_read_b64_tr_b16 v[24:25], v5 offset:8448\n"
"ds_read_b64_tr_b16 v[26:27], v5 offset:8960\n"
"v_mfma_f32_16x16x32_bf16 v[68:71], v[28:31], a[100:103], v[68:71]\n"
"v_rcp_f32_e32 v165, v165\n"
"v_mfma_f32_16x16x32_bf16 v[72:75], v[28:31], a[104:107], v[72:75]\n"
"v_mul_f32_e32 v181, v181, v165\n"
"v_fma_f32 v166, v166, s94, s94\n"
"v_mfma_f32_16x16x32_bf16 v[76:79], v[28:31], a[108:111], v[76:79]\n"
"v_rcp_f32_e32 v166, v166\n"
"v_mfma_f32_16x16x32_bf16 v[80:83], v[32:35], a[96:99], v[80:83]\n"
"ds_read_b64_tr_b16 v[28:29], v5 offset:12672\n"
"ds_read_b64_tr_b16 v[30:31], v5 offset:13184\n"
"v_mfma_f32_16x16x32_bf16 v[84:87], v[32:35], a[100:103], v[84:87]\n"
"v_cvt_pk_bf16_f32 v50, v180, v181\n"
"v_mul_f32_e32 v182, v182, v166\n"
"v_mfma_f32_16x16x32_bf16 v[88:91], v[32:35], a[104:107], v[88:91]\n"
"v_mul_f32_e32 v183, v167, v183\n"
"v_mul_f32_e32 v184, v168, v184\n"
"v_mfma_f32_16x16x32_bf16 v[92:95], v[32:35], a[108:111], v[92:95]\n"
"v_mul_f32_e32 v185, v169, v185\n"
"s_waitcnt lgkmcnt(4)\n"
"s_barrier\n"
"v_mfma_f32_16x16x32_bf16 v[96:99], v[36:39], a[48:51], v[96:99]\n"
"ds_read_b64_tr_b16 v[32:33], v6 offset:33856\n"
"ds_read_b64_tr_b16 v[34:35], v6 offset:34368\n"
"v_mfma_f32_16x16x32_bf16 v[100:103], v[36:39], a[52:55], v[100:103]\n"
"s_mov_b64 exec, 0xff\n"
"buffer_load_dword v22, v15, s[44:47], s84 offen offset:0\n"
"s_mov_b64 exec, -1\n"
"v_mfma_f32_16x16x32_bf16 v[104:107], v[36:39], a[56:59], v[104:107]\n"
"v_exp_f32_e64 v167, -v167\n"
"v_mfma_f32_16x16x32_bf16 v[108:111], v[36:39], a[60:63], v[108:111]\n"
"v_exp_f32_e64 v168, -v168\n"
"v_mfma_f32_16x16x32_bf16 v[112:115], v[40:43], a[48:51], v[112:115]\n"
"ds_read_b64_tr_b16 v[36:37], v6 offset:38080\n"
"ds_read_b64_tr_b16 v[38:39], v6 offset:38592\n"
"v_mfma_f32_16x16x32_bf16 v[116:119], v[40:43], a[52:55], v[116:119]\n"
"v_fma_f32 v167, v167, s94, s94\n"
"s_add_u32 s84, s84, 0x480\n"
"v_mfma_f32_16x16x32_bf16 v[120:123], v[40:43], a[56:59], v[120:123]\n"
"v_rcp_f32_e32 v167, v167\n"
"v_mfma_f32_16x16x32_bf16 v[124:127], v[40:43], a[60:63], v[124:127]\n"
"v_mul_f32_e32 v183, v183, v167\n"
"s_waitcnt lgkmcnt(4)\n"
"v_mfma_f32_16x16x32_bf16 v[64:67], v[24:27], a[112:115], v[64:67]\n"
"ds_read_b64_tr_b16 v[40:41], v7 offset:25376\n"
"ds_read_b64_tr_b16 v[42:43], v7 offset:25888\n"
"v_mfma_f32_16x16x32_bf16 v[68:71], v[24:27], a[116:119], v[68:71]\n"
"v_cvt_pk_bf16_f32 v51, v182, v183\n"
"v_fma_f32 v168, v168, s94, s94\n"
"v_mfma_f32_16x16x32_bf16 v[72:75], v[24:27], a[120:123], v[72:75]\n"
"s_add_u32 s90, s65, 0x4000\n"
"buffer_store_dwordx4 v[48:51], v0, s[48:51], s90 offen offset:1024 sc1\n"
"v_mfma_f32_16x16x32_bf16 v[76:79], v[24:27], a[124:127], v[76:79]\n"
"v_rcp_f32_e32 v168, v168\n"
"v_mfma_f32_16x16x32_bf16 v[80:83], v[28:31], a[112:115], v[80:83]\n"
"ds_read_b64_tr_b16 v[24:25], v7 offset:29600\n"
"ds_read_b64_tr_b16 v[26:27], v7 offset:30112\n"
"v_mfma_f32_16x16x32_bf16 v[84:87], v[28:31], a[116:119], v[84:87]\n"
"v_exp_f32_e64 v169, -v169\n"
"v_mfma_f32_16x16x32_bf16 v[88:91], v[28:31], a[120:123], v[88:91]\n"
"v_mul_f32_e32 v184, v184, v168\n"
"v_fma_f32 v169, v169, s94, s94\n"
"v_mfma_f32_16x16x32_bf16 v[92:95], v[28:31], a[124:127], v[92:95]\n"
"v_mul_f32_e32 v186, v170, v186\n"
"s_waitcnt lgkmcnt(4)\n"
"v_mfma_f32_16x16x32_bf16 v[128:131], v[32:35], v[224:227], 0\n"
"ds_read_b64_tr_b16 v[28:29], v5 offset:16896\n"
"ds_read_b64_tr_b16 v[30:31], v5 offset:17408\n"
"v_mfma_f32_16x16x32_bf16 v[132:135], v[32:35], v[228:231], 0\n"
"v_rcp_f32_e32 v169, v169\n"
"v_mfma_f32_16x16x32_bf16 v[136:139], v[32:35], v[232:235], 0\n"
"v_exp_f32_e64 v170, -v170\n"
"v_mfma_f32_16x16x32_bf16 v[140:143], v[32:35], v[236:239], 0\n"
"v_mul_f32_e32 v185, v185, v169\n"
"v_fma_f32 v170, v170, s94, s94\n"
"v_mfma_f32_16x16x32_bf16 v[144:147], v[36:39], v[224:227], 0\n"
"ds_read_b64_tr_b16 v[32:33], v5 offset:21120\n"
"ds_read_b64_tr_b16 v[34:35], v5 offset:21632\n"
"v_mfma_f32_16x16x32_bf16 v[148:151], v[36:39], v[228:231], 0\n"
"v_rcp_f32_e32 v170, v170\n"
"v_mfma_f32_16x16x32_bf16 v[152:155], v[36:39], v[232:235], 0\n"
"v_cvt_pk_bf16_f32 v54, v184, v185\n"
"v_mul_f32_e32 v186, v186, v170\n"
"v_mfma_f32_16x16x32_bf16 v[156:159], v[36:39], v[236:239], 0\n"
"v_mul_f32_e32 v187, v171, v187\n"
"s_waitcnt lgkmcnt(4)\n"
"v_mfma_f32_16x16x32_bf16 v[96:99], v[40:43], a[64:67], v[96:99]\n"
"ds_read_b64_tr_b16 v[36:37], v6 offset:42304\n"
"ds_read_b64_tr_b16 v[38:39], v6 offset:42816\n"
"v_mfma_f32_16x16x32_bf16 v[100:103], v[40:43], a[68:71], v[100:103]\n"
"v_exp_f32_e64 v171, -v171\n"
"v_mfma_f32_16x16x32_bf16 v[104:107], v[40:43], a[72:75], v[104:107]\n"
"v_fma_f32 v171, v171, s94, s94\n"
"v_mul_f32_e32 v188, v172, v188\n"
"v_mfma_f32_16x16x32_bf16 v[108:111], v[40:43], a[76:79], v[108:111]\n"
"v_rcp_f32_e32 v171, v171\n"
"v_mfma_f32_16x16x32_bf16 v[112:115], v[24:27], a[64:67], v[112:115]\n"
"ds_read_b64_tr_b16 v[40:41], v6 offset:46528\n"
"ds_read_b64_tr_b16 v[42:43], v6 offset:47040\n"
"v_mfma_f32_16x16x32_bf16 v[116:119], v[24:27], a[68:71], v[116:119]\n"
"v_exp_f32_e64 v172, -v172\n"
"v_mfma_f32_16x16x32_bf16 v[120:123], v[24:27], a[72:75], v[120:123]\n"
"v_mul_f32_e32 v187, v187, v171\n"
"v_fma_f32 v172, v172, s94, s94\n"
"v_mfma_f32_16x16x32_bf16 v[124:127], v[24:27], a[76:79], v[124:127]\n"
"v_cvt_pk_bf16_f32 v55, v186, v187\n"
"s_waitcnt lgkmcnt(4)\n"
"v_mfma_f32_16x16x32_bf16 v[64:67], v[28:31], a[128:131], v[64:67]\n"
"ds_read_b64_tr_b16 v[24:25], v5 offset:25344\n"
"ds_read_b64_tr_b16 v[26:27], v5 offset:25856\n"
"v_mfma_f32_16x16x32_bf16 v[68:71], v[28:31], a[132:135], v[68:71]\n"
"s_add_u32 s90, s66, 0x4000\n"
"buffer_store_dwordx4 v[52:55], v0, s[48:51], s90 offen offset:0 sc1\n"
"v_mfma_f32_16x16x32_bf16 v[72:75], v[28:31], a[136:139], v[72:75]\n"
"v_rcp_f32_e32 v172, v172\n"
"v_mfma_f32_16x16x32_bf16 v[76:79], v[28:31], a[140:143], v[76:79]\n"
"v_mul_f32_e32 v188, v188, v172\n"
"v_mul_f32_e32 v189, v173, v189\n"
"v_mfma_f32_16x16x32_bf16 v[80:83], v[32:35], a[128:131], v[80:83]\n"
"ds_read_b64_tr_b16 v[28:29], v5 offset:29568\n"
"ds_read_b64_tr_b16 v[30:31], v5 offset:30080\n"
"v_mfma_f32_16x16x32_bf16 v[84:87], v[32:35], a[132:135], v[84:87]\n"
"v_exp_f32_e64 v173, -v173\n"
"v_mfma_f32_16x16x32_bf16 v[88:91], v[32:35], a[136:139], v[88:91]\n"
"v_fma_f32 v173, v173, s94, s94\n"
"v_mul_f32_e32 v190, v174, v190\n"
"v_mfma_f32_16x16x32_bf16 v[92:95], v[32:35], a[140:143], v[92:95]\n"
"v_mul_f32_e32 v191, v175, v191\n"
"s_waitcnt lgkmcnt(4)\n"
"v_mfma_f32_16x16x32_bf16 v[128:131], v[36:39], v[240:243], v[128:131]\n"
"ds_read_b64_tr_b16 v[32:33], v5 offset:32\n"
"ds_read_b64_tr_b16 v[34:35], v5 offset:544\n"
"v_mfma_f32_16x16x32_bf16 v[132:135], v[36:39], v[244:247], v[132:135]\n"
"v_rcp_f32_e32 v173, v173\n"
"v_mfma_f32_16x16x32_bf16 v[136:139], v[36:39], v[248:251], v[136:139]\n"
"v_exp_f32_e64 v174, -v174\n"
"v_mfma_f32_16x16x32_bf16 v[140:143], v[36:39], v[252:255], v[140:143]\n"
"v_mul_f32_e32 v189, v189, v173\n"
"v_fma_f32 v174, v174, s94, s94\n"
"v_mfma_f32_16x16x32_bf16 v[144:147], v[40:43], v[240:243], v[144:147]\n"
"ds_read_b64_tr_b16 v[36:37], v5 offset:4256\n"
"ds_read_b64_tr_b16 v[38:39], v5 offset:4768\n"
"v_mfma_f32_16x16x32_bf16 v[148:151], v[40:43], v[244:247], v[148:151]\n"
"v_rcp_f32_e32 v174, v174\n"
"v_mfma_f32_16x16x32_bf16 v[152:155], v[40:43], v[248:251], v[152:155]\n"
"v_cvt_pk_bf16_f32 v58, v188, v189\n"
"v_mul_f32_e32 v190, v190, v174\n"
"v_mfma_f32_16x16x32_bf16 v[156:159], v[40:43], v[252:255], v[156:159]\n"
"v_mul_f32_e32 v208, v192, v208\n"
"s_waitcnt lgkmcnt(4)\n"
"v_mfma_f32_16x16x32_bf16 v[64:67], v[24:27], a[144:147], v[64:67]\n"
"ds_read_b64_tr_b16 v[40:41], v6 offset:50752\n"
"ds_read_b64_tr_b16 v[42:43], v6 offset:51264\n"
"v_mfma_f32_16x16x32_bf16 v[68:71], v[24:27], a[148:151], v[68:71]\n"
"v_exp_f32_e64 v175, -v175\n"
"v_mfma_f32_16x16x32_bf16 v[72:75], v[24:27], a[152:155], v[72:75]\n"
"v_exp_f32_e64 v192, -v192\n"
"v_mfma_f32_16x16x32_bf16 v[76:79], v[24:27], a[156:159], v[76:79]\n"
"buffer_load_dword v20, v11, s[48:51], s70 offen sc1\n"
"v_fma_f32 v175, v175, s94, s94\n"
"v_mfma_f32_16x16x32_bf16 v[80:83], v[28:31], a[144:147], v[80:83]\n"
"ds_read_b64_tr_b16 v[24:25], v6 offset:54976\n"
"ds_read_b64_tr_b16 v[26:27], v6 offset:55488\n"
"v_mfma_f32_16x16x32_bf16 v[84:87], v[28:31], a[148:151], v[84:87]\n"
"v_rcp_f32_e32 v175, v175\n"
"v_mfma_f32_16x16x32_bf16 v[88:91], v[28:31], a[152:155], v[88:91]\n"
"v_mul_f32_e32 v191, v191, v175\n"
"v_fma_f32 v192, v192, s94, s94\n"
"v_mfma_f32_16x16x32_bf16 v[92:95], v[28:31], a[156:159], v[92:95]\n"
"v_cvt_pk_bf16_f32 v59, v190, v191\n"
"s_waitcnt lgkmcnt(4)\n"
"v_mfma_f32_16x16x32_bf16 v[96:99], v[32:35], a[80:83], v[96:99]\n"
"ds_read_b64_tr_b16 v[28:29], v5 offset:33792\n"
"ds_read_b64_tr_b16 v[30:31], v5 offset:34304\n"
"v_mfma_f32_16x16x32_bf16 v[100:103], v[32:35], a[84:87], v[100:103]\n"
"s_add_u32 s90, s66, 0x4000\n"
"buffer_store_dwordx4 v[56:59], v0, s[48:51], s90 offen offset:1024 sc1\n"
"v_mfma_f32_16x16x32_bf16 v[104:107], v[32:35], a[88:91], v[104:107]\n"
"v_rcp_f32_e32 v192, v192\n"
"v_mfma_f32_16x16x32_bf16 v[108:111], v[32:35], a[92:95], v[108:111]\n"
"v_mul_f32_e32 v208, v208, v192\n"
"v_mul_f32_e32 v209, v193, v209\n"
"v_mfma_f32_16x16x32_bf16 v[112:115], v[36:39], a[80:83], v[112:115]\n"
"ds_read_b64_tr_b16 v[32:33], v5 offset:38016\n"
"ds_read_b64_tr_b16 v[34:35], v5 offset:38528\n"
"v_mfma_f32_16x16x32_bf16 v[116:119], v[36:39], a[84:87], v[116:119]\n"
"v_exp_f32_e64 v193, -v193\n"
"v_mfma_f32_16x16x32_bf16 v[120:123], v[36:39], a[88:91], v[120:123]\n"
"v_fma_f32 v193, v193, s94, s94\n"
"v_mul_f32_e32 v210, v194, v210\n"
"v_mfma_f32_16x16x32_bf16 v[124:127], v[36:39], a[92:95], v[124:127]\n"
"v_mul_f32_e32 v211, v195, v211\n"
"s_waitcnt vmcnt(14) lgkmcnt(4)\n"
"s_barrier\n"
"v_mfma_f32_16x16x32_bf16 v[128:131], v[40:43], a[0:3], v[128:131]\n"
"ds_read_b64_tr_b16 v[36:37], v7 offset:33824\n"
"ds_read_b64_tr_b16 v[38:39], v7 offset:34336\n"
"v_mfma_f32_16x16x32_bf16 v[132:135], v[40:43], a[4:7], v[132:135]\n"
"v_rcp_f32_e32 v193, v193\n"
"v_mfma_f32_16x16x32_bf16 v[136:139], v[40:43], a[8:11], v[136:139]\n"
"v_exp_f32_e64 v194, -v194\n"
"v_mfma_f32_16x16x32_bf16 v[140:143], v[40:43], a[12:15], v[140:143]\n"
"v_mul_f32_e32 v209, v209, v193\n"
"v_fma_f32 v194, v194, s94, s94\n"
"v_mfma_f32_16x16x32_bf16 v[144:147], v[24:27], a[0:3], v[144:147]\n"
"ds_read_b64_tr_b16 v[40:41], v7 offset:38048\n"
"ds_read_b64_tr_b16 v[42:43], v7 offset:38560\n"
"v_mfma_f32_16x16x32_bf16 v[148:151], v[24:27], a[4:7], v[148:151]\n"
"v_rcp_f32_e32 v194, v194\n"
"v_mfma_f32_16x16x32_bf16 v[152:155], v[24:27], a[8:11], v[152:155]\n"
"v_cvt_pk_bf16_f32 v48, v208, v209\n"
"v_mul_f32_e32 v210, v210, v194\n"
"v_mfma_f32_16x16x32_bf16 v[156:159], v[24:27], a[12:15], v[156:159]\n"
"v_mul_f32_e32 v212, v196, v212\n"
"s_waitcnt lgkmcnt(4)\n"
"v_mfma_f32_16x16x32_bf16 v[64:67], v[28:31], a[160:163], v[64:67]\n"
"ds_read_b64_tr_b16 v[24:25], v7 offset:64\n"
"ds_read_b64_tr_b16 v[26:27], v7 offset:576\n"
"v_mfma_f32_16x16x32_bf16 v[68:71], v[28:31], a[164:167], v[68:71]\n"
"v_exp_f32_e64 v195, -v195\n"
"v_mfma_f32_16x16x32_bf16 v[72:75], v[28:31], a[168:171], v[72:75]\n"
"v_exp_f32_e64 v196, -v196\n"
"v_mfma_f32_16x16x32_bf16 v[76:79], v[28:31], a[172:175], v[76:79]\n"
"v_fma_f32 v195, v195, s94, s94\n"
"v_fma_f32 v196, v196, s94, s94\n"
"v_mfma_f32_16x16x32_bf16 v[80:83], v[32:35], a[160:163], v[80:83]\n"
"ds_read_b64_tr_b16 v[28:29], v7 offset:4288\n"
"ds_read_b64_tr_b16 v[30:31], v7 offset:4800\n"
"v_mfma_f32_16x16x32_bf16 v[84:87], v[32:35], a[164:167], v[84:87]\n"
"v_rcp_f32_e32 v195, v195\n"
"v_mfma_f32_16x16x32_bf16 v[88:91], v[32:35], a[168:171], v[88:91]\n"
"v_rcp_f32_e32 v196, v196\n"
"v_mfma_f32_16x16x32_bf16 v[92:95], v[32:35], a[172:175], v[92:95]\n"
"v_mul_f32_e32 v211, v211, v195\n"
"s_waitcnt lgkmcnt(4)\n"
"v_mfma_f32_16x16x32_bf16 v[96:99], v[36:39], a[96:99], v[96:99]\n"
"ds_read_b64_tr_b16 v[32:33], v5 offset:42240\n"
"ds_read_b64_tr_b16 v[34:35], v5 offset:42752\n"
"v_mfma_f32_16x16x32_bf16 v[100:103], v[36:39], a[100:103], v[100:103]\n"
"s_mov_b64 exec, 0xff\n"
"buffer_load_dword v22, v15, s[44:47], s85 offen offset:0\n"
"s_mov_b64 exec, -1\n"
"v_mfma_f32_16x16x32_bf16 v[104:107], v[36:39], a[104:107], v[104:107]\n"
"v_cvt_pk_bf16_f32 v49, v210, v211\n"
"v_mul_f32_e32 v212, v212, v196\n"
"v_mfma_f32_16x16x32_bf16 v[108:111], v[36:39], a[108:111], v[108:111]\n"
"v_mul_f32_e32 v213, v197, v213\n"
"v_mul_f32_e32 v214, v198, v214\n"
"v_mfma_f32_16x16x32_bf16 v[112:115], v[40:43], a[96:99], v[112:115]\n"
"ds_read_b64_tr_b16 v[36:37], v5 offset:46464\n"
"ds_read_b64_tr_b16 v[38:39], v5 offset:46976\n"
"v_mfma_f32_16x16x32_bf16 v[116:119], v[40:43], a[100:103], v[116:119]\n"
"v_exp_f32_e64 v197, -v197\n"
"v_mfma_f32_16x16x32_bf16 v[120:123], v[40:43], a[104:107], v[120:123]\n"
"v_exp_f32_e64 v198, -v198\n"
"v_mfma_f32_16x16x32_bf16 v[124:127], v[40:43], a[108:111], v[124:127]\n"
"v_fma_f32 v197, v197, s94, s94\n"
"s_waitcnt lgkmcnt(4)\n"
"v_mfma_f32_16x16x32_bf16 v[128:131], v[24:27], a[16:19], v[128:131]\n"
"ds_read_b64_tr_b16 v[40:41], v5 offset:50688\n"
"ds_read_b64_tr_b16 v[42:43], v5 offset:51200\n"
"v_mfma_f32_16x16x32_bf16 v[132:135], v[24:27], a[20:23], v[132:135]\n"
"v_rcp_f32_e32 v197, v197\n"
"v_mfma_f32_16x16x32_bf16 v[136:139], v[24:27], a[24:27], v[136:139]\n"
"v_mul_f32_e32 v213, v213, v197\n"
"v_fma_f32 v198, v198, s94, s94\n"
"v_mfma_f32_16x16x32_bf16 v[140:143], v[24:27], a[28:31], v[140:143]\n"
"v_rcp_f32_e32 v198, v198\n"
"v_mfma_f32_16x16x32_bf16 v[144:147], v[28:31], a[16:19], v[144:147]\n"
"ds_read_b64_tr_b16 v[24:25], v5 offset:54912\n"
"ds_read_b64_tr_b16 v[26:27], v5 offset:55424\n"
"v_mfma_f32_16x16x32_bf16 v[148:151], v[28:31], a[20:23], v[148:151]\n"
"v_cvt_pk_bf16_f32 v50, v212, v213\n"
"v_mul_f32_e32 v214, v214, v198\n"
"v_mfma_f32_16x16x32_bf16 v[152:155], v[28:31], a[24:27], v[152:155]\n"
"v_mul_f32_e32 v215, v199, v215\n"
"v_mul_f32_e32 v216, v200, v216\n"
"v_mfma_f32_16x16x32_bf16 v[156:159], v[28:31], a[28:31], v[156:159]\n"
"v_mul_f32_e32 v217, v201, v217\n"
"s_waitcnt lgkmcnt(4)\n"
"v_mfma_f32_16x16x32_bf16 v[64:67], v[32:35], a[176:179], v[64:67]\n"
"ds_read_b64_tr_b16 v[28:29], v6 offset:33888\n"
"ds_read_b64_tr_b16 v[30:31], v6 offset:34400\n"
"v_mfma_f32_16x16x32_bf16 v[68:71], v[32:35], a[180:183], v[68:71]\n"
"v_exp_f32_e64 v199, -v199\n"
"v_mfma_f32_16x16x32_bf16 v[72:75], v[32:35], a[184:187], v[72:75]\n"
"v_exp_f32_e64 v200, -v200\n"
"v_mfma_f32_16x16x32_bf16 v[76:79], v[32:35], a[188:191], v[76:79]\n"
"v_fma_f32 v199, v199, s94, s94\n"
"v_fma_f32 v200, v200, s94, s94\n"
"v_mfma_f32_16x16x32_bf16 v[80:83], v[36:39], a[176:179], v[80:83]\n"
"ds_read_b64_tr_b16 v[32:33], v6 offset:38112\n"
"ds_read_b64_tr_b16 v[34:35], v6 offset:38624\n"
"v_mfma_f32_16x16x32_bf16 v[84:87], v[36:39], a[180:183], v[84:87]\n"
"v_rcp_f32_e32 v199, v199\n"
"v_mfma_f32_16x16x32_bf16 v[88:91], v[36:39], a[184:187], v[88:91]\n"
"v_rcp_f32_e32 v200, v200\n"
"v_mfma_f32_16x16x32_bf16 v[92:95], v[36:39], a[188:191], v[92:95]\n"
"v_mul_f32_e32 v215, v215, v199\n"
"s_waitcnt lgkmcnt(4)\n"
"s_waitcnt vmcnt(2)\n"
"v_cmp_gt_u32_e32 vcc, 1, v20\n"
"s_cbranch_vccnz .LKF_sP1_%=\n"
".LKF_rP1_%=:\n"
"v_mfma_f32_16x16x32_bf16 v[64:67], v[40:43], a[192:195], v[64:67]\n"
"ds_read_b64_tr_b16 v[36:37], v5 offset:8480\n"
"ds_read_b64_tr_b16 v[38:39], v5 offset:8992\n"
"v_mfma_f32_16x16x32_bf16 v[68:71], v[40:43], a[196:199], v[68:71]\n"
"v_cvt_pk_bf16_f32 v51, v214, v215\n"
"v_mul_f32_e32 v216, v216, v200\n"
"v_mfma_f32_16x16x32_bf16 v[72:75], v[40:43], a[200:203], v[72:75]\n"
"v_exp_f32_e64 v201, -v201\n"
"v_mfma_f32_16x16x32_bf16 v[76:79], v[40:43], a[204:207], v[76:79]\n"
"v_permlane16_swap_b32_e32 v48, v50\n"
"v_permlane16_swap_b32_e32 v49, v51\n"
"v_mfma_f32_16x16x32_bf16 v[80:83], v[24:27], a[192:195], v[80:83]\n"
"ds_read_b64_tr_b16 v[40:41], v5 offset:12704\n"
"ds_read_b64_tr_b16 v[42:43], v5 offset:13216\n"
"v_mfma_f32_16x16x32_bf16 v[84:87], v[24:27], a[196:199], v[84:87]\n"
"buffer_store_dwordx4 v[48:51], v19, s[48:51], s67 offen sc1\n"
"v_fma_f32 v201, v201, s94, s94\n"
"v_mfma_f32_16x16x32_bf16 v[88:91], v[24:27], a[200:203], v[88:91]\n"
"v_rcp_f32_e32 v201, v201\n"
"v_mfma_f32_16x16x32_bf16 v[92:95], v[24:27], a[204:207], v[92:95]\n"
"v_mul_f32_e32 v217, v217, v201\n"
"s_waitcnt lgkmcnt(4)\n"
"v_mfma_f32_16x16x32_bf16 v[160:163], v[28:31], v[224:227], 0\n"
"ds_read_b64_tr_b16 v[24:25], v7 offset:8512\n"
"ds_read_b64_tr_b16 v[26:27], v7 offset:9024\n"
"v_mfma_f32_16x16x32_bf16 v[164:167], v[28:31], v[228:231], 0\n"
"v_cvt_pk_bf16_f32 v56, v216, v217\n"
"v_mul_f32_e32 v218, v202, v218\n"
"v_mfma_f32_16x16x32_bf16 v[168:171], v[28:31], v[232:235], 0\n"
"v_exp_f32_e64 v202, -v202\n"
"v_mfma_f32_16x16x32_bf16 v[172:175], v[28:31], v[236:239], 0\n"
"v_fma_f32 v202, v202, s94, s94\n"
"v_mul_f32_e32 v219, v203, v219\n"
"v_mfma_f32_16x16x32_bf16 v[176:179], v[32:35], v[224:227], 0\n"
"ds_read_b64_tr_b16 v[28:29], v7 offset:12736\n"
"ds_read_b64_tr_b16 v[30:31], v7 offset:13248\n"
"v_mfma_f32_16x16x32_bf16 v[180:183], v[32:35], v[228:231], 0\n"
"v_rcp_f32_e32 v202, v202\n"
"v_mfma_f32_16x16x32_bf16 v[184:187], v[32:35], v[232:235], 0\n"
"v_exp_f32_e64 v203, -v203\n"
"v_mfma_f32_16x16x32_bf16 v[188:191], v[32:35], v[236:239], 0\n"
"v_mul_f32_e32 v218, v218, v202\n"
"s_waitcnt lgkmcnt(4)\n"
"v_mfma_f32_16x16x32_bf16 v[96:99], v[36:39], a[112:115], v[96:99]\n"
"ds_read_b64_tr_b16 v[32:33], v6 offset:0\n"
"ds_read_b64_tr_b16 v[34:35], v6 offset:512\n"
"v_mfma_f32_16x16x32_bf16 v[100:103], v[36:39], a[116:119], v[100:103]\n"
"v_fma_f32 v203, v203, s94, s94\n"
"v_mul_f32_e32 v220, v204, v220\n"
"v_mfma_f32_16x16x32_bf16 v[104:107], v[36:39], a[120:123], v[104:107]\n"
"v_rcp_f32_e32 v203, v203\n"
"v_mfma_f32_16x16x32_bf16 v[108:111], v[36:39], a[124:127], v[108:111]\n"
"v_exp_f32_e64 v204, -v204\n"
"v_mfma_f32_16x16x32_bf16 v[112:115], v[40:43], a[112:115], v[112:115]\n"
"ds_read_b64_tr_b16 v[36:37], v6 offset:4224\n"
"ds_read_b64_tr_b16 v[38:39], v6 offset:4736\n"
"v_mfma_f32_16x16x32_bf16 v[116:119], v[40:43], a[116:119], v[116:119]\n"
"v_add_u32_e32 v8, s80, v8\n"
"v_mul_f32_e32 v219, v219, v203\n"
"v_mfma_f32_16x16x32_bf16 v[120:123], v[40:43], a[120:123], v[120:123]\n"
"v_cvt_pk_bf16_f32 v57, v218, v219\n"
"v_fma_f32 v204, v204, s94, s94\n"
"v_mfma_f32_16x16x32_bf16 v[124:127], v[40:43], a[124:127], v[124:127]\n"
"v_mul_f32_e32 v221, v205, v221\n"
"s_waitcnt lgkmcnt(4)\n"
"s_barrier\n"
"v_mfma_f32_16x16x32_bf16 v[128:131], v[24:27], a[32:35], v[128:131]\n"
"ds_read_b64_tr_b16 v[40:41], v6 offset:42336\n"
"ds_read_b64_tr_b16 v[42:43], v6 offset:42848\n"
"v_mfma_f32_16x16x32_bf16 v[132:135], v[24:27], a[36:39], v[132:135]\n"
"v_add_u32_e32 v9, s80, v9\n"
"s_add_u32 m0, s17, 0x16b00\n"
"v_mfma_f32_16x16x32_bf16 v[136:139], v[24:27], a[40:43], v[136:139]\n"
"s_add_u32 s88, s64, 0x0\n"
"buffer_load_dwordx4 v0, s[48:51], s88 offen lds\n"
"v_mfma_f32_16x16x32_bf16 v[140:143], v[24:27], a[44:47], v[140:143]\n"
"v_rcp_f32_e32 v204, v204\n"
"v_mfma_f32_16x16x32_bf16 v[144:147], v[28:31], a[32:35], v[144:147]\n"
"ds_read_b64_tr_b16 v[24:25], v6 offset:46560\n"
"ds_read_b64_tr_b16 v[26:27], v6 offset:47072\n"
"v_mfma_f32_16x16x32_bf16 v[148:151], v[28:31], a[36:39], v[148:151]\n"
"v_add_u32_e32 v10, s80, v10\n"
"buffer_load_dwordx4 v0, s[48:51], s88 offen offset:1024 lds\n"
"v_mfma_f32_16x16x32_bf16 v[152:155], v[28:31], a[40:43], v[152:155]\n"
"v_exp_f32_e64 v205, -v205\n"
"v_mfma_f32_16x16x32_bf16 v[156:159], v[28:31], a[44:47], v[156:159]\n"
"v_mul_f32_e32 v220, v220, v204\n"
"s_waitcnt lgkmcnt(4)\n"
"v_mfma_f32_16x16x32_bf16 v[64:67], v[32:35], a[208:211], v[64:67]\n"
"ds_read_b64_tr_b16 v[28:29], v6 offset:8448\n"
"ds_read_b64_tr_b16 v[30:31], v6 offset:8960\n"
"v_mfma_f32_16x16x32_bf16 v[68:71], v[32:35], a[212:215], v[68:71]\n"
"v_fma_f32 v205, v205, s94, s94\n"
"v_mul_f32_e32 v222, v206, v222\n"
"v_mfma_f32_16x16x32_bf16 v[72:75], v[32:35], a[216:219], v[72:75]\n"
"v_rcp_f32_e32 v205, v205\n"
"v_mfma_f32_16x16x32_bf16 v[76:79], v[32:35], a[220:223], v[76:79]\n"
"v_exp_f32_e64 v206, -v206\n"
"v_mfma_f32_16x16x32_bf16 v[80:83], v[36:39], a[208:211], v[80:83]\n"
"ds_read_b64_tr_b16 v[32:33], v6 offset:12672\n"
"ds_read_b64_tr_b16 v[34:35], v6 offset:13184\n"
"v_mfma_f32_16x16x32_bf16 v[84:87], v[36:39], a[212:215], v[84:87]\n"
"v_mul_f32_e32 v221, v221, v205\n"
"v_fma_f32 v206, v206, s94, s94\n"
"v_mfma_f32_16x16x32_bf16 v[88:91], v[36:39], a[216:219], v[88:91]\n"
"v_rcp_f32_e32 v206, v206\n"
"v_mfma_f32_16x16x32_bf16 v[92:95], v[36:39], a[220:223], v[92:95]\n"
"v_cvt_pk_bf16_f32 v58, v220, v221\n"
"s_waitcnt lgkmcnt(4)\n"
"v_mfma_f32_16x16x32_bf16 v[160:163], v[40:43], v[240:243], v[160:163]\n"
"ds_read_b64_tr_b16 v[36:37], v6 offset:50784\n"
"ds_read_b64_tr_b16 v[38:39], v6 offset:51296\n"
"v_mfma_f32_16x16x32_bf16 v[164:167], v[40:43], v[244:247], v[164:167]\n"
"s_mov_b64 exec, 0xff\n"
"buffer_load_dword v22, v15, s[48:51], s86 offen offset:0\n"
"buffer_load_dword v22, v15, s[48:51], s86 offen offset:128\n"
"s_mov_b64 exec, -1\n"
"v_mfma_f32_16x16x32_bf16 v[168:171], v[40:43], v[248:251], v[168:171]\n"
"v_mul_f32_e32 v222, v222, v206\n"
"v_mul_f32_e32 v223, v207, v223\n"
"v_mfma_f32_16x16x32_bf16 v[172:175], v[40:43], v[252:255], v[172:175]\n"
"v_exp_f32_e64 v207, -v207\n"
"v_mfma_f32_16x16x32_bf16 v[176:179], v[24:27], v[240:243], v[176:179]\n"
"ds_read_b64_tr_b16 v[40:41], v6 offset:55008\n"
"ds_read_b64_tr_b16 v[42:43], v6 offset:55520\n"
"v_mfma_f32_16x16x32_bf16 v[180:183], v[24:27], v[244:247], v[180:183]\n"
"v_fma_f32 v207, v207, s94, s94\n"
"v_mfma_f32_16x16x32_bf16 v[184:187], v[24:27], v[248:251], v[184:187]\n"
"v_rcp_f32_e32 v207, v207\n"
"v_mfma_f32_16x16x32_bf16 v[188:191], v[24:27], v[252:255], v[188:191]\n"
"v_mul_f32_e32 v223, v223, v207\n"
"s_waitcnt lgkmcnt(4)\n"
"v_mfma_f32_16x16x32_bf16 v[64:67], v[28:31], a[224:227], v[64:67]\n"
"ds_read_b64_tr_b16 v[24:25], v5 offset:16928\n"
"ds_read_b64_tr_b16 v[26:27], v5 offset:17440\n"
"v_mfma_f32_16x16x32_bf16 v[68:71], v[28:31], a[228:231], v[68:71]\n"
"v_cvt_pk_bf16_f32 v59, v222, v223\n"
"v_mfma_f32_16x16x32_bf16 v[72:75], v[28:31], a[232:235], v[72:75]\n"
"v_mfma_f32_16x16x32_bf16 v[76:79], v[28:31], a[236:239], v[76:79]\n"
"v_permlane16_swap_b32_e32 v56, v58\n"
"v_permlane16_swap_b32_e32 v57, v59\n"
"v_mfma_f32_16x16x32_bf16 v[80:83], v[32:35], a[224:227], v[80:83]\n"
"ds_read_b64_tr_b16 v[28:29], v5 offset:21152\n"
"ds_read_b64_tr_b16 v[30:31], v5 offset:21664\n"
"v_mfma_f32_16x16x32_bf16 v[84:87], v[32:35], a[228:231], v[84:87]\n"
"buffer_store_dwordx4 v[56:59], v19, s[48:51], s68 offen sc1\n"
"v_mfma_f32_16x16x32_bf16 v[88:91], v[32:35], a[232:235], v[88:91]\n"
"v_mfma_f32_16x16x32_bf16 v[92:95], v[32:35], a[236:239], v[92:95]\n"
"s_waitcnt lgkmcnt(4)\n"
"v_mfma_f32_16x16x32_bf16 v[160:163], v[36:39], a[0:3], v[160:163]\n"
"ds_read_b64_tr_b16 v[32:33], v7 offset:16960\n"
"ds_read_b64_tr_b16 v[34:35], v7 offset:17472\n"
"v_mfma_f32_16x16x32_bf16 v[164:167], v[36:39], a[4:7], v[164:167]\n"
"v_mfma_f32_16x16x32_bf16 v[168:171], v[36:39], a[8:11], v[168:171]\n"
"v_mfma_f32_16x16x32_bf16 v[172:175], v[36:39], a[12:15], v[172:175]\n"
"v_mfma_f32_16x16x32_bf16 v[176:179], v[40:43], a[0:3], v[176:179]\n"
"ds_read_b64_tr_b16 v[36:37], v7 offset:21184\n"
"ds_read_b64_tr_b16 v[38:39], v7 offset:21696\n"
"v_mfma_f32_16x16x32_bf16 v[180:183], v[40:43], a[4:7], v[180:183]\n"
"v_mfma_f32_16x16x32_bf16 v[184:187], v[40:43], a[8:11], v[184:187]\n"
"v_mfma_f32_16x16x32_bf16 v[188:191], v[40:43], a[12:15], v[188:191]\n"
"s_waitcnt lgkmcnt(4)\n"
"v_mfma_f32_16x16x32_bf16 v[96:99], v[24:27], a[128:131], v[96:99]\n"
"ds_read_b64_tr_b16 v[40:41], v6 offset:16896\n"
"ds_read_b64_tr_b16 v[42:43], v6 offset:17408\n"
"v_mfma_f32_16x16x32_bf16 v[100:103], v[24:27], a[132:135], v[100:103]\n"
"s_add_u32 s89, s71, 0x0\n"
"buffer_load_dwordx4 a[0:3], v0, s[48:51], s89 offen offset:0 sc1\n"
"v_mfma_f32_16x16x32_bf16 v[104:107], v[24:27], a[136:139], v[104:107]\n"
"v_mfma_f32_16x16x32_bf16 v[108:111], v[24:27], a[140:143], v[108:111]\n"
"v_mfma_f32_16x16x32_bf16 v[112:115], v[28:31], a[128:131], v[112:115]\n"
"ds_read_b64_tr_b16 v[24:25], v6 offset:21120\n"
"ds_read_b64_tr_b16 v[26:27], v6 offset:21632\n"
"v_mfma_f32_16x16x32_bf16 v[116:119], v[28:31], a[132:135], v[116:119]\n"
"buffer_load_dwordx4 a[4:7], v0, s[48:51], s89 offen offset:1024 sc1\n"
"v_mfma_f32_16x16x32_bf16 v[120:123], v[28:31], a[136:139], v[120:123]\n"
"v_mfma_f32_16x16x32_bf16 v[124:127], v[28:31], a[140:143], v[124:127]\n"
"s_waitcnt lgkmcnt(4)\n"
"v_mfma_f32_16x16x32_bf16 v[128:131], v[32:35], a[48:51], v[128:131]\n"
"ds_read_b64_tr_b16 v[28:29], v7 offset:96\n"
"ds_read_b64_tr_b16 v[30:31], v7 offset:608\n"
"v_mfma_f32_16x16x32_bf16 v[132:135], v[32:35], a[52:55], v[132:135]\n"
"buffer_load_dwordx4 a[8:11], v0, s[48:51], s89 offen offset:2048 sc1\n"
"v_mfma_f32_16x16x32_bf16 v[136:139], v[32:35], a[56:59], v[136:139]\n"
"v_mfma_f32_16x16x32_bf16 v[140:143], v[32:35], a[60:63], v[140:143]\n"
"v_mfma_f32_16x16x32_bf16 v[144:147], v[36:39], a[48:51], v[144:147]\n"
"ds_read_b64_tr_b16 v[32:33], v7 offset:4320\n"
"ds_read_b64_tr_b16 v[34:35], v7 offset:4832\n"
"v_mfma_f32_16x16x32_bf16 v[148:151], v[36:39], a[52:55], v[148:151]\n"
"buffer_load_dwordx4 a[12:15], v0, s[48:51], s89 offen offset:3072 sc1\n"
"v_mfma_f32_16x16x32_bf16 v[152:155], v[36:39], a[56:59], v[152:155]\n"
"v_mfma_f32_16x16x32_bf16 v[156:159], v[36:39], a[60:63], v[156:159]\n"
"s_waitcnt lgkmcnt(4)\n"
"v_mfma_f32_16x16x32_bf16 v[64:67], v[40:43], a[240:243], v[64:67]\n"
"ds_read_b64_tr_b16 v[36:37], v5 offset:25376\n"
"ds_read_b64_tr_b16 v[38:39], v5 offset:25888\n"
"v_mfma_f32_16x16x32_bf16 v[68:71], v[40:43], a[244:247], v[68:71]\n"
".LKF_pad5:\n"
"s_cbranch_execz .LKF_pad9\n"
"v_add_u32_e32 v2, 1248, v18\n"
"v_mfma_f32_16x16x32_bf16 v[72:75], v[40:43], a[248:251], v[72:75]\n"
"v_mfma_f32_16x16x32_bf16 v[76:79], v[40:43], a[252:255], v[76:79]\n"
"v_mfma_f32_16x16x32_bf16 v[80:83], v[24:27], a[240:243], v[80:83]\n"
"ds_read_b64_tr_b16 v[40:41], v5 offset:29600\n"
"ds_read_b64_tr_b16 v[42:43], v5 offset:30112\n"
"v_mfma_f32_16x16x32_bf16 v[84:87], v[24:27], a[244:247], v[84:87]\n"
"v_mfma_f32_16x16x32_bf16 v[88:91], v[24:27], a[248:251], v[88:91]\n"
"v_mfma_f32_16x16x32_bf16 v[92:95], v[24:27], a[252:255], v[92:95]\n"
"s_waitcnt lgkmcnt(4)\n"
"s_barrier\n"
"v_mfma_f32_16x16x32_bf16 v[160:163], v[28:31], a[16:19], v[160:163]\n"
"ds_read_b64_tr_b16 v[24:25], v7 offset:25408\n"
"ds_read_b64_tr_b16 v[26:27], v7 offset:25920\n"
"v_mfma_f32_16x16x32_bf16 v[164:167], v[28:31], a[20:23], v[164:167]\n"
"s_add_u32 m0, s17, 0x18c00\n"
"s_add_u32 s88, s61, 0x24000\n"
"v_mfma_f32_16x16x32_bf16 v[168:171], v[28:31], a[24:27], v[168:171]\n"
"buffer_load_dwordx4 v3, s[44:47], s88 offen lds\n"
"v_mfma_f32_16x16x32_bf16 v[172:175], v[28:31], a[28:31], v[172:175]\n"
"v_mfma_f32_16x16x32_bf16 v[176:179], v[32:35], a[16:19], v[176:179]\n"
"ds_read_b64_tr_b16 v[28:29], v7 offset:29632\n"
"ds_read_b64_tr_b16 v[30:31], v7 offset:30144\n"
"v_mfma_f32_16x16x32_bf16 v[180:183], v[32:35], a[20:23], v[180:183]\n"
"buffer_load_dwordx4 v4, s[44:47], s88 offen offset:1056 lds\n"
"v_mul_f32_e32 v80, v64, v80\n"
"v_mfma_f32_16x16x32_bf16 v[184:187], v[32:35], a[24:27], v[184:187]\n"
"v_exp_f32_e64 v64, -v64\n"
"v_mfma_f32_16x16x32_bf16 v[188:191], v[32:35], a[28:31], v[188:191]\n"
"v_fma_f32 v64, v64, s94, s94\n"
"s_waitcnt lgkmcnt(4)\n"
"v_mfma_f32_16x16x32_bf16 v[96:99], v[36:39], a[144:147], v[96:99]\n"
"ds_read_b64_tr_b16 v[32:33], v7 offset:8544\n"
"ds_read_b64_tr_b16 v[34:35], v7 offset:9056\n"
"v_mfma_f32_16x16x32_bf16 v[100:103], v[36:39], a[148:151], v[100:103]\n"
"s_add_u32 m0, s17, 0x1ad00\n"
"s_add_u32 s88, s61, 0x48000\n"
"v_mfma_f32_16x16x32_bf16 v[104:107], v[36:39], a[152:155], v[104:107]\n"
"v_rcp_f32_e32 v64, v64\n"
"v_mfma_f32_16x16x32_bf16 v[108:111], v[36:39], a[156:159], v[108:111]\n"
"buffer_load_dwordx4 v3, s[44:47], s88 offen lds\n"
"v_mul_f32_e32 v80, v80, v64\n"
"v_mfma_f32_16x16x32_bf16 v[112:115], v[40:43], a[144:147], v[112:115]\n"
"ds_read_b64_tr_b16 v[36:37], v7 offset:12768\n"
"ds_read_b64_tr_b16 v[38:39], v7 offset:13280\n"
"v_mfma_f32_16x16x32_bf16 v[116:119], v[40:43], a[148:151], v[116:119]\n"
"buffer_load_dwordx4 v4, s[44:47], s88 offen offset:1056 lds\n"
"v_mul_f32_e32 v81, v65, v81\n"
"v_mfma_f32_16x16x32_bf16 v[120:123], v[40:43], a[152:155], v[120:123]\n"
"v_exp_f32_e64 v65, -v65\n"
"v_mfma_f32_16x16x32_bf16 v[124:127], v[40:43], a[156:159], v[124:127]\n"
"v_fma_f32 v65, v65, s94, s94\n"
"s_waitcnt lgkmcnt(4)\n"
"v_mfma_f32_16x16x32_bf16 v[128:131], v[24:27], a[64:67], v[128:131]\n"
"ds_read_b64_tr_b16 v[40:41], v5 offset:33824\n"
"ds_read_b64_tr_b16 v[42:43], v5 offset:34336\n"
"v_mfma_f32_16x16x32_bf16 v[132:135], v[24:27], a[68:71], v[132:135]\n"
"s_add_u32 s89, s71, 0x1000\n"
"buffer_load_dwordx4 a[16:19], v0, s[48:51], s89 offen offset:0 sc1\n"
"v_mfma_f32_16x16x32_bf16 v[136:139], v[24:27], a[72:75], v[136:139]\n"
"v_rcp_f32_e32 v65, v65\n"
"v_mfma_f32_16x16x32_bf16 v[140:143], v[24:27], a[76:79], v[140:143]\n"
"v_mul_f32_e32 v81, v81, v65\n"
"v_mul_f32_e32 v82, v66, v82\n"
"v_mfma_f32_16x16x32_bf16 v[144:147], v[28:31], a[64:67], v[144:147]\n"
"ds_read_b64_tr_b16 v[24:25], v5 offset:38048\n"
"ds_read_b64_tr_b16 v[26:27], v5 offset:38560\n"
"v_mfma_f32_16x16x32_bf16 v[148:151], v[28:31], a[68:71], v[148:151]\n"
"v_mov_b32_e32 v21, 3\n"
"v_cvt_pk_bf16_f32 v44, v80, v81\n"
"v_mfma_f32_16x16x32_bf16 v[152:155], v[28:31], a[72:75], v[152:155]\n"
"s_waitcnt vmcnt(9)\n"
"buffer_store_dword v21, v12, s[48:51], s69 offen sc1\n"
"v_mfma_f32_16x16x32_bf16 v[156:159], v[28:31], a[76:79], v[156:159]\n"
"v_mul_f32_e32 v83, v67, v83\n"
"s_waitcnt lgkmcnt(4)\n"
"v_mfma_f32_16x16x32_bf16 v[160:163], v[32:35], a[32:35], v[160:163]\n"
"ds_read_b64_tr_b16 v[28:29], v5 offset:42272\n"
"ds_read_b64_tr_b16 v[30:31], v5 offset:42784\n"
"v_mfma_f32_16x16x32_bf16 v[164:167], v[32:35], a[36:39], v[164:167]\n"
"v_exp_f32_e64 v66, -v66\n"
"v_mfma_f32_16x16x32_bf16 v[168:171], v[32:35], a[40:43], v[168:171]\n"
"v_exp_f32_e64 v67, -v67\n"
"v_mfma_f32_16x16x32_bf16 v[172:175], v[32:35], a[44:47], v[172:175]\n"
"buffer_load_dwordx4 a[20:23], v0, s[48:51], s89 offen offset:1024 sc1\n"
"v_fma_f32 v66, v66, s94, s94\n"
"v_mfma_f32_16x16x32_bf16 v[176:179], v[36:39], a[32:35], v[176:179]\n"
"ds_read_b64_tr_b16 v[32:33], v5 offset:46496\n"
"ds_read_b64_tr_b16 v[34:35], v5 offset:47008\n"
"v_mfma_f32_16x16x32_bf16 v[180:183], v[36:39], a[36:39], v[180:183]\n"
"v_rcp_f32_e32 v66, v66\n"
"v_mfma_f32_16x16x32_bf16 v[184:187], v[36:39], a[40:43], v[184:187]\n"
"buffer_load_dwordx4 a[24:27], v0, s[48:51], s89 offen offset:2048 sc1\n"
"v_mul_f32_e32 v82, v82, v66\n"
"v_mfma_f32_16x16x32_bf16 v[188:191], v[36:39], a[44:47], v[188:191]\n"
"v_fma_f32 v67, v67, s94, s94\n"
"s_waitcnt lgkmcnt(4)\n"
"v_mfma_f32_16x16x32_bf16 v[96:99], v[40:43], a[160:163], v[96:99]\n"
"ds_read_b64_tr_b16 v[36:37], v7 offset:16992\n"
"ds_read_b64_tr_b16 v[38:39], v7 offset:17504\n"
"v_mfma_f32_16x16x32_bf16 v[100:103], v[40:43], a[164:167], v[100:103]\n"
"v_rcp_f32_e32 v67, v67\n"
"v_mfma_f32_16x16x32_bf16 v[104:107], v[40:43], a[168:171], v[104:107]\n"
"buffer_load_dwordx4 a[28:31], v0, s[48:51], s89 offen offset:3072 sc1\n"
"v_mul_f32_e32 v83, v83, v67\n"
"v_mfma_f32_16x16x32_bf16 v[108:111], v[40:43], a[172:175], v[108:111]\n"
"v_cvt_pk_bf16_f32 v45, v82, v83\n"
"v_mul_f32_e32 v84, v68, v84\n"
"v_mfma_f32_16x16x32_bf16 v[112:115], v[24:27], a[160:163], v[112:115]\n"
"ds_read_b64_tr_b16 v[40:41], v7 offset:21216\n"
"ds_read_b64_tr_b16 v[42:43], v7 offset:21728\n"
"v_mfma_f32_16x16x32_bf16 v[116:119], v[24:27], a[164:167], v[116:119]\n"
"s_add_u32 s89, s71, 0x2000\n"
"buffer_load_dwordx4 a[32:35], v0, s[48:51], s89 offen offset:0 sc1\n"
"v_mfma_f32_16x16x32_bf16 v[120:123], v[24:27], a[168:171], v[120:123]\n"
"v_exp_f32_e64 v68, -v68\n"
"v_mfma_f32_16x16x32_bf16 v[124:127], v[24:27], a[172:175], v[124:127]\n"
"v_fma_f32 v68, v68, s94, s94\n"
"s_waitcnt lgkmcnt(4)\n"
"v_mfma_f32_16x16x32_bf16 v[96:99], v[28:31], a[176:179], v[96:99]\n"
"ds_read_b64_tr_b16 v[24:25], v5 offset:64\n"
"ds_read_b64_tr_b16 v[26:27], v5 offset:576\n"
"v_mfma_f32_16x16x32_bf16 v[100:103], v[28:31], a[180:183], v[100:103]\n"
"v_rcp_f32_e32 v68, v68\n"
"v_mfma_f32_16x16x32_bf16 v[104:107], v[28:31], a[184:187], v[104:107]\n"
"buffer_load_dwordx4 a[36:39], v0, s[48:51], s89 offen offset:1024 sc1\n"
"v_mul_f32_e32 v84, v84, v68\n"
"v_mfma_f32_16x16x32_bf16 v[108:111], v[28:31], a[188:191], v[108:111]\n"
"v_mul_f32_e32 v85, v69, v85\n"
"v_mul_f32_e32 v86, v70, v86\n"
"v_mfma_f32_16x16x32_bf16 v[112:115], v[32:35], a[176:179], v[112:115]\n"
"ds_read_b64_tr_b16 v[28:29], v5 offset:4288\n"
"ds_read_b64_tr_b16 v[30:31], v5 offset:4800\n"
"v_mfma_f32_16x16x32_bf16 v[116:119], v[32:35], a[180:183], v[116:119]\n"
"v_exp_f32_e64 v69, -v69\n"
"v_mfma_f32_16x16x32_bf16 v[120:123], v[32:35], a[184:187], v[120:123]\n"
"v_exp_f32_e64 v70, -v70\n"
"v_mfma_f32_16x16x32_bf16 v[124:127], v[32:35], a[188:191], v[124:127]\n"
"v_fma_f32 v69, v69, s94, s94\n"
"s_waitcnt lgkmcnt(4)\n"
"v_mfma_f32_16x16x32_bf16 v[160:163], v[36:39], a[48:51], v[160:163]\n"
"ds_read_b64_tr_b16 v[32:33], v5 offset:50720\n"
"ds_read_b64_tr_b16 v[34:35], v5 offset:51232\n"
"v_mfma_f32_16x16x32_bf16 v[164:167], v[36:39], a[52:55], v[164:167]\n"
"v_rcp_f32_e32 v69, v69\n"
"v_mfma_f32_16x16x32_bf16 v[168:171], v[36:39], a[56:59], v[168:171]\n"
"buffer_load_dwordx4 a[40:43], v0, s[48:51], s89 offen offset:2048 sc1\n"
"v_mul_f32_e32 v85, v85, v69\n"
"v_mfma_f32_16x16x32_bf16 v[172:175], v[36:39], a[60:63], v[172:175]\n"
"v_cvt_pk_bf16_f32 v48, v84, v85\n"
"v_fma_f32 v70, v70, s94, s94\n"
"v_mfma_f32_16x16x32_bf16 v[176:179], v[40:43], a[48:51], v[176:179]\n"
"ds_read_b64_tr_b16 v[36:37], v5 offset:54944\n"
"ds_read_b64_tr_b16 v[38:39], v5 offset:55456\n"
"v_mfma_f32_16x16x32_bf16 v[180:183], v[40:43], a[52:55], v[180:183]\n"
"v_rcp_f32_e32 v70, v70\n"
"v_mfma_f32_16x16x32_bf16 v[184:187], v[40:43], a[56:59], v[184:187]\n"
"buffer_load_dwordx4 a[44:47], v0, s[48:51], s89 offen offset:3072 sc1\n"
"v_mul_f32_e32 v86, v86, v70\n"
"v_mfma_f32_16x16x32_bf16 v[188:191], v[40:43], a[60:63], v[188:191]\n"
"v_mul_f32_e32 v87, v71, v87\n"
"s_waitcnt lgkmcnt(4)\n"
"v_mfma_f32_16x16x32_bf16 v[128:131], v[24:27], a[80:83], v[128:131]\n"
"ds_read_b64_tr_b16 v[40:41], v7 offset:25440\n"
"ds_read_b64_tr_b16 v[42:43], v7 offset:25952\n"
"v_mfma_f32_16x16x32_bf16 v[132:135], v[24:27], a[84:87], v[132:135]\n"
"s_add_u32 s89, s71, 0x3000\n"
"buffer_load_dwordx4 a[48:51], v0, s[48:51], s89 offen offset:0 sc1\n"
"v_mfma_f32_16x16x32_bf16 v[136:139], v[24:27], a[88:91], v[136:139]\n"
"v_exp_f32_e64 v71, -v71\n"
"v_mfma_f32_16x16x32_bf16 v[140:143], v[24:27], a[92:95], v[140:143]\n"
"v_fma_f32 v71, v71, s94, s94\n"
"v_mul_f32_e32 v88, v72, v88\n"
"v_mfma_f32_16x16x32_bf16 v[144:147], v[28:31], a[80:83], v[144:147]\n"
"ds_read_b64_tr_b16 v[24:25], v7 offset:29664\n"
"ds_read_b64_tr_b16 v[26:27], v7 offset:30176\n"
"v_mfma_f32_16x16x32_bf16 v[148:151], v[28:31], a[84:87], v[148:151]\n"
"v_rcp_f32_e32 v71, v71\n"
"v_mfma_f32_16x16x32_bf16 v[152:155], v[28:31], a[88:91], v[152:155]\n"
"v_exp_f32_e64 v72, -v72\n"
"v_mfma_f32_16x16x32_bf16 v[156:159], v[28:31], a[92:95], v[156:159]\n"
"buffer_load_dwordx4 a[52:55], v0, s[48:51], s89 offen offset:1024 sc1\n"
"s_waitcnt lgkmcnt(4)\n"
"s_barrier\n"
"v_mfma_f32_16x16x32_bf16 v[96:99], v[32:35], a[192:195], v[96:99]\n"
"ds_read_b64_tr_b16 v[28:29], v6 offset:32\n"
"ds_read_b64_tr_b16 v[30:31], v6 offset:544\n"
"v_mfma_f32_16x16x32_bf16 v[100:103], v[32:35], a[196:199], v[100:103]\n"
"s_add_u32 m0, s17, 0x1ce00\n"
"s_add_u32 s88, s61, 0x6c000\n"
"v_mfma_f32_16x16x32_bf16 v[104:107], v[32:35], a[200:203], v[104:107]\n"
"buffer_load_dwordx4 v3, s[44:47], s88 offen lds\n"
"v_mul_f32_e32 v87, v87, v71\n"
"v_mfma_f32_16x16x32_bf16 v[108:111], v[32:35], a[204:207], v[108:111]\n"
"v_cvt_pk_bf16_f32 v49, v86, v87\n"
"v_fma_f32 v72, v72, s94, s94\n"
"v_mfma_f32_16x16x32_bf16 v[112:115], v[36:39], a[192:195], v[112:115]\n"
"ds_read_b64_tr_b16 v[32:33], v6 offset:4256\n"
"ds_read_b64_tr_b16 v[34:35], v6 offset:4768\n"
"v_mfma_f32_16x16x32_bf16 v[116:119], v[36:39], a[196:199], v[116:119]\n"
"v_rcp_f32_e32 v72, v72\n"
"v_mfma_f32_16x16x32_bf16 v[120:123], v[36:39], a[200:203], v[120:123]\n"
"v_mul_f32_e32 v88, v88, v72\n"
"v_mul_f32_e32 v89, v73, v89\n"
"v_mfma_f32_16x16x32_bf16 v[124:127], v[36:39], a[204:207], v[124:127]\n"
"v_mul_f32_e32 v90, v74, v90\n"
"s_waitcnt lgkmcnt(4)\n"
"v_mfma_f32_16x16x32_bf16 v[160:163], v[40:43], a[64:67], v[160:163]\n"
"ds_read_b64_tr_b16 v[36:37], v7 offset:33856\n"
"ds_read_b64_tr_b16 v[38:39], v7 offset:34368\n"
"v_mfma_f32_16x16x32_bf16 v[164:167], v[40:43], a[68:71], v[164:167]\n"
"v_exp_f32_e64 v73, -v73\n"
"v_mfma_f32_16x16x32_bf16 v[168:171], v[40:43], a[72:75], v[168:171]\n"
"v_exp_f32_e64 v74, -v74\n"
"v_mfma_f32_16x16x32_bf16 v[172:175], v[40:43], a[76:79], v[172:175]\n"
"buffer_load_dwordx4 v4, s[44:47], s88 offen offset:1056 lds\n"
"v_fma_f32 v73, v73, s94, s94\n"
"v_mfma_f32_16x16x32_bf16 v[176:179], v[24:27], a[64:67], v[176:179]\n"
"ds_read_b64_tr_b16 v[40:41], v7 offset:38080\n"
"ds_read_b64_tr_b16 v[42:43], v7 offset:38592\n"
"v_mfma_f32_16x16x32_bf16 v[180:183], v[24:27], a[68:71], v[180:183]\n"
"s_add_u32 m0, s17, 0x1ef00\n"
"s_add_u32 s88, s60, 0x0\n"
"v_mfma_f32_16x16x32_bf16 v[184:187], v[24:27], a[72:75], v[184:187]\n"
"v_rcp_f32_e32 v73, v73\n"
"v_mfma_f32_16x16x32_bf16 v[188:191], v[24:27], a[76:79], v[188:191]\n"
"buffer_load_dwordx4 v3, s[44:47], s88 offen lds\n"
"s_waitcnt lgkmcnt(4)\n"
"v_mfma_f32_16x16x32_bf16 v[96:99], v[28:31], a[208:211], v[96:99]\n"
"ds_read_b64_tr_b16 v[24:25], v6 offset:8480\n"
"ds_read_b64_tr_b16 v[26:27], v6 offset:8992\n"
"v_mfma_f32_16x16x32_bf16 v[100:103], v[28:31], a[212:215], v[100:103]\n"
"buffer_load_dwordx4 v4, s[44:47], s88 offen offset:1056 lds\n"
"v_mul_f32_e32 v89, v89, v73\n"
"v_mfma_f32_16x16x32_bf16 v[104:107], v[28:31], a[216:219], v[104:107]\n"
"v_cvt_pk_bf16_f32 v52, v88, v89\n"
"v_fma_f32 v74, v74, s94, s94\n"
"v_mfma_f32_16x16x32_bf16 v[108:111], v[28:31], a[220:223], v[108:111]\n"
"v_rcp_f32_e32 v74, v74\n"
"v_mfma_f32_16x16x32_bf16 v[112:115], v[32:35], a[208:211], v[112:115]\n"
"ds_read_b64_tr_b16 v[28:29], v6 offset:12704\n"
"ds_read_b64_tr_b16 v[30:31], v6 offset:13216\n"
"v_mfma_f32_16x16x32_bf16 v[116:119], v[32:35], a[212:215], v[116:119]\n"
"buffer_load_dwordx4 a[56:59], v0, s[48:51], s89 offen offset:2048 sc1\n"
"v_mul_f32_e32 v90, v90, v74\n"
"v_mfma_f32_16x16x32_bf16 v[120:123], v[32:35], a[216:219], v[120:123]\n"
"v_mul_f32_e32 v91, v75, v91\n"
"v_mul_f32_e32 v92, v76, v92\n"
"v_mfma_f32_16x16x32_bf16 v[124:127], v[32:35], a[220:223], v[124:127]\n"
"v_mul_f32_e32 v93, v77, v93\n"
"s_waitcnt lgkmcnt(4)\n"
"v_mfma_f32_16x16x32_bf16 v[128:131], v[36:39], a[96:99], v[128:131]\n"
"ds_read_b64_tr_b16 v[32:33], v5 offset:96\n"
"ds_read_b64_tr_b16 v[34:35], v5 offset:608\n"
"v_mfma_f32_16x16x32_bf16 v[132:135], v[36:39], a[100:103], v[132:135]\n"
"v_exp_f32_e64 v75, -v75\n"
"v_mfma_f32_16x16x32_bf16 v[136:139], v[36:39], a[104:107], v[136:139]\n"
"v_exp_f32_e64 v76, -v76\n"
"v_mfma_f32_16x16x32_bf16 v[140:143], v[36:39], a[108:111], v[140:143]\n"
"buffer_load_dwordx4 a[60:63], v0, s[48:51], s89 offen offset:3072 sc1\n"
"v_fma_f32 v75, v75, s94, s94\n"
"v_mfma_f32_16x16x32_bf16 v[144:147], v[40:43], a[96:99], v[144:147]\n"
"ds_read_b64_tr_b16 v[36:37], v5 offset:4320\n"
"ds_read_b64_tr_b16 v[38:39], v5 offset:4832\n"
"v_mfma_f32_16x16x32_bf16 v[148:151], v[40:43], a[100:103], v[148:151]\n"
"v_rcp_f32_e32 v75, v75\n"
"v_mfma_f32_16x16x32_bf16 v[152:155], v[40:43], a[104:107], v[152:155]\n"
"v_mul_f32_e32 v91, v91, v75\n"
"v_fma_f32 v76, v76, s94, s94\n"
"v_mfma_f32_16x16x32_bf16 v[156:159], v[40:43], a[108:111], v[156:159]\n"
"v_cvt_pk_bf16_f32 v53, v90, v91\n"
"s_waitcnt lgkmcnt(4)\n"
"v_mfma_f32_16x16x32_bf16 v[96:99], v[24:27], a[224:227], v[96:99]\n"
"ds_read_b64_tr_b16 v[40:41], v5 offset:8512\n"
"ds_read_b64_tr_b16 v[42:43], v5 offset:9024\n"
"v_mfma_f32_16x16x32_bf16 v[100:103], v[24:27], a[228:231], v[100:103]\n"
"s_add_u32 m0, s17, 0x21000\n"
"s_add_u32 s88, s60, 0x24000\n"
"v_mfma_f32_16x16x32_bf16 v[104:107], v[24:27], a[232:235], v[104:107]\n"
"v_rcp_f32_e32 v76, v76\n"
"v_mfma_f32_16x16x32_bf16 v[108:111], v[24:27], a[236:239], v[108:111]\n"
"buffer_load_dwordx4 v3, s[44:47], s88 offen lds\n"
"v_mul_f32_e32 v92, v92, v76\n"
"v_mfma_f32_16x16x32_bf16 v[112:115], v[28:31], a[224:227], v[112:115]\n"
"ds_read_b64_tr_b16 v[24:25], v5 offset:12736\n"
"ds_read_b64_tr_b16 v[26:27], v5 offset:13248\n"
"v_mfma_f32_16x16x32_bf16 v[116:119], v[28:31], a[228:231], v[116:119]\n"
"v_exp_f32_e64 v77, -v77\n"
"v_mfma_f32_16x16x32_bf16 v[120:123], v[28:31], a[232:235], v[120:123]\n"
"buffer_load_dwordx4 v4, s[44:47], s88 offen offset:1056 lds\n"
"v_fma_f32 v77, v77, s94, s94\n"
"v_mfma_f32_16x16x32_bf16 v[124:127], v[28:31], a[236:239], v[124:127]\n"
"v_mul_f32_e32 v94, v78, v94\n"
"s_waitcnt lgkmcnt(4)\n"
"v_mfma_f32_16x16x32_bf16 v[160:163], v[32:35], a[80:83], v[160:163]\n"
"ds_read_b64_tr_b16 v[28:29], v6 offset:16928\n"
"ds_read_b64_tr_b16 v[30:31], v6 offset:17440\n"
"v_mfma_f32_16x16x32_bf16 v[164:167], v[32:35], a[84:87], v[164:167]\n"
"v_rcp_f32_e32 v77, v77\n"
"v_mfma_f32_16x16x32_bf16 v[168:171], v[32:35], a[88:91], v[168:171]\n"
"v_exp_f32_e64 v78, -v78\n"
"v_mfma_f32_16x16x32_bf16 v[172:175], v[32:35], a[92:95], v[172:175]\n"
"v_mul_f32_e32 v93, v93, v77\n"
"v_fma_f32 v78, v78, s94, s94\n"
"v_mfma_f32_16x16x32_bf16 v[176:179], v[36:39], a[80:83], v[176:179]\n"
"ds_read_b64_tr_b16 v[32:33], v6 offset:21152\n"
"ds_read_b64_tr_b16 v[34:35], v6 offset:21664\n"
"v_mfma_f32_16x16x32_bf16 v[180:183], v[36:39], a[84:87], v[180:183]\n"
"v_rcp_f32_e32 v78, v78\n"
"v_mfma_f32_16x16x32_bf16 v[184:187], v[36:39], a[88:91], v[184:187]\n"
"v_cvt_pk_bf16_f32 v56, v92, v93\n"
"v_mul_f32_e32 v94, v94, v78\n"
"v_mfma_f32_16x16x32_bf16 v[188:191], v[36:39], a[92:95], v[188:191]\n"
"v_mul_f32_e32 v95, v79, v95\n"
"s_waitcnt lgkmcnt(4)\n"
"v_mfma_f32_16x16x32_bf16 v[128:131], v[40:43], a[112:115], v[128:131]\n"
"ds_read_b64_tr_b16 v[36:37], v5 offset:16960\n"
"ds_read_b64_tr_b16 v[38:39], v5 offset:17472\n"
"v_mfma_f32_16x16x32_bf16 v[132:135], v[40:43], a[116:119], v[132:135]\n"
"v_exp_f32_e64 v79, -v79\n"
"v_mfma_f32_16x16x32_bf16 v[136:139], v[40:43], a[120:123], v[136:139]\n"
"v_fma_f32 v79, v79, s94, s94\n"
"v_mfma_f32_16x16x32_bf16 v[140:143], v[40:43], a[124:127], v[140:143]\n"
"v_rcp_f32_e32 v79, v79\n"
"v_mfma_f32_16x16x32_bf16 v[144:147], v[24:27], a[112:115], v[144:147]\n"
"ds_read_b64_tr_b16 v[40:41], v5 offset:21184\n"
"ds_read_b64_tr_b16 v[42:43], v5 offset:21696\n"
"v_mfma_f32_16x16x32_bf16 v[148:151], v[24:27], a[116:119], v[148:151]\n"
"v_mul_f32_e32 v95, v95, v79\n"
"v_mfma_f32_16x16x32_bf16 v[152:155], v[24:27], a[120:123], v[152:155]\n"
"v_cvt_pk_bf16_f32 v57, v94, v95\n"
"v_mfma_f32_16x16x32_bf16 v[156:159], v[24:27], a[124:127], v[156:159]\n"
"s_waitcnt lgkmcnt(4)\n"
"v_mfma_f32_16x16x32_bf16 v[96:99], v[28:31], a[240:243], v[96:99]\n"
"ds_read_b64_tr_b16 v[24:25], v7 offset:33888\n"
"ds_read_b64_tr_b16 v[26:27], v7 offset:34400\n"
"v_mfma_f32_16x16x32_bf16 v[100:103], v[28:31], a[244:247], v[100:103]\n"
"v_mfma_f32_16x16x32_bf16 v[104:107], v[28:31], a[248:251], v[104:107]\n"
"v_mfma_f32_16x16x32_bf16 v[108:111], v[28:31], a[252:255], v[108:111]\n"
"v_mfma_f32_16x16x32_bf16 v[112:115], v[32:35], a[240:243], v[112:115]\n"
"ds_read_b64_tr_b16 v[28:29], v7 offset:38112\n"
"ds_read_b64_tr_b16 v[30:31], v7 offset:38624\n"
"v_mfma_f32_16x16x32_bf16 v[116:119], v[32:35], a[244:247], v[116:119]\n"
"v_mfma_f32_16x16x32_bf16 v[120:123], v[32:35], a[248:251], v[120:123]\n"
"v_mfma_f32_16x16x32_bf16 v[124:127], v[32:35], a[252:255], v[124:127]\n"
"s_waitcnt lgkmcnt(4)\n"
"s_barrier\n"
"v_mfma_f32_16x16x32_bf16 v[128:131], v[36:39], a[128:131], v[128:131]\n"
"ds_read_b64_tr_b16 v[32:33], v5 offset:25408\n"
"ds_read_b64_tr_b16 v[34:35], v5 offset:25920\n"
"v_mfma_f32_16x16x32_bf16 v[132:135], v[36:39], a[132:135], v[132:135]\n"
"s_add_u32 m0, s17, 0x0\n"
"s_add_u32 s88, s60, 0x48000\n"
"v_mfma_f32_16x16x32_bf16 v[136:139], v[36:39], a[136:139], v[136:139]\n"
"buffer_load_dwordx4 v3, s[44:47], s88 offen lds\n"
"v_mfma_f32_16x16x32_bf16 v[140:143], v[36:39], a[140:143], v[140:143]\n"
"v_mfma_f32_16x16x32_bf16 v[144:147], v[40:43], a[128:131], v[144:147]\n"
"ds_read_b64_tr_b16 v[36:37], v5 offset:29632\n"
"ds_read_b64_tr_b16 v[38:39], v5 offset:30144\n"
"v_mfma_f32_16x16x32_bf16 v[148:151], v[40:43], a[132:135], v[148:151]\n"
"buffer_load_dwordx4 v4, s[44:47], s88 offen offset:1056 lds\n"
"v_mul_f32_e32 v112, v96, v112\n"
"v_mfma_f32_16x16x32_bf16 v[152:155], v[40:43], a[136:139], v[152:155]\n"
"v_exp_f32_e64 v96, -v96\n"
"v_mfma_f32_16x16x32_bf16 v[156:159], v[40:43], a[140:143], v[156:159]\n"
"v_fma_f32 v96, v96, s94, s94\n"
"s_waitcnt lgkmcnt(4)\n"
"v_mfma_f32_16x16x32_bf16 v[160:163], v[24:27], a[96:99], v[160:163]\n"
"ds_read_b64_tr_b16 v[40:41], v5 offset:33856\n"
"ds_read_b64_tr_b16 v[42:43], v5 offset:34368\n"
"v_mfma_f32_16x16x32_bf16 v[164:167], v[24:27], a[100:103], v[164:167]\n"
"buffer_load_dword v20, v11, s[48:51], s70 offen sc1\n"
"s_add_u32 m0, s17, 0x23100\n"
"s_add_u32 s88, s60, 0x6c000\n"
"v_mfma_f32_16x16x32_bf16 v[168:171], v[24:27], a[104:107], v[168:171]\n"
"v_rcp_f32_e32 v96, v96\n"
"v_mfma_f32_16x16x32_bf16 v[172:175], v[24:27], a[108:111], v[172:175]\n"
"buffer_load_dwordx4 v3, s[44:47], s88 offen lds\n"
"v_mul_f32_e32 v112, v112, v96\n"
"v_mfma_f32_16x16x32_bf16 v[176:179], v[28:31], a[96:99], v[176:179]\n"
"ds_read_b64_tr_b16 v[24:25], v5 offset:38080\n"
"ds_read_b64_tr_b16 v[26:27], v5 offset:38592\n"
"v_mfma_f32_16x16x32_bf16 v[180:183], v[28:31], a[100:103], v[180:183]\n"
"buffer_load_dwordx4 v4, s[44:47], s88 offen offset:1056 lds\n"
"v_mul_f32_e32 v113, v97, v113\n"
"v_mul_f32_e32 v114, v98, v114\n"
"v_mfma_f32_16x16x32_bf16 v[184:187], v[28:31], a[104:107], v[184:187]\n"
"v_exp_f32_e64 v97, -v97\n"
"v_mfma_f32_16x16x32_bf16 v[188:191], v[28:31], a[108:111], v[188:191]\n"
"v_fma_f32 v97, v97, s94, s94\n"
"s_waitcnt lgkmcnt(4)\n"
"v_mfma_f32_16x16x32_bf16 v[128:131], v[32:35], a[144:147], v[128:131]\n"
"ds_read_b64_tr_b16 v[28:29], v5 offset:8544\n"
"ds_read_b64_tr_b16 v[30:31], v5 offset:9056\n"
"v_mfma_f32_16x16x32_bf16 v[132:135], v[32:35], a[148:151], v[132:135]\n"
"v_rcp_f32_e32 v97, v97\n"
"v_mul_f32_e32 v115, v99, v115\n"
"v_mfma_f32_16x16x32_bf16 v[136:139], v[32:35], a[152:155], v[136:139]\n"
"v_exp_f32_e64 v98, -v98\n"
"v_mfma_f32_16x16x32_bf16 v[140:143], v[32:35], a[156:159], v[140:143]\n"
"v_mul_f32_e32 v113, v113, v97\n"
"v_fma_f32 v98, v98, s94, s94\n"
"v_mfma_f32_16x16x32_bf16 v[144:147], v[36:39], a[144:147], v[144:147]\n"
"ds_read_b64_tr_b16 v[32:33], v5 offset:12768\n"
"ds_read_b64_tr_b16 v[34:35], v5 offset:13280\n"
"v_mfma_f32_16x16x32_bf16 v[148:151], v[36:39], a[148:151], v[148:151]\n"
"v_cvt_pk_bf16_f32 v46, v112, v113\n"
"v_rcp_f32_e32 v98, v98\n"
"v_mfma_f32_16x16x32_bf16 v[152:155], v[36:39], a[152:155], v[152:155]\n"
"v_exp_f32_e64 v99, -v99\n"
"v_mfma_f32_16x16x32_bf16 v[156:159], v[36:39], a[156:159], v[156:159]\n"
"v_mul_f32_e32 v114, v114, v98\n"
"s_waitcnt lgkmcnt(4)\n"
"v_mfma_f32_16x16x32_bf16 v[128:131], v[40:43], a[160:163], v[128:131]\n"
"ds_read_b64_tr_b16 v[36:37], v5 offset:42304\n"
"ds_read_b64_tr_b16 v[38:39], v5 offset:42816\n"
"v_mfma_f32_16x16x32_bf16 v[132:135], v[40:43], a[164:167], v[132:135]\n"
"v_fma_f32 v99, v99, s94, s94\n"
"v_mul_f32_e32 v116, v100, v116\n"
"v_mul_f32_e32 v117, v101, v117\n"
"v_mfma_f32_16x16x32_bf16 v[136:139], v[40:43], a[168:171], v[136:139]\n"
"v_rcp_f32_e32 v99, v99\n"
"v_mfma_f32_16x16x32_bf16 v[140:143], v[40:43], a[172:175], v[140:143]\n"
"v_exp_f32_e64 v100, -v100\n"
"v_mfma_f32_16x16x32_bf16 v[144:147], v[24:27], a[160:163], v[144:147]\n"
"ds_read_b64_tr_b16 v[40:41], v5 offset:46528\n"
"ds_read_b64_tr_b16 v[42:43], v5 offset:47040\n"
"v_mfma_f32_16x16x32_bf16 v[148:151], v[24:27], a[164:167], v[148:151]\n"
"v_mul_f32_e32 v115, v115, v99\n"
"v_exp_f32_e64 v101, -v101\n"
"v_mfma_f32_16x16x32_bf16 v[152:155], v[24:27], a[168:171], v[152:155]\n"
"v_cvt_pk_bf16_f32 v47, v114, v115\n"
"v_fma_f32 v100, v100, s94, s94\n"
"v_mfma_f32_16x16x32_bf16 v[156:159], v[24:27], a[172:175], v[156:159]\n"
"v_fma_f32 v101, v101, s94, s94\n"
"s_waitcnt lgkmcnt(4)\n"
"v_mfma_f32_16x16x32_bf16 v[160:163], v[28:31], a[112:115], v[160:163]\n"
"ds_read_b64_tr_b16 v[24:25], v5 offset:50752\n"
"ds_read_b64_tr_b16 v[26:27], v5 offset:51264\n"
"v_mfma_f32_16x16x32_bf16 v[164:167], v[28:31], a[116:119], v[164:167]\n"
"s_add_u32 s90, s65, 0xa000\n"
"buffer_store_dwordx4 v[44:47], v0, s[48:51], s90 offen offset:0 sc1\n"
"v_mul_f32_e32 v118, v102, v118\n"
"v_mfma_f32_16x16x32_bf16 v[168:171], v[28:31], a[120:123], v[168:171]\n"
"v_rcp_f32_e32 v100, v100\n"
"v_mfma_f32_16x16x32_bf16 v[172:175], v[28:31], a[124:127], v[172:175]\n"
"v_rcp_f32_e32 v101, v101\n"
"v_mfma_f32_16x16x32_bf16 v[176:179], v[32:35], a[112:115], v[176:179]\n"
"ds_read_b64_tr_b16 v[28:29], v5 offset:54976\n"
"ds_read_b64_tr_b16 v[30:31], v5 offset:55488\n"
"v_mfma_f32_16x16x32_bf16 v[180:183], v[32:35], a[116:119], v[180:183]\n"
"v_mul_f32_e32 v116, v116, v100\n"
"v_exp_f32_e64 v102, -v102\n"
"v_mfma_f32_16x16x32_bf16 v[184:187], v[32:35], a[120:123], v[184:187]\n"
"v_mul_f32_e32 v117, v117, v101\n"
"v_fma_f32 v102, v102, s94, s94\n"
"v_mfma_f32_16x16x32_bf16 v[188:191], v[32:35], a[124:127], v[188:191]\n"
"v_cvt_pk_bf16_f32 v50, v116, v117\n"
"s_waitcnt lgkmcnt(4)\n"
"v_mfma_f32_16x16x32_bf16 v[128:131], v[36:39], a[176:179], v[128:131]\n"
"ds_read_b64_tr_b16 v[32:33], v5 offset:16992\n"
"ds_read_b64_tr_b16 v[34:35], v5 offset:17504\n"
"v_mfma_f32_16x16x32_bf16 v[132:135], v[36:39], a[180:183], v[132:135]\n"
"v_rcp_f32_e32 v102, v102\n"
"v_mul_f32_e32 v119, v103, v119\n"
"v_mfma_f32_16x16x32_bf16 v[136:139], v[36:39], a[184:187], v[136:139]\n"
"v_exp_f32_e64 v103, -v103\n"
"v_mfma_f32_16x16x32_bf16 v[140:143], v[36:39], a[188:191], v[140:143]\n"
"v_mul_f32_e32 v118, v118, v102\n"
"v_fma_f32 v103, v103, s94, s94\n"
"v_mfma_f32_16x16x32_bf16 v[144:147], v[40:43], a[176:179], v[144:147]\n"
"ds_read_b64_tr_b16 v[36:37], v5 offset:21216\n"
"ds_read_b64_tr_b16 v[38:39], v5 offset:21728\n"
"v_mfma_f32_16x16x32_bf16 v[148:151], v[40:43], a[180:183], v[148:151]\n"
"v_rcp_f32_e32 v103, v103\n"
"v_mul_f32_e32 v120, v104, v120\n"
"v_mfma_f32_16x16x32_bf16 v[152:155], v[40:43], a[184:187], v[152:155]\n"
"v_exp_f32_e64 v104, -v104\n"
"v_mfma_f32_16x16x32_bf16 v[156:159], v[40:43], a[188:191], v[156:159]\n"
"v_mul_f32_e32 v119, v119, v103\n"
"s_waitcnt lgkmcnt(4)\n"
"v_mfma_f32_16x16x32_bf16 v[128:131], v[24:27], a[192:195], v[128:131]\n"
"ds_read_b64_tr_b16 v[40:41], v6 offset:64\n"
"ds_read_b64_tr_b16 v[42:43], v6 offset:576\n"
"v_mfma_f32_16x16x32_bf16 v[132:135], v[24:27], a[196:199], v[132:135]\n"
"v_cvt_pk_bf16_f32 v51, v118, v119\n"
"s_add_u32 s90, s65, 0xa000\n"
"buffer_store_dwordx4 v[48:51], v0, s[48:51], s90 offen offset:1024 sc1\n"
"v_mfma_f32_16x16x32_bf16 v[136:139], v[24:27], a[200:203], v[136:139]\n"
"v_fma_f32 v104, v104, s94, s94\n"
"v_mul_f32_e32 v121, v105, v121\n"
"v_mfma_f32_16x16x32_bf16 v[140:143], v[24:27], a[204:207], v[140:143]\n"
"v_rcp_f32_e32 v104, v104\n"
"v_mfma_f32_16x16x32_bf16 v[144:147], v[28:31], a[192:195], v[144:147]\n"
"ds_read_b64_tr_b16 v[24:25], v6 offset:4288\n"
"ds_read_b64_tr_b16 v[26:27], v6 offset:4800\n"
"v_mfma_f32_16x16x32_bf16 v[148:151], v[28:31], a[196:199], v[148:151]\n"
"v_mul_f32_e32 v120, v120, v104\n"
"v_exp_f32_e64 v105, -v105\n"
"v_mfma_f32_16x16x32_bf16 v[152:155], v[28:31], a[200:203], v[152:155]\n"
"v_fma_f32 v105, v105, s94, s94\n"
"v_mul_f32_e32 v122, v106, v122\n"
"v_mfma_f32_16x16x32_bf16 v[156:159], v[28:31], a[204:207], v[156:159]\n"
"v_mul_f32_e32 v123, v107, v123\n"
"s_waitcnt lgkmcnt(4)\n"
"v_mfma_f32_16x16x32_bf16 v[160:163], v[32:35], a[128:131], v[160:163]\n"
"ds_read_b64_tr_b16 v[28:29], v6 offset:8512\n"
"ds_read_b64_tr_b16 v[30:31], v6 offset:9024\n"
"v_mfma_f32_16x16x32_bf16 v[164:167], v[32:35], a[132:135], v[164:167]\n"
"v_rcp_f32_e32 v105, v105\n"
"v_mul_f32_e32 v124, v108, v124\n"
"v_mfma_f32_16x16x32_bf16 v[168:171], v[32:35], a[136:139], v[168:171]\n"
"v_exp_f32_e64 v106, -v106\n"
"v_mfma_f32_16x16x32_bf16 v[172:175], v[32:35], a[140:143], v[172:175]\n"
"v_mul_f32_e32 v121, v121, v105\n"
"v_fma_f32 v106, v106, s94, s94\n"
"v_mfma_f32_16x16x32_bf16 v[176:179], v[36:39], a[128:131], v[176:179]\n"
"ds_read_b64_tr_b16 v[32:33], v6 offset:12736\n"
"ds_read_b64_tr_b16 v[34:35], v6 offset:13248\n"
"v_mfma_f32_16x16x32_bf16 v[180:183], v[36:39], a[132:135], v[180:183]\n"
"v_cvt_pk_bf16_f32 v54, v120, v121\n"
"v_rcp_f32_e32 v106, v106\n"
"v_mfma_f32_16x16x32_bf16 v[184:187], v[36:39], a[136:139], v[184:187]\n"
"v_exp_f32_e64 v107, -v107\n"
"v_mfma_f32_16x16x32_bf16 v[188:191], v[36:39], a[140:143], v[188:191]\n"
"v_mul_f32_e32 v122, v122, v106\n"
"s_waitcnt lgkmcnt(4)\n"
"s_barrier\n"
"s_waitcnt vmcnt(4)\n"
"v_cmp_gt_u32_e32 vcc, 3, v20\n"
"s_cbranch_vccnz .LKF_sP3_%=\n"
".LKF_rP3_%=:\n"
"v_mfma_f32_16x16x32_bf16 v[128:131], v[40:43], a[208:211], v[128:131]\n"
"ds_read_b64_tr_b16 v[36:37], v5 offset:25440\n"
"ds_read_b64_tr_b16 v[38:39], v5 offset:25952\n"
"v_mfma_f32_16x16x32_bf16 v[132:135], v[40:43], a[212:215], v[132:135]\n"
"s_add_u32 m0, s17, 0x2100\n"
"s_add_u32 s88, s61, 0x9000\n"
"buffer_load_dwordx4 v3, s[44:47], s88 offen lds\n"
"v_mfma_f32_16x16x32_bf16 v[136:139], v[40:43], a[216:219], v[136:139]\n"
"v_exp_f32_e64 v108, -v108\n"
"v_mfma_f32_16x16x32_bf16 v[140:143], v[40:43], a[220:223], v[140:143]\n"
"v_fma_f32 v107, v107, s94, s94\n"
"v_fma_f32 v108, v108, s94, s94\n"
"v_mfma_f32_16x16x32_bf16 v[144:147], v[24:27], a[208:211], v[144:147]\n"
"ds_read_b64_tr_b16 v[40:41], v5 offset:29664\n"
"ds_read_b64_tr_b16 v[42:43], v5 offset:30176\n"
"v_mfma_f32_16x16x32_bf16 v[148:151], v[24:27], a[212:215], v[148:151]\n"
"v_rcp_f32_e32 v107, v107\n"
"buffer_load_dwordx4 v4, s[44:47], s88 offen offset:1056 lds\n"
"v_mfma_f32_16x16x32_bf16 v[152:155], v[24:27], a[216:219], v[152:155]\n"
"v_rcp_f32_e32 v108, v108\n"
"v_mfma_f32_16x16x32_bf16 v[156:159], v[24:27], a[220:223], v[156:159]\n"
"v_mul_f32_e32 v123, v123, v107\n"
"s_waitcnt lgkmcnt(4)\n"
"v_mfma_f32_16x16x32_bf16 v[128:131], v[28:31], a[224:227], v[128:131]\n"
"ds_read_b64_tr_b16 v[24:25], v6 offset:16960\n"
"ds_read_b64_tr_b16 v[26:27], v6 offset:17472\n"
"v_mfma_f32_16x16x32_bf16 v[132:135], v[28:31], a[228:231], v[132:135]\n"
"s_add_u32 m0, s17, 0x4200\n"
"s_add_u32 s88, s64, 0x2000\n"
"buffer_load_dwordx4 v0, s[48:51], s88 offen lds\n"
"v_mfma_f32_16x16x32_bf16 v[136:139], v[28:31], a[232:235], v[136:139]\n"
"v_cvt_pk_bf16_f32 v55, v122, v123\n"
"v_mul_f32_e32 v124, v124, v108\n"
"v_mfma_f32_16x16x32_bf16 v[140:143], v[28:31], a[236:239], v[140:143]\n"
"s_add_u32 s90, s66, 0xa000\n"
"buffer_store_dwordx4 v[52:55], v0, s[48:51], s90 offen offset:0 sc1\n"
"v_mfma_f32_16x16x32_bf16 v[144:147], v[32:35], a[224:227], v[144:147]\n"
"ds_read_b64_tr_b16 v[28:29], v6 offset:21184\n"
"ds_read_b64_tr_b16 v[30:31], v6 offset:21696\n"
"v_mfma_f32_16x16x32_bf16 v[148:151], v[32:35], a[228:231], v[148:151]\n"
"buffer_load_dwordx4 v0, s[48:51], s88 offen offset:1024 lds\n"
"v_mul_f32_e32 v125, v109, v125\n"
"v_mul_f32_e32 v126, v110, v126\n"
".LKF_pad6:\n"
"s_cbranch_execz .LK3_ret\n"
"v_mfma_f32_16x16x32_bf16 v[152:155], v[32:35], a[232:235], v[152:155]\n"
"v_exp_f32_e64 v109, -v109\n"
"v_mfma_f32_16x16x32_bf16 v[156:159], v[32:35], a[236:239], v[156:159]\n"
"v_fma_f32 v109, v109, s94, s94\n"
"s_waitcnt lgkmcnt(4)\n"
"v_mfma_f32_16x16x32_bf16 v[160:163], v[36:39], a[144:147], v[160:163]\n"
"ds_read_b64_tr_b16 v[32:33], v5 offset:33888\n"
"ds_read_b64_tr_b16 v[34:35], v5 offset:34400\n"
"v_mfma_f32_16x16x32_bf16 v[164:167], v[36:39], a[148:151], v[164:167]\n"
"s_add_u32 m0, s17, 0x25200\n"
"s_add_u32 s88, s60, 0x9000\n"
"buffer_load_dwordx4 v3, s[44:47], s88 offen lds\n"
"v_mfma_f32_16x16x32_bf16 v[168:171], v[36:39], a[152:155], v[168:171]\n"
"v_rcp_f32_e32 v109, v109\n"
"v_mfma_f32_16x16x32_bf16 v[172:175], v[36:39], a[156:159], v[172:175]\n"
"v_exp_f32_e64 v110, -v110\n"
"v_mfma_f32_16x16x32_bf16 v[176:179], v[40:43], a[144:147], v[176:179]\n"
"ds_read_b64_tr_b16 v[36:37], v5 offset:38112\n"
"ds_read_b64_tr_b16 v[38:39], v5 offset:38624\n"
"v_mfma_f32_16x16x32_bf16 v[180:183], v[40:43], a[148:151], v[180:183]\n"
"buffer_load_dwordx4 v4, s[44:47], s88 offen offset:1056 lds\n"
"v_mul_f32_e32 v125, v125, v109\n"
"v_fma_f32 v110, v110, s94, s94\n"
"v_mfma_f32_16x16x32_bf16 v[184:187], v[40:43], a[152:155], v[184:187]\n"
"v_rcp_f32_e32 v110, v110\n"
"v_mfma_f32_16x16x32_bf16 v[188:191], v[40:43], a[156:159], v[188:191]\n"
"v_cvt_pk_bf16_f32 v58, v124, v125\n"
"s_waitcnt lgkmcnt(4)\n"
"v_mfma_f32_16x16x32_bf16 v[128:131], v[24:27], a[240:243], v[128:131]\n"
"ds_read_b64_tr_b16 v[40:41], v5 offset:42336\n"
"ds_read_b64_tr_b16 v[42:43], v5 offset:42848\n"
"v_mfma_f32_16x16x32_bf16 v[132:135], v[24:27], a[244:247], v[132:135]\n"
"s_add_u32 s89, s71, 0x4000\n"
"buffer_load_dwordx4 a[64:67], v0, s[48:51], s89 offen offset:0 sc1\n"
"v_mul_f32_e32 v126, v126, v110\n"
"v_mfma_f32_16x16x32_bf16 v[136:139], v[24:27], a[248:251], v[136:139]\n"
"v_mul_f32_e32 v127, v111, v127\n"
"v_mfma_f32_16x16x32_bf16 v[140:143], v[24:27], a[252:255], v[140:143]\n"
"v_exp_f32_e64 v111, -v111\n"
"v_mfma_f32_16x16x32_bf16 v[144:147], v[28:31], a[240:243], v[144:147]\n"
"ds_read_b64_tr_b16 v[24:25], v5 offset:46560\n"
"ds_read_b64_tr_b16 v[26:27], v5 offset:47072\n"
"v_mfma_f32_16x16x32_bf16 v[148:151], v[28:31], a[244:247], v[148:151]\n"
"buffer_load_dwordx4 a[68:71], v0, s[48:51], s89 offen offset:1024 sc1\n"
"v_fma_f32 v111, v111, s94, s94\n"
"v_mfma_f32_16x16x32_bf16 v[152:155], v[28:31], a[248:251], v[152:155]\n"
"v_rcp_f32_e32 v111, v111\n"
"v_mfma_f32_16x16x32_bf16 v[156:159], v[28:31], a[252:255], v[156:159]\n"
"v_mul_f32_e32 v127, v127, v111\n"
"s_waitcnt lgkmcnt(4)\n"
"v_mfma_f32_16x16x32_bf16 v[160:163], v[32:35], a[160:163], v[160:163]\n"
"ds_read_b64_tr_b16 v[28:29], v5 offset:50784\n"
"ds_read_b64_tr_b16 v[30:31], v5 offset:51296\n"
"v_mfma_f32_16x16x32_bf16 v[164:167], v[32:35], a[164:167], v[164:167]\n"
"buffer_load_dwordx4 a[72:75], v0, s[48:51], s89 offen offset:2048 sc1\n"
"v_cvt_pk_bf16_f32 v59, v126, v127\n"
"v_mfma_f32_16x16x32_bf16 v[168:171], v[32:35], a[168:171], v[168:171]\n"
"s_add_u32 s90, s66, 0xa000\n"
"buffer_store_dwordx4 v[56:59], v0, s[48:51], s90 offen offset:1024 sc1\n"
"v_mfma_f32_16x16x32_bf16 v[172:175], v[32:35], a[172:175], v[172:175]\n"
"v_mfma_f32_16x16x32_bf16 v[176:179], v[36:39], a[160:163], v[176:179]\n"
"ds_read_b64_tr_b16 v[32:33], v5 offset:55008\n"
"ds_read_b64_tr_b16 v[34:35], v5 offset:55520\n"
"v_mfma_f32_16x16x32_bf16 v[180:183], v[36:39], a[164:167], v[180:183]\n"
"buffer_load_dwordx4 a[76:79], v0, s[48:51], s89 offen offset:3072 sc1\n"
"v_mul_f32_e32 v144, v128, v144\n"
"v_mul_f32_e32 v145, v129, v145\n"
"v_mfma_f32_16x16x32_bf16 v[184:187], v[36:39], a[168:171], v[184:187]\n"
"v_exp_f32_e64 v128, -v128\n"
"v_mfma_f32_16x16x32_bf16 v[188:191], v[36:39], a[172:175], v[188:191]\n"
"v_fma_f32 v128, v128, s94, s94\n"
"s_waitcnt lgkmcnt(4)\n"
"v_mfma_f32_16x16x32_bf16 v[160:163], v[40:43], a[176:179], v[160:163]\n"
"ds_read_b64_tr_b16 v[36:37], v6 offset:96\n"
"ds_read_b64_tr_b16 v[38:39], v6 offset:608\n"
"v_mfma_f32_16x16x32_bf16 v[164:167], v[40:43], a[180:183], v[164:167]\n"
"s_add_u32 s89, s71, 0x5000\n"
"v_rcp_f32_e32 v128, v128\n"
"v_mfma_f32_16x16x32_bf16 v[168:171], v[40:43], a[184:187], v[168:171]\n"
"v_exp_f32_e64 v129, -v129\n"
"v_mfma_f32_16x16x32_bf16 v[172:175], v[40:43], a[188:191], v[172:175]\n"
"buffer_load_dwordx4 a[80:83], v0, s[48:51], s89 offen offset:0 sc1\n"
"v_mul_f32_e32 v144, v144, v128\n"
"v_mfma_f32_16x16x32_bf16 v[176:179], v[24:27], a[176:179], v[176:179]\n"
"ds_read_b64_tr_b16 v[40:41], v6 offset:4320\n"
"ds_read_b64_tr_b16 v[42:43], v6 offset:4832\n"
"v_mfma_f32_16x16x32_bf16 v[180:183], v[24:27], a[180:183], v[180:183]\n"
"buffer_load_dwordx4 a[84:87], v0, s[48:51], s89 offen offset:1024 sc1\n"
"v_fma_f32 v129, v129, s94, s94\n"
"v_mul_f32_e32 v146, v130, v146\n"
"v_mfma_f32_16x16x32_bf16 v[184:187], v[24:27], a[184:187], v[184:187]\n"
"v_rcp_f32_e32 v129, v129\n"
"v_mfma_f32_16x16x32_bf16 v[188:191], v[24:27], a[188:191], v[188:191]\n"
"v_mul_f32_e32 v145, v145, v129\n"
"s_waitcnt lgkmcnt(4)\n"
"s_barrier\n"
"v_mfma_f32_16x16x32_bf16 v[160:163], v[28:31], a[192:195], v[160:163]\n"
"ds_read_b64_tr_b16 v[24:25], v6 offset:8544\n"
"ds_read_b64_tr_b16 v[26:27], v6 offset:9056\n"
"v_mfma_f32_16x16x32_bf16 v[164:167], v[28:31], a[196:199], v[164:167]\n"
"s_add_u32 m0, s17, 0x6300\n"
"s_add_u32 s88, s61, 0x2d000\n"
"buffer_load_dwordx4 v3, s[44:47], s88 offen lds\n"
"v_mfma_f32_16x16x32_bf16 v[168:171], v[28:31], a[200:203], v[168:171]\n"
"v_exp_f32_e64 v130, -v130\n"
"v_mfma_f32_16x16x32_bf16 v[172:175], v[28:31], a[204:207], v[172:175]\n"
"v_cvt_pk_bf16_f32 v44, v144, v145\n"
"v_fma_f32 v130, v130, s94, s94\n"
"v_mfma_f32_16x16x32_bf16 v[176:179], v[32:35], a[192:195], v[176:179]\n"
"ds_read_b64_tr_b16 v[28:29], v6 offset:12768\n"
"ds_read_b64_tr_b16 v[30:31], v6 offset:13280\n"
"v_mfma_f32_16x16x32_bf16 v[180:183], v[32:35], a[196:199], v[180:183]\n"
"v_rcp_f32_e32 v130, v130\n"
"buffer_load_dwordx4 v4, s[44:47], s88 offen offset:1056 lds\n"
"v_mfma_f32_16x16x32_bf16 v[184:187], v[32:35], a[200:203], v[184:187]\n"
"v_mul_f32_e32 v146, v146, v130\n"
"v_mul_f32_e32 v147, v131, v147\n"
"v_mfma_f32_16x16x32_bf16 v[188:191], v[32:35], a[204:207], v[188:191]\n"
"v_mul_f32_e32 v148, v132, v148\n"
"s_waitcnt lgkmcnt(4)\n"
"v_mfma_f32_16x16x32_bf16 v[160:163], v[36:39], a[208:211], v[160:163]\n"
"ds_read_b64_tr_b16 v[32:33], v6 offset:16992\n"
"ds_read_b64_tr_b16 v[34:35], v6 offset:17504\n"
"v_mfma_f32_16x16x32_bf16 v[164:167], v[36:39], a[212:215], v[164:167]\n"
"s_add_u32 m0, s17, 0x8400\n"
"s_add_u32 s88, s60, 0x2d000\n"
"buffer_load_dwordx4 v3, s[44:47], s88 offen lds\n"
"v_mfma_f32_16x16x32_bf16 v[168:171], v[36:39], a[216:219], v[168:171]\n"
"v_exp_f32_e64 v131, -v131\n"
"v_mfma_f32_16x16x32_bf16 v[172:175], v[36:39], a[220:223], v[172:175]\n"
"v_exp_f32_e64 v132, -v132\n"
"v_mfma_f32_16x16x32_bf16 v[176:179], v[40:43], a[208:211], v[176:179]\n"
"ds_read_b64_tr_b16 v[36:37], v6 offset:21216\n"
"ds_read_b64_tr_b16 v[38:39], v6 offset:21728\n"
"v_mfma_f32_16x16x32_bf16 v[180:183], v[40:43], a[212:215], v[180:183]\n"
"buffer_load_dwordx4 v4, s[44:47], s88 offen offset:1056 lds\n"
"v_fma_f32 v131, v131, s94, s94\n"
"v_fma_f32 v132, v132, s94, s94\n"
"v_mfma_f32_16x16x32_bf16 v[184:187], v[40:43], a[216:219], v[184:187]\n"
"v_rcp_f32_e32 v131, v131\n"
"v_mfma_f32_16x16x32_bf16 v[188:191], v[40:43], a[220:223], v[188:191]\n"
"v_mul_f32_e32 v147, v147, v131\n"
"s_waitcnt lgkmcnt(4)\n"
"v_mfma_f32_16x16x32_bf16 v[160:163], v[24:27], a[224:227], v[160:163]\n"
"v_add_u32_e32 v5, s79, v5\n"
"v_add_u32_e32 v6, s79, v6\n"
"v_mfma_f32_16x16x32_bf16 v[164:167], v[24:27], a[228:231], v[164:167]\n"
"v_add_u32_e32 v7, s79, v7\n"
"ds_read_b64_tr_b16 v[40:41], v6 offset:25344\n"
"ds_read_b64_tr_b16 v[42:43], v6 offset:25856\n"
"v_mfma_f32_16x16x32_bf16 v[168:171], v[24:27], a[232:235], v[168:171]\n"
"s_add_u32 m0, s17, 0xa500\n"
"s_add_u32 s88, s61, 0x51000\n"
"v_mfma_f32_16x16x32_bf16 v[172:175], v[24:27], a[236:239], v[172:175]\n"
"buffer_load_dwordx4 v3, s[44:47], s88 offen lds\n"
"v_cvt_pk_bf16_f32 v45, v146, v147\n"
"v_mfma_f32_16x16x32_bf16 v[176:179], v[28:31], a[224:227], v[176:179]\n"
"ds_read_b64_tr_b16 v[24:25], v6 offset:25376\n"
"ds_read_b64_tr_b16 v[26:27], v6 offset:25888\n"
"v_mfma_f32_16x16x32_bf16 v[180:183], v[28:31], a[228:231], v[180:183]\n"
"v_rcp_f32_e32 v132, v132\n"
"buffer_load_dwordx4 v4, s[44:47], s88 offen offset:1056 lds\n"
"v_mfma_f32_16x16x32_bf16 v[184:187], v[28:31], a[232:235], v[184:187]\n"
"v_mul_f32_e32 v148, v148, v132\n"
"v_mul_f32_e32 v149, v133, v149\n"
"v_mfma_f32_16x16x32_bf16 v[188:191], v[28:31], a[236:239], v[188:191]\n"
"v_mul_f32_e32 v150, v134, v150\n"
"s_waitcnt lgkmcnt(4)\n"
"v_mfma_f32_16x16x32_bf16 v[160:163], v[32:35], a[240:243], v[160:163]\n"
"ds_read_b64_tr_b16 v[28:29], v6 offset:25408\n"
"ds_read_b64_tr_b16 v[30:31], v6 offset:25920\n"
"v_mfma_f32_16x16x32_bf16 v[164:167], v[32:35], a[244:247], v[164:167]\n"
"v_exp_f32_e64 v133, -v133\n"
"buffer_load_dwordx4 a[88:91], v0, s[48:51], s89 offen offset:2048 sc1\n"
"v_mfma_f32_16x16x32_bf16 v[168:171], v[32:35], a[248:251], v[168:171]\n"
"v_exp_f32_e64 v134, -v134\n"
"v_mfma_f32_16x16x32_bf16 v[172:175], v[32:35], a[252:255], v[172:175]\n"
"v_fma_f32 v133, v133, s94, s94\n"
"v_fma_f32 v134, v134, s94, s94\n"
"v_mfma_f32_16x16x32_bf16 v[176:179], v[36:39], a[240:243], v[176:179]\n"
"ds_read_b64_tr_b16 v[32:33], v6 offset:25440\n"
"ds_read_b64_tr_b16 v[34:35], v6 offset:25952\n"
"v_mfma_f32_16x16x32_bf16 v[180:183], v[36:39], a[244:247], v[180:183]\n"
"v_rcp_f32_e32 v133, v133\n"
"buffer_load_dwordx4 a[92:95], v0, s[48:51], s89 offen offset:3072 sc1\n"
"v_mfma_f32_16x16x32_bf16 v[184:187], v[36:39], a[248:251], v[184:187]\n"
"v_rcp_f32_e32 v134, v134\n"
"v_mfma_f32_16x16x32_bf16 v[188:191], v[36:39], a[252:255], v[188:191]\n"
"v_mul_f32_e32 v149, v149, v133\n"
"s_waitcnt lgkmcnt(4)\n"
"v_mfma_f32_16x16x32_bf16 v[192:195], v[40:43], a[0:3], 0\n"
"ds_read_b128 v[36:39], v9 offset:33792\n"
"v_cvt_pk_bf16_f32 v48, v148, v149\n"
"v_mfma_f32_16x16x32_bf16 v[196:199], v[40:43], a[4:7], 0\n"
"v_mul_f32_e32 v150, v150, v134\n"
"v_mul_f32_e32 v151, v135, v151\n"
"v_mul_f32_e32 v152, v136, v152\n"
"v_mfma_f32_16x16x32_bf16 v[80:83], v[40:43], a[8:11], 0\n"
"v_exp_f32_e64 v135, -v135\n"
"v_mfma_f32_16x16x32_bf16 v[84:87], v[40:43], a[12:15], 0\n"
"v_exp_f32_e64 v136, -v136\n"
"v_mfma_f32_16x16x32_bf16 v[200:203], v[24:27], a[0:3], 0\n"
"ds_read_b64_tr_b16 v[40:41], v6 offset:42240\n"
"ds_read_b64_tr_b16 v[42:43], v6 offset:42752\n"
"v_mfma_f32_16x16x32_bf16 v[204:207], v[24:27], a[4:7], 0\n"
"s_add_u32 s89, s71, 0x6000\n"
"buffer_load_dwordx4 a[96:99], v0, s[48:51], s89 offen offset:0 sc1\n"
"v_fma_f32 v135, v135, s94, s94\n"
"v_mfma_f32_16x16x32_bf16 v[88:91], v[24:27], a[8:11], 0\n"
"v_rcp_f32_e32 v135, v135\n"
"v_mfma_f32_16x16x32_bf16 v[92:95], v[24:27], a[12:15], 0\n"
"v_mul_f32_e32 v151, v151, v135\n"
"s_waitcnt lgkmcnt(3)\n"
"v_mfma_f32_16x16x32_bf16 v[208:211], v[28:31], a[0:3], 0\n"
"ds_read_b64_tr_b16 v[24:25], v6 offset:42272\n"
"ds_read_b64_tr_b16 v[26:27], v6 offset:42784\n"
"v_mfma_f32_16x16x32_bf16 v[212:215], v[28:31], a[4:7], 0\n"
"v_mov_b32_e32 v21, 4\n"
"s_waitcnt vmcnt(12)\n"
"buffer_store_dword v21, v12, s[48:51], s69 offen sc1\n"
"v_mfma_f32_16x16x32_bf16 a[224:227], v[28:31], a[8:11], 0\n"
"buffer_load_dwordx4 a[100:103], v0, s[48:51], s89 offen offset:1024 sc1\n"
"v_cvt_pk_bf16_f32 v49, v150, v151\n"
"v_mfma_f32_16x16x32_bf16 a[228:231], v[28:31], a[12:15], 0\n"
"v_fma_f32 v136, v136, s94, s94\n"
"v_mul_f32_e32 v153, v137, v153\n"
"v_mfma_f32_16x16x32_bf16 v[216:219], v[32:35], a[0:3], 0\n"
"ds_read_b64_tr_b16 v[28:29], v6 offset:42304\n"
"ds_read_b64_tr_b16 v[30:31], v6 offset:42816\n"
"v_mfma_f32_16x16x32_bf16 v[220:223], v[32:35], a[4:7], 0\n"
"v_rcp_f32_e32 v136, v136\n"
"buffer_load_dwordx4 a[104:107], v0, s[48:51], s89 offen offset:2048 sc1\n"
"v_mfma_f32_16x16x32_bf16 a[232:235], v[32:35], a[8:11], 0\n"
"v_exp_f32_e64 v137, -v137\n"
"v_mfma_f32_16x16x32_bf16 a[236:239], v[32:35], a[12:15], 0\n"
"v_mul_f32_e32 v152, v152, v136\n"
"s_waitcnt lgkmcnt(4)\n"
"s_barrier\n"
"v_mfma_f32_16x16x32_bf16 v[224:227], v[36:39], a[0:3], 0\n"
"ds_read_b64_tr_b16 v[32:33], v6 offset:42336\n"
"ds_read_b64_tr_b16 v[34:35], v6 offset:42848\n"
"v_mfma_f32_16x16x32_bf16 v[228:231], v[36:39], a[4:7], 0\n"
"s_add_u32 m0, s17, 0xc600\n"
"s_add_u32 s88, s60, 0x51000\n"
"buffer_load_dwordx4 v3, s[44:47], s88 offen lds\n"
"v_mfma_f32_16x16x32_bf16 v[232:235], v[36:39], a[8:11], 0\n"
"v_fma_f32 v137, v137, s94, s94\n"
"v_mul_f32_e32 v154, v138, v154\n"
"v_mfma_f32_16x16x32_bf16 v[236:239], v[36:39], a[12:15], 0\n"
"v_rcp_f32_e32 v137, v137\n"
"v_mfma_f32_16x16x32_bf16 v[192:195], v[40:43], a[16:19], v[192:195]\n"
"ds_read_b128 v[36:39], v9 offset:35904\n"
"buffer_load_dwordx4 v4, s[44:47], s88 offen offset:1056 lds\n"
"v_mfma_f32_16x16x32_bf16 v[196:199], v[40:43], a[20:23], v[196:199]\n"
"v_mul_f32_e32 v153, v153, v137\n"
"v_exp_f32_e64 v138, -v138\n"
"v_mfma_f32_16x16x32_bf16 v[80:83], v[40:43], a[24:27], v[80:83]\n"
"v_cvt_pk_bf16_f32 v52, v152, v153\n"
"v_fma_f32 v138, v138, s94, s94\n"
"v_mfma_f32_16x16x32_bf16 v[84:87], v[40:43], a[28:31], v[84:87]\n"
"v_mul_f32_e32 v155, v139, v155\n"
"s_waitcnt lgkmcnt(3)\n"
"v_mfma_f32_16x16x32_bf16 v[200:203], v[24:27], a[16:19], v[200:203]\n"
"ds_read_b64_tr_b16 v[40:41], v6 offset:50688\n"
"ds_read_b64_tr_b16 v[42:43], v6 offset:51200\n"
"v_mfma_f32_16x16x32_bf16 v[204:207], v[24:27], a[20:23], v[204:207]\n"
"s_add_u32 m0, s17, 0xe700\n"
"s_add_u32 s88, s61, 0x75000\n"
"buffer_load_dwordx4 v3, s[44:47], s88 offen lds\n"
"v_mfma_f32_16x16x32_bf16 v[88:91], v[24:27], a[24:27], v[88:91]\n"
"v_rcp_f32_e32 v138, v138\n"
"v_mfma_f32_16x16x32_bf16 v[92:95], v[24:27], a[28:31], v[92:95]\n"
"v_exp_f32_e64 v139, -v139\n"
"v_mfma_f32_16x16x32_bf16 v[208:211], v[28:31], a[16:19], v[208:211]\n"
"ds_read_b64_tr_b16 v[24:25], v6 offset:50720\n"
"ds_read_b64_tr_b16 v[26:27], v6 offset:51232\n"
"v_mfma_f32_16x16x32_bf16 v[212:215], v[28:31], a[20:23], v[212:215]\n"
"buffer_load_dwordx4 v4, s[44:47], s88 offen offset:1056 lds\n"
"v_mul_f32_e32 v154, v154, v138\n"
"v_fma_f32 v139, v139, s94, s94\n"
"v_mfma_f32_16x16x32_bf16 a[224:227], v[28:31], a[24:27], a[224:227]\n"
"v_rcp_f32_e32 v139, v139\n"
"v_mfma_f32_16x16x32_bf16 a[228:231], v[28:31], a[28:31], a[228:231]\n"
"v_mul_f32_e32 v155, v155, v139\n"
"s_waitcnt lgkmcnt(4)\n"
"v_mfma_f32_16x16x32_bf16 v[216:219], v[32:35], a[16:19], v[216:219]\n"
"ds_read_b64_tr_b16 v[28:29], v6 offset:50752\n"
"ds_read_b64_tr_b16 v[30:31], v6 offset:51264\n"
"v_mfma_f32_16x16x32_bf16 v[220:223], v[32:35], a[20:23], v[220:223]\n"
"s_add_u32 m0, s17, 0x10800\n"
"s_add_u32 s88, s60, 0x75000\n"
"buffer_load_dwordx4 v3, s[44:47], s88 offen lds\n"
"v_mfma_f32_16x16x32_bf16 a[232:235], v[32:35], a[24:27], a[232:235]\n"
"v_cvt_pk_bf16_f32 v53, v154, v155\n"
"v_mul_f32_e32 v156, v140, v156\n"
"v_mfma_f32_16x16x32_bf16 a[236:239], v[32:35], a[28:31], a[236:239]\n"
"v_exp_f32_e64 v140, -v140\n"
"v_mfma_f32_16x16x32_bf16 v[224:227], v[36:39], a[16:19], v[224:227]\n"
"ds_read_b64_tr_b16 v[32:33], v6 offset:50784\n"
"ds_read_b64_tr_b16 v[34:35], v6 offset:51296\n"
"v_mfma_f32_16x16x32_bf16 v[228:231], v[36:39], a[20:23], v[228:231]\n"
"buffer_load_dwordx4 v4, s[44:47], s88 offen offset:1056 lds\n"
"v_fma_f32 v140, v140, s94, s94\n"
"v_mul_f32_e32 v157, v141, v157\n"
"v_mfma_f32_16x16x32_bf16 v[232:235], v[36:39], a[24:27], v[232:235]\n"
"v_rcp_f32_e32 v140, v140\n"
"v_mfma_f32_16x16x32_bf16 v[236:239], v[36:39], a[28:31], v[236:239]\n"
"v_mul_f32_e32 v156, v156, v140\n"
"s_waitcnt lgkmcnt(4)\n"
"v_mfma_f32_16x16x32_bf16 v[192:195], v[40:43], a[32:35], v[192:195]\n"
"ds_read_b128 v[36:39], v9 offset:38016\n"
"buffer_load_dwordx4 a[108:111], v0, s[48:51], s89 offen offset:3072 sc1\n"
"v_mfma_f32_16x16x32_bf16 v[196:199], v[40:43], a[36:39], v[196:199]\n"
"v_exp_f32_e64 v141, -v141\n"
"v_mul_f32_e32 v158, v142, v158\n"
"v_mfma_f32_16x16x32_bf16 v[80:83], v[40:43], a[40:43], v[80:83]\n"
"v_exp_f32_e64 v142, -v142\n"
"v_mfma_f32_16x16x32_bf16 v[84:87], v[40:43], a[44:47], v[84:87]\n"
"v_fma_f32 v141, v141, s94, s94\n"
"v_fma_f32 v142, v142, s94, s94\n"
"v_mfma_f32_16x16x32_bf16 v[200:203], v[24:27], a[32:35], v[200:203]\n"
"ds_read_b64_tr_b16 v[40:41], v7 offset:0\n"
"ds_read_b64_tr_b16 v[42:43], v7 offset:512\n"
"v_mfma_f32_16x16x32_bf16 v[204:207], v[24:27], a[36:39], v[204:207]\n"
"s_add_u32 s89, s71, 0x7000\n"
"v_rcp_f32_e32 v141, v141\n"
"v_mfma_f32_16x16x32_bf16 v[88:91], v[24:27], a[40:43], v[88:91]\n"
"v_rcp_f32_e32 v142, v142\n"
"v_mfma_f32_16x16x32_bf16 v[92:95], v[24:27], a[44:47], v[92:95]\n"
"buffer_load_dwordx4 a[112:115], v0, s[48:51], s89 offen offset:0 sc1\n"
"s_waitcnt lgkmcnt(3)\n"
"v_mfma_f32_16x16x32_bf16 v[208:211], v[28:31], a[32:35], v[208:211]\n"
"ds_read_b64_tr_b16 v[24:25], v7 offset:32\n"
"ds_read_b64_tr_b16 v[26:27], v7 offset:544\n"
"v_mfma_f32_16x16x32_bf16 v[212:215], v[28:31], a[36:39], v[212:215]\n"
"buffer_load_dwordx4 a[116:119], v0, s[48:51], s89 offen offset:1024 sc1\n"
"v_mul_f32_e32 v157, v157, v141\n"
"v_mul_f32_e32 v158, v158, v142\n"
"v_mfma_f32_16x16x32_bf16 a[224:227], v[28:31], a[40:43], a[224:227]\n"
"v_cvt_pk_bf16_f32 v56, v156, v157\n"
"v_mul_f32_e32 v159, v143, v159\n"
"v_mfma_f32_16x16x32_bf16 a[228:231], v[28:31], a[44:47], a[228:231]\n"
"v_exp_f32_e64 v143, -v143\n"
"v_mfma_f32_16x16x32_bf16 v[216:219], v[32:35], a[32:35], v[216:219]\n"
"ds_read_b64_tr_b16 v[28:29], v7 offset:64\n"
"ds_read_b64_tr_b16 v[30:31], v7 offset:576\n"
"v_mfma_f32_16x16x32_bf16 v[220:223], v[32:35], a[36:39], v[220:223]\n"
"buffer_load_dwordx4 a[120:123], v0, s[48:51], s89 offen offset:2048 sc1\n"
"v_fma_f32 v143, v143, s94, s94\n"
"v_mul_f32_e32 v176, v160, v176\n"
"v_mfma_f32_16x16x32_bf16 a[232:235], v[32:35], a[40:43], a[232:235]\n"
"v_rcp_f32_e32 v143, v143\n"
"v_mfma_f32_16x16x32_bf16 a[236:239], v[32:35], a[44:47], a[236:239]\n"
"v_mul_f32_e32 v159, v159, v143\n"
"s_waitcnt lgkmcnt(4)\n"
"v_mfma_f32_16x16x32_bf16 v[224:227], v[36:39], a[32:35], v[224:227]\n"
"ds_read_b64_tr_b16 v[32:33], v7 offset:96\n"
"ds_read_b64_tr_b16 v[34:35], v7 offset:608\n"
"v_mfma_f32_16x16x32_bf16 v[228:231], v[36:39], a[36:39], v[228:231]\n"
"v_cvt_pk_bf16_f32 v57, v158, v159\n"
"v_exp_f32_e64 v160, -v160\n"
"v_mfma_f32_16x16x32_bf16 v[232:235], v[36:39], a[40:43], v[232:235]\n"
"buffer_load_dwordx4 a[124:127], v0, s[48:51], s89 offen offset:3072 sc1\n"
"v_fma_f32 v160, v160, s94, s94\n"
"v_mfma_f32_16x16x32_bf16 v[236:239], v[36:39], a[44:47], v[236:239]\n"
"v_rcp_f32_e32 v160, v160\n"
"v_mfma_f32_16x16x32_bf16 v[192:195], v[40:43], a[48:51], v[192:195]\n"
"ds_read_b128 v[36:39], v9 offset:40128\n"
"s_add_u32 m0, s17, 0x12900\n"
"v_mfma_f32_16x16x32_bf16 v[196:199], v[40:43], a[52:55], v[196:199]\n"
"s_add_u32 s88, s63, 0x0\n"
"buffer_load_dwordx4 v18, s[44:47], s88 offen lds\n"
"v_mul_f32_e32 v176, v176, v160\n"
"v_mfma_f32_16x16x32_bf16 v[80:83], v[40:43], a[56:59], v[80:83]\n"
"v_mul_f32_e32 v177, v161, v177\n"
"v_mul_f32_e32 v178, v162, v178\n"
"v_mfma_f32_16x16x32_bf16 v[84:87], v[40:43], a[60:63], v[84:87]\n"
"v_mul_f32_e32 v179, v163, v179\n"
"s_waitcnt lgkmcnt(3)\n"
"v_mfma_f32_16x16x32_bf16 v[200:203], v[24:27], a[48:51], v[200:203]\n"
"ds_read_b64_tr_b16 v[40:41], v7 offset:8448\n"
"ds_read_b64_tr_b16 v[42:43], v7 offset:8960\n"
"v_mfma_f32_16x16x32_bf16 v[204:207], v[24:27], a[52:55], v[204:207]\n"
"v_exp_f32_e64 v161, -v161\n"
"buffer_load_dwordx4 v2, s[44:47], s88 offen offset:1056 lds\n"
"v_mfma_f32_16x16x32_bf16 v[88:91], v[24:27], a[56:59], v[88:91]\n"
"v_exp_f32_e64 v162, -v162\n"
"v_mfma_f32_16x16x32_bf16 v[92:95], v[24:27], a[60:63], v[92:95]\n"
"v_fma_f32 v161, v161, s94, s94\n"
"v_fma_f32 v162, v162, s94, s94\n"
"v_mfma_f32_16x16x32_bf16 v[208:211], v[28:31], a[48:51], v[208:211]\n"
"ds_read_b64_tr_b16 v[24:25], v7 offset:8480\n"
"ds_read_b64_tr_b16 v[26:27], v7 offset:8992\n"
"v_mfma_f32_16x16x32_bf16 v[212:215], v[28:31], a[52:55], v[212:215]\n"
"s_add_u32 m0, s17, 0x14a00\n"
"s_add_u32 s88, s64, 0x4000\n"
"buffer_load_dwordx4 v0, s[48:51], s88 offen lds\n"
"v_mfma_f32_16x16x32_bf16 a[224:227], v[28:31], a[56:59], a[224:227]\n"
"v_rcp_f32_e32 v161, v161\n"
"v_mfma_f32_16x16x32_bf16 a[228:231], v[28:31], a[60:63], a[228:231]\n"
"v_mul_f32_e32 v177, v177, v161\n"
"s_waitcnt lgkmcnt(4)\n"
"v_mfma_f32_16x16x32_bf16 v[216:219], v[32:35], a[48:51], v[216:219]\n"
"ds_read_b64_tr_b16 v[28:29], v7 offset:8512\n"
"ds_read_b64_tr_b16 v[30:31], v7 offset:9024\n"
"v_mfma_f32_16x16x32_bf16 v[220:223], v[32:35], a[52:55], v[220:223]\n"
"v_cvt_pk_bf16_f32 v46, v176, v177\n"
"v_rcp_f32_e32 v162, v162\n"
"v_mfma_f32_16x16x32_bf16 a[232:235], v[32:35], a[56:59], a[232:235]\n"
"v_exp_f32_e64 v163, -v163\n"
"v_mfma_f32_16x16x32_bf16 a[236:239], v[32:35], a[60:63], a[236:239]\n"
"buffer_load_dwordx4 v0, s[48:51], s88 offen offset:1024 lds\n"
"v_mul_f32_e32 v178, v178, v162\n"
"v_mfma_f32_16x16x32_bf16 v[224:227], v[36:39], a[48:51], v[224:227]\n"
"ds_read_b64_tr_b16 v[32:33], v7 offset:8544\n"
"ds_read_b64_tr_b16 v[34:35], v7 offset:9056\n"
"v_mfma_f32_16x16x32_bf16 v[228:231], v[36:39], a[52:55], v[228:231]\n"
"s_add_u32 s89, s71, 0x8000\n"
"buffer_load_dwordx4 a[128:131], v0, s[48:51], s89 offen offset:0 sc1\n"
"v_fma_f32 v163, v163, s94, s94\n"
"v_mfma_f32_16x16x32_bf16 v[232:235], v[36:39], a[56:59], v[232:235]\n"
"v_rcp_f32_e32 v163, v163\n"
"v_mfma_f32_16x16x32_bf16 v[236:239], v[36:39], a[60:63], v[236:239]\n"
"v_mul_f32_e32 v179, v179, v163\n"
"s_waitcnt lgkmcnt(4)\n"
"s_barrier\n"
"v_mfma_f32_16x16x32_bf16 v[64:67], v[40:43], a[0:3], 0\n"
"ds_read_b64_tr_b16 v[36:37], v7 offset:16896\n"
"ds_read_b64_tr_b16 v[38:39], v7 offset:17408\n"
"v_mfma_f32_16x16x32_bf16 v[68:71], v[40:43], a[4:7], 0\n"
"s_add_u32 m0, s17, 0x16b00\n"
"s_add_u32 s88, s63, 0x9000\n"
"buffer_load_dwordx4 v18, s[44:47], s88 offen lds\n"
"v_mfma_f32_16x16x32_bf16 a[240:243], v[40:43], a[8:11], 0\n"
"v_cvt_pk_bf16_f32 v47, v178, v179\n"
"v_mul_f32_e32 v180, v164, v180\n"
"v_mfma_f32_16x16x32_bf16 a[244:247], v[40:43], a[12:15], 0\n"
"s_add_u32 s90, s65, 0xe000\n"
"buffer_store_dwordx4 v[44:47], v0, s[48:51], s90 offen offset:0 sc1\n"
"v_mfma_f32_16x16x32_bf16 v[72:75], v[24:27], a[0:3], 0\n"
"ds_read_b64_tr_b16 v[40:41], v7 offset:16928\n"
"ds_read_b64_tr_b16 v[42:43], v7 offset:17440\n"
"v_mfma_f32_16x16x32_bf16 v[76:79], v[24:27], a[4:7], 0\n"
"v_exp_f32_e64 v164, -v164\n"
"v_mul_f32_e32 v181, v165, v181\n"
"v_mfma_f32_16x16x32_bf16 a[248:251], v[24:27], a[8:11], 0\n"
"v_exp_f32_e64 v165, -v165\n"
"v_mfma_f32_16x16x32_bf16 a[252:255], v[24:27], a[12:15], 0\n"
"v_fma_f32 v164, v164, s94, s94\n"
"s_waitcnt lgkmcnt(4)\n"
"v_mfma_f32_16x16x32_bf16 v[240:243], v[28:31], a[0:3], 0\n"
"ds_read_b64_tr_b16 v[24:25], v7 offset:16960\n"
"ds_read_b64_tr_b16 v[26:27], v7 offset:17472\n"
"v_mfma_f32_16x16x32_bf16 v[244:247], v[28:31], a[4:7], 0\n"
"buffer_load_dword v20, v11, s[48:51], s70 offen sc1\n"
"v_rcp_f32_e32 v164, v164\n"
"v_mfma_f32_16x16x32_bf16 v[96:99], v[28:31], a[8:11], 0\n"
"buffer_load_dwordx4 v2, s[44:47], s88 offen offset:1056 lds\n"
"v_mul_f32_e32 v180, v180, v164\n"
"v_mfma_f32_16x16x32_bf16 v[100:103], v[28:31], a[12:15], 0\n"
"v_fma_f32 v165, v165, s94, s94\n"
"v_mul_f32_e32 v182, v166, v182\n"
"v_mfma_f32_16x16x32_bf16 v[248:251], v[32:35], a[0:3], 0\n"
"ds_read_b64_tr_b16 v[28:29], v7 offset:16992\n"
"ds_read_b64_tr_b16 v[30:31], v7 offset:17504\n"
"v_mfma_f32_16x16x32_bf16 v[252:255], v[32:35], a[4:7], 0\n"
"s_add_u32 m0, s17, 0x18c00\n"
"s_add_u32 s88, s61, 0x12000\n"
"buffer_load_dwordx4 v3, s[44:47], s88 offen lds\n"
"v_mfma_f32_16x16x32_bf16 v[104:107], v[32:35], a[8:11], 0\n"
"v_rcp_f32_e32 v165, v165\n"
"v_mfma_f32_16x16x32_bf16 v[108:111], v[32:35], a[12:15], 0\n"
"v_mul_f32_e32 v181, v181, v165\n"
"s_waitcnt lgkmcnt(4)\n"
"v_mfma_f32_16x16x32_bf16 v[64:67], v[36:39], a[16:19], v[64:67]\n"
"ds_read_b64_tr_b16 v[32:33], v5 offset:0\n"
"ds_read_b64_tr_b16 v[34:35], v5 offset:512\n"
"v_mfma_f32_16x16x32_bf16 v[68:71], v[36:39], a[20:23], v[68:71]\n"
"v_cvt_pk_bf16_f32 v50, v180, v181\n"
"v_exp_f32_e64 v166, -v166\n"
"v_mfma_f32_16x16x32_bf16 a[240:243], v[36:39], a[24:27], a[240:243]\n"
"buffer_load_dwordx4 v4, s[44:47], s88 offen offset:1056 lds\n"
"v_fma_f32 v166, v166, s94, s94\n"
"v_mfma_f32_16x16x32_bf16 a[244:247], v[36:39], a[28:31], a[244:247]\n"
"v_rcp_f32_e32 v166, v166\n"
"v_mfma_f32_16x16x32_bf16 v[72:75], v[40:43], a[16:19], v[72:75]\n"
"ds_read_b64_tr_b16 v[36:37], v5 offset:32\n"
"ds_read_b64_tr_b16 v[38:39], v5 offset:544\n"
"v_mfma_f32_16x16x32_bf16 v[76:79], v[40:43], a[20:23], v[76:79]\n"
"buffer_load_dwordx4 a[132:135], v0, s[48:51], s89 offen offset:1024 sc1\n"
"v_mul_f32_e32 v182, v182, v166\n"
"v_mul_f32_e32 v183, v167, v183\n"
"v_mfma_f32_16x16x32_bf16 a[248:251], v[40:43], a[24:27], a[248:251]\n"
"v_exp_f32_e64 v167, -v167\n"
"v_mfma_f32_16x16x32_bf16 a[252:255], v[40:43], a[28:31], a[252:255]\n"
"v_fma_f32 v167, v167, s94, s94\n"
"s_waitcnt lgkmcnt(4)\n"
"v_mfma_f32_16x16x32_bf16 v[240:243], v[24:27], a[16:19], v[240:243]\n"
"ds_read_b64_tr_b16 v[40:41], v5 offset:64\n"
"ds_read_b64_tr_b16 v[42:43], v5 offset:576\n"
"v_mfma_f32_16x16x32_bf16 v[244:247], v[24:27], a[20:23], v[244:247]\n"
"v_rcp_f32_e32 v167, v167\n"
"buffer_load_dwordx4 a[136:139], v0, s[48:51], s89 offen offset:2048 sc1\n"
"v_mfma_f32_16x16x32_bf16 v[96:99], v[24:27], a[24:27], v[96:99]\n"
"v_mul_f32_e32 v183, v183, v167\n"
"v_mul_f32_e32 v184, v168, v184\n"
"v_mfma_f32_16x16x32_bf16 v[100:103], v[24:27], a[28:31], v[100:103]\n"
"v_exp_f32_e64 v168, -v168\n"
"v_mfma_f32_16x16x32_bf16 v[248:251], v[28:31], a[16:19], v[248:251]\n"
"ds_read_b64_tr_b16 v[24:25], v5 offset:96\n"
"ds_read_b64_tr_b16 v[26:27], v5 offset:608\n"
"v_mfma_f32_16x16x32_bf16 v[252:255], v[28:31], a[20:23], v[252:255]\n"
"buffer_load_dwordx4 a[140:143], v0, s[48:51], s89 offen offset:3072 sc1\n"
"v_cvt_pk_bf16_f32 v51, v182, v183\n"
"v_fma_f32 v168, v168, s94, s94\n"
"v_mfma_f32_16x16x32_bf16 v[104:107], v[28:31], a[24:27], v[104:107]\n"
"s_add_u32 s90, s65, 0xe000\n"
"buffer_store_dwordx4 v[48:51], v0, s[48:51], s90 offen offset:1024 sc1\n"
"v_mfma_f32_16x16x32_bf16 v[108:111], v[28:31], a[28:31], v[108:111]\n"
"v_mul_f32_e32 v185, v169, v185\n"
"s_waitcnt lgkmcnt(4)\n"
"v_mfma_f32_16x16x32_bf16 v[64:67], v[32:35], a[32:35], v[64:67]\n"
"ds_read_b64_tr_b16 v[28:29], v7 offset:25344\n"
"ds_read_b64_tr_b16 v[30:31], v7 offset:25856\n"
"v_mfma_f32_16x16x32_bf16 v[68:71], v[32:35], a[36:39], v[68:71]\n"
"s_add_u32 s89, s71, 0x9000\n"
"v_rcp_f32_e32 v168, v168\n"
"v_mfma_f32_16x16x32_bf16 a[240:243], v[32:35], a[40:43], a[240:243]\n"
"v_exp_f32_e64 v169, -v169\n"
"v_mfma_f32_16x16x32_bf16 a[244:247], v[32:35], a[44:47], a[244:247]\n"
"buffer_load_dwordx4 a[144:147], v0, s[48:51], s89 offen offset:0 sc1\n"
"v_mul_f32_e32 v184, v184, v168\n"
"v_mfma_f32_16x16x32_bf16 v[72:75], v[36:39], a[32:35], v[72:75]\n"
"ds_read_b64_tr_b16 v[32:33], v7 offset:25376\n"
"ds_read_b64_tr_b16 v[34:35], v7 offset:25888\n"
"v_mfma_f32_16x16x32_bf16 v[76:79], v[36:39], a[36:39], v[76:79]\n"
"buffer_load_dwordx4 a[148:151], v0, s[48:51], s89 offen offset:1024 sc1\n"
"v_fma_f32 v169, v169, s94, s94\n"
"v_mul_f32_e32 v186, v170, v186\n"
"v_mfma_f32_16x16x32_bf16 a[248:251], v[36:39], a[40:43], a[248:251]\n"
"v_rcp_f32_e32 v169, v169\n"
"v_mfma_f32_16x16x32_bf16 a[252:255], v[36:39], a[44:47], a[252:255]\n"
"v_mul_f32_e32 v185, v185, v169\n"
"s_waitcnt lgkmcnt(4)\n"
"v_mfma_f32_16x16x32_bf16 v[240:243], v[40:43], a[32:35], v[240:243]\n"
"ds_read_b64_tr_b16 v[36:37], v7 offset:25408\n"
"ds_read_b64_tr_b16 v[38:39], v7 offset:25920\n"
"v_mfma_f32_16x16x32_bf16 v[244:247], v[40:43], a[36:39], v[244:247]\n"
"v_cvt_pk_bf16_f32 v54, v184, v185\n"
"v_exp_f32_e64 v170, -v170\n"
"v_mfma_f32_16x16x32_bf16 v[96:99], v[40:43], a[40:43], v[96:99]\n"
"buffer_load_dwordx4 a[152:155], v0, s[48:51], s89 offen offset:2048 sc1\n"
"v_fma_f32 v170, v170, s94, s94\n"
"v_mfma_f32_16x16x32_bf16 v[100:103], v[40:43], a[44:47], v[100:103]\n"
"v_rcp_f32_e32 v170, v170\n"
"v_mfma_f32_16x16x32_bf16 v[248:251], v[24:27], a[32:35], v[248:251]\n"
"ds_read_b64_tr_b16 v[40:41], v7 offset:25440\n"
"ds_read_b64_tr_b16 v[42:43], v7 offset:25952\n"
"v_mfma_f32_16x16x32_bf16 v[252:255], v[24:27], a[36:39], v[252:255]\n"
"buffer_load_dwordx4 a[156:159], v0, s[48:51], s89 offen offset:3072 sc1\n"
"v_mul_f32_e32 v186, v186, v170\n"
"v_mul_f32_e32 v187, v171, v187\n"
"v_mfma_f32_16x16x32_bf16 v[104:107], v[24:27], a[40:43], v[104:107]\n"
"v_exp_f32_e64 v171, -v171\n"
"v_mfma_f32_16x16x32_bf16 v[108:111], v[24:27], a[44:47], v[108:111]\n"
"v_fma_f32 v171, v171, s94, s94\n"
"s_waitcnt lgkmcnt(4)\n"
"v_mfma_f32_16x16x32_bf16 v[64:67], v[28:31], a[48:51], v[64:67]\n"
"ds_read_b64_tr_b16 v[24:25], v5 offset:8448\n"
"ds_read_b64_tr_b16 v[26:27], v5 offset:8960\n"
"v_mfma_f32_16x16x32_bf16 v[68:71], v[28:31], a[52:55], v[68:71]\n"
"s_add_u32 m0, s17, 0x1ad00\n"
"s_add_u32 s88, s61, 0x36000\n"
".LKF_pad7:\n"
"s_cbranch_execz .LK3_ret\n"
"buffer_load_dwordx4 v3, s[44:47], s88 offen lds\n"
"v_mfma_f32_16x16x32_bf16 a[240:243], v[28:31], a[56:59], a[240:243]\n"
"v_rcp_f32_e32 v171, v171\n"
"v_mfma_f32_16x16x32_bf16 a[244:247], v[28:31], a[60:63], a[244:247]\n"
"v_mul_f32_e32 v187, v187, v171\n"
"v_mul_f32_e32 v188, v172, v188\n"
"v_mfma_f32_16x16x32_bf16 v[72:75], v[32:35], a[48:51], v[72:75]\n"
"ds_read_b64_tr_b16 v[28:29], v5 offset:8480\n"
"ds_read_b64_tr_b16 v[30:31], v5 offset:8992\n"
"v_mfma_f32_16x16x32_bf16 v[76:79], v[32:35], a[52:55], v[76:79]\n"
"v_cvt_pk_bf16_f32 v55, v186, v187\n"
"s_add_u32 s90, s66, 0xe000\n"
"buffer_store_dwordx4 v[52:55], v0, s[48:51], s90 offen offset:0 sc1\n"
"v_mfma_f32_16x16x32_bf16 a[248:251], v[32:35], a[56:59], a[248:251]\n"
"v_exp_f32_e64 v172, -v172\n"
"v_mfma_f32_16x16x32_bf16 a[252:255], v[32:35], a[60:63], a[252:255]\n"
"buffer_load_dwordx4 v4, s[44:47], s88 offen offset:1056 lds\n"
"s_waitcnt lgkmcnt(4)\n"
"v_mfma_f32_16x16x32_bf16 v[240:243], v[36:39], a[48:51], v[240:243]\n"
"ds_read_b64_tr_b16 v[32:33], v5 offset:8512\n"
"ds_read_b64_tr_b16 v[34:35], v5 offset:9024\n"
"v_mfma_f32_16x16x32_bf16 v[244:247], v[36:39], a[52:55], v[244:247]\n"
"s_add_u32 m0, s17, 0x1ce00\n"
"s_add_u32 s88, s61, 0x5a000\n"
"buffer_load_dwordx4 v3, s[44:47], s88 offen lds\n"
"v_mfma_f32_16x16x32_bf16 v[96:99], v[36:39], a[56:59], v[96:99]\n"
"v_fma_f32 v172, v172, s94, s94\n"
"v_mul_f32_e32 v189, v173, v189\n"
"v_mfma_f32_16x16x32_bf16 v[100:103], v[36:39], a[60:63], v[100:103]\n"
"v_rcp_f32_e32 v172, v172\n"
"v_mfma_f32_16x16x32_bf16 v[248:251], v[40:43], a[48:51], v[248:251]\n"
"ds_read_b64_tr_b16 v[36:37], v5 offset:8544\n"
"ds_read_b64_tr_b16 v[38:39], v5 offset:9056\n"
"v_mfma_f32_16x16x32_bf16 v[252:255], v[40:43], a[52:55], v[252:255]\n"
"v_mul_f32_e32 v188, v188, v172\n"
"v_exp_f32_e64 v173, -v173\n"
"v_mfma_f32_16x16x32_bf16 v[104:107], v[40:43], a[56:59], v[104:107]\n"
"buffer_load_dwordx4 v4, s[44:47], s88 offen offset:1056 lds\n"
"v_fma_f32 v173, v173, s94, s94\n"
"v_mfma_f32_16x16x32_bf16 v[108:111], v[40:43], a[60:63], v[108:111]\n"
"v_mul_f32_e32 v190, v174, v190\n"
"s_waitcnt vmcnt(41) lgkmcnt(4)\n"
"s_barrier\n"
"s_waitcnt vmcnt(16)\n"
"v_cmp_gt_u32_e32 vcc, 4, v20\n"
"s_cbranch_vccnz .LKF_sP4_%=\n"
".LKF_rP4_%=:\n"
"v_mfma_f32_16x16x32_bf16 v[192:195], v[24:27], a[64:67], v[192:195]\n"
"ds_read_b128 v[40:43], v8 offset:16896\n"
"s_add_u32 m0, s17, 0x0\n"
"v_mfma_f32_16x16x32_bf16 v[196:199], v[24:27], a[68:71], v[196:199]\n"
"s_add_u32 s88, s64, 0x6000\n"
"v_rcp_f32_e32 v173, v173\n"
"v_mfma_f32_16x16x32_bf16 v[80:83], v[24:27], a[72:75], v[80:83]\n"
"v_exp_f32_e64 v174, -v174\n"
"v_mfma_f32_16x16x32_bf16 v[84:87], v[24:27], a[76:79], v[84:87]\n"
"buffer_load_dwordx4 v0, s[48:51], s88 offen lds\n"
"v_mul_f32_e32 v189, v189, v173\n"
"v_mfma_f32_16x16x32_bf16 v[200:203], v[28:31], a[64:67], v[200:203]\n"
"ds_read_b64_tr_b16 v[24:25], v7 offset:33792\n"
"ds_read_b64_tr_b16 v[26:27], v7 offset:34304\n"
"v_mfma_f32_16x16x32_bf16 v[204:207], v[28:31], a[68:71], v[204:207]\n"
"buffer_load_dwordx4 v0, s[48:51], s88 offen offset:1024 lds\n"
"v_cvt_pk_bf16_f32 v58, v188, v189\n"
"v_fma_f32 v174, v174, s94, s94\n"
"v_mfma_f32_16x16x32_bf16 v[88:91], v[28:31], a[72:75], v[88:91]\n"
"v_rcp_f32_e32 v174, v174\n"
"v_mfma_f32_16x16x32_bf16 v[92:95], v[28:31], a[76:79], v[92:95]\n"
"v_mul_f32_e32 v190, v190, v174\n"
"s_waitcnt lgkmcnt(3)\n"
"v_mfma_f32_16x16x32_bf16 v[208:211], v[32:35], a[64:67], v[208:211]\n"
"ds_read_b64_tr_b16 v[28:29], v7 offset:33824\n"
"ds_read_b64_tr_b16 v[30:31], v7 offset:34336\n"
"v_mfma_f32_16x16x32_bf16 v[212:215], v[32:35], a[68:71], v[212:215]\n"
"s_add_u32 s89, s71, 0xa000\n"
"buffer_load_dwordx4 a[160:163], v0, s[48:51], s89 offen offset:0 sc1\n"
"v_mul_f32_e32 v191, v175, v191\n"
"v_mfma_f32_16x16x32_bf16 a[224:227], v[32:35], a[72:75], a[224:227]\n"
"v_exp_f32_e64 v175, -v175\n"
"v_mfma_f32_16x16x32_bf16 a[228:231], v[32:35], a[76:79], a[228:231]\n"
"v_fma_f32 v175, v175, s94, s94\n"
"v_mfma_f32_16x16x32_bf16 v[216:219], v[36:39], a[64:67], v[216:219]\n"
"ds_read_b64_tr_b16 v[32:33], v7 offset:33856\n"
"ds_read_b64_tr_b16 v[34:35], v7 offset:34368\n"
"v_mfma_f32_16x16x32_bf16 v[220:223], v[36:39], a[68:71], v[220:223]\n"
"v_rcp_f32_e32 v175, v175\n"
"buffer_load_dwordx4 a[164:167], v0, s[48:51], s89 offen offset:1024 sc1\n"
"v_mfma_f32_16x16x32_bf16 a[232:235], v[36:39], a[72:75], a[232:235]\n"
"v_mul_f32_e32 v191, v191, v175\n"
"v_mfma_f32_16x16x32_bf16 a[236:239], v[36:39], a[76:79], a[236:239]\n"
"v_cvt_pk_bf16_f32 v59, v190, v191\n"
"s_waitcnt lgkmcnt(4)\n"
"v_mfma_f32_16x16x32_bf16 v[224:227], v[40:43], a[64:67], v[224:227]\n"
"ds_read_b64_tr_b16 v[36:37], v7 offset:33888\n"
"ds_read_b64_tr_b16 v[38:39], v7 offset:34400\n"
"v_mfma_f32_16x16x32_bf16 v[228:231], v[40:43], a[68:71], v[228:231]\n"
"s_add_u32 s90, s66, 0xe000\n"
"buffer_store_dwordx4 v[56:59], v0, s[48:51], s90 offen offset:1024 sc1\n"
"buffer_load_dwordx4 a[168:171], v0, s[48:51], s89 offen offset:2048 sc1\n"
"v_mfma_f32_16x16x32_bf16 v[232:235], v[40:43], a[72:75], v[232:235]\n"
"v_mfma_f32_16x16x32_bf16 v[236:239], v[40:43], a[76:79], v[236:239]\n"
"v_mfma_f32_16x16x32_bf16 v[64:67], v[24:27], a[64:67], v[64:67]\n"
"ds_read_b64_tr_b16 v[40:41], v5 offset:25344\n"
"ds_read_b64_tr_b16 v[42:43], v5 offset:25856\n"
"v_mfma_f32_16x16x32_bf16 v[68:71], v[24:27], a[68:71], v[68:71]\n"
"buffer_load_dwordx4 a[172:175], v0, s[48:51], s89 offen offset:3072 sc1\n"
"v_mfma_f32_16x16x32_bf16 a[240:243], v[24:27], a[72:75], a[240:243]\n"
"v_mfma_f32_16x16x32_bf16 a[244:247], v[24:27], a[76:79], a[244:247]\n"
"s_waitcnt lgkmcnt(4)\n"
"v_mfma_f32_16x16x32_bf16 v[72:75], v[28:31], a[64:67], v[72:75]\n"
"ds_read_b64_tr_b16 v[24:25], v5 offset:25376\n"
"ds_read_b64_tr_b16 v[26:27], v5 offset:25888\n"
"v_mfma_f32_16x16x32_bf16 v[76:79], v[28:31], a[68:71], v[76:79]\n"
"v_mfma_f32_16x16x32_bf16 a[248:251], v[28:31], a[72:75], a[248:251]\n"
"v_mfma_f32_16x16x32_bf16 a[252:255], v[28:31], a[76:79], a[252:255]\n"
"v_mfma_f32_16x16x32_bf16 v[240:243], v[32:35], a[64:67], v[240:243]\n"
"ds_read_b64_tr_b16 v[28:29], v5 offset:25408\n"
"ds_read_b64_tr_b16 v[30:31], v5 offset:25920\n"
"v_mfma_f32_16x16x32_bf16 v[244:247], v[32:35], a[68:71], v[244:247]\n"
"s_add_u32 s89, s71, 0xb000\n"
"buffer_load_dwordx4 a[176:179], v0, s[48:51], s89 offen offset:0 sc1\n"
"v_mfma_f32_16x16x32_bf16 v[96:99], v[32:35], a[72:75], v[96:99]\n"
"v_mfma_f32_16x16x32_bf16 v[100:103], v[32:35], a[76:79], v[100:103]\n"
"s_waitcnt lgkmcnt(4)\n"
"v_mfma_f32_16x16x32_bf16 v[248:251], v[36:39], a[64:67], v[248:251]\n"
"ds_read_b64_tr_b16 v[32:33], v5 offset:25440\n"
"ds_read_b64_tr_b16 v[34:35], v5 offset:25952\n"
"v_mfma_f32_16x16x32_bf16 v[252:255], v[36:39], a[68:71], v[252:255]\n"
"buffer_load_dwordx4 a[180:183], v0, s[48:51], s89 offen offset:1024 sc1\n"
"v_mfma_f32_16x16x32_bf16 v[104:107], v[36:39], a[72:75], v[104:107]\n"
"v_mfma_f32_16x16x32_bf16 v[108:111], v[36:39], a[76:79], v[108:111]\n"
"v_mfma_f32_16x16x32_bf16 v[192:195], v[40:43], a[80:83], v[192:195]\n"
"ds_read_b128 v[36:39], v8 offset:19008\n"
"buffer_load_dwordx4 a[184:187], v0, s[48:51], s89 offen offset:2048 sc1\n"
"v_mfma_f32_16x16x32_bf16 v[196:199], v[40:43], a[84:87], v[196:199]\n"
"v_mfma_f32_16x16x32_bf16 v[80:83], v[40:43], a[88:91], v[80:83]\n"
"v_mfma_f32_16x16x32_bf16 v[84:87], v[40:43], a[92:95], v[84:87]\n"
"s_waitcnt lgkmcnt(3)\n"
"v_mfma_f32_16x16x32_bf16 v[200:203], v[24:27], a[80:83], v[200:203]\n"
"ds_read_b64_tr_b16 v[40:41], v5 offset:33792\n"
"ds_read_b64_tr_b16 v[42:43], v5 offset:34304\n"
"v_mfma_f32_16x16x32_bf16 v[204:207], v[24:27], a[84:87], v[204:207]\n"
"buffer_load_dwordx4 a[188:191], v0, s[48:51], s89 offen offset:3072 sc1\n"
"v_mfma_f32_16x16x32_bf16 v[88:91], v[24:27], a[88:91], v[88:91]\n"
"v_mfma_f32_16x16x32_bf16 v[92:95], v[24:27], a[92:95], v[92:95]\n"
"v_mfma_f32_16x16x32_bf16 v[208:211], v[28:31], a[80:83], v[208:211]\n"
"ds_read_b64_tr_b16 v[24:25], v5 offset:33824\n"
"ds_read_b64_tr_b16 v[26:27], v5 offset:34336\n"
"v_mfma_f32_16x16x32_bf16 v[212:215], v[28:31], a[84:87], v[212:215]\n"
"s_add_u32 s89, s71, 0xc000\n"
"buffer_load_dwordx4 a[192:195], v0, s[48:51], s89 offen offset:0 sc1\n"
"v_mfma_f32_16x16x32_bf16 a[224:227], v[28:31], a[88:91], a[224:227]\n"
"v_mfma_f32_16x16x32_bf16 a[228:231], v[28:31], a[92:95], a[228:231]\n"
"s_waitcnt lgkmcnt(4)\n"
"v_mfma_f32_16x16x32_bf16 v[216:219], v[32:35], a[80:83], v[216:219]\n"
"ds_read_b64_tr_b16 v[28:29], v5 offset:33856\n"
"ds_read_b64_tr_b16 v[30:31], v5 offset:34368\n"
"v_mfma_f32_16x16x32_bf16 v[220:223], v[32:35], a[84:87], v[220:223]\n"
"buffer_load_dwordx4 a[196:199], v0, s[48:51], s89 offen offset:1024 sc1\n"
"v_mfma_f32_16x16x32_bf16 a[232:235], v[32:35], a[88:91], a[232:235]\n"
"v_mfma_f32_16x16x32_bf16 a[236:239], v[32:35], a[92:95], a[236:239]\n"
"v_mfma_f32_16x16x32_bf16 v[224:227], v[36:39], a[80:83], v[224:227]\n"
"ds_read_b64_tr_b16 v[32:33], v5 offset:33888\n"
"ds_read_b64_tr_b16 v[34:35], v5 offset:34400\n"
"v_mfma_f32_16x16x32_bf16 v[228:231], v[36:39], a[84:87], v[228:231]\n"
"buffer_load_dwordx4 a[200:203], v0, s[48:51], s89 offen offset:2048 sc1\n"
"v_mfma_f32_16x16x32_bf16 v[232:235], v[36:39], a[88:91], v[232:235]\n"
"v_mfma_f32_16x16x32_bf16 v[236:239], v[36:39], a[92:95], v[236:239]\n"
"s_waitcnt lgkmcnt(4)\n"
"v_mfma_f32_16x16x32_bf16 v[64:67], v[40:43], a[80:83], v[64:67]\n"
"ds_read_b64_tr_b16 v[36:37], v5 offset:42240\n"
"ds_read_b64_tr_b16 v[38:39], v5 offset:42752\n"
"v_mfma_f32_16x16x32_bf16 v[68:71], v[40:43], a[84:87], v[68:71]\n"
"buffer_load_dwordx4 a[204:207], v0, s[48:51], s89 offen offset:3072 sc1\n"
"v_mfma_f32_16x16x32_bf16 a[240:243], v[40:43], a[88:91], a[240:243]\n"
"v_mfma_f32_16x16x32_bf16 a[244:247], v[40:43], a[92:95], a[244:247]\n"
"v_mfma_f32_16x16x32_bf16 v[72:75], v[24:27], a[80:83], v[72:75]\n"
"ds_read_b64_tr_b16 v[40:41], v5 offset:42272\n"
"ds_read_b64_tr_b16 v[42:43], v5 offset:42784\n"
"v_mfma_f32_16x16x32_bf16 v[76:79], v[24:27], a[84:87], v[76:79]\n"
"s_add_u32 m0, s17, 0x1ef00\n"
"s_add_u32 s88, s61, 0x7e000\n"
"v_mfma_f32_16x16x32_bf16 a[248:251], v[24:27], a[88:91], a[248:251]\n"
"buffer_load_dwordx4 v3, s[44:47], s88 offen lds\n"
"v_mfma_f32_16x16x32_bf16 a[252:255], v[24:27], a[92:95], a[252:255]\n"
"s_waitcnt lgkmcnt(4)\n"
"s_barrier\n"
"v_mfma_f32_16x16x32_bf16 v[240:243], v[28:31], a[80:83], v[240:243]\n"
"ds_read_b64_tr_b16 v[24:25], v5 offset:42304\n"
"ds_read_b64_tr_b16 v[26:27], v5 offset:42816\n"
"v_mfma_f32_16x16x32_bf16 v[244:247], v[28:31], a[84:87], v[244:247]\n"
"buffer_load_dwordx4 v4, s[44:47], s88 offen offset:1056 lds\n"
"v_mfma_f32_16x16x32_bf16 v[96:99], v[28:31], a[88:91], v[96:99]\n"
"v_mfma_f32_16x16x32_bf16 v[100:103], v[28:31], a[92:95], v[100:103]\n"
"v_mfma_f32_16x16x32_bf16 v[248:251], v[32:35], a[80:83], v[248:251]\n"
"ds_read_b64_tr_b16 v[28:29], v5 offset:42336\n"
"ds_read_b64_tr_b16 v[30:31], v5 offset:42848\n"
"v_mfma_f32_16x16x32_bf16 v[252:255], v[32:35], a[84:87], v[252:255]\n"
"s_add_u32 m0, s17, 0x21000\n"
"s_add_u32 s88, s64, 0x8000\n"
"v_mfma_f32_16x16x32_bf16 v[104:107], v[32:35], a[88:91], v[104:107]\n"
"buffer_load_dwordx4 v0, s[48:51], s88 offen lds\n"
"v_mfma_f32_16x16x32_bf16 v[108:111], v[32:35], a[92:95], v[108:111]\n"
"s_waitcnt lgkmcnt(4)\n"
"v_mfma_f32_16x16x32_bf16 v[192:195], v[36:39], a[96:99], v[192:195]\n"
"ds_read_b128 v[32:35], v8 offset:21120\n"
"v_mov_b32_e32 v21, 5\n"
"v_mfma_f32_16x16x32_bf16 v[196:199], v[36:39], a[100:103], v[196:199]\n"
"s_waitcnt vmcnt(13)\n"
"buffer_store_dword v21, v12, s[48:51], s69 offen sc1\n"
"v_mfma_f32_16x16x32_bf16 v[80:83], v[36:39], a[104:107], v[80:83]\n"
"buffer_load_dwordx4 v0, s[48:51], s88 offen offset:1024 lds\n"
"v_mfma_f32_16x16x32_bf16 v[84:87], v[36:39], a[108:111], v[84:87]\n"
"v_mfma_f32_16x16x32_bf16 v[200:203], v[40:43], a[96:99], v[200:203]\n"
"ds_read_b64_tr_b16 v[36:37], v5 offset:50688\n"
"ds_read_b64_tr_b16 v[38:39], v5 offset:51200\n"
"v_mfma_f32_16x16x32_bf16 v[204:207], v[40:43], a[100:103], v[204:207]\n"
"s_add_u32 m0, s17, 0x23100\n"
"s_add_u32 s88, s61, 0x1b000\n"
"v_mfma_f32_16x16x32_bf16 v[88:91], v[40:43], a[104:107], v[88:91]\n"
"buffer_load_dwordx4 v3, s[44:47], s88 offen lds\n"
"v_mfma_f32_16x16x32_bf16 v[92:95], v[40:43], a[108:111], v[92:95]\n"
"s_waitcnt lgkmcnt(3)\n"
"v_mfma_f32_16x16x32_bf16 v[208:211], v[24:27], a[96:99], v[208:211]\n"
"ds_read_b64_tr_b16 v[40:41], v5 offset:50720\n"
"ds_read_b64_tr_b16 v[42:43], v5 offset:51232\n"
"v_mfma_f32_16x16x32_bf16 v[212:215], v[24:27], a[100:103], v[212:215]\n"
"buffer_load_dwordx4 v4, s[44:47], s88 offen offset:1056 lds\n"
"v_mfma_f32_16x16x32_bf16 a[224:227], v[24:27], a[104:107], a[224:227]\n"
"v_mfma_f32_16x16x32_bf16 a[228:231], v[24:27], a[108:111], a[228:231]\n"
"v_mfma_f32_16x16x32_bf16 v[216:219], v[28:31], a[96:99], v[216:219]\n"
"ds_read_b64_tr_b16 v[24:25], v5 offset:50752\n"
"ds_read_b64_tr_b16 v[26:27], v5 offset:51264\n"
"v_mfma_f32_16x16x32_bf16 v[220:223], v[28:31], a[100:103], v[220:223]\n"
"s_add_u32 s89, s71, 0xd000\n"
"buffer_load_dwordx4 a[208:211], v0, s[48:51], s89 offen offset:0 sc1\n"
"v_mfma_f32_16x16x32_bf16 a[232:235], v[28:31], a[104:107], a[232:235]\n"
"v_mfma_f32_16x16x32_bf16 a[236:239], v[28:31], a[108:111], a[236:239]\n"
"s_waitcnt lgkmcnt(4)\n"
"v_mfma_f32_16x16x32_bf16 v[224:227], v[32:35], a[96:99], v[224:227]\n"
"ds_read_b64_tr_b16 v[28:29], v5 offset:50784\n"
"ds_read_b64_tr_b16 v[30:31], v5 offset:51296\n"
"v_mfma_f32_16x16x32_bf16 v[228:231], v[32:35], a[100:103], v[228:231]\n"
"buffer_load_dwordx4 a[212:215], v0, s[48:51], s89 offen offset:1024 sc1\n"
"v_mfma_f32_16x16x32_bf16 v[232:235], v[32:35], a[104:107], v[232:235]\n"
"v_mfma_f32_16x16x32_bf16 v[236:239], v[32:35], a[108:111], v[236:239]\n"
"v_mfma_f32_16x16x32_bf16 v[64:67], v[36:39], a[96:99], v[64:67]\n"
"ds_read_b64_tr_b16 v[32:33], v6 offset:0\n"
"ds_read_b64_tr_b16 v[34:35], v6 offset:512\n"
"v_mfma_f32_16x16x32_bf16 v[68:71], v[36:39], a[100:103], v[68:71]\n"
"buffer_load_dwordx4 a[216:219], v0, s[48:51], s89 offen offset:2048 sc1\n"
"v_mfma_f32_16x16x32_bf16 a[240:243], v[36:39], a[104:107], a[240:243]\n"
"v_mfma_f32_16x16x32_bf16 a[244:247], v[36:39], a[108:111], a[244:247]\n"
"s_waitcnt lgkmcnt(4)\n"
"v_mfma_f32_16x16x32_bf16 v[72:75], v[40:43], a[96:99], v[72:75]\n"
"ds_read_b64_tr_b16 v[36:37], v6 offset:32\n"
"ds_read_b64_tr_b16 v[38:39], v6 offset:544\n"
"v_mfma_f32_16x16x32_bf16 v[76:79], v[40:43], a[100:103], v[76:79]\n"
"buffer_load_dwordx4 a[220:223], v0, s[48:51], s89 offen offset:3072 sc1\n"
"v_mfma_f32_16x16x32_bf16 a[248:251], v[40:43], a[104:107], a[248:251]\n"
"v_mfma_f32_16x16x32_bf16 a[252:255], v[40:43], a[108:111], a[252:255]\n"
"v_mfma_f32_16x16x32_bf16 v[240:243], v[24:27], a[96:99], v[240:243]\n"
"ds_read_b64_tr_b16 v[40:41], v6 offset:64\n"
"ds_read_b64_tr_b16 v[42:43], v6 offset:576\n"
"v_mfma_f32_16x16x32_bf16 v[244:247], v[24:27], a[100:103], v[244:247]\n"
"s_add_u32 m0, s17, 0x2100\n"
"s_add_u32 s88, s61, 0x3f000\n"
"v_mfma_f32_16x16x32_bf16 v[96:99], v[24:27], a[104:107], v[96:99]\n"
"buffer_load_dwordx4 v3, s[44:47], s88 offen lds\n"
"v_mfma_f32_16x16x32_bf16 v[100:103], v[24:27], a[108:111], v[100:103]\n"
"s_waitcnt lgkmcnt(4)\n"
"v_mfma_f32_16x16x32_bf16 v[248:251], v[28:31], a[96:99], v[248:251]\n"
"ds_read_b64_tr_b16 v[24:25], v6 offset:96\n"
"ds_read_b64_tr_b16 v[26:27], v6 offset:608\n"
"v_mfma_f32_16x16x32_bf16 v[252:255], v[28:31], a[100:103], v[252:255]\n"
"buffer_load_dwordx4 v4, s[44:47], s88 offen offset:1056 lds\n"
"v_mfma_f32_16x16x32_bf16 v[104:107], v[28:31], a[104:107], v[104:107]\n"
"v_mfma_f32_16x16x32_bf16 v[108:111], v[28:31], a[108:111], v[108:111]\n"
"v_mfma_f32_16x16x32_bf16 v[192:195], v[32:35], a[112:115], v[192:195]\n"
"ds_read_b128 v[28:31], v8 offset:23232\n"
"v_mfma_f32_16x16x32_bf16 v[196:199], v[32:35], a[116:119], v[196:199]\n"
"v_mfma_f32_16x16x32_bf16 v[80:83], v[32:35], a[120:123], v[80:83]\n"
"v_mfma_f32_16x16x32_bf16 v[84:87], v[32:35], a[124:127], v[84:87]\n"
"s_waitcnt lgkmcnt(3)\n"
"v_mfma_f32_16x16x32_bf16 v[200:203], v[36:39], a[112:115], v[200:203]\n"
"ds_read_b64_tr_b16 v[32:33], v6 offset:8448\n"
"ds_read_b64_tr_b16 v[34:35], v6 offset:8960\n"
"v_mfma_f32_16x16x32_bf16 v[204:207], v[36:39], a[116:119], v[204:207]\n"
"s_add_u32 m0, s17, 0x6300\n"
"s_add_u32 s88, s61, 0x63000\n"
"v_mfma_f32_16x16x32_bf16 v[88:91], v[36:39], a[120:123], v[88:91]\n"
"buffer_load_dwordx4 v3, s[44:47], s88 offen lds\n"
"v_mfma_f32_16x16x32_bf16 v[92:95], v[36:39], a[124:127], v[92:95]\n"
"v_mfma_f32_16x16x32_bf16 v[208:211], v[40:43], a[112:115], v[208:211]\n"
"ds_read_b64_tr_b16 v[36:37], v6 offset:8480\n"
"ds_read_b64_tr_b16 v[38:39], v6 offset:8992\n"
"v_mfma_f32_16x16x32_bf16 v[212:215], v[40:43], a[116:119], v[212:215]\n"
"buffer_load_dwordx4 v4, s[44:47], s88 offen offset:1056 lds\n"
"v_mfma_f32_16x16x32_bf16 a[224:227], v[40:43], a[120:123], a[224:227]\n"
"v_mfma_f32_16x16x32_bf16 a[228:231], v[40:43], a[124:127], a[228:231]\n"
"s_waitcnt lgkmcnt(4)\n"
"v_mfma_f32_16x16x32_bf16 v[216:219], v[24:27], a[112:115], v[216:219]\n"
"ds_read_b64_tr_b16 v[40:41], v6 offset:8512\n"
"ds_read_b64_tr_b16 v[42:43], v6 offset:9024\n"
"v_mfma_f32_16x16x32_bf16 v[220:223], v[24:27], a[116:119], v[220:223]\n"
"s_add_u32 m0, s17, 0x25200\n"
"s_add_u32 s88, s61, 0x87000\n"
"v_mfma_f32_16x16x32_bf16 a[232:235], v[24:27], a[120:123], a[232:235]\n"
"buffer_load_dwordx4 v3, s[44:47], s88 offen lds\n"
"v_mfma_f32_16x16x32_bf16 a[236:239], v[24:27], a[124:127], a[236:239]\n"
"v_mfma_f32_16x16x32_bf16 v[224:227], v[28:31], a[112:115], v[224:227]\n"
"ds_read_b64_tr_b16 v[24:25], v6 offset:8544\n"
"ds_read_b64_tr_b16 v[26:27], v6 offset:9056\n"
"v_mfma_f32_16x16x32_bf16 v[228:231], v[28:31], a[116:119], v[228:231]\n"
"buffer_load_dwordx4 v4, s[44:47], s88 offen offset:1056 lds\n"
"v_mfma_f32_16x16x32_bf16 v[232:235], v[28:31], a[120:123], v[232:235]\n"
"v_mfma_f32_16x16x32_bf16 v[236:239], v[28:31], a[124:127], v[236:239]\n"
"s_waitcnt lgkmcnt(4)\n"
"s_barrier\n"
"v_mfma_f32_16x16x32_bf16 v[64:67], v[32:35], a[112:115], v[64:67]\n"
"ds_read_b64_tr_b16 v[28:29], v6 offset:16896\n"
"ds_read_b64_tr_b16 v[30:31], v6 offset:17408\n"
"v_mfma_f32_16x16x32_bf16 v[68:71], v[32:35], a[116:119], v[68:71]\n"
"buffer_load_dword v20, v11, s[48:51], s70 offen sc1\n"
"s_add_u32 m0, s17, 0x4200\n"
"v_mfma_f32_16x16x32_bf16 a[240:243], v[32:35], a[120:123], a[240:243]\n"
"s_add_u32 s88, s62, 0x0\n"
"buffer_load_dwordx4 v18, s[44:47], s88 offen lds\n"
"v_mfma_f32_16x16x32_bf16 a[244:247], v[32:35], a[124:127], a[244:247]\n"
"v_mfma_f32_16x16x32_bf16 v[72:75], v[36:39], a[112:115], v[72:75]\n"
"ds_read_b64_tr_b16 v[32:33], v6 offset:16928\n"
"ds_read_b64_tr_b16 v[34:35], v6 offset:17440\n"
"v_mfma_f32_16x16x32_bf16 v[76:79], v[36:39], a[116:119], v[76:79]\n"
"buffer_load_dwordx4 v2, s[44:47], s88 offen offset:1056 lds\n"
"v_mfma_f32_16x16x32_bf16 a[248:251], v[36:39], a[120:123], a[248:251]\n"
"v_mfma_f32_16x16x32_bf16 a[252:255], v[36:39], a[124:127], a[252:255]\n"
"s_waitcnt lgkmcnt(4)\n"
"v_mfma_f32_16x16x32_bf16 v[240:243], v[40:43], a[112:115], v[240:243]\n"
"ds_read_b64_tr_b16 v[36:37], v6 offset:16960\n"
"ds_read_b64_tr_b16 v[38:39], v6 offset:17472\n"
"v_mfma_f32_16x16x32_bf16 v[244:247], v[40:43], a[116:119], v[244:247]\n"
"s_add_u32 m0, s17, 0x8400\n"
"s_add_u32 s88, s62, 0x9000\n"
"v_mfma_f32_16x16x32_bf16 v[96:99], v[40:43], a[120:123], v[96:99]\n"
"buffer_load_dwordx4 v18, s[44:47], s88 offen lds\n"
"v_mfma_f32_16x16x32_bf16 v[100:103], v[40:43], a[124:127], v[100:103]\n"
"v_mfma_f32_16x16x32_bf16 v[248:251], v[24:27], a[112:115], v[248:251]\n"
"ds_read_b64_tr_b16 v[40:41], v6 offset:16992\n"
"ds_read_b64_tr_b16 v[42:43], v6 offset:17504\n"
"v_mfma_f32_16x16x32_bf16 v[252:255], v[24:27], a[116:119], v[252:255]\n"
"buffer_load_dwordx4 v2, s[44:47], s88 offen offset:1056 lds\n"
"v_mfma_f32_16x16x32_bf16 v[104:107], v[24:27], a[120:123], v[104:107]\n"
"v_mfma_f32_16x16x32_bf16 v[108:111], v[24:27], a[124:127], v[108:111]\n"
"s_waitcnt lgkmcnt(4)\n"
"v_mfma_f32_16x16x32_bf16 v[192:195], v[28:31], a[128:131], v[192:195]\n"
"ds_read_b128 v[24:27], v9 offset:25344\n"
"s_add_u32 m0, s17, 0xa500\n"
"v_mfma_f32_16x16x32_bf16 v[196:199], v[28:31], a[132:135], v[196:199]\n"
"s_add_u32 s88, s60, 0x12000\n"
"buffer_load_dwordx4 v3, s[44:47], s88 offen lds\n"
"v_mfma_f32_16x16x32_bf16 v[80:83], v[28:31], a[136:139], v[80:83]\n"
"buffer_load_dwordx4 v[144:147], v0, s[48:51], s73 offen offset:0 sc1\n"
"v_mfma_f32_16x16x32_bf16 v[84:87], v[28:31], a[140:143], v[84:87]\n"
"v_mfma_f32_16x16x32_bf16 v[200:203], v[32:35], a[128:131], v[200:203]\n"
"ds_read_b64_tr_b16 v[28:29], v6 offset:33792\n"
"ds_read_b64_tr_b16 v[30:31], v6 offset:34304\n"
"v_mfma_f32_16x16x32_bf16 v[204:207], v[32:35], a[132:135], v[204:207]\n"
"buffer_load_dwordx4 v4, s[44:47], s88 offen offset:1056 lds\n"
"v_mfma_f32_16x16x32_bf16 v[88:91], v[32:35], a[136:139], v[88:91]\n"
"v_mfma_f32_16x16x32_bf16 v[92:95], v[32:35], a[140:143], v[92:95]\n"
"s_waitcnt lgkmcnt(3)\n"
"v_mfma_f32_16x16x32_bf16 v[208:211], v[36:39], a[128:131], v[208:211]\n"
"ds_read_b64_tr_b16 v[32:33], v6 offset:33824\n"
"ds_read_b64_tr_b16 v[34:35], v6 offset:34336\n"
"v_mfma_f32_16x16x32_bf16 v[212:215], v[36:39], a[132:135], v[212:215]\n"
"s_add_u32 m0, s17, 0xc600\n"
"s_add_u32 s88, s60, 0x36000\n"
"v_mfma_f32_16x16x32_bf16 a[224:227], v[36:39], a[136:139], a[224:227]\n"
"buffer_load_dwordx4 v3, s[44:47], s88 offen lds\n"
"buffer_load_dwordx4 v[148:151], v0, s[48:51], s73 offen offset:1024 sc1\n"
"v_mfma_f32_16x16x32_bf16 a[228:231], v[36:39], a[140:143], a[228:231]\n"
"v_mfma_f32_16x16x32_bf16 v[216:219], v[40:43], a[128:131], v[216:219]\n"
"ds_read_b64_tr_b16 v[36:37], v6 offset:33856\n"
"ds_read_b64_tr_b16 v[38:39], v6 offset:34368\n"
"v_mfma_f32_16x16x32_bf16 v[220:223], v[40:43], a[132:135], v[220:223]\n"
"buffer_load_dwordx4 v4, s[44:47], s88 offen offset:1056 lds\n"
"v_mfma_f32_16x16x32_bf16 a[232:235], v[40:43], a[136:139], a[232:235]\n"
"v_mfma_f32_16x16x32_bf16 a[236:239], v[40:43], a[140:143], a[236:239]\n"
"s_waitcnt lgkmcnt(4)\n"
"v_mfma_f32_16x16x32_bf16 v[224:227], v[24:27], a[128:131], v[224:227]\n"
"ds_read_b64_tr_b16 v[40:41], v6 offset:33888\n"
"ds_read_b64_tr_b16 v[42:43], v6 offset:34400\n"
"v_mfma_f32_16x16x32_bf16 v[228:231], v[24:27], a[132:135], v[228:231]\n"
"s_add_u32 m0, s17, 0xe700\n"
"s_add_u32 s88, s60, 0x5a000\n"
"v_mfma_f32_16x16x32_bf16 v[232:235], v[24:27], a[136:139], v[232:235]\n"
"buffer_load_dwordx4 v3, s[44:47], s88 offen lds\n"
"buffer_load_dwordx2 v[152:153], v0, s[48:51], s74 offen offset:0 sc1\n"
"v_mfma_f32_16x16x32_bf16 v[236:239], v[24:27], a[140:143], v[236:239]\n"
"v_mfma_f32_16x16x32_bf16 v[192:195], v[28:31], a[144:147], v[192:195]\n"
"ds_read_b128 v[24:27], v9 offset:27456\n"
"buffer_load_dwordx4 v4, s[44:47], s88 offen offset:1056 lds\n"
"v_mfma_f32_16x16x32_bf16 v[196:199], v[28:31], a[148:151], v[196:199]\n"
"v_mfma_f32_16x16x32_bf16 v[80:83], v[28:31], a[152:155], v[80:83]\n"
"v_mfma_f32_16x16x32_bf16 v[84:87], v[28:31], a[156:159], v[84:87]\n"
"s_waitcnt lgkmcnt(3)\n"
"v_mfma_f32_16x16x32_bf16 v[200:203], v[32:35], a[144:147], v[200:203]\n"
"ds_read_b64_tr_b16 v[28:29], v6 offset:42240\n"
"ds_read_b64_tr_b16 v[30:31], v6 offset:42752\n"
"v_mfma_f32_16x16x32_bf16 v[204:207], v[32:35], a[148:151], v[204:207]\n"
"buffer_load_dwordx2 v[154:155], v0, s[48:51], s74 offen offset:1024 sc1\n"
"v_mfma_f32_16x16x32_bf16 v[88:91], v[32:35], a[152:155], v[88:91]\n"
"v_mfma_f32_16x16x32_bf16 v[92:95], v[32:35], a[156:159], v[92:95]\n"
"v_mfma_f32_16x16x32_bf16 v[208:211], v[36:39], a[144:147], v[208:211]\n"
"ds_read_b64_tr_b16 v[32:33], v6 offset:42272\n"
"ds_read_b64_tr_b16 v[34:35], v6 offset:42784\n"
"v_mfma_f32_16x16x32_bf16 v[212:215], v[36:39], a[148:151], v[212:215]\n"
"v_mfma_f32_16x16x32_bf16 a[224:227], v[36:39], a[152:155], a[224:227]\n"
"v_mfma_f32_16x16x32_bf16 a[228:231], v[36:39], a[156:159], a[228:231]\n"
"s_waitcnt lgkmcnt(4)\n"
"s_waitcnt vmcnt(14)\n"
"v_cmp_gt_u32_e32 vcc, 5, v20\n"
"s_cbranch_vccnz .LKF_sP5_%=\n"
".LKF_rP5_%=:\n"
"v_mfma_f32_16x16x32_bf16 v[216:219], v[40:43], a[144:147], v[216:219]\n"
"ds_read_b64_tr_b16 v[36:37], v6 offset:42304\n"
"ds_read_b64_tr_b16 v[38:39], v6 offset:42816\n"
"v_mfma_f32_16x16x32_bf16 v[220:223], v[40:43], a[148:151], v[220:223]\n"
"s_add_u32 s89, s71, 0xe000\n"
"buffer_load_dwordx4 a[0:3], v0, s[48:51], s89 offen offset:0 sc1\n"
"v_mfma_f32_16x16x32_bf16 a[232:235], v[40:43], a[152:155], a[232:235]\n"
"buffer_load_dwordx4 v[156:159], v14, s[48:51], s75 offen offset:0\n"
"v_mfma_f32_16x16x32_bf16 a[236:239], v[40:43], a[156:159], a[236:239]\n"
"v_mfma_f32_16x16x32_bf16 v[224:227], v[24:27], a[144:147], v[224:227]\n"
"ds_read_b64_tr_b16 v[40:41], v6 offset:42336\n"
"ds_read_b64_tr_b16 v[42:43], v6 offset:42848\n"
"v_mfma_f32_16x16x32_bf16 v[228:231], v[24:27], a[148:151], v[228:231]\n"
"buffer_load_dwordx4 a[4:7], v0, s[48:51], s89 offen offset:1024 sc1\n"
"v_mfma_f32_16x16x32_bf16 v[232:235], v[24:27], a[152:155], v[232:235]\n"
"v_mfma_f32_16x16x32_bf16 v[236:239], v[24:27], a[156:159], v[236:239]\n"
"s_waitcnt lgkmcnt(4)\n"
"s_barrier\n"
"v_mfma_f32_16x16x32_bf16 v[192:195], v[28:31], a[160:163], v[192:195]\n"
"ds_read_b128 v[24:27], v9 offset:29568\n"
"buffer_load_dwordx4 a[8:11], v0, s[48:51], s89 offen offset:2048 sc1\n"
"v_mfma_f32_16x16x32_bf16 v[196:199], v[28:31], a[164:167], v[196:199]\n"
"buffer_load_dwordx4 v[160:163], v14, s[48:51], s75 offen offset:64\n"
"v_mfma_f32_16x16x32_bf16 v[80:83], v[28:31], a[168:171], v[80:83]\n"
"v_mfma_f32_16x16x32_bf16 v[84:87], v[28:31], a[172:175], v[84:87]\n"
"v_mfma_f32_16x16x32_bf16 v[200:203], v[32:35], a[160:163], v[200:203]\n"
"ds_read_b64_tr_b16 v[28:29], v6 offset:50688\n"
"ds_read_b64_tr_b16 v[30:31], v6 offset:51200\n"
"v_mfma_f32_16x16x32_bf16 v[204:207], v[32:35], a[164:167], v[204:207]\n"
"buffer_load_dwordx4 a[12:15], v0, s[48:51], s89 offen offset:3072 sc1\n"
"v_mfma_f32_16x16x32_bf16 v[88:91], v[32:35], a[168:171], v[88:91]\n"
"v_mfma_f32_16x16x32_bf16 v[92:95], v[32:35], a[172:175], v[92:95]\n"
"s_waitcnt lgkmcnt(3)\n"
"v_mfma_f32_16x16x32_bf16 v[208:211], v[36:39], a[160:163], v[208:211]\n"
"ds_read_b64_tr_b16 v[32:33], v6 offset:50720\n"
"ds_read_b64_tr_b16 v[34:35], v6 offset:51232\n"
"v_mfma_f32_16x16x32_bf16 v[212:215], v[36:39], a[164:167], v[212:215]\n"
"s_add_u32 s89, s71, 0xf000\n"
"buffer_load_dwordx4 a[16:19], v0, s[48:51], s89 offen offset:0 sc1\n"
"v_mfma_f32_16x16x32_bf16 a[224:227], v[36:39], a[168:171], a[224:227]\n"
"buffer_load_dwordx4 v[164:167], v14, s[48:51], s76 offen offset:0\n"
"v_mfma_f32_16x16x32_bf16 a[228:231], v[36:39], a[172:175], a[228:231]\n"
"v_mfma_f32_16x16x32_bf16 v[216:219], v[40:43], a[160:163], v[216:219]\n"
"ds_read_b64_tr_b16 v[36:37], v6 offset:50752\n"
"ds_read_b64_tr_b16 v[38:39], v6 offset:51264\n"
"v_mfma_f32_16x16x32_bf16 v[220:223], v[40:43], a[164:167], v[220:223]\n"
"buffer_load_dwordx4 a[20:23], v0, s[48:51], s89 offen offset:1024 sc1\n"
"v_mfma_f32_16x16x32_bf16 a[232:235], v[40:43], a[168:171], a[232:235]\n"
"v_mfma_f32_16x16x32_bf16 a[236:239], v[40:43], a[172:175], a[236:239]\n"
"s_waitcnt lgkmcnt(4)\n"
"v_mfma_f32_16x16x32_bf16 v[224:227], v[24:27], a[160:163], v[224:227]\n"
"ds_read_b64_tr_b16 v[40:41], v6 offset:50784\n"
"ds_read_b64_tr_b16 v[42:43], v6 offset:51296\n"
"v_mfma_f32_16x16x32_bf16 v[228:231], v[24:27], a[164:167], v[228:231]\n"
"buffer_load_dwordx4 a[24:27], v0, s[48:51], s89 offen offset:2048 sc1\n"
"v_mfma_f32_16x16x32_bf16 v[232:235], v[24:27], a[168:171], v[232:235]\n"
"v_mfma_f32_16x16x32_bf16 v[236:239], v[24:27], a[172:175], v[236:239]\n"
"v_mfma_f32_16x16x32_bf16 v[192:195], v[28:31], a[176:179], v[192:195]\n"
"ds_read_b128 v[24:27], v9 offset:31680\n"
"buffer_load_dwordx4 a[28:31], v0, s[48:51], s89 offen offset:3072 sc1\n"
"v_mfma_f32_16x16x32_bf16 v[196:199], v[28:31], a[180:183], v[196:199]\n"
"v_mfma_f32_16x16x32_bf16 v[80:83], v[28:31], a[184:187], v[80:83]\n"
"v_mfma_f32_16x16x32_bf16 v[84:87], v[28:31], a[188:191], v[84:87]\n"
"s_waitcnt lgkmcnt(3)\n"
"v_mfma_f32_16x16x32_bf16 v[200:203], v[32:35], a[176:179], v[200:203]\n"
"ds_read_b64_tr_b16 v[28:29], v7 offset:0\n"
"ds_read_b64_tr_b16 v[30:31], v7 offset:512\n"
"v_mfma_f32_16x16x32_bf16 v[204:207], v[32:35], a[180:183], v[204:207]\n"
"s_add_u32 s89, s71, 0x10000\n"
"buffer_load_dwordx4 a[32:35], v0, s[48:51], s89 offen offset:0 sc1\n"
"v_mfma_f32_16x16x32_bf16 v[88:91], v[32:35], a[184:187], v[88:91]\n"
"v_mfma_f32_16x16x32_bf16 v[92:95], v[32:35], a[188:191], v[92:95]\n"
"v_mfma_f32_16x16x32_bf16 v[208:211], v[36:39], a[176:179], v[208:211]\n"
"ds_read_b64_tr_b16 v[32:33], v7 offset:32\n"
"ds_read_b64_tr_b16 v[34:35], v7 offset:544\n"
"v_mfma_f32_16x16x32_bf16 v[212:215], v[36:39], a[180:183], v[212:215]\n"
"buffer_load_dwordx4 a[36:39], v0, s[48:51], s89 offen offset:1024 sc1\n"
"v_mfma_f32_16x16x32_bf16 a[224:227], v[36:39], a[184:187], a[224:227]\n"
"v_mfma_f32_16x16x32_bf16 a[228:231], v[36:39], a[188:191], a[228:231]\n"
"s_waitcnt lgkmcnt(4)\n"
"v_mfma_f32_16x16x32_bf16 v[216:219], v[40:43], a[176:179], v[216:219]\n"
"ds_read_b64_tr_b16 v[36:37], v7 offset:64\n"
"ds_read_b64_tr_b16 v[38:39], v7 offset:576\n"
"v_mfma_f32_16x16x32_bf16 v[220:223], v[40:43], a[180:183], v[220:223]\n"
"buffer_load_dwordx4 a[40:43], v0, s[48:51], s89 offen offset:2048 sc1\n"
"v_mfma_f32_16x16x32_bf16 a[232:235], v[40:43], a[184:187], a[232:235]\n"
"v_mfma_f32_16x16x32_bf16 a[236:239], v[40:43], a[188:191], a[236:239]\n"
"v_mfma_f32_16x16x32_bf16 v[224:227], v[24:27], a[176:179], v[224:227]\n"
"ds_read_b64_tr_b16 v[40:41], v7 offset:96\n"
"ds_read_b64_tr_b16 v[42:43], v7 offset:608\n"
"v_mfma_f32_16x16x32_bf16 v[228:231], v[24:27], a[180:183], v[228:231]\n"
"buffer_load_dwordx4 a[44:47], v0, s[48:51], s89 offen offset:3072 sc1\n"
"v_mfma_f32_16x16x32_bf16 v[232:235], v[24:27], a[184:187], v[232:235]\n"
"v_mfma_f32_16x16x32_bf16 v[236:239], v[24:27], a[188:191], v[236:239]\n"
"s_waitcnt lgkmcnt(4)\n"
"v_mfma_f32_16x16x32_bf16 v[192:195], v[28:31], a[192:195], v[192:195]\n"
"ds_read_b128 v[24:27], v8 offset:0\n"
"s_add_u32 s89, s71, 0x11000\n"
"v_mfma_f32_16x16x32_bf16 v[196:199], v[28:31], a[196:199], v[196:199]\n"
"buffer_load_dwordx4 a[48:51], v0, s[48:51], s89 offen offset:0 sc1\n"
"v_mfma_f32_16x16x32_bf16 v[80:83], v[28:31], a[200:203], v[80:83]\n"
"v_mfma_f32_16x16x32_bf16 v[84:87], v[28:31], a[204:207], v[84:87]\n"
"s_barrier\n"
"v_mfma_f32_16x16x32_bf16 v[200:203], v[32:35], a[192:195], v[200:203]\n"
"ds_read_b64_tr_b16 v[28:29], v7 offset:8448\n"
"ds_read_b64_tr_b16 v[30:31], v7 offset:8960\n"
"v_mfma_f32_16x16x32_bf16 v[204:207], v[32:35], a[196:199], v[204:207]\n"
"buffer_load_dwordx4 a[52:55], v0, s[48:51], s89 offen offset:1024 sc1\n"
"v_mfma_f32_16x16x32_bf16 v[88:91], v[32:35], a[200:203], v[88:91]\n"
"v_mfma_f32_16x16x32_bf16 v[92:95], v[32:35], a[204:207], v[92:95]\n"
"s_waitcnt lgkmcnt(3)\n"
"v_mfma_f32_16x16x32_bf16 v[208:211], v[36:39], a[192:195], v[208:211]\n"
"ds_read_b64_tr_b16 v[32:33], v7 offset:8480\n"
"ds_read_b64_tr_b16 v[34:35], v7 offset:8992\n"
"v_mfma_f32_16x16x32_bf16 v[212:215], v[36:39], a[196:199], v[212:215]\n"
"buffer_load_dwordx4 a[56:59], v0, s[48:51], s89 offen offset:2048 sc1\n"
"v_mfma_f32_16x16x32_bf16 a[224:227], v[36:39], a[200:203], a[224:227]\n"
"v_mfma_f32_16x16x32_bf16 a[228:231], v[36:39], a[204:207], a[228:231]\n"
"v_mfma_f32_16x16x32_bf16 v[216:219], v[40:43], a[192:195], v[216:219]\n"
"ds_read_b64_tr_b16 v[36:37], v7 offset:8512\n"
".LKF_pad8:\n"
"s_cbranch_execz .LK3_ret\n"
"ds_read_b64_tr_b16 v[38:39], v7 offset:9024\n"
"v_mfma_f32_16x16x32_bf16 v[220:223], v[40:43], a[196:199], v[220:223]\n"
"buffer_load_dwordx4 a[60:63], v0, s[48:51], s89 offen offset:3072 sc1\n"
"v_mfma_f32_16x16x32_bf16 a[232:235], v[40:43], a[200:203], a[232:235]\n"
"v_mfma_f32_16x16x32_bf16 a[236:239], v[40:43], a[204:207], a[236:239]\n"
"s_waitcnt lgkmcnt(4)\n"
"v_mfma_f32_16x16x32_bf16 v[224:227], v[24:27], a[192:195], v[224:227]\n"
"ds_read_b64_tr_b16 v[40:41], v7 offset:8544\n"
"ds_read_b64_tr_b16 v[42:43], v7 offset:9056\n"
"v_mfma_f32_16x16x32_bf16 v[228:231], v[24:27], a[196:199], v[228:231]\n"
"s_add_u32 m0, s17, 0x10800\n"
"s_add_u32 s88, s60, 0x7e000\n"
"v_mfma_f32_16x16x32_bf16 v[232:235], v[24:27], a[200:203], v[232:235]\n"
"buffer_load_dwordx4 v3, s[44:47], s88 offen lds\n"
"v_mfma_f32_16x16x32_bf16 v[236:239], v[24:27], a[204:207], v[236:239]\n"
"v_mfma_f32_16x16x32_bf16 v[192:195], v[28:31], a[208:211], v[192:195]\n"
"ds_read_b128 v[24:27], v8 offset:2112\n"
"buffer_load_dwordx4 v4, s[44:47], s88 offen offset:1056 lds\n"
"v_mfma_f32_16x16x32_bf16 v[196:199], v[28:31], a[212:215], v[196:199]\n"
"v_mfma_f32_16x16x32_bf16 v[80:83], v[28:31], a[216:219], v[80:83]\n"
"v_mfma_f32_16x16x32_bf16 v[84:87], v[28:31], a[220:223], v[84:87]\n"
"s_waitcnt lgkmcnt(3)\n"
"v_mfma_f32_16x16x32_bf16 v[200:203], v[32:35], a[208:211], v[200:203]\n"
"ds_read_b128 v[28:31], v8 offset:4224\n"
"s_add_u32 m0, s17, 0x16b00\n"
"v_mfma_f32_16x16x32_bf16 v[204:207], v[32:35], a[212:215], v[204:207]\n"
"s_add_u32 s88, s60, 0x1b000\n"
"buffer_load_dwordx4 v3, s[44:47], s88 offen lds\n"
"v_mfma_f32_16x16x32_bf16 v[88:91], v[32:35], a[216:219], v[88:91]\n"
"s_waitcnt vmcnt(14)\n"
"v_mfma_f32_16x16x32_bf16 v[92:95], v[32:35], a[220:223], v[92:95]\n"
"v_max3_u32 v22, v144, v148, v152\n"
"v_max_u32_e32 v22, v22, v154\n"
"v_cmp_eq_u32_e32 vcc, -1, v22\n"
"s_cbranch_vccnz .LKF_sX_%=\n"
".LKF_rX_%=:\n"
"v_mfma_f32_16x16x32_bf16 v[208:211], v[36:39], a[208:211], v[208:211]\n"
"ds_read_b128 v[32:35], v8 offset:6336\n"
"buffer_load_dwordx4 v4, s[44:47], s88 offen offset:1056 lds\n"
"v_mfma_f32_16x16x32_bf16 v[212:215], v[36:39], a[212:215], v[212:215]\n"
"v_mov_b32_e32 v58, 0\n"
"v_mov_b32_e32 v59, 0\n"
"v_mfma_f32_16x16x32_bf16 a[224:227], v[36:39], a[216:219], a[224:227]\n"
"v_lshlrev_b32_e32 v168, 16, v152\n"
"v_and_b32_e32 v169, 0xffff0000, v152\n"
"v_mfma_f32_16x16x32_bf16 a[228:231], v[36:39], a[220:223], a[228:231]\n"
"v_lshlrev_b32_e32 v170, 16, v153\n"
"s_waitcnt lgkmcnt(2)\n"
"v_mfma_f32_16x16x32_bf16 v[216:219], v[40:43], a[208:211], v[216:219]\n"
"ds_read_b128 v[36:39], v10 offset:16896\n"
"v_and_b32_e32 v171, 0xffff0000, v153\n"
"v_mfma_f32_16x16x32_bf16 v[220:223], v[40:43], a[212:215], v[220:223]\n"
"v_lshlrev_b32_e32 v172, 16, v154\n"
"v_and_b32_e32 v173, 0xffff0000, v154\n"
"v_mfma_f32_16x16x32_bf16 a[232:235], v[40:43], a[216:219], a[232:235]\n"
"v_lshlrev_b32_e32 v174, 16, v155\n"
"v_and_b32_e32 v175, 0xffff0000, v155\n"
"v_mfma_f32_16x16x32_bf16 a[236:239], v[40:43], a[220:223], a[236:239]\n"
"v_lshlrev_b32_e32 v176, 16, v144\n"
"v_and_b32_e32 v177, 0xffff0000, v144\n"
"v_mfma_f32_16x16x32_bf16 v[224:227], v[24:27], a[208:211], v[224:227]\n"
"ds_read_b128 v[40:43], v10 offset:19008\n"
"s_add_u32 m0, s17, 0x14a00\n"
"v_mfma_f32_16x16x32_bf16 v[228:231], v[24:27], a[212:215], v[228:231]\n"
"s_add_u32 s88, s60, 0x3f000\n"
"buffer_load_dwordx4 v3, s[44:47], s88 offen lds\n"
"v_mfma_f32_16x16x32_bf16 v[232:235], v[24:27], a[216:219], v[232:235]\n"
"v_lshlrev_b32_e32 v178, 16, v145\n"
"v_and_b32_e32 v179, 0xffff0000, v145\n"
"v_mfma_f32_16x16x32_bf16 v[236:239], v[24:27], a[220:223], v[236:239]\n"
"v_lshlrev_b32_e32 v180, 16, v148\n"
"s_waitcnt lgkmcnt(2)\n"
"v_mfma_f32_16x16x32_bf16 v[224:227], v[28:31], a[0:3], v[224:227]\n"
"ds_read_b64_tr_b16 v[24:25], v7 offset:25344\n"
"ds_read_b64_tr_b16 v[26:27], v7 offset:25856\n"
"v_mfma_f32_16x16x32_bf16 v[228:231], v[28:31], a[4:7], v[228:231]\n"
"buffer_load_dwordx4 v4, s[44:47], s88 offen offset:1056 lds\n"
"v_and_b32_e32 v181, 0xffff0000, v148\n"
"v_mfma_f32_16x16x32_bf16 v[232:235], v[28:31], a[8:11], v[232:235]\n"
"v_lshlrev_b32_e32 v182, 16, v149\n"
"v_and_b32_e32 v183, 0xffff0000, v149\n"
"v_mfma_f32_16x16x32_bf16 v[236:239], v[28:31], a[12:15], v[236:239]\n"
"v_lshlrev_b32_e32 v184, 16, v146\n"
"v_and_b32_e32 v185, 0xffff0000, v146\n"
"s_waitcnt vmcnt(14)\n"
"s_barrier\n"
"v_mfma_f32_16x16x32_bf16 v[224:227], v[32:35], a[16:19], v[224:227]\n"
"ds_read_b64_tr_b16 v[28:29], v5 offset:8448\n"
"ds_read_b64_tr_b16 v[30:31], v5 offset:8960\n"
"v_mfma_f32_16x16x32_bf16 v[228:231], v[32:35], a[20:23], v[228:231]\n"
"s_add_u32 m0, s17, 0x18c00\n"
"s_add_u32 s88, s60, 0x63000\n"
"v_mfma_f32_16x16x32_bf16 v[232:235], v[32:35], a[24:27], v[232:235]\n"
"buffer_load_dwordx4 v3, s[44:47], s88 offen lds\n"
"v_lshlrev_b32_e32 v186, 16, v147\n"
"v_mfma_f32_16x16x32_bf16 v[236:239], v[32:35], a[28:31], v[236:239]\n"
"v_and_b32_e32 v187, 0xffff0000, v147\n"
"s_waitcnt vmcnt(11) lgkmcnt(4)\n"
"v_mfma_f32_16x16x32_bf16 v[224:227], v[36:39], a[32:35], v[224:227]\n"
"ds_read_b64_tr_b16 v[32:33], v5 offset:25344\n"
"ds_read_b64_tr_b16 v[34:35], v5 offset:25856\n"
"v_mfma_f32_16x16x32_bf16 v[228:231], v[36:39], a[36:39], v[228:231]\n"
"buffer_load_dwordx4 v4, s[44:47], s88 offen offset:1056 lds\n"
"v_lshlrev_b32_e32 v188, 16, v150\n"
"v_mfma_f32_16x16x32_bf16 v[232:235], v[36:39], a[40:43], v[232:235]\n"
"v_and_b32_e32 v189, 0xffff0000, v150\n"
"v_lshlrev_b32_e32 v190, 16, v151\n"
"v_mfma_f32_16x16x32_bf16 v[236:239], v[36:39], a[44:47], v[236:239]\n"
"v_and_b32_e32 v191, 0xffff0000, v151\n"
"s_waitcnt vmcnt(8)\n"
"v_mfma_f32_16x16x32_bf16 v[224:227], v[40:43], a[48:51], v[224:227]\n"
"ds_read_b64_tr_b16 v[36:37], v7 offset:33792\n"
"ds_read_b64_tr_b16 v[38:39], v7 offset:34304\n"
"v_mfma_f32_16x16x32_bf16 v[228:231], v[40:43], a[52:55], v[228:231]\n"
"s_add_u32 m0, s17, 0x1ce00\n"
"s_add_u32 s88, s60, 0x87000\n"
"v_mfma_f32_16x16x32_bf16 v[232:235], v[40:43], a[56:59], v[232:235]\n"
"buffer_load_dwordx4 v3, s[44:47], s88 offen lds\n"
"v_mfma_f32_16x16x32_bf16 v[236:239], v[40:43], a[60:63], v[236:239]\n"
"s_waitcnt lgkmcnt(4)\n"
"v_mfma_f32_16x16x32_bf16 v[192:195], v[24:27], a[0:3], v[192:195]\n"
"ds_read_b64_tr_b16 v[40:41], v7 offset:25376\n"
"ds_read_b64_tr_b16 v[42:43], v7 offset:25888\n"
"v_mfma_f32_16x16x32_bf16 v[196:199], v[24:27], a[4:7], v[196:199]\n"
"buffer_load_dwordx4 v4, s[44:47], s88 offen offset:1056 lds\n"
"v_mfma_f32_16x16x32_bf16 v[80:83], v[24:27], a[8:11], v[80:83]\n"
"v_mfma_f32_16x16x32_bf16 v[84:87], v[24:27], a[12:15], v[84:87]\n"
"v_mfma_f32_16x16x32_bf16 v[192:195], v[28:31], a[16:19], v[192:195]\n"
"ds_read_b64_tr_b16 v[24:25], v5 offset:8480\n"
"ds_read_b64_tr_b16 v[26:27], v5 offset:8992\n"
"v_mfma_f32_16x16x32_bf16 v[196:199], v[28:31], a[20:23], v[196:199]\n"
"v_add_u32_e32 v15, 0x12900, v16\n"
"v_mfma_f32_16x16x32_bf16 v[80:83], v[28:31], a[24:27], v[80:83]\n"
"ds_write_b128 v15, v[232:235] offset:0\n"
"v_mfma_f32_16x16x32_bf16 v[84:87], v[28:31], a[28:31], v[84:87]\n"
"s_waitcnt lgkmcnt(5)\n"
"v_mfma_f32_16x16x32_bf16 v[192:195], v[32:35], a[32:35], v[192:195]\n"
"ds_read_b64_tr_b16 v[28:29], v5 offset:25376\n"
"ds_read_b64_tr_b16 v[30:31], v5 offset:25888\n"
"v_mfma_f32_16x16x32_bf16 v[196:199], v[32:35], a[36:39], v[196:199]\n"
"ds_write_b128 v15, v[236:239] offset:1024\n"
"v_mfma_f32_16x16x32_bf16 v[80:83], v[32:35], a[40:43], v[80:83]\n"
"v_mfma_f32_16x16x32_bf16 v[84:87], v[32:35], a[44:47], v[84:87]\n"
"v_mfma_f32_16x16x32_bf16 v[192:195], v[36:39], a[48:51], v[192:195]\n"
"ds_read_b64_tr_b16 v[32:33], v7 offset:33824\n"
"ds_read_b64_tr_b16 v[34:35], v7 offset:34336\n"
"v_mfma_f32_16x16x32_bf16 v[196:199], v[36:39], a[52:55], v[196:199]\n"
"v_mfma_f32_16x16x32_bf16 v[80:83], v[36:39], a[56:59], v[80:83]\n"
"v_mfma_f32_16x16x32_bf16 v[84:87], v[36:39], a[60:63], v[84:87]\n"
"s_waitcnt lgkmcnt(6)\n"
"v_mfma_f32_16x16x32_bf16 v[200:203], v[40:43], a[0:3], v[200:203]\n"
"ds_read_b64_tr_b16 v[36:37], v7 offset:25408\n"
"ds_read_b64_tr_b16 v[38:39], v7 offset:25920\n"
"v_mfma_f32_16x16x32_bf16 v[204:207], v[40:43], a[4:7], v[204:207]\n"
"v_mfma_f32_16x16x32_bf16 v[88:91], v[40:43], a[8:11], v[88:91]\n"
"v_mfma_f32_16x16x32_bf16 v[92:95], v[40:43], a[12:15], v[92:95]\n"
"v_mfma_f32_16x16x32_bf16 v[200:203], v[24:27], a[16:19], v[200:203]\n"
"ds_read_b64_tr_b16 v[40:41], v5 offset:8512\n"
"ds_read_b64_tr_b16 v[42:43], v5 offset:9024\n"
"v_mfma_f32_16x16x32_bf16 v[204:207], v[24:27], a[20:23], v[204:207]\n"
"v_add_u32_e32 v15, 0x1ad00, v16\n"
"v_mfma_f32_16x16x32_bf16 v[88:91], v[24:27], a[24:27], v[88:91]\n"
"ds_write_b128 v15, v[80:83] offset:0\n"
"v_mfma_f32_16x16x32_bf16 v[92:95], v[24:27], a[28:31], v[92:95]\n"
"s_waitcnt lgkmcnt(5)\n"
"v_mfma_f32_16x16x32_bf16 v[200:203], v[28:31], a[32:35], v[200:203]\n"
"ds_read_b64_tr_b16 v[24:25], v5 offset:25408\n"
"ds_read_b64_tr_b16 v[26:27], v5 offset:25920\n"
"v_mfma_f32_16x16x32_bf16 v[204:207], v[28:31], a[36:39], v[204:207]\n"
"ds_write_b128 v15, v[84:87] offset:1024\n"
"v_mfma_f32_16x16x32_bf16 v[88:91], v[28:31], a[40:43], v[88:91]\n"
"v_mfma_f32_16x16x32_bf16 v[92:95], v[28:31], a[44:47], v[92:95]\n"
"s_waitcnt lgkmcnt(0)\n"
"s_barrier\n"
"v_mfma_f32_16x16x32_bf16 v[200:203], v[32:35], a[48:51], v[200:203]\n"
"ds_read_b64_tr_b16 v[28:29], v7 offset:33856\n"
"ds_read_b64_tr_b16 v[30:31], v7 offset:34368\n"
"v_mfma_f32_16x16x32_bf16 v[204:207], v[32:35], a[52:55], v[204:207]\n"
"v_add_u32_e32 v15, 0x1ad00, v17\n"
"ds_read_b128 v[80:83], v15 offset:0\n"
"ds_read_b128 v[84:87], v15 offset:1024\n"
"v_add_u32_e32 v15, 0x12900, v17\n"
"v_mfma_f32_16x16x32_bf16 v[88:91], v[32:35], a[56:59], v[88:91]\n"
"ds_read_b128 v[232:235], v15 offset:0\n"
"ds_read_b128 v[236:239], v15 offset:1024\n"
"v_mfma_f32_16x16x32_bf16 v[92:95], v[32:35], a[60:63], v[92:95]\n"
"v_mfma_f32_16x16x32_bf16 v[208:211], v[36:39], a[0:3], v[208:211]\n"
"ds_read_b64_tr_b16 v[32:33], v7 offset:25440\n"
"ds_read_b64_tr_b16 v[34:35], v7 offset:25952\n"
"v_mfma_f32_16x16x32_bf16 v[212:215], v[36:39], a[4:7], v[212:215]\n"
"s_waitcnt lgkmcnt(4)\n"
"v_add_f32_e32 v192, v192, v80\n"
"v_add_f32_e32 v193, v193, v81\n"
"v_add_f32_e32 v194, v194, v82\n"
"v_mfma_f32_16x16x32_bf16 a[224:227], v[36:39], a[8:11], a[224:227]\n"
"v_add_f32_e32 v195, v195, v83\n"
"v_add_f32_e32 v196, v196, v84\n"
"v_mfma_f32_16x16x32_bf16 a[228:231], v[36:39], a[12:15], a[228:231]\n"
"v_add_f32_e32 v197, v197, v85\n"
"v_add_f32_e32 v198, v198, v86\n"
"v_mfma_f32_16x16x32_bf16 v[208:211], v[40:43], a[16:19], v[208:211]\n"
"ds_read_b64_tr_b16 v[36:37], v5 offset:8544\n"
"ds_read_b64_tr_b16 v[38:39], v5 offset:9056\n"
"v_mfma_f32_16x16x32_bf16 v[212:215], v[40:43], a[20:23], v[212:215]\n"
"v_add_u32_e32 v15, 0x1ef00, v16\n"
"ds_write_b128 v15, v[88:91] offset:0\n"
"v_mfma_f32_16x16x32_bf16 a[224:227], v[40:43], a[24:27], a[224:227]\n"
"ds_write_b128 v15, v[92:95] offset:1024\n"
"v_mfma_f32_16x16x32_bf16 a[228:231], v[40:43], a[28:31], a[228:231]\n"
"v_add_f32_e32 v199, v199, v87\n"
"v_mfma_f32_16x16x32_bf16 v[208:211], v[24:27], a[32:35], v[208:211]\n"
"ds_read_b64_tr_b16 v[40:41], v5 offset:25440\n"
"ds_read_b64_tr_b16 v[42:43], v5 offset:25952\n"
"v_mfma_f32_16x16x32_bf16 v[212:215], v[24:27], a[36:39], v[212:215]\n"
"v_cvt_pk_bf16_f32 v80, v192, v193\n"
"v_cvt_pk_bf16_f32 v81, v194, v195\n"
"v_cvt_pk_bf16_f32 v82, v196, v197\n"
"v_cvt_pk_bf16_f32 v83, v198, v199\n"
"v_mfma_f32_16x16x32_bf16 a[224:227], v[24:27], a[40:43], a[224:227]\n"
"s_xor_b32 s90, s72, 0x5000\n"
"buffer_store_dwordx4 v[80:83], v0, s[48:51], s90 offen offset:0 sc1\n"
"v_mfma_f32_16x16x32_bf16 a[228:231], v[24:27], a[44:47], a[228:231]\n"
"s_waitcnt lgkmcnt(8)\n"
"v_add_f32_e32 v224, v224, v232\n"
"v_mfma_f32_16x16x32_bf16 v[208:211], v[28:31], a[48:51], v[208:211]\n"
"ds_read_b64_tr_b16 v[24:25], v7 offset:33888\n"
"ds_read_b64_tr_b16 v[26:27], v7 offset:34400\n"
"v_mfma_f32_16x16x32_bf16 v[212:215], v[28:31], a[52:55], v[212:215]\n"
"v_add_f32_e32 v225, v225, v233\n"
"v_add_f32_e32 v226, v226, v234\n"
"v_add_f32_e32 v227, v227, v235\n"
"v_add_f32_e32 v228, v228, v236\n"
"v_mfma_f32_16x16x32_bf16 a[224:227], v[28:31], a[56:59], a[224:227]\n"
"v_add_f32_e32 v229, v229, v237\n"
"v_add_f32_e32 v230, v230, v238\n"
"v_mfma_f32_16x16x32_bf16 a[228:231], v[28:31], a[60:63], a[228:231]\n"
"v_add_f32_e32 v231, v231, v239\n"
"s_waitcnt lgkmcnt(6)\n"
"v_mfma_f32_16x16x32_bf16 v[216:219], v[32:35], a[0:3], v[216:219]\n"
"ds_read_b64_tr_b16 v[28:29], v5 offset:16960\n"
"ds_read_b64_tr_b16 v[30:31], v5 offset:17472\n"
"v_mfma_f32_16x16x32_bf16 v[220:223], v[32:35], a[4:7], v[220:223]\n"
"v_cvt_pk_bf16_f32 v232, v224, v225\n"
"v_cvt_pk_bf16_f32 v233, v226, v227\n"
"v_cvt_pk_bf16_f32 v234, v228, v229\n"
"v_cvt_pk_bf16_f32 v235, v230, v231\n"
"v_mfma_f32_16x16x32_bf16 a[232:235], v[32:35], a[8:11], a[232:235]\n"
"buffer_store_dwordx4 v[232:235], v0, s[48:51], s72 offen offset:0 sc1\n"
"v_mfma_f32_16x16x32_bf16 a[236:239], v[32:35], a[12:15], a[236:239]\n"
"v_mfma_f32_16x16x32_bf16 v[216:219], v[36:39], a[16:19], v[216:219]\n"
"ds_read_b64_tr_b16 v[32:33], v5 offset:16992\n"
"ds_read_b64_tr_b16 v[34:35], v5 offset:17504\n"
"v_mfma_f32_16x16x32_bf16 v[220:223], v[36:39], a[20:23], v[220:223]\n"
"ds_write_b128 v16, a[224:227] offset:0\n"
"ds_write_b128 v16, a[228:231] offset:1024\n"
"v_mfma_f32_16x16x32_bf16 a[232:235], v[36:39], a[24:27], a[232:235]\n"
"v_mfma_f32_16x16x32_bf16 a[236:239], v[36:39], a[28:31], a[236:239]\n"
"s_waitcnt lgkmcnt(6)\n"
"v_mfma_f32_16x16x32_bf16 v[216:219], v[40:43], a[32:35], v[216:219]\n"
"ds_read_b64_tr_b16 v[36:37], v5 offset:33856\n"
"ds_read_b64_tr_b16 v[38:39], v5 offset:34368\n"
"v_mfma_f32_16x16x32_bf16 v[220:223], v[40:43], a[36:39], v[220:223]\n"
"v_mfma_f32_16x16x32_bf16 a[232:235], v[40:43], a[40:43], a[232:235]\n"
"v_mfma_f32_16x16x32_bf16 a[236:239], v[40:43], a[44:47], a[236:239]\n"
"s_waitcnt lgkmcnt(2)\n"
"s_barrier\n"
"v_mfma_f32_16x16x32_bf16 v[216:219], v[24:27], a[48:51], v[216:219]\n"
"ds_read_b64_tr_b16 v[40:41], v5 offset:33888\n"
"ds_read_b64_tr_b16 v[42:43], v5 offset:34400\n"
"v_mfma_f32_16x16x32_bf16 v[220:223], v[24:27], a[52:55], v[220:223]\n"
"v_add_u32_e32 v15, 0x1ef00, v17\n"
"ds_read_b128 v[88:91], v15 offset:0\n"
"ds_read_b128 v[92:95], v15 offset:1024\n"
"ds_read_b128 v[112:115], v17 offset:0\n"
"v_mfma_f32_16x16x32_bf16 a[232:235], v[24:27], a[56:59], a[232:235]\n"
"ds_read_b128 v[116:119], v17 offset:1024\n"
"v_mfma_f32_16x16x32_bf16 a[236:239], v[24:27], a[60:63], a[236:239]\n"
"v_mfma_f32_16x16x32_bf16 v[240:243], v[28:31], a[128:131], v[240:243]\n"
"ds_read_b64_tr_b16 v[24:25], v5 offset:42304\n"
"ds_read_b64_tr_b16 v[26:27], v5 offset:42816\n"
"v_mfma_f32_16x16x32_bf16 v[244:247], v[28:31], a[132:135], v[244:247]\n"
"s_waitcnt lgkmcnt(4)\n"
"v_add_f32_e32 v200, v200, v88\n"
"v_add_f32_e32 v201, v201, v89\n"
"v_add_f32_e32 v202, v202, v90\n"
"v_mfma_f32_16x16x32_bf16 v[96:99], v[28:31], a[136:139], v[96:99]\n"
"v_add_f32_e32 v203, v203, v91\n"
"v_add_f32_e32 v204, v204, v92\n"
"v_mfma_f32_16x16x32_bf16 v[100:103], v[28:31], a[140:143], v[100:103]\n"
"v_add_f32_e32 v205, v205, v93\n"
"v_add_f32_e32 v206, v206, v94\n"
"v_mfma_f32_16x16x32_bf16 v[248:251], v[32:35], a[128:131], v[248:251]\n"
"ds_read_b64_tr_b16 v[28:29], v5 offset:42336\n"
"ds_read_b64_tr_b16 v[30:31], v5 offset:42848\n"
"v_mfma_f32_16x16x32_bf16 v[252:255], v[32:35], a[132:135], v[252:255]\n"
"v_add_u32_e32 v15, 0x21000, v16\n"
"ds_write_b128 v15, a[232:235] offset:0\n"
"v_mfma_f32_16x16x32_bf16 v[104:107], v[32:35], a[136:139], v[104:107]\n"
"ds_write_b128 v15, a[236:239] offset:1024\n"
"v_mfma_f32_16x16x32_bf16 v[108:111], v[32:35], a[140:143], v[108:111]\n"
"v_add_f32_e32 v207, v207, v95\n"
"v_mfma_f32_16x16x32_bf16 v[240:243], v[36:39], a[144:147], v[240:243]\n"
"ds_read_b64_tr_b16 v[32:33], v5 offset:50752\n"
"ds_read_b64_tr_b16 v[34:35], v5 offset:51264\n"
"v_mfma_f32_16x16x32_bf16 v[244:247], v[36:39], a[148:151], v[244:247]\n"
"v_cvt_pk_bf16_f32 v88, v200, v201\n"
"v_cvt_pk_bf16_f32 v89, v202, v203\n"
"v_cvt_pk_bf16_f32 v90, v204, v205\n"
"v_cvt_pk_bf16_f32 v91, v206, v207\n"
"v_mfma_f32_16x16x32_bf16 v[96:99], v[36:39], a[152:155], v[96:99]\n"
"s_xor_b32 s90, s72, 0x5000\n"
"buffer_store_dwordx4 v[88:91], v0, s[48:51], s90 offen offset:1024 sc1\n"
"v_mfma_f32_16x16x32_bf16 v[100:103], v[36:39], a[156:159], v[100:103]\n"
"s_waitcnt lgkmcnt(8)\n"
"v_add_f32_e32 v208, v208, v112\n"
"s_waitcnt vmcnt(7) lgkmcnt(2)\n"
"s_barrier\n"
"v_mfma_f32_16x16x32_bf16 v[248:251], v[40:43], a[144:147], v[248:251]\n"
"ds_read_b64_tr_b16 v[36:37], v5 offset:50784\n"
"ds_read_b64_tr_b16 v[38:39], v5 offset:51296\n"
"v_mfma_f32_16x16x32_bf16 v[252:255], v[40:43], a[148:151], v[252:255]\n"
"v_add_u32_e32 v15, 0x21000, v17\n"
"ds_read_b128 v[120:123], v15 offset:0\n"
"ds_read_b128 v[124:127], v15 offset:1024\n"
"v_add_f32_e32 v209, v209, v113\n"
"v_mfma_f32_16x16x32_bf16 v[104:107], v[40:43], a[152:155], v[104:107]\n"
"v_add_f32_e32 v210, v210, v114\n"
"v_add_f32_e32 v211, v211, v115\n"
"v_mfma_f32_16x16x32_bf16 v[108:111], v[40:43], a[156:159], v[108:111]\n"
"v_add_f32_e32 v212, v212, v116\n"
"v_mfma_f32_16x16x32_bf16 v[240:243], v[24:27], a[160:163], v[240:243]\n"
"ds_read_b64_tr_b16 v[40:41], v6 offset:64\n"
"ds_read_b64_tr_b16 v[42:43], v6 offset:576\n"
"v_mfma_f32_16x16x32_bf16 v[244:247], v[24:27], a[164:167], v[244:247]\n"
"v_add_f32_e32 v213, v213, v117\n"
"v_add_f32_e32 v214, v214, v118\n"
"v_add_f32_e32 v215, v215, v119\n"
"v_cvt_pk_bf16_f32 v112, v208, v209\n"
"v_mfma_f32_16x16x32_bf16 v[96:99], v[24:27], a[168:171], v[96:99]\n"
"v_cvt_pk_bf16_f32 v113, v210, v211\n"
"v_cvt_pk_bf16_f32 v114, v212, v213\n"
"v_mfma_f32_16x16x32_bf16 v[100:103], v[24:27], a[172:175], v[100:103]\n"
"v_cvt_pk_bf16_f32 v115, v214, v215\n"
"v_mfma_f32_16x16x32_bf16 v[248:251], v[28:31], a[160:163], v[248:251]\n"
"ds_read_b64_tr_b16 v[24:25], v6 offset:96\n"
"ds_read_b64_tr_b16 v[26:27], v6 offset:608\n"
"v_mfma_f32_16x16x32_bf16 v[252:255], v[28:31], a[164:167], v[252:255]\n"
"s_xor_b32 s90, s72, 0x7800\n"
"buffer_store_dwordx4 v[112:115], v0, s[48:51], s90 offen offset:0 sc1\n"
"s_waitcnt lgkmcnt(4)\n"
"v_add_f32_e32 v216, v216, v120\n"
"v_mfma_f32_16x16x32_bf16 v[104:107], v[28:31], a[168:171], v[104:107]\n"
"v_add_f32_e32 v217, v217, v121\n"
"v_add_f32_e32 v218, v218, v122\n"
"v_mfma_f32_16x16x32_bf16 v[108:111], v[28:31], a[172:175], v[108:111]\n"
"v_add_f32_e32 v219, v219, v123\n"
"v_mfma_f32_16x16x32_bf16 v[240:243], v[32:35], a[176:179], v[240:243]\n"
"ds_read_b64_tr_b16 v[28:29], v6 offset:8512\n"
"ds_read_b64_tr_b16 v[30:31], v6 offset:9024\n"
"v_mfma_f32_16x16x32_bf16 v[244:247], v[32:35], a[180:183], v[244:247]\n"
"v_add_f32_e32 v220, v220, v124\n"
"v_add_f32_e32 v221, v221, v125\n"
"v_add_f32_e32 v222, v222, v126\n"
"v_add_f32_e32 v223, v223, v127\n"
"v_mfma_f32_16x16x32_bf16 v[96:99], v[32:35], a[184:187], v[96:99]\n"
"v_cvt_pk_bf16_f32 v120, v216, v217\n"
"v_cvt_pk_bf16_f32 v121, v218, v219\n"
"v_mfma_f32_16x16x32_bf16 v[100:103], v[32:35], a[188:191], v[100:103]\n"
"v_cvt_pk_bf16_f32 v122, v220, v221\n"
"v_cvt_pk_bf16_f32 v123, v222, v223\n"
"v_mfma_f32_16x16x32_bf16 v[248:251], v[36:39], a[176:179], v[248:251]\n"
"ds_read_b64_tr_b16 v[32:33], v6 offset:8544\n"
"ds_read_b64_tr_b16 v[34:35], v6 offset:9056\n"
"v_mfma_f32_16x16x32_bf16 v[252:255], v[36:39], a[180:183], v[252:255]\n"
"s_xor_b32 s90, s72, 0x7800\n"
"buffer_store_dwordx4 v[120:123], v0, s[48:51], s90 offen offset:1024 sc1\n"
"v_mfma_f32_16x16x32_bf16 v[104:107], v[36:39], a[184:187], v[104:107]\n"
"v_mfma_f32_16x16x32_bf16 v[108:111], v[36:39], a[188:191], v[108:111]\n"
"s_waitcnt lgkmcnt(4)\n"
"v_mfma_f32_16x16x32_bf16 v[240:243], v[40:43], a[192:195], v[240:243]\n"
"ds_read_b64_tr_b16 v[36:37], v6 offset:33856\n"
"ds_read_b64_tr_b16 v[38:39], v6 offset:34368\n"
"v_mfma_f32_16x16x32_bf16 v[244:247], v[40:43], a[196:199], v[244:247]\n"
"v_mfma_f32_16x16x32_bf16 v[96:99], v[40:43], a[200:203], v[96:99]\n"
"v_mfma_f32_16x16x32_bf16 v[100:103], v[40:43], a[204:207], v[100:103]\n"
"v_mfma_f32_16x16x32_bf16 v[248:251], v[24:27], a[192:195], v[248:251]\n"
"ds_read_b64_tr_b16 v[40:41], v6 offset:25408\n"
"ds_read_b64_tr_b16 v[42:43], v6 offset:25920\n"
"v_mfma_f32_16x16x32_bf16 v[252:255], v[24:27], a[196:199], v[252:255]\n"
"v_mfma_f32_16x16x32_bf16 v[104:107], v[24:27], a[200:203], v[104:107]\n"
"v_mfma_f32_16x16x32_bf16 v[108:111], v[24:27], a[204:207], v[108:111]\n"
"s_waitcnt vmcnt(5) lgkmcnt(4)\n"
"s_barrier\n"
"v_mfma_f32_16x16x32_bf16 v[240:243], v[28:31], a[208:211], v[240:243]\n"
"ds_read_b64_tr_b16 v[24:25], v6 offset:42304\n"
"ds_read_b64_tr_b16 v[26:27], v6 offset:42816\n"
"v_mfma_f32_16x16x32_bf16 v[244:247], v[28:31], a[212:215], v[244:247]\n"
"v_mfma_f32_16x16x32_bf16 v[96:99], v[28:31], a[216:219], v[96:99]\n"
"v_mfma_f32_16x16x32_bf16 v[100:103], v[28:31], a[220:223], v[100:103]\n"
"v_mfma_f32_16x16x32_bf16 v[248:251], v[32:35], a[208:211], v[248:251]\n"
"ds_read_b64_tr_b16 v[28:29], v7 offset:64\n"
"ds_read_b64_tr_b16 v[30:31], v7 offset:576\n"
"v_mfma_f32_16x16x32_bf16 v[252:255], v[32:35], a[212:215], v[252:255]\n"
"v_mfma_f32_16x16x32_bf16 v[104:107], v[32:35], a[216:219], v[104:107]\n"
"v_mfma_f32_16x16x32_bf16 v[108:111], v[32:35], a[220:223], v[108:111]\n"
"s_waitcnt lgkmcnt(4)\n"
"v_mfma_f32_16x16x32_bf16 v[240:243], v[36:39], a[0:3], v[240:243]\n"
"ds_read_b64_tr_b16 v[32:33], v6 offset:33888\n"
"ds_read_b64_tr_b16 v[34:35], v6 offset:34400\n"
"v_mfma_f32_16x16x32_bf16 v[244:247], v[36:39], a[4:7], v[244:247]\n"
"v_mfma_f32_16x16x32_bf16 v[96:99], v[36:39], a[8:11], v[96:99]\n"
"v_mfma_f32_16x16x32_bf16 v[100:103], v[36:39], a[12:15], v[100:103]\n"
"v_mfma_f32_16x16x32_bf16 v[240:243], v[40:43], a[16:19], v[240:243]\n"
"ds_read_b64_tr_b16 v[36:37], v6 offset:25440\n"
"ds_read_b64_tr_b16 v[38:39], v6 offset:25952\n"
"v_mfma_f32_16x16x32_bf16 v[244:247], v[40:43], a[20:23], v[244:247]\n"
"v_mfma_f32_16x16x32_bf16 v[96:99], v[40:43], a[24:27], v[96:99]\n"
"v_mfma_f32_16x16x32_bf16 v[100:103], v[40:43], a[28:31], v[100:103]\n"
"s_waitcnt lgkmcnt(4)\n"
"v_mfma_f32_16x16x32_bf16 v[240:243], v[24:27], a[32:35], v[240:243]\n"
"ds_read_b64_tr_b16 v[40:41], v6 offset:42336\n"
"ds_read_b64_tr_b16 v[42:43], v6 offset:42848\n"
"v_mfma_f32_16x16x32_bf16 v[244:247], v[24:27], a[36:39], v[244:247]\n"
"v_mfma_f32_16x16x32_bf16 v[96:99], v[24:27], a[40:43], v[96:99]\n"
"v_mfma_f32_16x16x32_bf16 v[100:103], v[24:27], a[44:47], v[100:103]\n"
"v_mfma_f32_16x16x32_bf16 v[240:243], v[28:31], a[48:51], v[240:243]\n"
"ds_read_b64_tr_b16 v[24:25], v7 offset:96\n"
"ds_read_b64_tr_b16 v[26:27], v7 offset:608\n"
"v_mfma_f32_16x16x32_bf16 v[244:247], v[28:31], a[52:55], v[244:247]\n"
"s_xor_b32 s90, s72, 0x2000\n"
"buffer_load_dwordx4 v[192:195], v0, s[48:51], s90 offen sc1\n"
"s_xor_b32 s90, s72, 0x4000\n"
"buffer_load_dwordx4 v[196:199], v0, s[48:51], s90 offen sc1\n"
"s_xor_b32 s90, s72, 0x6000\n"
"buffer_load_dwordx4 v[200:203], v0, s[48:51], s90 offen sc1\n"
"v_mfma_f32_16x16x32_bf16 v[96:99], v[28:31], a[56:59], v[96:99]\n"
"v_mfma_f32_16x16x32_bf16 v[100:103], v[28:31], a[60:63], v[100:103]\n"
"s_waitcnt lgkmcnt(4)\n"
"v_mfma_f32_16x16x32_bf16 v[248:251], v[32:35], a[0:3], v[248:251]\n"
"ds_read_b64_tr_b16 v[28:29], v5 offset:16896\n"
"ds_read_b64_tr_b16 v[30:31], v5 offset:17408\n"
"v_mfma_f32_16x16x32_bf16 v[252:255], v[32:35], a[4:7], v[252:255]\n"
"v_mfma_f32_16x16x32_bf16 v[104:107], v[32:35], a[8:11], v[104:107]\n"
"v_mfma_f32_16x16x32_bf16 v[108:111], v[32:35], a[12:15], v[108:111]\n"
"v_mfma_f32_16x16x32_bf16 v[248:251], v[36:39], a[16:19], v[248:251]\n"
"ds_read_b64_tr_b16 v[32:33], v5 offset:33792\n"
"ds_read_b64_tr_b16 v[34:35], v5 offset:34304\n"
"v_mfma_f32_16x16x32_bf16 v[252:255], v[36:39], a[20:23], v[252:255]\n"
"ds_write_b128 v16, v[96:99] offset:8448\n"
"v_mfma_f32_16x16x32_bf16 v[104:107], v[36:39], a[24:27], v[104:107]\n"
"ds_write_b128 v16, v[100:103] offset:9472\n"
"v_mfma_f32_16x16x32_bf16 v[108:111], v[36:39], a[28:31], v[108:111]\n"
"s_waitcnt lgkmcnt(6)\n"
"v_mfma_f32_16x16x32_bf16 v[248:251], v[40:43], a[32:35], v[248:251]\n"
"ds_read_b64_tr_b16 v[36:37], v5 offset:42240\n"
"ds_read_b64_tr_b16 v[38:39], v5 offset:42752\n"
"v_mfma_f32_16x16x32_bf16 v[252:255], v[40:43], a[36:39], v[252:255]\n"
"s_add_u32 s90, s72, 0x1000\n"
"buffer_load_dwordx4 v[80:83], v0, s[48:51], s90 offen offset:0 sc1\n"
"buffer_load_dwordx4 v[84:87], v0, s[48:51], s90 offen offset:1024 sc1\n"
"v_mfma_f32_16x16x32_bf16 v[104:107], v[40:43], a[40:43], v[104:107]\n"
"v_mfma_f32_16x16x32_bf16 v[108:111], v[40:43], a[44:47], v[108:111]\n"
"v_mfma_f32_16x16x32_bf16 v[248:251], v[24:27], a[48:51], v[248:251]\n"
"ds_read_b64_tr_b16 v[40:41], v5 offset:50688\n"
"ds_read_b64_tr_b16 v[42:43], v5 offset:51200\n"
"v_mfma_f32_16x16x32_bf16 v[252:255], v[24:27], a[52:55], v[252:255]\n"
"s_add_u32 s90, s72, 0x1800\n"
"buffer_load_dwordx4 v[88:91], v0, s[48:51], s90 offen offset:0 sc1\n"
"buffer_load_dwordx4 v[92:95], v0, s[48:51], s90 offen offset:1024 sc1\n"
"v_mfma_f32_16x16x32_bf16 v[104:107], v[24:27], a[56:59], v[104:107]\n"
"v_mfma_f32_16x16x32_bf16 v[108:111], v[24:27], a[60:63], v[108:111]\n"
"s_waitcnt lgkmcnt(6)\n"
"v_mfma_f32_16x16x32_bf16 v[64:67], v[28:31], a[128:131], v[64:67]\n"
"ds_read_b64_tr_b16 v[24:25], v6 offset:0\n"
"ds_read_b64_tr_b16 v[26:27], v6 offset:512\n"
"v_mfma_f32_16x16x32_bf16 v[68:71], v[28:31], a[132:135], v[68:71]\n"
"v_mfma_f32_16x16x32_bf16 a[240:243], v[28:31], a[136:139], a[240:243]\n"
"v_mfma_f32_16x16x32_bf16 a[244:247], v[28:31], a[140:143], a[244:247]\n"
"v_mfma_f32_16x16x32_bf16 v[64:67], v[32:35], a[144:147], v[64:67]\n"
"ds_read_b64_tr_b16 v[28:29], v6 offset:8448\n"
"ds_read_b64_tr_b16 v[30:31], v6 offset:8960\n"
"v_mfma_f32_16x16x32_bf16 v[68:71], v[32:35], a[148:151], v[68:71]\n"
"ds_write_b128 v16, v[104:107] offset:25344\n"
"v_mfma_f32_16x16x32_bf16 a[240:243], v[32:35], a[152:155], a[240:243]\n"
"ds_write_b128 v16, v[108:111] offset:26368\n"
"v_mfma_f32_16x16x32_bf16 a[244:247], v[32:35], a[156:159], a[244:247]\n"
"s_waitcnt lgkmcnt(0)\n"
"s_barrier\n"
"v_mfma_f32_16x16x32_bf16 v[64:67], v[36:39], a[160:163], v[64:67]\n"
"ds_read_b64_tr_b16 v[32:33], v6 offset:33792\n"
"ds_read_b64_tr_b16 v[34:35], v6 offset:34304\n"
"v_mfma_f32_16x16x32_bf16 v[68:71], v[36:39], a[164:167], v[68:71]\n"
"ds_read_b128 v[96:99], v17 offset:8448\n"
"ds_read_b128 v[100:103], v17 offset:9472\n"
"ds_read_b128 v[104:107], v17 offset:25344\n"
"ds_read_b128 v[108:111], v17 offset:26368\n"
"v_mfma_f32_16x16x32_bf16 a[240:243], v[36:39], a[168:171], a[240:243]\n"
"v_mfma_f32_16x16x32_bf16 a[244:247], v[36:39], a[172:175], a[244:247]\n"
"v_mfma_f32_16x16x32_bf16 v[64:67], v[40:43], a[176:179], v[64:67]\n"
"ds_read_b64_tr_b16 v[36:37], v6 offset:25344\n"
"ds_read_b64_tr_b16 v[38:39], v6 offset:25856\n"
"v_mfma_f32_16x16x32_bf16 v[68:71], v[40:43], a[180:183], v[68:71]\n"
"s_waitcnt lgkmcnt(4)\n"
"v_add_f32_e32 v240, v240, v96\n"
"v_add_f32_e32 v241, v241, v97\n"
"v_add_f32_e32 v242, v242, v98\n"
"v_mfma_f32_16x16x32_bf16 a[240:243], v[40:43], a[184:187], a[240:243]\n"
"v_add_f32_e32 v243, v243, v99\n"
"v_add_f32_e32 v244, v244, v100\n"
"v_add_f32_e32 v245, v245, v101\n"
"v_mfma_f32_16x16x32_bf16 a[244:247], v[40:43], a[188:191], a[244:247]\n"
"v_add_f32_e32 v246, v246, v102\n"
"v_mfma_f32_16x16x32_bf16 v[64:67], v[24:27], a[192:195], v[64:67]\n"
"ds_read_b64_tr_b16 v[40:41], v6 offset:42240\n"
"ds_read_b64_tr_b16 v[42:43], v6 offset:42752\n"
"v_mfma_f32_16x16x32_bf16 v[68:71], v[24:27], a[196:199], v[68:71]\n"
"v_add_f32_e32 v247, v247, v103\n"
"v_cvt_pk_bf16_f32 v96, v240, v241\n"
"v_cvt_pk_bf16_f32 v97, v242, v243\n"
"v_cvt_pk_bf16_f32 v98, v244, v245\n"
"v_mfma_f32_16x16x32_bf16 a[240:243], v[24:27], a[200:203], a[240:243]\n"
"v_cvt_pk_bf16_f32 v99, v246, v247\n"
"s_xor_b32 s90, s72, 0x2800\n"
"buffer_store_dwordx4 v[96:99], v0, s[48:51], s90 offen offset:0 sc1\n"
"v_mfma_f32_16x16x32_bf16 a[244:247], v[24:27], a[204:207], a[244:247]\n"
"s_waitcnt lgkmcnt(4)\n"
"v_add_f32_e32 v248, v248, v104\n"
"v_mfma_f32_16x16x32_bf16 v[64:67], v[28:31], a[208:211], v[64:67]\n"
"ds_read_b64_tr_b16 v[24:25], v7 offset:0\n"
"ds_read_b64_tr_b16 v[26:27], v7 offset:512\n"
"v_mfma_f32_16x16x32_bf16 v[68:71], v[28:31], a[212:215], v[68:71]\n"
"v_add_f32_e32 v249, v249, v105\n"
"v_add_f32_e32 v250, v250, v106\n"
"v_add_f32_e32 v251, v251, v107\n"
"v_add_f32_e32 v252, v252, v108\n"
"v_mfma_f32_16x16x32_bf16 a[240:243], v[28:31], a[216:219], a[240:243]\n"
"v_add_f32_e32 v253, v253, v109\n"
"v_add_f32_e32 v254, v254, v110\n"
"v_add_f32_e32 v255, v255, v111\n"
"v_mfma_f32_16x16x32_bf16 a[244:247], v[28:31], a[220:223], a[244:247]\n"
"v_cvt_pk_bf16_f32 v104, v248, v249\n"
"s_waitcnt lgkmcnt(4)\n"
"v_mfma_f32_16x16x32_bf16 v[64:67], v[32:35], a[0:3], v[64:67]\n"
"ds_read_b64_tr_b16 v[28:29], v5 offset:16928\n"
"ds_read_b64_tr_b16 v[30:31], v5 offset:17440\n"
"v_mfma_f32_16x16x32_bf16 v[68:71], v[32:35], a[4:7], v[68:71]\n"
"v_cvt_pk_bf16_f32 v105, v250, v251\n"
"v_cvt_pk_bf16_f32 v106, v252, v253\n"
"v_mfma_f32_16x16x32_bf16 a[240:243], v[32:35], a[8:11], a[240:243]\n"
"v_cvt_pk_bf16_f32 v107, v254, v255\n"
"v_mfma_f32_16x16x32_bf16 a[244:247], v[32:35], a[12:15], a[244:247]\n"
"s_xor_b32 s90, s72, 0x2800\n"
"buffer_store_dwordx4 v[104:107], v0, s[48:51], s90 offen offset:1024 sc1\n"
"v_mfma_f32_16x16x32_bf16 v[64:67], v[36:39], a[16:19], v[64:67]\n"
"ds_read_b64_tr_b16 v[32:33], v5 offset:33824\n"
"ds_read_b64_tr_b16 v[34:35], v5 offset:34336\n"
"v_mfma_f32_16x16x32_bf16 v[68:71], v[36:39], a[20:23], v[68:71]\n"
"v_mfma_f32_16x16x32_bf16 a[240:243], v[36:39], a[24:27], a[240:243]\n"
"v_mfma_f32_16x16x32_bf16 a[244:247], v[36:39], a[28:31], a[244:247]\n"
"s_waitcnt lgkmcnt(4)\n"
"v_mfma_f32_16x16x32_bf16 v[64:67], v[40:43], a[32:35], v[64:67]\n"
"ds_read_b64_tr_b16 v[36:37], v5 offset:42272\n"
"ds_read_b64_tr_b16 v[38:39], v5 offset:42784\n"
"v_mfma_f32_16x16x32_bf16 v[68:71], v[40:43], a[36:39], v[68:71]\n"
"v_mfma_f32_16x16x32_bf16 a[240:243], v[40:43], a[40:43], a[240:243]\n"
"v_mfma_f32_16x16x32_bf16 a[244:247], v[40:43], a[44:47], a[244:247]\n"
"v_mfma_f32_16x16x32_bf16 v[64:67], v[24:27], a[48:51], v[64:67]\n"
"ds_read_b64_tr_b16 v[40:41], v5 offset:50720\n"
"ds_read_b64_tr_b16 v[42:43], v5 offset:51232\n"
".LKF_pad9:\n"
"s_cbranch_execz .LK3_ret\n"
"v_mfma_f32_16x16x32_bf16 v[68:71], v[24:27], a[52:55], v[68:71]\n"
"v_mfma_f32_16x16x32_bf16 a[240:243], v[24:27], a[56:59], a[240:243]\n"
"v_mfma_f32_16x16x32_bf16 a[244:247], v[24:27], a[60:63], a[244:247]\n"
"s_waitcnt lgkmcnt(4)\n"
"v_mfma_f32_16x16x32_bf16 v[72:75], v[28:31], a[128:131], v[72:75]\n"
"ds_read_b64_tr_b16 v[24:25], v6 offset:32\n"
"ds_read_b64_tr_b16 v[26:27], v6 offset:544\n"
"v_mfma_f32_16x16x32_bf16 v[76:79], v[28:31], a[132:135], v[76:79]\n"
"v_mfma_f32_16x16x32_bf16 a[248:251], v[28:31], a[136:139], a[248:251]\n"
"v_mfma_f32_16x16x32_bf16 a[252:255], v[28:31], a[140:143], a[252:255]\n"
"v_mfma_f32_16x16x32_bf16 v[72:75], v[32:35], a[144:147], v[72:75]\n"
"ds_read_b64_tr_b16 v[28:29], v6 offset:8480\n"
"ds_read_b64_tr_b16 v[30:31], v6 offset:8992\n"
"v_mfma_f32_16x16x32_bf16 v[76:79], v[32:35], a[148:151], v[76:79]\n"
"v_add_u32_e32 v15, 0x12900, v16\n"
"v_mfma_f32_16x16x32_bf16 a[248:251], v[32:35], a[152:155], a[248:251]\n"
"ds_write_b128 v15, a[240:243] offset:0\n"
"v_mfma_f32_16x16x32_bf16 a[252:255], v[32:35], a[156:159], a[252:255]\n"
"s_waitcnt lgkmcnt(5)\n"
"v_mfma_f32_16x16x32_bf16 v[72:75], v[36:39], a[160:163], v[72:75]\n"
"ds_read_b64_tr_b16 v[32:33], v6 offset:33824\n"
"ds_read_b64_tr_b16 v[34:35], v6 offset:34336\n"
"v_mfma_f32_16x16x32_bf16 v[76:79], v[36:39], a[164:167], v[76:79]\n"
"ds_write_b128 v15, a[244:247] offset:1024\n"
"v_mfma_f32_16x16x32_bf16 a[248:251], v[36:39], a[168:171], a[248:251]\n"
"v_mfma_f32_16x16x32_bf16 a[252:255], v[36:39], a[172:175], a[252:255]\n"
"s_waitcnt lgkmcnt(0)\n"
"s_barrier\n"
"v_mfma_f32_16x16x32_bf16 v[72:75], v[40:43], a[176:179], v[72:75]\n"
"ds_read_b64_tr_b16 v[36:37], v6 offset:25376\n"
"ds_read_b64_tr_b16 v[38:39], v6 offset:25888\n"
"v_mfma_f32_16x16x32_bf16 v[76:79], v[40:43], a[180:183], v[76:79]\n"
"v_add_u32_e32 v15, 0x12900, v17\n"
"ds_read_b128 v[128:131], v15 offset:0\n"
"v_mfma_f32_16x16x32_bf16 a[248:251], v[40:43], a[184:187], a[248:251]\n"
"ds_read_b128 v[132:135], v15 offset:1024\n"
"s_waitcnt vmcnt(6)\n"
"v_mfma_f32_16x16x32_bf16 a[252:255], v[40:43], a[188:191], a[252:255]\n"
"v_max3_u32 v22, v192, v193, v194\n"
"v_mfma_f32_16x16x32_bf16 v[72:75], v[24:27], a[192:195], v[72:75]\n"
"ds_read_b64_tr_b16 v[40:41], v6 offset:42272\n"
"ds_read_b64_tr_b16 v[42:43], v6 offset:42784\n"
"v_mfma_f32_16x16x32_bf16 v[76:79], v[24:27], a[196:199], v[76:79]\n"
"v_max3_u32 v22, v22, v195, v196\n"
"v_max3_u32 v22, v22, v197, v198\n"
"v_mfma_f32_16x16x32_bf16 a[248:251], v[24:27], a[200:203], a[248:251]\n"
"v_max3_u32 v22, v22, v199, v200\n"
"v_max3_u32 v22, v22, v201, v202\n"
"v_mfma_f32_16x16x32_bf16 a[252:255], v[24:27], a[204:207], a[252:255]\n"
"v_max_u32_e32 v22, v22, v203\n"
"v_cmp_eq_u32_e32 vcc, -1, v22\n"
"v_mfma_f32_16x16x32_bf16 v[72:75], v[28:31], a[208:211], v[72:75]\n"
"ds_read_b64_tr_b16 v[24:25], v7 offset:32\n"
"ds_read_b64_tr_b16 v[26:27], v7 offset:544\n"
"v_mfma_f32_16x16x32_bf16 v[76:79], v[28:31], a[212:215], v[76:79]\n"
"s_cbranch_vccnz .LKF_sQ0_%=\n"
".LKF_rQ0_%=:\n"
"s_waitcnt vmcnt(4)\n"
"v_mfma_f32_16x16x32_bf16 a[248:251], v[28:31], a[216:219], a[248:251]\n"
"v_max3_u32 v22, v80, v81, v82\n"
"v_max3_u32 v22, v22, v83, v84\n"
"v_mfma_f32_16x16x32_bf16 a[252:255], v[28:31], a[220:223], a[252:255]\n"
"v_max3_u32 v22, v22, v85, v86\n"
"v_max_u32_e32 v22, v22, v87\n"
"v_cmp_eq_u32_e32 vcc, -1, v22\n"
"s_cbranch_vccnz .LKF_sQ2_%=\n"
".LKF_rQ2_%=:\n"
"v_mfma_f32_16x16x32_bf16 v[224:227], v[60:63], v[192:195], v[224:227]\n"
"s_waitcnt lgkmcnt(4)\n"
"v_add_f32_e32 v64, v64, v128\n"
"v_mfma_f32_16x16x32_bf16 v[228:231], v[58:61], v[192:195], v[228:231]\n"
"v_add_f32_e32 v65, v65, v129\n"
"v_add_f32_e32 v66, v66, v130\n"
"v_mfma_f32_16x16x32_bf16 v[224:227], v[60:63], v[196:199], v[224:227]\n"
"v_add_f32_e32 v67, v67, v131\n"
"v_add_f32_e32 v68, v68, v132\n"
"v_mfma_f32_16x16x32_bf16 v[228:231], v[58:61], v[196:199], v[228:231]\n"
"v_add_f32_e32 v69, v69, v133\n"
"v_add_f32_e32 v70, v70, v134\n"
"v_mfma_f32_16x16x32_bf16 v[224:227], v[60:63], v[200:203], v[224:227]\n"
"v_add_f32_e32 v71, v71, v135\n"
"v_mfma_f32_16x16x32_bf16 v[228:231], v[58:61], v[200:203], v[228:231]\n"
"v_mfma_f32_16x16x32_bf16 v[72:75], v[32:35], a[0:3], v[72:75]\n"
"v_mfma_f32_16x16x32_bf16 v[76:79], v[32:35], a[4:7], v[76:79]\n"
"v_mfma_f32_16x16x32_bf16 a[248:251], v[32:35], a[8:11], a[248:251]\n"
"v_mfma_f32_16x16x32_bf16 a[252:255], v[32:35], a[12:15], a[252:255]\n"
"v_mfma_f32_16x16x32_bf16 v[72:75], v[36:39], a[16:19], v[72:75]\n"
"s_waitcnt vmcnt(2)\n"
"v_max3_u32 v22, v88, v89, v90\n"
"v_mfma_f32_16x16x32_bf16 v[76:79], v[36:39], a[20:23], v[76:79]\n"
"v_max3_u32 v22, v22, v91, v92\n"
"v_max3_u32 v22, v22, v93, v94\n"
"v_mfma_f32_16x16x32_bf16 a[248:251], v[36:39], a[24:27], a[248:251]\n"
"v_max_u32_e32 v22, v22, v95\n"
"v_cmp_eq_u32_e32 vcc, -1, v22\n"
"v_mfma_f32_16x16x32_bf16 a[252:255], v[36:39], a[28:31], a[252:255]\n"
"s_cbranch_vccnz .LKF_sQ6_%=\n"
".LKF_rQ6_%=:\n"
"v_mfma_f32_16x16x32_bf16 v[64:67], v[60:63], v[80:83], v[64:67]\n"
"v_mfma_f32_16x16x32_bf16 v[68:71], v[58:61], v[80:83], v[68:71]\n"
"v_mfma_f32_16x16x32_bf16 v[72:75], v[60:63], v[84:87], v[72:75]\n"
"v_mfma_f32_16x16x32_bf16 v[76:79], v[58:61], v[84:87], v[76:79]\n"
"s_waitcnt lgkmcnt(0)\n"
"v_mfma_f32_16x16x32_bf16 v[72:75], v[40:43], a[32:35], v[72:75]\n"
"s_add_u32 s90, s72, 0x800\n"
"buffer_load_dwordx4 v[208:211], v0, s[48:51], s90 offen offset:0 sc1\n"
"buffer_load_dwordx4 v[212:215], v0, s[48:51], s90 offen offset:1024 sc1\n"
"v_mfma_f32_16x16x32_bf16 v[76:79], v[40:43], a[36:39], v[76:79]\n"
"v_fma_f32 v224, v224, v164, v168\n"
"v_fma_f32 v225, v225, v165, v169\n"
"v_mfma_f32_16x16x32_bf16 a[248:251], v[40:43], a[40:43], a[248:251]\n"
"v_fma_f32 v226, v226, v166, v170\n"
"v_fma_f32 v227, v227, v167, v171\n"
"v_mfma_f32_16x16x32_bf16 a[252:255], v[40:43], a[44:47], a[252:255]\n"
"v_fma_f32 v228, v228, v164, v172\n"
"v_fma_f32 v229, v229, v165, v173\n"
"v_mfma_f32_16x16x32_bf16 v[72:75], v[24:27], a[48:51], v[72:75]\n"
"v_fma_f32 v230, v230, v166, v174\n"
"v_fma_f32 v231, v231, v167, v175\n"
"v_mfma_f32_16x16x32_bf16 v[76:79], v[24:27], a[52:55], v[76:79]\n"
"v_cvt_pk_bf16_f32 v168, v224, v225\n"
"v_cvt_pk_bf16_f32 v169, v226, v227\n"
"v_mfma_f32_16x16x32_bf16 a[248:251], v[24:27], a[56:59], a[248:251]\n"
"v_cvt_pk_bf16_f32 v170, v228, v229\n"
"v_cvt_pk_bf16_f32 v171, v230, v231\n"
"v_mfma_f32_16x16x32_bf16 a[252:255], v[24:27], a[60:63], a[252:255]\n"
"s_add_u32 s90, s78, 0x4800\n"
"s_mov_b32 exec_lo, s82\n"
"s_mov_b32 exec_hi, s82\n"
"buffer_store_dwordx2 v[168:169], v13, s[52:55], s78 offen offset:0\n"
"buffer_store_dwordx2 v[170:171], v13, s[52:55], s90 offen offset:0\n"
"s_mov_b64 exec, -1\n"
"v_mfma_f32_16x16x32_bf16 v[64:67], v[60:63], v[88:91], v[64:67]\n"
"v_mfma_f32_16x16x32_bf16 v[68:71], v[58:61], v[88:91], v[68:71]\n"
"v_mfma_f32_16x16x32_bf16 v[72:75], v[60:63], v[92:95], v[72:75]\n"
"v_mfma_f32_16x16x32_bf16 v[76:79], v[58:61], v[92:95], v[76:79]\n"
"s_nop 7\n"
"v_add_u32_e32 v15, 0x1ad00, v16\n"
"ds_write_b128 v15, a[248:251] offset:0\n"
"ds_write_b128 v15, a[252:255] offset:1024\n"
"v_add_u32_e32 v15, 0x1ad00, v17\n"
"s_waitcnt lgkmcnt(0)\n"
"s_barrier\n"
"ds_read_b128 v[136:139], v15 offset:0\n"
"ds_read_b128 v[140:143], v15 offset:1024\n"
"s_waitcnt vmcnt(2)\n"
"v_max3_u32 v22, v208, v209, v210\n"
"v_max3_u32 v22, v22, v211, v212\n"
"v_max3_u32 v22, v22, v213, v214\n"
"v_max_u32_e32 v22, v22, v215\n"
"v_cmp_eq_u32_e32 vcc, -1, v22\n"
"s_cbranch_vccnz .LKF_sQ7_%=\n"
".LKF_rQ7_%=:\n"
"v_mfma_f32_16x16x32_bf16 v[64:67], v[60:63], v[208:211], v[64:67]\n"
"v_mfma_f32_16x16x32_bf16 v[68:71], v[58:61], v[208:211], v[68:71]\n"
"s_waitcnt lgkmcnt(0)\n"
"v_add_f32_e32 v72, v72, v136\n"
"v_add_f32_e32 v73, v73, v137\n"
"v_add_f32_e32 v74, v74, v138\n"
"v_add_f32_e32 v75, v75, v139\n"
"v_add_f32_e32 v76, v76, v140\n"
"v_add_f32_e32 v77, v77, v141\n"
"v_add_f32_e32 v78, v78, v142\n"
"v_add_f32_e32 v79, v79, v143\n"
"s_nop 1\n"
"v_mfma_f32_16x16x32_bf16 v[72:75], v[60:63], v[212:215], v[72:75]\n"
"v_mfma_f32_16x16x32_bf16 v[76:79], v[58:61], v[212:215], v[76:79]\n"
"v_fma_f32 v64, v64, v156, v176\n"
"v_fma_f32 v65, v65, v157, v177\n"
"v_fma_f32 v66, v66, v158, v178\n"
"v_fma_f32 v67, v67, v159, v179\n"
"v_fma_f32 v68, v68, v156, v180\n"
"v_fma_f32 v69, v69, v157, v181\n"
"v_fma_f32 v70, v70, v158, v182\n"
"v_fma_f32 v71, v71, v159, v183\n"
"v_cvt_pk_bf16_f32 v176, v64, v65\n"
"v_cvt_pk_bf16_f32 v177, v66, v67\n"
"v_cvt_pk_bf16_f32 v178, v68, v69\n"
"v_cvt_pk_bf16_f32 v179, v70, v71\n"
"s_add_u32 s90, s77, 0x4800\n"
"buffer_store_dwordx2 v[176:177], v13, s[52:55], s77 offen offset:0\n"
"buffer_store_dwordx2 v[178:179], v13, s[52:55], s90 offen offset:0\n"
"v_fma_f32 v72, v72, v160, v184\n"
"v_fma_f32 v73, v73, v161, v185\n"
"v_fma_f32 v74, v74, v162, v186\n"
"v_fma_f32 v75, v75, v163, v187\n"
"v_fma_f32 v76, v76, v160, v188\n"
"v_fma_f32 v77, v77, v161, v189\n"
"v_fma_f32 v78, v78, v162, v190\n"
"v_fma_f32 v79, v79, v163, v191\n"
"v_cvt_pk_bf16_f32 v184, v72, v73\n"
"v_cvt_pk_bf16_f32 v185, v74, v75\n"
"v_cvt_pk_bf16_f32 v186, v76, v77\n"
"v_cvt_pk_bf16_f32 v187, v78, v79\n"
"s_add_u32 s90, s77, 0x4800\n"
"buffer_store_dwordx2 v[184:185], v13, s[52:55], s77 offen offset:32\n"
"buffer_store_dwordx2 v[186:187], v13, s[52:55], s90 offen offset:32\n"
"s_branch .LKF_end_%=\n"
".LKF_sH2_%=:\n"
"s_mov_b32 s91, 0x40000\n"
".LKF_lH2_%=:\n"
"s_sleep 2\n"
"s_add_u32 s90, s15, 0x2000\n"
"buffer_load_dwordx4 a[0:3], v0, s[48:51], s90 offen offset:0 sc1\n"
"buffer_load_dwordx4 a[4:7], v0, s[48:51], s90 offen offset:1024 sc1\n"
"buffer_load_dwordx4 a[8:11], v0, s[48:51], s90 offen offset:2048 sc1\n"
"buffer_load_dwordx4 a[12:15], v0, s[48:51], s90 offen offset:3072 sc1\n"
"s_waitcnt vmcnt(0)\n"
"s_sub_u32 s91, s91, 1\n"
"s_cbranch_scc1 .LKF_rH2_%=\n"
"v_accvgpr_read_b32 v44, a0\n"
"v_accvgpr_read_b32 v45, a4\n"
"v_accvgpr_read_b32 v46, a8\n"
"v_accvgpr_read_b32 v47, a12\n"
"v_max3_u32 v44, v44, v45, v46\n"
"v_max_u32_e32 v44, v44, v47\n"
"v_cmp_eq_u32_e32 vcc, -1, v44\n"
"s_cbranch_vccnz .LKF_lH2_%=\n"
"s_branch .LKF_rH2_%=\n"
".LKF_sH3_%=:\n"
"s_mov_b32 s91, 0x40000\n"
".LKF_lH3_%=:\n"
"s_sleep 2\n"
"s_add_u32 s90, s15, 0x3000\n"
"buffer_load_dwordx4 a[16:19], v0, s[48:51], s90 offen offset:0 sc1\n"
"buffer_load_dwordx4 a[20:23], v0, s[48:51], s90 offen offset:1024 sc1\n"
"buffer_load_dwordx4 a[24:27], v0, s[48:51], s90 offen offset:2048 sc1\n"
"buffer_load_dwordx4 a[28:31], v0, s[48:51], s90 offen offset:3072 sc1\n"
"s_waitcnt vmcnt(0)\n"
"s_sub_u32 s91, s91, 1\n"
"s_cbranch_scc1 .LKF_rH3_%=\n"
"v_accvgpr_read_b32 v44, a16\n"
"v_accvgpr_read_b32 v45, a20\n"
"v_accvgpr_read_b32 v46, a24\n"
"v_accvgpr_read_b32 v47, a28\n"
"v_max3_u32 v44, v44, v45, v46\n"
"v_max_u32_e32 v44, v44, v47\n"
"v_cmp_eq_u32_e32 vcc, -1, v44\n"
"s_cbranch_vccnz .LKF_lH3_%=\n"
"s_branch .LKF_rH3_%=\n"
".LKF_sH4_%=:\n"
"s_mov_b32 s91, 0x40000\n"
".LKF_lH4_%=:\n"
"s_sleep 2\n"
"s_add_u32 s90, s15, 0x4000\n"
"buffer_load_dwordx4 a[32:35], v0, s[48:51], s90 offen offset:0 sc1\n"
"buffer_load_dwordx4 a[36:39], v0, s[48:51], s90 offen offset:1024 sc1\n"
"buffer_load_dwordx4 a[40:43], v0, s[48:51], s90 offen offset:2048 sc1\n"
"buffer_load_dwordx4 a[44:47], v0, s[48:51], s90 offen offset:3072 sc1\n"
"s_waitcnt vmcnt(0)\n"
"s_sub_u32 s91, s91, 1\n"
"s_cbranch_scc1 .LKF_rH4_%=\n"
"v_accvgpr_read_b32 v44, a32\n"
"v_accvgpr_read_b32 v45, a36\n"
"v_accvgpr_read_b32 v46, a40\n"
"v_accvgpr_read_b32 v47, a44\n"
"v_max3_u32 v44, v44, v45, v46\n"
"v_max_u32_e32 v44, v44, v47\n"
"v_cmp_eq_u32_e32 vcc, -1, v44\n"
"s_cbranch_vccnz .LKF_lH4_%=\n"
"s_branch .LKF_rH4_%=\n"
".LKF_sH5_%=:\n"
"s_mov_b32 s91, 0x40000\n"
".LKF_lH5_%=:\n"
"s_sleep 2\n"
"s_add_u32 s90, s15, 0x5000\n"
"buffer_load_dwordx4 a[48:51], v0, s[48:51], s90 offen offset:0 sc1\n"
"buffer_load_dwordx4 a[52:55], v0, s[48:51], s90 offen offset:1024 sc1\n"
"buffer_load_dwordx4 a[56:59], v0, s[48:51], s90 offen offset:2048 sc1\n"
"buffer_load_dwordx4 a[60:63], v0, s[48:51], s90 offen offset:3072 sc1\n"
"s_waitcnt vmcnt(0)\n"
"s_sub_u32 s91, s91, 1\n"
"s_cbranch_scc1 .LKF_rH5_%=\n"
"v_accvgpr_read_b32 v44, a48\n"
"v_accvgpr_read_b32 v45, a52\n"
"v_accvgpr_read_b32 v46, a56\n"
"v_accvgpr_read_b32 v47, a60\n"
"v_max3_u32 v44, v44, v45, v46\n"
"v_max_u32_e32 v44, v44, v47\n"
"v_cmp_eq_u32_e32 vcc, -1, v44\n"
"s_cbranch_vccnz .LKF_lH5_%=\n"
"s_branch .LKF_rH5_%=\n"
".LKF_sH6_%=:\n"
"s_mov_b32 s91, 0x40000\n"
".LKF_lH6_%=:\n"
"s_sleep 2\n"
"s_add_u32 s90, s15, 0x6000\n"
"buffer_load_dwordx4 a[64:67], v0, s[48:51], s90 offen offset:0 sc1\n"
"buffer_load_dwordx4 a[68:71], v0, s[48:51], s90 offen offset:1024 sc1\n"
"buffer_load_dwordx4 a[72:75], v0, s[48:51], s90 offen offset:2048 sc1\n"
"buffer_load_dwordx4 a[76:79], v0, s[48:51], s90 offen offset:3072 sc1\n"
"s_waitcnt vmcnt(0)\n"
"s_sub_u32 s91, s91, 1\n"
"s_cbranch_scc1 .LKF_rH6_%=\n"
"v_accvgpr_read_b32 v44, a64\n"
"v_accvgpr_read_b32 v45, a68\n"
"v_accvgpr_read_b32 v46, a72\n"
"v_accvgpr_read_b32 v47, a76\n"
"v_max3_u32 v44, v44, v45, v46\n"
"v_max_u32_e32 v44, v44, v47\n"
"v_cmp_eq_u32_e32 vcc, -1, v44\n"
"s_cbranch_vccnz .LKF_lH6_%=\n"
"s_branch .LKF_rH6_%=\n"
".LKF_sH7_%=:\n"
"s_mov_b32 s91, 0x40000\n"
".LKF_lH7_%=:\n"
"s_sleep 2\n"
"s_add_u32 s90, s15, 0x7000\n"
"buffer_load_dwordx4 a[80:83], v0, s[48:51], s90 offen offset:0 sc1\n"
"buffer_load_dwordx4 a[84:87], v0, s[48:51], s90 offen offset:1024 sc1\n"
"buffer_load_dwordx4 a[88:91], v0, s[48:51], s90 offen offset:2048 sc1\n"
"buffer_load_dwordx4 a[92:95], v0, s[48:51], s90 offen offset:3072 sc1\n"
"s_waitcnt vmcnt(0)\n"
"s_sub_u32 s91, s91, 1\n"
"s_cbranch_scc1 .LKF_rH7_%=\n"
"v_accvgpr_read_b32 v44, a80\n"
"v_accvgpr_read_b32 v45, a84\n"
"v_accvgpr_read_b32 v46, a88\n"
"v_accvgpr_read_b32 v47, a92\n"
"v_max3_u32 v44, v44, v45, v46\n"
"v_max_u32_e32 v44, v44, v47\n"
"v_cmp_eq_u32_e32 vcc, -1, v44\n"
"s_cbranch_vccnz .LKF_lH7_%=\n"
"s_branch .LKF_rH7_%=\n"
".LKF_sH8_%=:\n"
"s_mov_b32 s91, 0x40000\n"
".LKF_lH8_%=:\n"
"s_sleep 2\n"
"s_add_u32 s90, s15, 0x8000\n"
"buffer_load_dwordx4 a[96:99], v0, s[48:51], s90 offen offset:0 sc1\n"
"buffer_load_dwordx4 a[100:103], v0, s[48:51], s90 offen offset:1024 sc1\n"
"buffer_load_dwordx4 a[104:107], v0, s[48:51], s90 offen offset:2048 sc1\n"
"buffer_load_dwordx4 a[108:111], v0, s[48:51], s90 offen offset:3072 sc1\n"
"s_waitcnt vmcnt(0)\n"
"s_sub_u32 s91, s91, 1\n"
"s_cbranch_scc1 .LKF_rH8_%=\n"
"v_accvgpr_read_b32 v44, a96\n"
"v_accvgpr_read_b32 v45, a100\n"
"v_accvgpr_read_b32 v46, a104\n"
"v_accvgpr_read_b32 v47, a108\n"
"v_max3_u32 v44, v44, v45, v46\n"
"v_max_u32_e32 v44, v44, v47\n"
"v_cmp_eq_u32_e32 vcc, -1, v44\n"
"s_cbranch_vccnz .LKF_lH8_%=\n"
"s_branch .LKF_rH8_%=\n"
".LKF_sH9_%=:\n"
"s_mov_b32 s91, 0x40000\n"
".LKF_lH9_%=:\n"
"s_sleep 2\n"
"s_add_u32 s90, s15, 0x9000\n"
"buffer_load_dwordx4 a[112:115], v0, s[48:51], s90 offen offset:0 sc1\n"
"buffer_load_dwordx4 a[116:119], v0, s[48:51], s90 offen offset:1024 sc1\n"
"buffer_load_dwordx4 a[120:123], v0, s[48:51], s90 offen offset:2048 sc1\n"
"buffer_load_dwordx4 a[124:127], v0, s[48:51], s90 offen offset:3072 sc1\n"
"s_waitcnt vmcnt(0)\n"
"s_sub_u32 s91, s91, 1\n"
"s_cbranch_scc1 .LKF_rH9_%=\n"
"v_accvgpr_read_b32 v44, a112\n"
"v_accvgpr_read_b32 v45, a116\n"
"v_accvgpr_read_b32 v46, a120\n"
"v_accvgpr_read_b32 v47, a124\n"
"v_max3_u32 v44, v44, v45, v46\n"
"v_max_u32_e32 v44, v44, v47\n"
"v_cmp_eq_u32_e32 vcc, -1, v44\n"
"s_cbranch_vccnz .LKF_lH9_%=\n"
"s_branch .LKF_rH9_%=\n"
".LKF_sH10_%=:\n"
"s_mov_b32 s91, 0x40000\n"
".LKF_lH10_%=:\n"
"s_sleep 2\n"
"s_add_u32 s90, s15, 0xa000\n"
"buffer_load_dwordx4 a[128:131], v0, s[48:51], s90 offen offset:0 sc1\n"
"buffer_load_dwordx4 a[132:135], v0, s[48:51], s90 offen offset:1024 sc1\n"
"buffer_load_dwordx4 a[136:139], v0, s[48:51], s90 offen offset:2048 sc1\n"
"buffer_load_dwordx4 a[140:143], v0, s[48:51], s90 offen offset:3072 sc1\n"
"s_waitcnt vmcnt(0)\n"
"s_sub_u32 s91, s91, 1\n"
"s_cbranch_scc1 .LKF_rH10_%=\n"
"v_accvgpr_read_b32 v44, a128\n"
"v_accvgpr_read_b32 v45, a132\n"
"v_accvgpr_read_b32 v46, a136\n"
"v_accvgpr_read_b32 v47, a140\n"
"v_max3_u32 v44, v44, v45, v46\n"
"v_max_u32_e32 v44, v44, v47\n"
"v_cmp_eq_u32_e32 vcc, -1, v44\n"
"s_cbranch_vccnz .LKF_lH10_%=\n"
"s_branch .LKF_rH10_%=\n"
".LKF_sH11_%=:\n"
"s_mov_b32 s91, 0x40000\n"
".LKF_lH11_%=:\n"
"s_sleep 2\n"
"s_add_u32 s90, s15, 0xb000\n"
"buffer_load_dwordx4 a[144:147], v0, s[48:51], s90 offen offset:0 sc1\n"
"buffer_load_dwordx4 a[148:151], v0, s[48:51], s90 offen offset:1024 sc1\n"
"buffer_load_dwordx4 a[152:155], v0, s[48:51], s90 offen offset:2048 sc1\n"
"buffer_load_dwordx4 a[156:159], v0, s[48:51], s90 offen offset:3072 sc1\n"
"s_waitcnt vmcnt(0)\n"
"s_sub_u32 s91, s91, 1\n"
"s_cbranch_scc1 .LKF_rH11_%=\n"
"v_accvgpr_read_b32 v44, a144\n"
"v_accvgpr_read_b32 v45, a148\n"
"v_accvgpr_read_b32 v46, a152\n"
"v_accvgpr_read_b32 v47, a156\n"
"v_max3_u32 v44, v44, v45, v46\n"
"v_max_u32_e32 v44, v44, v47\n"
"v_cmp_eq_u32_e32 vcc, -1, v44\n"
"s_cbranch_vccnz .LKF_lH11_%=\n"
"s_branch .LKF_rH11_%=\n"
".LKF_sH12_%=:\n"
"s_mov_b32 s91, 0x40000\n"
".LKF_lH12_%=:\n"
"s_sleep 2\n"
"s_add_u32 s90, s15, 0xc000\n"
"buffer_load_dwordx4 a[160:163], v0, s[48:51], s90 offen offset:0 sc1\n"
"buffer_load_dwordx4 a[164:167], v0, s[48:51], s90 offen offset:1024 sc1\n"
"buffer_load_dwordx4 a[168:171], v0, s[48:51], s90 offen offset:2048 sc1\n"
"buffer_load_dwordx4 a[172:175], v0, s[48:51], s90 offen offset:3072 sc1\n"
"s_waitcnt vmcnt(0)\n"
"s_sub_u32 s91, s91, 1\n"
"s_cbranch_scc1 .LKF_rH12_%=\n"
"v_accvgpr_read_b32 v44, a160\n"
"v_accvgpr_read_b32 v45, a164\n"
"v_accvgpr_read_b32 v46, a168\n"
"v_accvgpr_read_b32 v47, a172\n"
"v_max3_u32 v44, v44, v45, v46\n"
"v_max_u32_e32 v44, v44, v47\n"
"v_cmp_eq_u32_e32 vcc, -1, v44\n"
"s_cbranch_vccnz .LKF_lH12_%=\n"
"s_branch .LKF_rH12_%=\n"
".LKF_sH13_%=:\n"
"s_mov_b32 s91, 0x40000\n"
".LKF_lH13_%=:\n"
"s_sleep 2\n"
"s_add_u32 s90, s15, 0xd000\n"
"buffer_load_dwordx4 a[176:179], v0, s[48:51], s90 offen offset:0 sc1\n"
"buffer_load_dwordx4 a[180:183], v0, s[48:51], s90 offen offset:1024 sc1\n"
"buffer_load_dwordx4 a[184:187], v0, s[48:51], s90 offen offset:2048 sc1\n"
"buffer_load_dwordx4 a[188:191], v0, s[48:51], s90 offen offset:3072 sc1\n"
"s_waitcnt vmcnt(0)\n"
"s_sub_u32 s91, s91, 1\n"
"s_cbranch_scc1 .LKF_rH13_%=\n"
"v_accvgpr_read_b32 v44, a176\n"
"v_accvgpr_read_b32 v45, a180\n"
"v_accvgpr_read_b32 v46, a184\n"
"v_accvgpr_read_b32 v47, a188\n"
"v_max3_u32 v44, v44, v45, v46\n"
"v_max_u32_e32 v44, v44, v47\n"
"v_cmp_eq_u32_e32 vcc, -1, v44\n"
"s_cbranch_vccnz .LKF_lH13_%=\n"
"s_branch .LKF_rH13_%=\n"
".LKF_sH14_%=:\n"
"s_mov_b32 s91, 0x40000\n"
".LKF_lH14_%=:\n"
"s_sleep 2\n"
"s_add_u32 s90, s15, 0xe000\n"
"buffer_load_dwordx4 a[192:195], v0, s[48:51], s90 offen offset:0 sc1\n"
"buffer_load_dwordx4 a[196:199], v0, s[48:51], s90 offen offset:1024 sc1\n"
"buffer_load_dwordx4 a[200:203], v0, s[48:51], s90 offen offset:2048 sc1\n"
"buffer_load_dwordx4 a[204:207], v0, s[48:51], s90 offen offset:3072 sc1\n"
"s_waitcnt vmcnt(0)\n"
"s_sub_u32 s91, s91, 1\n"
"s_cbranch_scc1 .LKF_rH14_%=\n"
"v_accvgpr_read_b32 v44, a192\n"
"v_accvgpr_read_b32 v45, a196\n"
"v_accvgpr_read_b32 v46, a200\n"
"v_accvgpr_read_b32 v47, a204\n"
"v_max3_u32 v44, v44, v45, v46\n"
"v_max_u32_e32 v44, v44, v47\n"
"v_cmp_eq_u32_e32 vcc, -1, v44\n"
"s_cbranch_vccnz .LKF_lH14_%=\n"
"s_branch .LKF_rH14_%=\n"
".LKF_sH15_%=:\n"
"s_mov_b32 s91, 0x40000\n"
".LKF_lH15_%=:\n"
"s_sleep 2\n"
"s_add_u32 s90, s15, 0xf000\n"
"buffer_load_dwordx4 a[208:211], v0, s[48:51], s90 offen offset:0 sc1\n"
"buffer_load_dwordx4 a[212:215], v0, s[48:51], s90 offen offset:1024 sc1\n"
"buffer_load_dwordx4 a[216:219], v0, s[48:51], s90 offen offset:2048 sc1\n"
"buffer_load_dwordx4 a[220:223], v0, s[48:51], s90 offen offset:3072 sc1\n"
"s_waitcnt vmcnt(0)\n"
"s_sub_u32 s91, s91, 1\n"
"s_cbranch_scc1 .LKF_rH15_%=\n"
"v_accvgpr_read_b32 v44, a208\n"
"v_accvgpr_read_b32 v45, a212\n"
"v_accvgpr_read_b32 v46, a216\n"
"v_accvgpr_read_b32 v47, a220\n"
"v_max3_u32 v44, v44, v45, v46\n"
"v_max_u32_e32 v44, v44, v47\n"
"v_cmp_eq_u32_e32 vcc, -1, v44\n"
"s_cbranch_vccnz .LKF_lH15_%=\n"
"s_branch .LKF_rH15_%=\n"
".LKF_sH16_%=:\n"
"s_mov_b32 s91, 0x40000\n"
".LKF_lH16_%=:\n"
"s_sleep 2\n"
"s_add_u32 s90, s15, 0x10000\n"
"buffer_load_dwordx4 a[224:227], v0, s[48:51], s90 offen offset:0 sc1\n"
"buffer_load_dwordx4 a[228:231], v0, s[48:51], s90 offen offset:1024 sc1\n"
"buffer_load_dwordx4 a[232:235], v0, s[48:51], s90 offen offset:2048 sc1\n"
"buffer_load_dwordx4 a[236:239], v0, s[48:51], s90 offen offset:3072 sc1\n"
"s_waitcnt vmcnt(0)\n"
"s_sub_u32 s91, s91, 1\n"
"s_cbranch_scc1 .LKF_rH16_%=\n"
"v_accvgpr_read_b32 v44, a224\n"
"v_accvgpr_read_b32 v45, a228\n"
"v_accvgpr_read_b32 v46, a232\n"
"v_accvgpr_read_b32 v47, a236\n"
"v_accvgpr_read_b32 v48, a226\n"
"v_accvgpr_read_b32 v49, a230\n"
"v_accvgpr_read_b32 v50, a234\n"
"v_accvgpr_read_b32 v51, a238\n"
"v_max3_u32 v44, v44, v48, v49\n"
"v_max3_u32 v45, v45, v50, v51\n"
"v_max3_u32 v44, v44, v45, v46\n"
"v_max_u32_e32 v44, v44, v47\n"
"v_cmp_eq_u32_e32 vcc, -1, v44\n"
"s_cbranch_vccnz .LKF_lH16_%=\n"
"s_branch .LKF_rH16_%=\n"
".LKF_sH17_%=:\n"
"s_mov_b32 s91, 0x40000\n"
".LKF_lH17_%=:\n"
"s_sleep 2\n"
"s_add_u32 s90, s15, 0x11000\n"
"buffer_load_dwordx4 a[240:243], v0, s[48:51], s90 offen offset:0 sc1\n"
"buffer_load_dwordx4 a[244:247], v0, s[48:51], s90 offen offset:1024 sc1\n"
"buffer_load_dwordx4 a[248:251], v0, s[48:51], s90 offen offset:2048 sc1\n"
"buffer_load_dwordx4 a[252:255], v0, s[48:51], s90 offen offset:3072 sc1\n"
"s_waitcnt vmcnt(0)\n"
"s_sub_u32 s91, s91, 1\n"
"s_cbranch_scc1 .LKF_rH17_%=\n"
"v_accvgpr_read_b32 v44, a240\n"
"v_accvgpr_read_b32 v45, a244\n"
"v_accvgpr_read_b32 v46, a248\n"
"v_accvgpr_read_b32 v47, a252\n"
"v_accvgpr_read_b32 v48, a242\n"
"v_accvgpr_read_b32 v49, a246\n"
"v_accvgpr_read_b32 v50, a250\n"
"v_accvgpr_read_b32 v51, a254\n"
"v_max3_u32 v44, v44, v48, v49\n"
"v_max3_u32 v45, v45, v50, v51\n"
"v_max3_u32 v44, v44, v45, v46\n"
"v_max_u32_e32 v44, v44, v47\n"
"v_cmp_eq_u32_e32 vcc, -1, v44\n"
"s_cbranch_vccnz .LKF_lH17_%=\n"
"s_branch .LKF_rH17_%=\n"
".LKF_sP1_%=:\n"
"s_mov_b32 s91, 0x40000\n"
".LKF_lP1_%=:\n"
"s_sleep 2\n"
"buffer_load_dword v20, v11, s[48:51], s70 offen sc1\n"
"s_waitcnt vmcnt(0)\n"
"s_sub_u32 s91, s91, 1\n"
"s_cbranch_scc1 .LKF_rP1_%=\n"
"v_cmp_gt_u32_e32 vcc, 1, v20\n"
"s_cbranch_vccnz .LKF_lP1_%=\n"
"s_branch .LKF_rP1_%=\n"
".LKF_sP3_%=:\n"
"s_mov_b32 s91, 0x40000\n"
".LKF_lP3_%=:\n"
"s_sleep 2\n"
"buffer_load_dword v20, v11, s[48:51], s70 offen sc1\n"
"s_waitcnt vmcnt(0)\n"
"s_sub_u32 s91, s91, 1\n"
"s_cbranch_scc1 .LKF_rP3_%=\n"
"v_cmp_gt_u32_e32 vcc, 3, v20\n"
"s_cbranch_vccnz .LKF_lP3_%=\n"
"s_branch .LKF_rP3_%=\n"
".LKF_sP4_%=:\n"
"s_mov_b32 s91, 0x40000\n"
".LKF_lP4_%=:\n"
"s_sleep 2\n"
"buffer_load_dword v20, v11, s[48:51], s70 offen sc1\n"
"s_waitcnt vmcnt(0)\n"
"s_sub_u32 s91, s91, 1\n"
"s_cbranch_scc1 .LKF_rP4_%=\n"
"v_cmp_gt_u32_e32 vcc, 4, v20\n"
"s_cbranch_vccnz .LKF_lP4_%=\n"
"s_branch .LKF_rP4_%=\n"
".LKF_sP5_%=:\n"
"s_mov_b32 s91, 0x40000\n"
".LKF_lP5_%=:\n"
"s_sleep 2\n"
"buffer_load_dword v20, v11, s[48:51], s70 offen sc1\n"
"s_waitcnt vmcnt(0)\n"
"s_sub_u32 s91, s91, 1\n"
"s_cbranch_scc1 .LKF_rP5_%=\n"
"v_cmp_gt_u32_e32 vcc, 5, v20\n"
"s_cbranch_vccnz .LKF_lP5_%=\n"
"s_branch .LKF_rP5_%=\n"
".LKF_sX_%=:\n"
"s_mov_b32 s91, 0x40000\n"
".LKF_lX_%=:\n"
"s_sleep 2\n"
"buffer_load_dwordx4 v[144:147], v0, s[48:51], s73 offen offset:0 sc1\n"
"buffer_load_dwordx4 v[148:151], v0, s[48:51], s73 offen offset:1024 sc1\n"
"buffer_load_dwordx2 v[152:153], v0, s[48:51], s74 offen offset:0 sc1\n"
"buffer_load_dwordx2 v[154:155], v0, s[48:51], s74 offen offset:1024 sc1\n"
"s_waitcnt vmcnt(0)\n"
"s_sub_u32 s91, s91, 1\n"
"s_cbranch_scc1 .LKF_rX_%=\n"
"v_max3_u32 v22, v144, v148, v152\n"
"v_max_u32_e32 v22, v22, v154\n"
"v_cmp_eq_u32_e32 vcc, -1, v22\n"
"s_cbranch_vccnz .LKF_lX_%=\n"
"s_branch .LKF_rX_%=\n"
".LKF_sQ0_%=:\n"
"s_mov_b32 s91, 0x40000\n"
".LKF_lQ0_%=:\n"
"s_sleep 2\n"
"s_xor_b32 s90, s72, 0x2000\n"
"buffer_load_dwordx4 v[192:195], v0, s[48:51], s90 offen sc1\n"
"s_xor_b32 s90, s72, 0x4000\n"
"buffer_load_dwordx4 v[196:199], v0, s[48:51], s90 offen sc1\n"
"s_xor_b32 s90, s72, 0x6000\n"
"buffer_load_dwordx4 v[200:203], v0, s[48:51], s90 offen sc1\n"
"s_waitcnt vmcnt(0)\n"
"s_sub_u32 s91, s91, 1\n"
"s_cbranch_scc1 .LKF_rQ0_%=\n"
"v_max3_u32 v22, v192, v193, v194\n"
"v_max3_u32 v22, v22, v195, v196\n"
"v_max3_u32 v22, v22, v197, v198\n"
"v_max3_u32 v22, v22, v199, v200\n"
"v_max3_u32 v22, v22, v201, v202\n"
"v_max_u32_e32 v22, v22, v203\n"
"v_cmp_eq_u32_e32 vcc, -1, v22\n"
"s_cbranch_vccnz .LKF_lQ0_%=\n"
"s_branch .LKF_rQ0_%=\n"
".LKF_sQ2_%=:\n"
"s_mov_b32 s91, 0x40000\n"
".LKF_lQ2_%=:\n"
"s_sleep 2\n"
"s_add_u32 s90, s72, 0x1000\n"
"buffer_load_dwordx4 v[80:83], v0, s[48:51], s90 offen offset:0 sc1\n"
"buffer_load_dwordx4 v[84:87], v0, s[48:51], s90 offen offset:1024 sc1\n"
"s_waitcnt vmcnt(0)\n"
"s_sub_u32 s91, s91, 1\n"
"s_cbranch_scc1 .LKF_rQ2_%=\n"
"v_max3_u32 v22, v80, v81, v82\n"
"v_max3_u32 v22, v22, v83, v84\n"
"v_max3_u32 v22, v22, v85, v86\n"
"v_max_u32_e32 v22, v22, v87\n"
"v_cmp_eq_u32_e32 vcc, -1, v22\n"
"s_cbranch_vccnz .LKF_lQ2_%=\n"
"s_branch .LKF_rQ2_%=\n"
".LKF_sQ6_%=:\n"
"s_mov_b32 s91, 0x40000\n"
".LKF_lQ6_%=:\n"
"s_sleep 2\n"
"s_add_u32 s90, s72, 0x1800\n"
"buffer_load_dwordx4 v[88:91], v0, s[48:51], s90 offen offset:0 sc1\n"
"buffer_load_dwordx4 v[92:95], v0, s[48:51], s90 offen offset:1024 sc1\n"
"s_waitcnt vmcnt(0)\n"
"s_sub_u32 s91, s91, 1\n"
"s_cbranch_scc1 .LKF_rQ6_%=\n"
"v_max3_u32 v22, v88, v89, v90\n"
"v_max3_u32 v22, v22, v91, v92\n"
"v_max3_u32 v22, v22, v93, v94\n"
"v_max_u32_e32 v22, v22, v95\n"
"v_cmp_eq_u32_e32 vcc, -1, v22\n"
"s_cbranch_vccnz .LKF_lQ6_%=\n"
"s_branch .LKF_rQ6_%=\n"
".LKF_sQ7_%=:\n"
"s_mov_b32 s91, 0x40000\n"
".LKF_lQ7_%=:\n"
"s_sleep 2\n"
"s_add_u32 s90, s72, 0x800\n"
"buffer_load_dwordx4 v[208:211], v0, s[48:51], s90 offen offset:0 sc1\n"
"buffer_load_dwordx4 v[212:215], v0, s[48:51], s90 offen offset:1024 sc1\n"
"s_waitcnt vmcnt(0)\n"
"s_sub_u32 s91, s91, 1\n"
"s_cbranch_scc1 .LKF_rQ7_%=\n"
"v_max3_u32 v22, v208, v209, v210\n"
"v_max3_u32 v22, v22, v211, v212\n"
"v_max3_u32 v22, v22, v213, v214\n"
"v_max_u32_e32 v22, v22, v215\n"
"v_cmp_eq_u32_e32 vcc, -1, v22\n"
"s_cbranch_vccnz .LKF_lQ7_%=\n"
"s_branch .LKF_rQ7_%=\n"
".LKF_end:\n"
".LK3_bad_%=:\n"
"s_waitcnt vmcnt(0) lgkmcnt(0)\n"
"s_mov_b64 s[38:39], 0\n"
"s_nop 15\n"
"s_mov_b32 s90, s55\n"
"s_mov_b32 s92, 0\n"
"s_lshl_b32 s88, s80, 7\n"
"v_lshrrev_b32_e32 v129, 2, v0\n"
"v_and_b32_e32 v130, 0x7c, v129\n"
"v_add_u32_e32 v129, s88, v130\n"
"v_mov_b32_e32 v81, 0\n"
"v_mov_b32_e32 v82, 0\n"
".LK3_fg_%=:\n"
"buffer_load_dwordx4 v[76:79], v3, s[48:51], s90 offen\n"
"buffer_load_dword v80, v7, s[48:51], s90 offen\n"
"v_mov_b32_e32 v128, 0xff61b1e6\n"
"s_mov_b32 s88, s52\n"
"s_mov_b32 s89, s53\n"
"s_mov_b32 s93, 0\n"
"s_waitcnt vmcnt(0)\n"
"v_mov_b32_e32 v83, v80\n"
".LK3_ft_%=:\n"
"buffer_load_dwordx4 v[152:155], v0, s[48:51], s88 offen offset:0\n"
"buffer_load_dwordx4 v[156:159], v0, s[48:51], s88 offen offset:1024\n"
"buffer_load_dwordx4 v[160:163], v0, s[48:51], s88 offen offset:2048\n"
"buffer_load_dwordx4 v[164:167], v0, s[48:51], s88 offen offset:3072\n"
"buffer_load_dwordx4 v[168:171], v0, s[48:51], s89 offen\n"
"s_waitcnt vmcnt(0)\n"
"v_mfma_f32_32x32x16_bf16 v[24:39], v[152:155], v[76:79], 0\n"
"v_mfma_f32_32x32x8bf16_1k v[24:39], v[168:169], v[80:81], v[24:39]\n"
"s_nop 11\n"
"v_max3_f32 v128, v128, v24, v25\n"
"v_max3_f32 v128, v128, v26, v27\n"
"v_max3_f32 v128, v128, v28, v29\n"
"v_max3_f32 v128, v128, v30, v31\n"
"v_max3_f32 v128, v128, v32, v33\n"
"v_max3_f32 v128, v128, v34, v35\n"
"v_max3_f32 v128, v128, v36, v37\n"
"v_max3_f32 v128, v128, v38, v39\n"
"v_mfma_f32_32x32x16_bf16 v[24:39], v[156:159], v[76:79], 0\n"
"v_mfma_f32_32x32x8bf16_1k v[24:39], v[168:169], v[82:83], v[24:39]\n"
"s_nop 11\n"
"v_max3_f32 v128, v128, v24, v25\n"
"v_max3_f32 v128, v128, v26, v27\n"
"v_max3_f32 v128, v128, v28, v29\n"
"v_max3_f32 v128, v128, v30, v31\n"
"v_max3_f32 v128, v128, v32, v33\n"
"v_max3_f32 v128, v128, v34, v35\n"
"v_max3_f32 v128, v128, v36, v37\n"
"v_max3_f32 v128, v128, v38, v39\n"
"v_mfma_f32_32x32x16_bf16 v[24:39], v[160:163], v[76:79], 0\n"
"v_mfma_f32_32x32x8bf16_1k v[24:39], v[170:171], v[80:81], v[24:39]\n"
"s_nop 11\n"
"v_max3_f32 v128, v128, v24, v25\n"
"v_max3_f32 v128, v128, v26, v27\n"
"v_max3_f32 v128, v128, v28, v29\n"
"v_max3_f32 v128, v128, v30, v31\n"
"v_max3_f32 v128, v128, v32, v33\n"
"v_max3_f32 v128, v128, v34, v35\n"
"v_max3_f32 v128, v128, v36, v37\n"
"v_max3_f32 v128, v128, v38, v39\n"
"v_mfma_f32_32x32x16_bf16 v[24:39], v[164:167], v[76:79], 0\n"
"v_mfma_f32_32x32x8bf16_1k v[24:39], v[170:171], v[82:83], v[24:39]\n"
"s_nop 11\n"
"v_max3_f32 v128, v128, v24, v25\n"
"v_max3_f32 v128, v128, v26, v27\n"
"v_max3_f32 v128, v128, v28, v29\n"
"v_max3_f32 v128, v128, v30, v31\n"
"v_max3_f32 v128, v128, v32, v33\n"
"v_max3_f32 v128, v128, v34, v35\n"
"v_max3_f32 v128, v128, v36, v37\n"
"v_max3_f32 v128, v128, v38, v39\n"
"s_add_u32 s88, s88, 0x1000\n"
"s_add_u32 s89, s89, 0x200\n"
"s_add_u32 s93, s93, 1\n"
"s_cmp_lt_u32 s93, 4\n"
"s_cbranch_scc1 .LK3_ft_%=\n"
"v_mov_b32_e32 v24, v128\n"
"s_nop 1\n"
"v_permlane32_swap_b32_e32 v128, v24\n"
"s_nop 1\n"
"v_max_f32_e32 v128, v128, v24\n"
"ds_write_b32 v129, v128\n"
"v_add_u32_e32 v129, 0x200, v129\n"
"s_add_u32 s90, s90, 0x90\n"
"s_add_u32 s92, s92, 1\n"
"s_cmp_lt_u32 s92, 8\n"
"s_cbranch_scc1 .LK3_fg_%=\n"
"s_lshl_b32 s88, s80, 10\n"
"v_and_b32_e32 v129, 0x200, v0\n"
"v_add3_u32 v131, v129, v130, s88\n"
"s_waitcnt lgkmcnt(0)\n"
"s_barrier\n"
"ds_read_b32 v24, v131 offset:0\n"
"ds_read_b32 v25, v131 offset:128\n"
"ds_read_b32 v26, v131 offset:256\n"
"ds_read_b32 v27, v131 offset:384\n"
"v_lshrrev_b32_e32 v129, 2, v129\n"
"v_add_u32_e32 v130, 0x240010, v3\n"
"v_add_u32_e32 v129, v129, v130\n"
"v_cndmask_b32_e64 v7, v7, v130, s[36:37]\n"
"s_mul_i32 s88, s80, 0x120\n"
"s_add_u32 s88, s88, s55\n"
"s_waitcnt lgkmcnt(0)\n"
"v_max3_f32 v128, v24, v25, v26\n"
"v_max_f32_e32 v128, v128, v27\n"
"v_xor_b32_e32 v128, 0x80000000, v128\n"
"v_and_b32_e32 v24, 0xffff0000, v128\n"
"v_sub_f32_e32 v128, v128, v24\n"
"v_and_b32_e32 v26, 0xffff0000, v128\n"
"v_sub_f32_e32 v128, v128, v26\n"
"v_lshrrev_b32_e32 v24, 16, v24\n"
"v_or_b32_e32 v24, v24, v26\n"
"v_lshrrev_b32_e32 v25, 16, v128\n"
"buffer_store_dwordx2 v[24:25], v129, s[48:51], s88 offen offset:16\n"
"s_waitcnt vmcnt(0)\n"
"s_barrier\n"
"s_branch .LK3_top_%=\n"
".LKF_end_%=:\n"
    : "+{s[86:87]}"(ka_), "+{s85}"(b_)
    : "{s[0:1]}"(__builtin_amdgcn_kernarg_segment_ptr()), "{s2}"(blockIdx.x), "{v0}"(threadIdx.x)
    : "v1", "v2", "v3", "v4", "v5", "v6", "v7", "v8", "v9", "v10", "v11", "v12", "v13", "v14", "v15", "v16", "v17", "v18", "v19", "v20", "v21", "v22", "v23", "v24", "v25", "v26", "v27", "v28", "v29", "v30", "v31", "v32", "v33", "v34", "v35", "v36", "v37", "v38", "v39", "v40", "v41", "v42", "v43", "v44", "v45", "v46", "v47", "v48", "v49", "v50", "v51", "v52", "v53", "v54", "v55", "v56", "v57", "v58", "v59", "v60", "v61", "v62", "v63", "v64", "v65", "v66", "v67", "v68", "v69", "v70", "v71", "v72", "v73", "v74", "v75", "v76", "v77", "v78", "v79", "v80", "v81", "v82", "v83", "v84", "v85", "v86", "v87", "v88", "v89", "v90", "v91", "v92", "v93", "v94", "v95", "v96", "v97", "v98", "v99", "v100", "v101", "v102", "v103", "v104", "v105", "v106", "v107", "v108", "v109", "v110", "v111", "v112", "v113", "v114", "v115", "v116", "v117", "v118", "v119", "v120", "v121", "v122", "v123", "v124", "v125", "v126", "v127", "v128", "v129", "v130", "v131", "v132", "v133", "v134", "v135", "v136", "v137", "v138", "v139", "v140", "v141", "v142", "v143", "v144", "v145", "v146", "v147", "v148", "v149", "v150", "v151", "v152", "v153", "v154", "v155", "v156", "v157", "v158", "v159", "v160", "v161", "v162", "v163", "v164", "v165", "v166", "v167", "v168", "v169", "v170", "v171", "v172", "v173", "v174", "v175", "v176", "v177", "v178", "v179", "v180", "v181", "v182", "v183", "v184", "v185", "v186", "v187", "v188", "v189", "v190", "v191", "v192", "v193", "v194", "v195", "v196", "v197", "v198", "v199", "v200", "v201", "v202", "v203", "v204", "v205", "v206", "v207", "v208", "v209", "v210", "v211", "v212", "v213", "v214", "v215", "v216", "v217", "v218", "v219", "v220", "v221", "v222", "v223", "v224", "v225", "v226", "v227", "v228", "v229", "v230", "v231", "v232", "v233", "v234", "v235", "v236", "v237", "v238", "v239", "v240", "v241", "v242", "v243", "v244", "v245", "v246", "v247", "v248", "v249", "v250", "v251", "v252", "v253", "v254", "v255", "a0", "a1", "a2", "a3", "a4", "a5", "a6", "a7", "a8", "a9", "a10", "a11", "a12", "a13", "a14", "a15", "a16", "a17", "a18", "a19", "a20", "a21", "a22", "a23", "a24", "a25", "a26", "a27", "a28", "a29", "a30", "a31", "a32", "a33", "a34", "a35", "a36", "a37", "a38", "a39", "a40", "a41", "a42", "a43", "a44", "a45", "a46", "a47", "a48", "a49", "a50", "a51", "a52", "a53", "a54", "a55", "a56", "a57", "a58", "a59", "a60", "a61", "a62", "a63", "a64", "a65", "a66", "a67", "a68", "a69", "a70", "a71", "a72", "a73", "a74", "a75", "a76", "a77", "a78", "a79", "a80", "a81", "a82", "a83", "a84", "a85", "a86", "a87", "a88", "a89", "a90", "a91", "a92", "a93", "a94", "a95", "a96", "a97", "a98", "a99", "a100", "a101", "a102", "a103", "a104", "a105", "a106", "a107", "a108", "a109", "a110", "a111", "a112", "a113", "a114", "a115", "a116", "a117", "a118", "a119", "a120", "a121", "a122", "a123", "a124", "a125", "a126", "a127", "a128", "a129", "a130", "a131", "a132", "a133", "a134", "a135", "a136", "a137", "a138", "a139", "a140", "a141", "a142", "a143", "a144", "a145", "a146", "a147", "a148", "a149", "a150", "a151", "a152", "a153", "a154", "a155", "a156", "a157", "a158", "a159", "a160", "a161", "a162", "a163", "a164", "a165", "a166", "a167", "a168", "a169", "a170", "a171", "a172", "a173", "a174", "a175", "a176", "a177", "a178", "a179", "a180", "a181", "a182", "a183", "a184", "a185", "a186", "a187", "a188", "a189", "a190", "a191", "a192", "a193", "a194", "a195", "a196", "a197", "a198", "a199", "a200", "a201", "a202", "a203", "a204", "a205", "a206", "a207", "a208", "a209", "a210", "a211", "a212", "a213", "a214", "a215", "a216", "a217", "a218", "a219", "a220", "a221", "a222", "a223", "a224", "a225", "a226", "a227", "a228", "a229", "a230", "a231", "a232", "a233", "a234", "a235", "a236", "a237", "a238", "a239", "a240", "a241", "a242", "a243", "a244", "a245", "a246", "a247", "a248", "a249", "a250", "a251", "a252", "a253", "a254", "a255", "s14", "s15", "s16", "s17", "s18", "s19", "s20", "s21", "s22", "s23", "s24", "s25", "s26", "s27", "s28", "s29", "s30", "s31", "s32", "s33", "s34", "s35", "s36", "s37", "s38", "s39", "s40", "s41", "s42", "s43", "s44", "s45", "s46", "s47", "s48", "s49", "s50", "s51", "s52", "s53", "s54", "s55", "s56", "s57", "s58", "s59", "s60", "s61", "s62", "s63", "s64", "s65", "s66", "s67", "s68", "s69", "s70", "s71", "s72", "s73", "s74", "s75", "s76", "s77", "s78", "s79", "s80", "s81", "s82", "s83", "s84", "s88", "s89", "s90", "s91", "s92", "s93", "s94", "s95", "vcc", "scc", "memory"
  );
}

extern "C" void kernel_launch(void* const* d_in, const int* in_sizes, int n_in, void* d_out, int out_size, void* d_ws, size_t ws_size, hipStream_t stream) {
  const u16* x = (const u16*)d_in[0]; const u16* wq = (const u16*)d_in[1]; const u16* wk = (const u16*)d_in[2]; const u16* wv = (const u16*)d_in[3]; const u16* wo = (const u16*)d_in[4];
  const u16* wg = (const u16*)d_in[6]; const u16* wu = (const u16*)d_in[7]; const u16* wd = (const u16*)d_in[8]; const u16* c = (const u16*)d_in[9]; const u16* wmod = (const u16*)d_in[10];
  char* ws = (char*)d_ws;
  (void)hipFuncSetAttribute((const void*)k3f, hipFuncAttributeMaxDynamicSharedMemorySize, 163840);
  (void)hipFuncSetAttribute((const void*)k12_qkv, hipFuncAttributeMaxDynamicSharedMemorySize, L1_SIZE);
  k12_qkv<<<256, 512, L1_SIZE, stream>>>(c, wmod, wq, wk, wv, wo, x, wg, wu, wd, ws);
  k3f<<<256, 256, 163840, stream>>>(x, wg, wu, ws, wd, (u16*)d_out);
}
```
